```python
import torch

def _softmax(x, axis=-1):
    return torch.softmax(x, dim=axis)
def _gelu(x):
    return torch.nn.functional.gelu(x, approximate="tanh")
def _silu(x):
    return torch.nn.functional.silu(x)
def _sigmoid(x):
    return torch.sigmoid(x)
def _layernorm(x, axis=-1, eps=1e-5):
    m = x.mean(dim=axis, keepdim=True)
    v = x.var(dim=axis, keepdim=True, unbiased=False)
    return (x - m) / torch.sqrt(v + eps)
def _scatter_add(tgt, idx, src, axis=0):
    return tgt.clone().index_add_(axis, idx, src)
def _segment_sum(src, seg, n):
    out = torch.zeros((n,) + src.shape[1:], dtype=src.dtype)
    return out.index_add_(0, seg, src)
def _pad(x, pw):
    flat = tuple(v for pair in reversed(pw) for v in pair)
    return torch.nn.functional.pad(x, flat)

kernel_name = 'standalone__fft_conv__vision'

def setup_inputs():

    _w = (lambda a: (lambda t: t.to(torch.bfloat16) if t.is_floating_point() else t)(torch.as_tensor(a)))
    H = W = 224
    C = 96
    K = 7
    return {
        "x": _w((lambda *s: torch.randn(*s, dtype=torch.bfloat16) * 0.05)(1, H, W, C)),
        "w": _w((lambda *s: torch.randn(*s, dtype=torch.bfloat16) * 0.05)(K, K, C, C)),
    }

def reference(x, w):
    B, H, W, C = x.shape
    K = w.shape[0]
    wp = _pad(w, ((0, H - K), (0, W - K), (0, 0), (0, 0)))
    Xf = (lambda x, axes=(-2,-1): torch.fft.rfft2(x.float(), dim=axes))(x, axes=(1, 2))
    Wf = (lambda x, axes=(-2,-1): torch.fft.rfft2(x.float(), dim=axes))(wp, axes=(0, 1))
    Yf = torch.einsum('bhwi,hwio->bhwo', Xf, Wf)
    y = (lambda x, s, axes=(-2,-1): torch.fft.irfft2(x, s=s, dim=axes).to(torch.bfloat16))(Yf, (H, W), axes=(1, 2))
    return y
```

```cpp
#include <hip/hip_runtime.h>
#include <hip/hip_bf16.h>


#define K_BODY \
    "s_load_dwordx2 s[16:17], s[0:1], 0x0\n\t" \
    "s_load_dwordx2 s[4:5], s[0:1], 0x10\n\t" \
    "s_load_dwordx2 s[12:13], s[0:1], 0x8\n\t" \
    "s_load_dwordx2 s[8:9], s[0:1], 0x18\n\t" \
    "s_getpc_b64 s[22:23]\n\t" \
    ".Lpc_%=:\n\t" \
    "s_cmp_ge_u32 s2, 16\n\t" \
    "s_cbranch_scc1 .Le1_%=\n\t" \
    "s_mov_b32 m0, 157312\n\t" \
    "v_lshlrev_b32 v8, 6, v0\n\t" \
    "v_min_u32 v8, .Lfin_%=-.Lpc_%=, v8\n\t" \
    "global_load_lds_dword v8, s[22:23]\n\t" \
    ".Le1_%=:\n\t" \
    "v_readfirstlane_b32 s24, v0\n\t" \
    "s_and_b32 s25, s2, 7\n\t" \
    "s_lshl_b32 s25, s25, 5\n\t" \
    "s_lshr_b32 s37, s2, 3\n\t" \
    "s_add_u32 s25, s25, s37\n\t" \
    "s_min_u32 s28, s25, 251\n\t" \
    "s_mul_i32 s26, s28, 57\n\t" \
    "s_lshr_b32 s26, s26, 9\n\t" \
    "s_mul_i32 s37, s26, 9\n\t" \
    "s_sub_u32 s27, s28, s37\n\t" \
    "v_and_b32 v0, 63, v0\n\t" \
    "v_lshlrev_b32 v0, 4, v0\n\t" \
    "s_lshr_b32 s24, s24, 6\n\t" \
    "s_sub_u32 s37, s24, 4\n\t" \
    "s_cbranch_scc1 .Le2_%=\n\t" \
    "s_cmp_ge_u32 s25, 252\n\t" \
    "s_cbranch_scc1 .Lpb_%=\n\t" \
    "s_mul_i32 m0, s37, 12288\n\t" \
    "s_lshl2_add_u32 s37, s26, s37\n\t" \
    "s_mul_i32 s37, s37, 86016\n\t" \
    "s_mul_i32 s38, s27, 4992\n\t" \
    "s_add_u32 s37, s37, s38\n\t" \
    "s_addk_i32 s37, 896\n\t" \
    "v_add_u32 v9, s37, v0\n\t" \
    "s_cmp_eq_u32 s27, 0\n\t" \
    "s_cselect_b32 s38, 43008, 0\n\t" \
    "s_sub_u32 s39, s38, 2048\n\t" \
    "v_add_u32 v7, s39, v9\n\t" \
    "v_subrev_u32 v8, 0x400, v9\n\t" \
    "s_mov_b64 exec, 0xff\n\t" \
    "v_add_u32 v8, s38, v8\n\t" \
    "s_mov_b64 exec, -1\n\t" \
    "s_mov_b32 s18, 9633792\n\t" \
    "s_mov_b32 s19, 0x20000\n\t" \
    "s_mov_b32 s39, 43008\n\t" \
    "s_waitcnt lgkmcnt(0)\n\t" \
    "buffer_load_dwordx4 v7, s[16:19], 0 offen offset:0 nt lds\n\t" \
    "s_add_u32 m0, m0, 0x400\n\t" \
    "s_nop 0\n\t" \
    "buffer_load_dwordx4 v8, s[16:19], 0 offen offset:0 nt lds\n\t" \
    "s_add_u32 m0, m0, 0x400\n\t" \
    "s_nop 0\n\t" \
    "buffer_load_dwordx4 v9, s[16:19], 0 offen offset:0 nt lds\n\t" \
    "buffer_load_dwordx4 v9, s[16:19], 0 offen offset:1024 nt lds\n\t" \
    "buffer_load_dwordx4 v9, s[16:19], 0 offen offset:2048 nt lds\n\t" \
    "buffer_load_dwordx4 v9, s[16:19], 0 offen offset:3072 nt lds\n\t" \
    "s_add_u32 m0, m0, 0x1000\n\t" \
    "s_nop 0\n\t" \
    "buffer_load_dwordx4 v7, s[16:19], s39 offen offset:0 nt lds\n\t" \
    "s_add_u32 m0, m0, 0x400\n\t" \
    "s_nop 0\n\t" \
    "buffer_load_dwordx4 v8, s[16:19], s39 offen offset:0 nt lds\n\t" \
    "s_add_u32 m0, m0, 0x400\n\t" \
    "s_nop 0\n\t" \
    "buffer_load_dwordx4 v9, s[16:19], s39 offen offset:0 nt lds\n\t" \
    "buffer_load_dwordx4 v9, s[16:19], s39 offen offset:1024 nt lds\n\t" \
    "buffer_load_dwordx4 v9, s[16:19], s39 offen offset:2048 nt lds\n\t" \
    "buffer_load_dwordx4 v9, s[16:19], s39 offen offset:3072 nt lds\n\t" \
    ".Le2_%=:\n\t" \
    "s_mov_b32 s6, 34668544\n\t" \
    "s_mov_b32 s7, 0x20000\n\t" \
    "v_lshrrev_b32 v8, 7, v0\n\t" \
    "v_and_b32 v9, 0x70, v0\n\t" \
    "v_lshl_or_b32 v2, v8, 15, v9\n\t" \
    "s_lshl_b32 s32, s2, 7\n\t" \
    "s_add_u32 s32, s32, 33554432\n\t" \
    "s_cmp_ge_u32 s24, 4\n\t" \
    "s_cbranch_scc1 .Le3_%=\n\t" \
    "v_mov_b32 v12, 0\n\t" \
    "v_mov_b32 v13, 0\n\t" \
    "v_mov_b32 v14, 0\n\t" \
    "v_mov_b32 v15, 0\n\t" \
    "s_lshl_b32 s37, s24, 18\n\t" \
    "s_add_u32 s37, s37, s32\n\t" \
    "s_xor_b32 s38, s24, 2\n\t" \
    "s_lshr_b32 s39, s2, 3\n\t" \
    "s_or_b32 s38, s38, s39\n\t" \
    "s_memrealtime s[22:23]\n\t" \
    "s_waitcnt lgkmcnt(0)\n\t" \
    "s_cbranch_scc1 .Le4_%=\n\t" \
    "v_lshl_or_b32 v8, v8, 10, v9\n\t" \
    "v_mov_b32 v16, s22\n\t" \
    "v_mov_b32 v17, s22\n\t" \
    "v_mov_b32 v18, s22\n\t" \
    "v_mov_b32 v19, s22\n\t" \
    "s_add_u32 s38, s32, 1089536\n\t" \
    "buffer_store_dwordx4 v[16:19], v8, s[4:7], s38 offen sc1\n\t" \
    ".Le4_%=:\n\t" \
    "buffer_store_dwordx4 v[12:15], v2, s[4:7], s37 offen sc1\n\t" \
    ".Le3_%=:\n\t" \
    "s_mul_i32 s30, s28, 293\n\t" \
    "s_lshr_b32 s30, s30, 11\n\t" \
    "s_mul_i32 s37, s30, 7\n\t" \
    "s_sub_u32 s28, s28, s37\n\t" \
    "s_mul_i32 s29, s30, 43\n\t" \
    "s_lshr_b32 s29, s29, 7\n\t" \
    "s_mul_i32 s37, s29, 3\n\t" \
    "s_sub_u32 s30, s30, s37\n\t" \
    "s_and_b32 s37, s29, 12\n\t" \
    "s_add_u32 s37, s37, s29\n\t" \
    "s_mul_i32 s31, s37, 768\n\t" \
    "s_lshl_b32 s37, s30, 6\n\t" \
    "s_add_u32 s31, s31, s37\n\t" \
    "s_add_u32 s41, s29, s30\n\t" \
    "s_add_u32 s41, s41, s28\n\t" \
    "s_and_b32 s41, s41, 3\n\t" \
    "s_mov_b32 s14, 903168\n\t" \
    "s_mov_b32 s15, 0x20000\n\t" \
    "v_lshrrev_b32 v10, 6, v0\n\t" \
    "v_lshrrev_b32 v11, 9, v0\n\t" \
    "v_sub_u32 v8, v10, v11\n\t" \
    "v_mul_u32_u24 v8, 18432, v8\n\t" \
    "v_and_b32 v9, 0x30, v0\n\t" \
    "v_lshlrev_b32 v11, 5, v11\n\t" \
    "v_bitop3_b32 v1, v8, v9, v11 bitop3:0xf6\n\t" \
    "s_mov_b32 exec_lo, 0xf0000000\n\t" \
    "s_mov_b32 exec_hi, 0xf0000000\n\t" \
    "v_bfrev_b32 v1, 1\n\t" \
    "s_mov_b64 exec, -1\n\t" \
    "s_cmp_ge_u32 s24, 4\n\t" \
    "s_cbranch_scc1 .Le9_%=\n\t" \
    "v_bfe_u32 v20, v0, 4, 4\n\t" \
    "v_and_b32 v10, 12, v10\n\t" \
    "v_cvt_f32_ubyte0 v21, v10\n\t" \
    "v_and_b32 v22, 1, v20\n\t" \
    "v_cvt_f32_ubyte0 v22, v22\n\t" \
    "v_mul_f32 v22, 0x3e800000, v22\n\t" \
    "v_lshrrev_b32 v23, 1, v20\n\t" \
    "v_mov_b32 v32, 0\n\t" \
    "v_mov_b32 v33, -1\n\t" \
    "v_mov_b32 v31, 0x3e800000\n\t" \
    "s_mov_b32 s65, 0\n\t" \
    "s_mov_b32 s40, 0\n\t" \
    "s_cmp_eq_u32 s24, 1\n\t" \
    "s_cbranch_scc1 .Lm1_%=\n\t" \
    "s_cmp_eq_u32 s24, 2\n\t" \
    "s_cbranch_scc1 .Lm2_%=\n\t" \
    "s_cmp_eq_u32 s24, 3\n\t" \
    "s_cbranch_scc1 .Lm3_%=\n\t" \
    "s_add_u32 s37, s32, 1048576\n\t" \
    "s_mov_b64 exec, 0xff\n\t" \
    "buffer_store_dwordx4 v[12:15], v0, s[4:7], s37 offen sc1\n\t" \
    "s_mov_b64 exec, -1\n\t" \
    "v_or_b32 v24, 1, v20\n\t" \
    "v_cvt_f32_ubyte0 v24, v24\n\t" \
    "v_mul_f32 v24, 0x3c800000, v24\n\t" \
    "v_add_f32 v25, v21, v21\n\t" \
    "v_add_f32 v26, 2.0, v25\n\t" \
    "v_add_f32 v27, 4.0, v25\n\t" \
    "v_add_f32 v28, 4.0, v26\n\t" \
    "v_mov_b32 v29, v22\n\t" \
    "v_mov_b32 v30, v22\n\t" \
    "v_mov_b32 v31, v24\n\t" \
    "s_mov_b32 s64, 0x3e800000\n\t" \
    "s_mov_b32 s65, 0x3e800000\n\t" \
    "v_add_u32 v34, 154880, v0\n\t" \
    "s_branch .Ltl_%=\n\t" \
    ".Lm1_%=:\n\t" \
    "v_lshrrev_b32 v24, 5, v0\n\t" \
    "v_and_b32 v24, 12, v24\n\t" \
    "v_and_or_b32 v24, v20, 3, v24\n\t" \
    "v_cvt_f32_ubyte0 v24, v24\n\t" \
    "v_mul_f32 v24, 0x3d924925, v24\n\t" \
    "v_mul_f32 v32, 0x41000000, v24\n\t" \
    "v_lshrrev_b32 v27, 3, v11\n\t" \
    "v_cvt_f32_ubyte0 v27, v27\n\t" \
    "v_add_f32 v25, -2.0, v27\n\t" \
    "v_add_f32 v26, -1.0, v27\n\t" \
    "v_add_f32 v28, 1.0, v27\n\t" \
    "v_and_b32 v29, 4, v20\n\t" \
    "v_cvt_f32_ubyte0 v29, v29\n\t" \
    "v_mul_f32 v29, 0x3d800000, v29\n\t" \
    "v_mov_b32 v30, v29\n\t" \
    "v_mov_b32 v31, 0xbe800000\n\t" \
    "s_mov_b32 s64, 0\n\t" \
    "s_mov_b32 exec_hi, 0\n\t" \
    "v_mov_b32 v33, 0\n\t" \
    "s_mov_b64 exec, -1\n\t" \
    "v_add_u32 v34, 149760, v0\n\t" \
    "s_branch .Ltl_%=\n\t" \
    ".Lm2_%=:\n\t" \
    "v_add_u32 v24, 6, v20\n\t" \
    "v_cvt_f32_ubyte0 v24, v24\n\t" \
    "v_mul_f32 v24, 0x3d000000, v24\n\t" \
    "v_add_f32 v25, 0.5, v21\n\t" \
    "v_add_f32 v26, 1.0, v25\n\t" \
    "v_add_f32 v27, 2.0, v25\n\t" \
    "v_add_f32 v28, 2.0, v26\n\t" \
    "v_mov_b32 v29, 0\n\t" \
    "v_mov_b32 v30, 0\n\t" \
    "s_mov_b32 s64, 0.5\n\t" \
    "v_add_u32 v34, 152832, v0\n\t" \
    "s_branch .Ltl_%=\n\t" \
    ".Lm3_%=:\n\t" \
    "s_mov_b32 s37, 34635776\n\t" \
    "buffer_load_dword v3, v12, s[4:7], s37 offen sc1\n\t" \
    "v_lshrrev_b32 v8, 7, v0\n\t" \
    "v_mul_u32_u24 v8, 96, v8\n\t" \
    "v_and_b32 v9, 16, v0\n\t" \
    "v_bfe_u32 v10, v0, 5, 2\n\t" \
    "v_add3_u32 v8, v8, v9, v10\n\t" \
    "v_mul_u32_u24 v8, 192, v8\n\t" \
    "s_mul_i32 s37, s28, 129024\n\t" \
    "s_add_u32 s37, s37, s31\n\t" \
    "s_mov_b32 m0, 157312\n\t" \
    "s_mov_b32 exec_hi, 0xffffff\n\t" \
    "buffer_load_dword v8, s[12:15], s37 offen lds\n\t" \
    "s_mov_b64 exec, -1\n\t" \
    "v_lshrrev_b32 v8, 2, v0\n\t" \
    "v_and_b32 v8, 28, v8\n\t" \
    "v_add_u32 v8, 156928, v8\n\t" \
    "ds_write_b32 v8, v12\n\t" \
    "v_add_u32 v24, 6, v23\n\t" \
    "v_cvt_f32_ubyte0 v24, v24\n\t" \
    "v_mul_f32 v24, 0x3d924925, v24\n\t" \
    "v_mov_b32 v25, v21\n\t" \
    "v_add_f32 v26, 1.0, v21\n\t" \
    "v_add_f32 v27, 2.0, v21\n\t" \
    "v_add_f32 v28, 2.0, v26\n\t" \
    "v_sub_f32 v29, 0, v22\n\t" \
    "v_sub_f32 v30, 0, v22\n\t" \
    "s_mov_b32 exec_lo, 0\n\t" \
    "v_fma_f32 v25, v21, 0.5, 4.0\n\t" \
    "v_add_f32 v27, 1.0, v25\n\t" \
    "v_fmaak_f32 v26, v21, v31, 0x41200000\n\t" \
    "v_mov_b32 v28, v26\n\t" \
    "v_add_f32 v30, v31, v30\n\t" \
    "s_mov_b64 exec, -1\n\t" \
    "s_mov_b32 s40, 0x11\n\t" \
    "v_add_u32 v34, 151808, v0\n\t" \
    ".Ltl_%=:\n\t" \
    "v_fma_f32 v36, v24, v25, v29\n\t" \
    "v_fma_f32 v38, v24, v26, v29\n\t" \
    "v_fma_f32 v40, v24, v27, v29\n\t" \
    "v_fma_f32 v42, v24, v28, v30\n\t" \
    "v_add_f32 v37, v36, v31\n\t" \
    "v_add_f32 v39, v38, v31\n\t" \
    "v_add_f32 v41, v40, v31\n\t" \
    "v_add_f32 v43, v42, v31\n\t" \
    "v_cos_f32 v36, v36\n\t" \
    "v_cos_f32 v37, v37\n\t" \
    "v_cos_f32 v38, v38\n\t" \
    "v_cos_f32 v39, v39\n\t" \
    "v_cos_f32 v40, v40\n\t" \
    "v_cos_f32 v41, v41\n\t" \
    "v_cos_f32 v42, v42\n\t" \
    "v_cos_f32 v43, v43\n\t" \
    "s_bitcmp0_b32 s40, 4\n\t" \
    "s_cbranch_scc1 .Lt2_%=\n\t" \
    "v_mul_f32 v36, 0x3b924925, v36\n\t" \
    "v_mul_f32 v37, 0x3b924925, v37\n\t" \
    "v_mul_f32 v38, 0x3b924925, v38\n\t" \
    "v_mul_f32 v39, 0x3b924925, v39\n\t" \
    "v_mul_f32 v40, 0x3b924925, v40\n\t" \
    "v_mul_f32 v41, 0x3b924925, v41\n\t" \
    "v_mul_f32 v42, 0x3b924925, v42\n\t" \
    "v_mul_f32 v43, 0x3b924925, v43\n\t" \
    ".Lt2_%=:\n\t" \
    "v_cvt_pk_bf16_f32 v44, v36, v37\n\t" \
    "v_cvt_pk_bf16_f32 v45, v38, v39\n\t" \
    "v_cvt_pk_bf16_f32 v46, v40, v41\n\t" \
    "v_cvt_pk_bf16_f32 v47, v42, v43\n\t" \
    "v_and_b32 v44, v33, v44\n\t" \
    "v_and_b32 v45, v33, v45\n\t" \
    "v_mov_b32 v33, -1\n\t" \
    "v_add_f32 v24, s64, v24\n\t" \
    "v_add_f32 v31, s65, v31\n\t" \
    "v_add_f32 v29, v32, v29\n\t" \
    "v_add_f32 v30, v32, v30\n\t" \
    "ds_write_b128 v34, v[44:47]\n\t" \
    "s_add_u32 s40, s40, 1\n\t" \
    "s_bitcmp1_b32 s40, 0\n\t" \
    "v_add_u32 v34, 0x400, v34\n\t" \
    "s_cbranch_scc1 .Ltl_%=\n\t" \
    "s_bitcmp1_b32 s40, 2\n\t" \
    "s_cbranch_scc1 .Lt3_%=\n\t" \
    "s_mov_b32 s40, 2\n\t" \
    "s_lshl2_add_u32 s37, s28, s24\n\t" \
    "s_lshl_b32 s37, s37, 2\n\t" \
    "v_lshrrev_b32 v25, 2, v20\n\t" \
    "v_add_u32 v25, s37, v25\n\t" \
    "v_mul_u32_u24 v26, 147, v25\n\t" \
    "v_lshrrev_b32 v26, 10, v26\n\t" \
    "v_lshl_or_b32 v24, v26, 1, 1\n\t" \
    "v_cvt_f32_ubyte0 v24, v24\n\t" \
    "v_mul_f32 v24, 0x3c800000, v24\n\t" \
    "v_mul_u32_u24 v26, 7, v26\n\t" \
    "v_sub_u32 v25, v25, v26\n\t" \
    "v_xor_b32 v22, s41, v20\n\t" \
    "v_bfe_u32 v32, v22, 1, 1\n\t" \
    "v_and_b32 v22, 1, v22\n\t" \
    "v_cvt_f32_ubyte0 v22, v22\n\t" \
    "v_mul_f32 v22, 0x3e800000, v22\n\t" \
    "v_lshl_or_b32 v32, v25, 1, v32\n\t" \
    "v_cvt_f32_ubyte0 v32, v32\n\t" \
    "v_mul_f32 v32, 0x3e924925, v32\n\t" \
    "v_mul_f32 v29, 0x3d800000, v21\n\t" \
    "v_fma_f32 v29, v29, v32, v22\n\t" \
    "v_mov_b32 v30, v29\n\t" \
    "v_mov_b32 v31, v24\n\t" \
    "v_mov_b32 v25, 0\n\t" \
    "v_mov_b32 v26, 2.0\n\t" \
    "v_mov_b32 v27, 4.0\n\t" \
    "v_mov_b32 v28, 0x40c00000\n\t" \
    "s_mov_b32 s64, 0\n\t" \
    "s_mov_b32 s65, 0\n\t" \
    "s_lshl_b32 s37, s24, 11\n\t" \
    "s_add_u32 s37, s37, 49152\n\t" \
    "v_add_u32 v34, s37, v0\n\t" \
    "s_branch .Ltl_%=\n\t" \
    ".Lt3_%=:\n\t" \
    "s_cmp_lg_u32 s24, 3\n\t" \
    "s_cbranch_scc1 .Le9_%=\n\t" \
    "s_and_b32 s37, s2, 7\n\t" \
    "s_lshl_b32 s37, s37, 10\n\t" \
    "s_add_u32 s37, s37, 34643968\n\t" \
    "s_add_u32 s22, s4, s37\n\t" \
    "s_addc_u32 s23, s5, 0\n\t" \
    "s_load_dword s56, s[22:23], 0x0\n\t" \
    "s_load_dword s57, s[22:23], 0x80\n\t" \
    "s_load_dword s58, s[22:23], 0x100\n\t" \
    "s_load_dword s59, s[22:23], 0x180\n\t" \
    "s_load_dword s60, s[22:23], 0x200\n\t" \
    "s_load_dword s61, s[22:23], 0x280\n\t" \
    "s_load_dword s62, s[22:23], 0x300\n\t" \
    "s_load_dword s63, s[22:23], 0x380\n\t" \
    ".Le9_%=:\n\t" \
    "s_lshr_b32 s37, s24, 2\n\t" \
    "s_and_b32 s38, s24, 2\n\t" \
    "s_mul_i32 s39, s37, 24576\n\t" \
    "s_lshl4_add_u32 s39, s38, s39\n\t" \
    "v_lshrrev_b32 v4, 6, v0\n\t" \
    "v_and_b32 v5, 12, v4\n\t" \
    "v_add_u32 v6, v4, v5\n\t" \
    "v_lshrrev_b32 v7, 1, v0\n\t" \
    "v_and_b32 v7, 24, v7\n\t" \
    "v_mul_u32_u24 v4, 192, v6\n\t" \
    "v_add3_u32 v8, v4, v7, s39\n\t" \
    "v_mov_b32 v9, 156928\n\t" \
    "v_add_u32 v10, 149760, v0\n\t" \
    "s_lshl2_add_u32 s37, s37, s38\n\t" \
    "s_lshl_b32 s37, s37, 7\n\t" \
    "s_mul_i32 s53, s27, 28\n\t" \
    "s_add_u32 s53, s53, s26\n\t" \
    "s_andn2_b32 s39, s53, 7\n\t" \
    "s_add_u32 s53, s53, s39\n\t" \
    "s_lshl_b32 s53, s53, 10\n\t" \
    "s_add_u32 s37, s37, s53\n\t" \
    "s_and_b32 s38, s24, 1\n\t" \
    "s_lshl_b32 s39, s38, 13\n\t" \
    "s_add_u32 s37, s37, s39\n\t" \
    "s_cmp_lt_u32 s25, 252\n\t" \
    "s_cselect_b32 s43, 0, 0x80000000\n\t" \
    "s_add_u32 s37, s37, s43\n\t" \
    "v_lshrrev_b32 v11, 8, v0\n\t" \
    "v_and_b32 v12, 0xf0, v0\n\t" \
    "v_mul_u32_u24 v4, 8486912, v11\n\t" \
    "v_add3_u32 v13, v4, v12, s37\n\t" \
    "s_lshl_b32 s39, s38, 10\n\t" \
    "v_add_u32 v14, s39, v10\n\t" \
    "s_lshl_b32 s39, s38, 5\n\t" \
    "s_add_u32 s39, s39, 116736\n\t" \
    "v_lshl_add_u32 v4, v6, 6, v7\n\t" \
    "v_lshrrev_b32 v5, 3, v0\n\t" \
    "v_and_b32 v5, 32, v5\n\t" \
    "v_xor_b32 v5, s39, v5\n\t" \
    "v_add_u32 v15, v4, v5\n\t" \
    "s_cmp_lg_u32 s24, 3\n\t" \
    "s_cbranch_scc1 .Lr9_%=\n\t" \
    "s_mov_b32 s40, 0\n\t" \
    ".Lr1_%=:\n\t" \
    "s_waitcnt lgkmcnt(0)\n\t" \
    "s_memrealtime s[22:23]\n\t" \
    "s_waitcnt lgkmcnt(0)\n\t" \
    "s_mov_b32 s37, 1000\n\t" \
    "s_mov_b32 s38, 0\n\t" \
    "s_sub_u32 s39, s22, s56\n\t" \
    "s_min_u32 s37, s37, s39\n\t" \
    "s_max_u32 s38, s38, s39\n\t" \
    "s_sub_u32 s39, s22, s57\n\t" \
    "s_min_u32 s37, s37, s39\n\t" \
    "s_max_u32 s38, s38, s39\n\t" \
    "s_sub_u32 s39, s22, s58\n\t" \
    "s_min_u32 s37, s37, s39\n\t" \
    "s_max_u32 s38, s38, s39\n\t" \
    "s_sub_u32 s39, s22, s59\n\t" \
    "s_min_u32 s37, s37, s39\n\t" \
    "s_max_u32 s38, s38, s39\n\t" \
    "s_sub_u32 s39, s22, s60\n\t" \
    "s_min_u32 s37, s37, s39\n\t" \
    "s_max_u32 s38, s38, s39\n\t" \
    "s_sub_u32 s39, s22, s61\n\t" \
    "s_min_u32 s37, s37, s39\n\t" \
    "s_max_u32 s38, s38, s39\n\t" \
    "s_sub_u32 s39, s22, s62\n\t" \
    "s_min_u32 s37, s37, s39\n\t" \
    "s_max_u32 s38, s38, s39\n\t" \
    "s_sub_u32 s39, s22, s63\n\t" \
    "s_min_u32 s37, s37, s39\n\t" \
    "s_max_u32 s38, s38, s39\n\t" \
    "s_cmp_gt_u32 s38, 1000\n\t" \
    "s_cbranch_scc1 .Lr2_%=\n\t" \
    ".Lr3_%=:\n\t" \
    "s_sub_u32 s35, s22, s37\n\t" \
    "s_waitcnt vmcnt(1)\n\t" \
    "v_readfirstlane_b32 s34, v3\n\t" \
    "s_mul_i32 s34, s34, 1664525\n\t" \
    "s_add_u32 s34, s34, 1013904223\n\t" \
    "s_or_b32 s34, s34, 1\n\t" \
    "v_mov_b32 v20, s34\n\t" \
    "v_mov_b32 v21, s35\n\t" \
    "ds_write_b64 v9, v[20:21] offset:32\n\t" \
    ".Lr9_%=:\n\t" \
    "s_waitcnt vmcnt(0)\n\t" \
    "s_waitcnt lgkmcnt(0)\n\t" \
    "s_barrier\n\t" \
    "ds_read_b128 v[24:27], v14 offset:5120\n\t" \
    "ds_read_b64_tr_b16 v[28:29], v8 offset:0\n\t" \
    "ds_read_b64_tr_b16 v[30:31], v8 offset:768\n\t" \
    "ds_read_b64_tr_b16 v[32:33], v8 offset:12288\n\t" \
    "ds_read_b64_tr_b16 v[34:35], v8 offset:13056\n\t" \
    "ds_read_b64_tr_b16 v[36:37], v8 offset:6144\n\t" \
    "ds_read_b64_tr_b16 v[38:39], v8 offset:6912\n\t" \
    "ds_read_b64_tr_b16 v[40:41], v8 offset:18432\n\t" \
    "ds_read_b64_tr_b16 v[42:43], v8 offset:19200\n\t" \
    "ds_read_b128 v[44:47], v9 offset:32\n\t" \
    "ds_read_b64_tr_b16 v[48:49], v8 offset:64\n\t" \
    "ds_read_b64_tr_b16 v[50:51], v8 offset:832\n\t" \
    "ds_read_b64_tr_b16 v[52:53], v8 offset:12352\n\t" \
    "ds_read_b64_tr_b16 v[54:55], v8 offset:13120\n\t" \
    "s_and_b32 s37, s24, 1\n\t" \
    "s_lshl_b32 s37, s37, 4\n\t" \
    "s_lshr_b32 s39, s24, 1\n\t" \
    "s_add_u32 s37, s37, s39\n\t" \
    "s_mul_i32 s37, s37, 192\n\t" \
    "s_add_u32 s37, s37, s31\n\t" \
    "s_lshl_b32 s39, s24, 12\n\t" \
    "s_add_u32 m0, s39, 116736\n\t" \
    "s_add_u32 s38, s37, 0\n\t" \
    "buffer_load_dwordx4 v1, s[12:15], s38 offen lds\n\t" \
    "s_add_u32 m0, s39, 117760\n\t" \
    "s_add_u32 s38, s37, 258048\n\t" \
    "buffer_load_dwordx4 v1, s[12:15], s38 offen lds\n\t" \
    "s_add_u32 m0, s39, 118784\n\t" \
    "s_add_u32 s38, s37, 516096\n\t" \
    "buffer_load_dwordx4 v1, s[12:15], s38 offen lds\n\t" \
    "s_add_u32 m0, s39, 119808\n\t" \
    "s_add_u32 s38, s37, 774144\n\t" \
    "buffer_load_dwordx4 v1, s[12:15], s38 offen lds\n\t" \
    "s_waitcnt lgkmcnt(11)\n\t" \
    "v_mfma_f32_16x16x32_bf16 v[56:59], v[24:27], v[28:31], 0\n\t" \
    "s_waitcnt lgkmcnt(9)\n\t" \
    "v_mfma_f32_16x16x32_bf16 v[60:63], v[24:27], v[32:35], 0\n\t" \
    "s_waitcnt lgkmcnt(7)\n\t" \
    "v_mfma_f32_16x16x32_bf16 v[64:67], v[24:27], v[36:39], 0\n\t" \
    "s_waitcnt lgkmcnt(5)\n\t" \
    "v_mfma_f32_16x16x32_bf16 v[68:71], v[24:27], v[40:43], 0\n\t" \
    "ds_read_b64_tr_b16 v[72:73], v8 offset:6208\n\t" \
    "ds_read_b64_tr_b16 v[74:75], v8 offset:6976\n\t" \
    "ds_read_b64_tr_b16 v[76:77], v8 offset:18496\n\t" \
    "ds_read_b64_tr_b16 v[78:79], v8 offset:19264\n\t" \
    "s_waitcnt lgkmcnt(8)\n\t" \
    "v_readfirstlane_b32 s34, v44\n\t" \
    "v_readfirstlane_b32 s35, v45\n\t" \
    "s_lshl_b32 s37, s24, 8\n\t" \
    "s_add_u32 s37, s37, s2\n\t" \
    "s_cmp_eq_u32 s37, 255\n\t" \
    "s_cbranch_scc1 .Lpp_%=\n\t" \
    ".Lpq_%=:\n\t" \
    "v_add_u32 v56, 0x8000, v56\n\t" \
    "v_add_u32 v58, 0x8000, v58\n\t" \
    "v_add_u32 v60, 0x8000, v60\n\t" \
    "v_add_u32 v62, 0x8000, v62\n\t" \
    "v_cvt_pk_bf16_f32 v88, v56, v57\n\t" \
    "v_cvt_pk_bf16_f32 v90, v60, v61\n\t" \
    "v_cvt_pk_bf16_f32 v89, v64, v65\n\t" \
    "v_cvt_pk_bf16_f32 v91, v68, v69\n\t" \
    "v_and_b32 v88, -2, v88\n\t" \
    "v_and_b32 v90, -2, v90\n\t" \
    "v_cvt_pk_bf16_f32 v92, v58, v59\n\t" \
    "v_cvt_pk_bf16_f32 v94, v62, v63\n\t" \
    "v_cvt_pk_bf16_f32 v93, v66, v67\n\t" \
    "v_cvt_pk_bf16_f32 v95, v70, v71\n\t" \
    "v_and_b32 v92, -2, v92\n\t" \
    "v_and_b32 v94, -2, v94\n\t" \
    "ds_read_b64_tr_b16 v[96:97], v8 offset:128\n\t" \
    "ds_read_b64_tr_b16 v[98:99], v8 offset:896\n\t" \
    "ds_read_b64_tr_b16 v[100:101], v8 offset:12416\n\t" \
    "ds_read_b64_tr_b16 v[102:103], v8 offset:13184\n\t" \
    "ds_read_b64_tr_b16 v[104:105], v8 offset:6272\n\t" \
    "ds_read_b64_tr_b16 v[106:107], v8 offset:7040\n\t" \
    "ds_read_b64_tr_b16 v[108:109], v8 offset:18560\n\t" \
    "ds_read_b64_tr_b16 v[110:111], v8 offset:19328\n\t" \
    ".Lh25_%=:\n\t" \
    "s_memrealtime s[22:23]\n\t" \
    "s_waitcnt lgkmcnt(0)\n\t" \
    "s_sub_u32 s22, s22, s35\n\t" \
    "s_cmp_lt_u32 s22, 250\n\t" \
    "s_cbranch_scc1 .Lh25_%=\n\t" \
    "buffer_store_dwordx4 v[88:91], v13, s[4:7], 0 offen offset:0 sc1\n\t" \
    "s_mov_b32 s42, 4243456\n\t" \
    "buffer_store_dwordx4 v[92:95], v13, s[4:7], s42 offen offset:0 sc1\n\t" \
    "v_mfma_f32_16x16x32_bf16 v[112:115], v[24:27], v[48:51], 0\n\t" \
    "v_mfma_f32_16x16x32_bf16 v[116:119], v[24:27], v[52:55], 0\n\t" \
    "v_mfma_f32_16x16x32_bf16 v[120:123], v[24:27], v[72:75], 0\n\t" \
    "v_mfma_f32_16x16x32_bf16 v[124:127], v[24:27], v[76:79], 0\n\t" \
    "v_mfma_f32_16x16x32_bf16 v[128:131], v[24:27], v[96:99], 0\n\t" \
    "v_mfma_f32_16x16x32_bf16 v[132:135], v[24:27], v[100:103], 0\n\t" \
    "v_mfma_f32_16x16x32_bf16 v[136:139], v[24:27], v[104:107], 0\n\t" \
    "v_mfma_f32_16x16x32_bf16 v[140:143], v[24:27], v[108:111], 0\n\t" \
    "s_and_b32 s37, s24, 6\n\t" \
    "s_lshl_b32 s37, s37, 10\n\t" \
    "v_add_u32 v3, s37, v0\n\t" \
    "ds_read_b128 v[144:147], v3 offset:49152\n\t" \
    "ds_read_b128 v[148:151], v3 offset:50176\n\t" \
    "v_add_u32 v112, 0x8000, v112\n\t" \
    "v_add_u32 v114, 0x8000, v114\n\t" \
    "v_add_u32 v116, 0x8000, v116\n\t" \
    "v_add_u32 v118, 0x8000, v118\n\t" \
    "v_cvt_pk_bf16_f32 v152, v112, v113\n\t" \
    "v_cvt_pk_bf16_f32 v154, v116, v117\n\t" \
    "v_cvt_pk_bf16_f32 v153, v120, v121\n\t" \
    "v_cvt_pk_bf16_f32 v155, v124, v125\n\t" \
    "v_and_b32 v152, -2, v152\n\t" \
    "v_and_b32 v154, -2, v154\n\t" \
    "v_cvt_pk_bf16_f32 v156, v114, v115\n\t" \
    "v_cvt_pk_bf16_f32 v158, v118, v119\n\t" \
    "v_cvt_pk_bf16_f32 v157, v122, v123\n\t" \
    "v_cvt_pk_bf16_f32 v159, v126, v127\n\t" \
    "v_and_b32 v156, -2, v156\n\t" \
    "v_and_b32 v158, -2, v158\n\t" \
    "v_add_u32 v128, 0x8000, v128\n\t" \
    "v_add_u32 v130, 0x8000, v130\n\t" \
    "v_add_u32 v132, 0x8000, v132\n\t" \
    "v_add_u32 v134, 0x8000, v134\n\t" \
    "v_cvt_pk_bf16_f32 v160, v128, v129\n\t" \
    "v_cvt_pk_bf16_f32 v162, v132, v133\n\t" \
    "v_cvt_pk_bf16_f32 v161, v136, v137\n\t" \
    "v_cvt_pk_bf16_f32 v163, v140, v141\n\t" \
    "v_and_b32 v160, -2, v160\n\t" \
    "v_and_b32 v162, -2, v162\n\t" \
    "v_cvt_pk_bf16_f32 v164, v130, v131\n\t" \
    "v_cvt_pk_bf16_f32 v166, v134, v135\n\t" \
    "v_cvt_pk_bf16_f32 v165, v138, v139\n\t" \
    "v_cvt_pk_bf16_f32 v167, v142, v143\n\t" \
    "v_and_b32 v164, -2, v164\n\t" \
    "v_and_b32 v166, -2, v166\n\t" \
    "v_mov_b32 v168, s34\n\t" \
    "v_mov_b32 v169, s34\n\t" \
    "v_mov_b32 v170, s34\n\t" \
    "v_mov_b32 v171, s34\n\t" \
    ".Lh26_%=:\n\t" \
    "s_memrealtime s[22:23]\n\t" \
    "s_waitcnt lgkmcnt(0)\n\t" \
    "s_sub_u32 s22, s22, s35\n\t" \
    "s_cmp_lt_u32 s22, 305\n\t" \
    "s_cbranch_scc1 .Lh26_%=\n\t" \
    "s_mov_b32 s42, 524288\n\t" \
    "buffer_store_dwordx4 v[152:155], v13, s[4:7], s42 offen offset:0 sc1\n\t" \
    "s_mov_b32 s42, 4767744\n\t" \
    "buffer_store_dwordx4 v[156:159], v13, s[4:7], s42 offen offset:0 sc1\n\t" \
    "s_waitcnt vmcnt(4)\n\t" \
    "; wait\n\t" \
    "s_waitcnt lgkmcnt(0)\n\t" \
    "s_barrier\n\t" \
    "ds_read_b64_tr_b16 v[172:173], v15 offset:0\n\t" \
    "ds_read_b64_tr_b16 v[174:175], v15 offset:256\n\t" \
    "ds_read_b64_tr_b16 v[176:177], v15 offset:2048\n\t" \
    "ds_read_b64_tr_b16 v[178:179], v15 offset:2304\n\t" \
    "ds_read_b64_tr_b16 v[180:181], v15 offset:4096\n\t" \
    "ds_read_b64_tr_b16 v[182:183], v15 offset:4352\n\t" \
    "ds_read_b64_tr_b16 v[184:185], v15 offset:6144\n\t" \
    "ds_read_b64_tr_b16 v[186:187], v15 offset:6400\n\t" \
    "ds_read_b64_tr_b16 v[188:189], v15 offset:8192\n\t" \
    "ds_read_b64_tr_b16 v[190:191], v15 offset:8448\n\t" \
    "ds_read_b64_tr_b16 v[4:5], v15 offset:10240\n\t" \
    "ds_read_b64_tr_b16 v[6:7], v15 offset:10496\n\t" \
    "ds_read_b64_tr_b16 v[16:17], v15 offset:12288\n\t" \
    "ds_read_b64_tr_b16 v[18:19], v15 offset:12544\n\t" \
    "s_waitcnt lgkmcnt(12)\n\t" \
    "v_mfma_f32_16x16x32_bf16 v[20:23], v[144:147], v[172:175], 0\n\t" \
    "ds_read_b64_tr_b16 v[28:29], v15 offset:14336\n\t" \
    "ds_read_b64_tr_b16 v[30:31], v15 offset:14592\n\t" \
    "s_waitcnt lgkmcnt(12)\n\t" \
    "v_mfma_f32_16x16x32_bf16 v[20:23], v[148:151], v[176:179], v[20:23]\n\t" \
    "ds_read_b64_tr_b16 v[32:33], v15 offset:16384\n\t" \
    "ds_read_b64_tr_b16 v[34:35], v15 offset:16640\n\t" \
    "s_waitcnt lgkmcnt(12)\n\t" \
    "v_mfma_f32_16x16x32_bf16 v[36:39], v[144:147], v[180:183], 0\n\t" \
    "ds_read_b64_tr_b16 v[40:41], v15 offset:18432\n\t" \
    "ds_read_b64_tr_b16 v[42:43], v15 offset:18688\n\t" \
    "s_waitcnt lgkmcnt(12)\n\t" \
    "v_mfma_f32_16x16x32_bf16 v[36:39], v[148:151], v[184:187], v[36:39]\n\t" \
    "ds_read_b64_tr_b16 v[44:45], v15 offset:20480\n\t" \
    "ds_read_b64_tr_b16 v[46:47], v15 offset:20736\n\t" \
    "s_waitcnt lgkmcnt(12)\n\t" \
    "v_mfma_f32_16x16x32_bf16 v[80:83], v[144:147], v[188:191], 0\n\t" \
    "ds_read_b64_tr_b16 v[84:85], v15 offset:22528\n\t" \
    "ds_read_b64_tr_b16 v[86:87], v15 offset:22784\n\t" \
    "s_waitcnt lgkmcnt(12)\n\t" \
    "v_mfma_f32_16x16x32_bf16 v[80:83], v[148:151], v[4:7], v[80:83]\n\t" \
    "ds_read_b64_tr_b16 v[56:57], v15 offset:24576\n\t" \
    "ds_read_b64_tr_b16 v[58:59], v15 offset:24832\n\t" \
    "s_waitcnt lgkmcnt(12)\n\t" \
    "v_mfma_f32_16x16x32_bf16 v[60:63], v[144:147], v[16:19], 0\n\t" \
    "ds_read_b64_tr_b16 v[64:65], v15 offset:26624\n\t" \
    "ds_read_b64_tr_b16 v[66:67], v15 offset:26880\n\t" \
    "s_waitcnt lgkmcnt(12)\n\t" \
    "v_mfma_f32_16x16x32_bf16 v[60:63], v[148:151], v[28:31], v[60:63]\n\t" \
    "ds_read_b64_tr_b16 v[68:69], v15 offset:28672\n\t" \
    "ds_read_b64_tr_b16 v[70:71], v15 offset:28928\n\t" \
    ".Lh27_%=:\n\t" \
    "s_memrealtime s[22:23]\n\t" \
    "s_waitcnt lgkmcnt(0)\n\t" \
    "s_sub_u32 s22, s22, s35\n\t" \
    "s_cmp_lt_u32 s22, 360\n\t" \
    "s_cbranch_scc1 .Lh27_%=\n\t" \
    "s_mov_b32 s42, 1048576\n\t" \
    "buffer_store_dwordx4 v[160:163], v13, s[4:7], s42 offen offset:0 sc1\n\t" \
    "s_mov_b32 s42, 5292032\n\t" \
    "buffer_store_dwordx4 v[164:167], v13, s[4:7], s42 offen offset:0 sc1\n\t" \
    "s_and_b32 s52, s2, 7\n\t" \
    "s_lshl_b32 s52, s52, 7\n\t" \
    "s_add_u32 s52, s52, 34636800\n\t" \
    "s_buffer_load_dword s36, s[4:7], s52\n\t" \
    "v_mfma_f32_16x16x32_bf16 v[88:91], v[144:147], v[32:35], 0\n\t" \
    "ds_read_b64_tr_b16 v[92:93], v15 offset:30720\n\t" \
    "ds_read_b64_tr_b16 v[94:95], v15 offset:30976\n\t" \
    "v_mfma_f32_16x16x32_bf16 v[88:91], v[148:151], v[40:43], v[88:91]\n\t" \
    "v_cvt_pk_bf16_f32 v48, v20, v36\n\t" \
    "v_cvt_pk_bf16_f32 v52, v21, v37\n\t" \
    "v_mfma_f32_16x16x32_bf16 v[72:75], v[144:147], v[44:47], 0\n\t" \
    "v_cvt_pk_bf16_f32 v76, v22, v38\n\t" \
    "v_cvt_pk_bf16_f32 v96, v23, v39\n\t" \
    "v_mfma_f32_16x16x32_bf16 v[72:75], v[148:151], v[84:87], v[72:75]\n\t" \
    "v_cvt_pk_bf16_f32 v49, v80, v60\n\t" \
    "v_cvt_pk_bf16_f32 v53, v81, v61\n\t" \
    "v_mfma_f32_16x16x32_bf16 v[100:103], v[144:147], v[56:59], 0\n\t" \
    "v_cvt_pk_bf16_f32 v77, v82, v62\n\t" \
    "v_cvt_pk_bf16_f32 v97, v83, v63\n\t" \
    "v_mfma_f32_16x16x32_bf16 v[100:103], v[148:151], v[64:67], v[100:103]\n\t" \
    "v_mfma_f32_16x16x32_bf16 v[104:107], v[144:147], v[68:71], 0\n\t" \
    "s_waitcnt lgkmcnt(0)\n\t" \
    "v_mfma_f32_16x16x32_bf16 v[104:107], v[148:151], v[92:95], v[104:107]\n\t" \
    "v_cvt_pk_bf16_f32 v50, v88, v72\n\t" \
    "v_cvt_pk_bf16_f32 v54, v89, v73\n\t" \
    "v_cvt_pk_bf16_f32 v78, v90, v74\n\t" \
    "v_cvt_pk_bf16_f32 v98, v91, v75\n\t" \
    "s_nop 3\n\t" \
    "v_cvt_pk_bf16_f32 v51, v100, v104\n\t" \
    "v_cvt_pk_bf16_f32 v55, v101, v105\n\t" \
    "v_cvt_pk_bf16_f32 v79, v102, v106\n\t" \
    "v_cvt_pk_bf16_f32 v99, v103, v107\n\t" \
    "s_lshr_b32 s37, s29, 2\n\t" \
    "s_mul_i32 s39, s30, 3\n\t" \
    "s_add_u32 s37, s37, s39\n\t" \
    "s_mul_i32 s37, s37, 114688\n\t" \
    "s_and_b32 s39, s29, 3\n\t" \
    "s_lshl_b32 s39, s39, 8\n\t" \
    "s_add_u32 s37, s37, s39\n\t" \
    "s_add_u32 s37, s37, s43\n\t" \
    "s_add_u32 s37, s37, 2097152\n\t" \
    "s_and_b32 s39, s24, 6\n\t" \
    "s_lshl3_add_u32 s39, s28, s39\n\t" \
    "s_lshl_b32 s39, s39, 1\n\t" \
    "v_add_u32 v14, s39, v11\n\t" \
    "v_mul_u32_u24 v1, 147, v14\n\t" \
    "v_lshrrev_b32 v1, 10, v1\n\t" \
    "s_and_b32 s39, s24, 1\n\t" \
    "v_subrev_u32 v8, s39, v1\n\t" \
    "v_mul_i32_i24 v8, 7, v8\n\t" \
    "v_sub_u32 v14, v14, v8\n\t" \
    "v_and_b32 v8, 14, v14\n\t" \
    "v_add_lshl_u32 v14, v14, v8, 12\n\t" \
    "v_and_b32 v8, 7, v1\n\t" \
    "v_mul_u32_u24 v8, 4243456, v8\n\t" \
    "v_add3_u32 v14, v14, v8, v12\n\t" \
    "v_and_b32 v1, 8, v1\n\t" \
    "v_xor_b32 v1, 8, v1\n\t" \
    "v_lshl_add_u32 v14, v1, 10, v14\n\t" \
    "v_add_u32 v3, s37, v14\n\t" \
    "s_lshl_b32 s43, s41, 10\n\t" \
    "s_cmp_lt_u32 s30, 3\n\t" \
    "s_cselect_b64 s[20:21], -1, 0\n\t" \
    "s_cmp_eq_u32 s24, 0\n\t" \
    "s_cbranch_scc1 .Lj29_%=\n\t" \
    ".Lh28_%=:\n\t" \
    "s_memrealtime s[22:23]\n\t" \
    "s_waitcnt lgkmcnt(0)\n\t" \
    "s_sub_u32 s22, s22, s35\n\t" \
    "s_cmp_lt_u32 s22, 420\n\t" \
    "s_cbranch_scc1 .Lh28_%=\n\t" \
    ".Lj29_%=:\n\t" \
    "s_waitcnt lgkmcnt(0)\n\t" \
    "s_xor_b32 s42, s43, 0\n\t" \
    "buffer_store_dwordx4 v[48:51], v3, s[4:7], s42 offen offset:0 sc1\n\t" \
    "s_xor_b32 s42, s43, 1024\n\t" \
    "buffer_store_dwordx4 v[52:55], v3, s[4:7], s42 offen offset:0 sc1\n\t" \
    "s_mov_b32 s40, 0\n\t" \
    "s_waitcnt lgkmcnt(0)\n\t" \
    ".Lr4_%=:\n\t" \
    "s_xor_b32 s36, s36, s34\n\t" \
    "s_andn2_b32 s37, s36, 14\n\t" \
    "s_cbranch_scc1 .Lr5_%=\n\t" \
    ".Lr6_%=:\n\t" \
    "s_bfe_u32 s36, s36, 0x30001\n\t" \
    "s_and_b32 s33, s2, 7\n\t" \
    "s_add_u32 s56, s2, s36\n\t" \
    "s_and_b32 s56, s56, 7\n\t" \
    "s_lshr_b32 s37, s2, 7\n\t" \
    "s_lshl1_add_u32 s56, s56, s37\n\t" \
    "s_lshl_b32 s33, s33, 15\n\t" \
    "s_add_u32 s33, s33, 33554432\n\t" \
    "s_and_b32 s37, s2, 0x70\n\t" \
    "s_lshl_b32 s37, s37, 1\n\t" \
    "s_lshl2_add_u32 s57, s24, s37\n\t" \
    "s_min_u32 s58, s57, 248\n\t" \
    "s_mul_i32 s59, s58, 147\n\t" \
    "s_lshr_b32 s59, s59, 12\n\t" \
    "s_mul_i32 s37, s59, 28\n\t" \
    "s_sub_u32 s60, s58, s37\n\t" \
    "s_and_b32 s37, s56, 7\n\t" \
    "s_mul_i32 s48, s37, 4243456\n\t" \
    "s_and_b32 s37, s56, 8\n\t" \
    "s_lshl_b32 s37, s37, 10\n\t" \
    "s_add_u32 s48, s48, s37\n\t" \
    "s_add_u32 s49, s48, 524288\n\t" \
    "s_add_u32 s50, s48, 1048576\n\t" \
    "s_andn2_b32 s39, s58, 7\n\t" \
    "s_add_u32 s51, s58, s39\n\t" \
    "s_lshl_b32 s51, s51, 10\n\t" \
    "s_sub_u32 s37, s60, 1\n\t" \
    "s_cselect_b32 s52, 27, -1\n\t" \
    "s_add_u32 s52, s52, s58\n\t" \
    "s_andn2_b32 s39, s52, 7\n\t" \
    "s_add_u32 s52, s52, s39\n\t" \
    "s_lshl_b32 s52, s52, 10\n\t" \
    "v_lshrrev_b32 v8, 4, v0\n\t" \
    "v_min_u32 v8, 4, v8\n\t" \
    "v_add_u32 v8, s37, v8\n\t" \
    "v_add_u32 v1, 28, v8\n\t" \
    "v_min_u32 v8, v8, v1\n\t" \
    "v_mad_u32_u24 v8, v8, 9, s59\n\t" \
    "v_and_b32 v14, 31, v8\n\t" \
    "v_lshrrev_b32 v13, 5, v8\n\t" \
    "v_lshlrev_b32 v13, 7, v13\n\t" \
    "v_lshl_or_b32 v15, v14, 10, v13\n\t" \
    "v_lshrrev_b32 v24, 9, v0\n\t" \
    "v_and_b32 v25, 0x2f0, v0\n\t" \
    "v_and_b32 v26, 1, v11\n\t" \
    "v_lshl_add_u32 v27, v26, 11, v25\n\t" \
    "v_add_u32 v1, s51, v27\n\t" \
    "s_sub_u32 s37, s51, s52\n\t" \
    "s_add_u32 s37, s37, 1024\n\t" \
    "v_mul_i32_i24 v26, s37, v26\n\t" \
    "v_add3_u32 v8, v26, v25, s52\n\t" \
    "s_mul_i32 s44, s24, 7296\n\t" \
    "s_waitcnt vmcnt(2)\n\t" \
    "s_mov_b64 exec, 1\n\t" \
    "ds_add_rtn_u32 v14, v9, v9 offset:8\n\t" \
    "s_add_u32 s37, s32, 524288\n\t" \
    "s_waitcnt lgkmcnt(0)\n\t" \
    "v_cmp_eq_u32 vcc, 1098496, v14\n\t" \
    "s_mov_b64 exec, -1\n\t" \
    "s_nop 0\n\t" \
    "s_cbranch_vccz .La13_%=\n\t" \
    "buffer_store_dwordx4 v[168:171], v2, s[4:7], s37 offen sc1\n\t" \
    ".La13_%=:\n\t" \
    "s_mov_b64 exec, -1\n\t" \
    "s_lshl_b32 s37, s24, 2\n\t" \
    "s_lshl1_add_u32 s39, s24, 8\n\t" \
    "s_min_u32 s61, s37, s39\n\t" \
    "s_add_u32 s37, s37, 2\n\t" \
    "s_add_u32 s39, s24, 20\n\t" \
    "s_cmp_lt_u32 s24, 4\n\t" \
    "s_cselect_b32 s62, s37, s39\n\t" \
    "s_bfe_u32 s63, s2, 0x10003\n\t" \
    "s_xor_b32 s37, s48, 0x2000\n\t" \
    "s_add_u32 s37, s37, 2097152\n\t" \
    "s_mul_i32 s47, s63, 28\n\t" \
    "s_add_u32 s45, s47, s61\n\t" \
    "s_add_u32 s46, s47, s62\n\t" \
    "s_andn2_b32 s39, s45, 7\n\t" \
    "s_add_u32 s45, s45, s39\n\t" \
    "s_lshl_b32 s45, s45, 10\n\t" \
    "s_andn2_b32 s39, s46, 7\n\t" \
    "s_add_u32 s46, s46, s39\n\t" \
    "s_lshl_b32 s46, s46, 10\n\t" \
    "s_add_u32 s45, s45, s37\n\t" \
    "s_add_u32 s46, s46, s37\n\t" \
    "s_cmp_lt_u32 s24, 4\n\t" \
    "s_cselect_b32 s47, s46, 0x80000000\n\t" \
    "v_add_u32 v13, s45, v0\n\t" \
    "v_add_u32 v15, s46, v0\n\t" \
    "v_add_u32 v27, s47, v0\n\t" \
    "v_lshlrev_b32 v25, 4, v11\n\t" \
    "v_lshrrev_b32 v26, 6, v12\n\t" \
    "v_mul_u32_u24 v26, 7296, v26\n\t" \
    "v_and_b32 v14, 16, v0\n\t" \
    "v_mul_u32_u24 v14, 226, v14\n\t" \
    "v_and_b32 v108, 32, v0\n\t" \
    "v_lshl_add_u32 v14, v108, 1, v14\n\t" \
    "v_add3_u32 v26, v26, v14, v25\n\t" \
    "s_lshl_b32 s37, s61, 7\n\t" \
    "v_add_u32 v109, s37, v26\n\t" \
    "s_and_b32 s37, s62, 30\n\t" \
    "s_lshl_b32 s37, s37, 7\n\t" \
    "v_add_u32 v110, s37, v26\n\t" \
    "s_lshr_b32 s37, s24, 2\n\t" \
    "s_mul_i32 s37, s37, 6\n\t" \
    "s_mul_i32 s39, s56, 7\n\t" \
    "s_add_u32 s37, s37, s39\n\t" \
    "s_lshr_b32 s37, s37, 4\n\t" \
    "s_and_b32 s39, s24, 3\n\t" \
    "s_mul_i32 s39, s39, 63\n\t" \
    "s_add_u32 s37, s37, s39\n\t" \
    "v_lshrrev_b32 v14, 4, v0\n\t" \
    "v_min_u32 v14, 8, v14\n\t" \
    "v_mad_u32_u24 v14, v14, 7, s37\n\t" \
    "v_and_b32 v111, 31, v14\n\t" \
    "v_lshrrev_b32 v26, 5, v14\n\t" \
    "v_lshlrev_b32 v26, 7, v26\n\t" \
    "v_lshl_or_b32 v112, v111, 10, v26\n\t" \
    "s_mul_i32 s37, s56, 7\n\t" \
    "s_lshr_b32 s39, s62, 2\n\t" \
    "s_add_u32 s39, s39, s37\n\t" \
    "s_lshr_b32 s39, s39, 4\n\t" \
    "s_lshr_b32 s38, s61, 2\n\t" \
    "s_add_u32 s37, s37, s38\n\t" \
    "s_lshr_b32 s37, s37, 4\n\t" \
    "s_sub_u32 s39, s39, s37\n\t" \
    "s_add_u32 s37, s37, 14\n\t" \
    "v_lshrrev_b32 v14, 4, v12\n\t" \
    "v_min_u32 v14, 11, v14\n\t" \
    "v_mad_u32_u24 v14, v14, 21, s37\n\t" \
    "v_and_b32 v108, 1, v11\n\t" \
    "v_mad_u32_u24 v14, v108, s39, v14\n\t" \
    "v_and_b32 v111, 31, v14\n\t" \
    "v_lshrrev_b32 v26, 5, v14\n\t" \
    "v_lshlrev_b32 v26, 7, v26\n\t" \
    "v_lshl_or_b32 v113, v111, 10, v26\n\t" \
    "s_cmp_eq_u32 s24, 0\n\t" \
    "s_cbranch_scc1 .Lj31_%=\n\t" \
    ".Lh30_%=:\n\t" \
    "s_memrealtime s[22:23]\n\t" \
    "s_waitcnt lgkmcnt(0)\n\t" \
    "s_sub_u32 s22, s22, s35\n\t" \
    "s_cmp_lt_u32 s22, 480\n\t" \
    "s_cbranch_scc1 .Lh30_%=\n\t" \
    ".Lj31_%=:\n\t" \
    "s_waitcnt lgkmcnt(0)\n\t" \
    "s_xor_b32 s42, s43, 2048\n\t" \
    "buffer_store_dwordx4 v[76:79], v3, s[4:7], s42 offen offset:0 sc1\n\t" \
    "s_xor_b32 s42, s43, 3072\n\t" \
    "buffer_store_dwordx4 v[96:99], v3, s[4:7], s42 offen offset:0 sc1\n\t" \
    "s_mov_b32 s1, 0\n\t" \
    "s_mov_b32 s41, 0x18000\n\t" \
    "s_lshl_b32 s37, s63, 9\n\t" \
    "s_add_u32 s51, s51, s37\n\t" \
    "s_cmp_lt_u32 s57, 252\n\t" \
    "s_cselect_b32 s51, s51, 0x80000000\n\t" \
    "s_and_b32 s37, s24, 3\n\t" \
    "s_mul_i32 s37, s37, 8486912\n\t" \
    "s_add_u32 s53, s53, s37\n\t" \
    "s_and_b32 s37, s24, 4\n\t" \
    "s_lshl_b32 s37, s37, 11\n\t" \
    "s_add_u32 s53, s53, s37\n\t" \
    "s_and_b32 s37, s2, 0x87\n\t" \
    "s_lshl4_add_u32 s37, s24, s37\n\t" \
    "s_lshl_b32 s55, s37, 7\n\t" \
    "v_and_b32 v114, 16, v0\n\t" \
    "v_lshlrev_b32 v115, 6, v114\n\t" \
    "s_lshl3_add_u32 s37, s26, s24\n\t" \
    "s_mul_i32 s37, s37, 43008\n\t" \
    "s_mul_i32 s39, s27, 4992\n\t" \
    "s_add_u32 s37, s37, s39\n\t" \
    "s_add_u32 s8, s8, s37\n\t" \
    "s_addc_u32 s9, s9, 0\n\t" \
    "s_cmp_eq_u32 s27, 8\n\t" \
    "s_movk_i32 s10, 4992\n\t" \
    "s_cselect_b32 s10, 3072, s10\n\t" \
    "s_mov_b32 s11, 0x20000\n\t" \
    "v_mad_u32_u24 v108, v12, 12, v25\n\t" \
    "v_and_b32 v14, 0x1f0, v0\n\t" \
    "v_lshl_add_u32 v14, v24, 11, v14\n\t" \
    "v_add_u32 v111, s51, v14\n\t" \
    "s_mul_i32 s37, s24, 2064\n\t" \
    "v_and_b32 v14, 0x300, v0\n\t" \
    "v_add3_u32 v26, v14, v0, s37\n\t" \
    "s_lshl_b32 s37, s37, 1\n\t" \
    "v_lshlrev_b32 v14, 7, v24\n\t" \
    "v_xor_b32 v14, v14, v0\n\t" \
    "v_add_u32 v113, s37, v14\n\t" \
    "v_lshrrev_b32 v14, 5, v12\n\t" \
    "v_mul_u32_u24 v14, 2064, v14\n\t" \
    "v_lshl_add_u32 v14, v114, 2, v14\n\t" \
    "s_lshl_b32 s37, s24, 8\n\t" \
    "v_add3_u32 v3, v14, v25, s37\n\t" \
    "v_bfe_u32 v14, v0, 7, 1\n\t" \
    "v_lshl_add_u32 v14, v11, 2, v14\n\t" \
    "v_mul_u32_u24 v14, 2064, v14\n\t" \
    "v_lshrrev_b32 v24, 3, v0\n\t" \
    "v_and_or_b32 v14, v24, 8, v14\n\t" \
    "s_and_b32 s37, s24, 3\n\t" \
    "s_lshl_b32 s37, s37, 6\n\t" \
    "s_and_b32 s39, s24, 4\n\t" \
    "s_lshl_b32 s39, s39, 8\n\t" \
    "s_or_b32 s37, s37, s39\n\t" \
    "v_and_or_b32 v24, v0, 32, s37\n\t" \
    "v_mul_u32_u24 v114, 40, v114\n\t" \
    "v_xor_b32 v24, v24, v114\n\t" \
    "v_add_u32 v12, v14, v24\n\t" \
    "s_and_b32 s37, s2, 0x70\n\t" \
    "s_lshl_b32 s37, s37, 1\n\t" \
    "v_lshrrev_b32 v25, 7, v0\n\t" \
    "v_lshl_add_u32 v25, v25, 2, s37\n\t" \
    "v_min_u32 v25, 248, v25\n\t" \
    "v_mul_u32_u24 v11, 147, v25\n\t" \
    "v_lshrrev_b32 v11, 12, v11\n\t" \
    "v_mul_u32_u24 v114, 28, v11\n\t" \
    "v_sub_u32 v25, v25, v114\n\t" \
    "v_bfe_u32 v114, v0, 4, 3\n\t" \
    "v_min_u32 v114, 4, v114\n\t" \
    "v_add3_u32 v25, v25, v114, -1\n\t" \
    "v_add_u32 v114, 28, v25\n\t" \
    "v_min_u32 v25, v25, v114\n\t" \
    "v_mad_u32_u24 v25, v25, 9, v11\n\t" \
    "v_and_b32 v14, 31, v25\n\t" \
    "v_lshrrev_b32 v24, 5, v25\n\t" \
    "v_lshlrev_b32 v24, 7, v24\n\t" \
    "v_lshl_or_b32 v116, v14, 10, v24\n\t" \
    "s_nop 0\n\t" \
    "v_readlane_b32 s12, v112, 0\n\t" \
    "v_readlane_b32 s13, v112, 1\n\t" \
    "v_readlane_b32 s14, v112, 2\n\t" \
    "v_readlane_b32 s15, v112, 3\n\t" \
    "v_readlane_b32 s16, v112, 4\n\t" \
    "v_readlane_b32 s17, v112, 5\n\t" \
    "v_readlane_b32 s18, v112, 6\n\t" \
    "v_readlane_b32 s19, v112, 7\n\t" \
    "v_readlane_b32 s3, v112, 8\n\t" \
    "s_add_u32 s12, s12, s33\n\t" \
    "s_add_u32 s13, s13, s33\n\t" \
    "s_add_u32 s14, s14, s33\n\t" \
    "s_add_u32 s15, s15, s33\n\t" \
    "s_add_u32 s16, s16, s33\n\t" \
    "s_add_u32 s17, s17, s33\n\t" \
    "s_add_u32 s18, s18, s33\n\t" \
    "s_add_u32 s19, s19, s33\n\t" \
    "s_add_u32 s3, s3, s33\n\t" \
    "ds_read_b128 v[120:123], v10 offset:0\n\t" \
    "ds_read_b128 v[124:127], v10 offset:1024\n\t" \
    ".Lh32_%=:\n\t" \
    "s_memrealtime s[22:23]\n\t" \
    "s_waitcnt lgkmcnt(0)\n\t" \
    "s_sub_u32 s22, s22, s35\n\t" \
    "s_cmp_lt_u32 s22, 510\n\t" \
    "s_cbranch_scc1 .Lh32_%=\n\t" \
    "s_cmp_lg_u32 s24, 0\n\t" \
    "s_cbranch_scc1 .Lb15_%=\n\t" \
    "s_mov_b32 s40, 0\n\t" \
    "s_add_u32 s38, s33, 524288\n\t" \
    ".Ll15_%=:\n\t" \
    "buffer_load_dword v117, v116, s[4:7], s38 offen sc1\n\t" \
    "s_waitcnt vmcnt(0)\n\t" \
    "v_cmp_ne_u32 vcc, s34, v117\n\t" \
    "s_add_u32 s40, s40, 1\n\t" \
    "s_cmp_ge_u32 s40, 8192\n\t" \
    "s_cbranch_scc1 .Lb15_%=\n\t" \
    "s_cbranch_vccz .Lb15_%=\n\t" \
    "s_sleep 2\n\t" \
    "s_branch .Ll15_%=\n\t" \
    ".Lb15_%=:\n\t" \
    "s_waitcnt vmcnt(0)\n\t" \
    "s_mov_b64 exec, 1\n\t" \
    "ds_add_rtn_u32 v118, v9, v9 offset:12\n\t" \
    "s_add_u32 s37, s32, 786432\n\t" \
    "s_waitcnt lgkmcnt(0)\n\t" \
    "v_cmp_eq_u32 vcc, 1098496, v118\n\t" \
    "s_mov_b64 exec, -1\n\t" \
    "s_nop 0\n\t" \
    "s_cbranch_vccz .La18_%=\n\t" \
    "buffer_store_dwordx4 v[168:171], v2, s[4:7], s37 offen sc1\n\t" \
    ".La18_%=:\n\t" \
    "s_mov_b64 exec, -1\n\t" \
    "s_waitcnt lgkmcnt(0)\n\t" \
    "s_barrier\n\t" \
    "buffer_load_dwordx4 v[128:131], v8, s[4:7], s48 offen offset:0 sc1\n\t" \
    "buffer_load_dwordx4 v[132:135], v1, s[4:7], s48 offen offset:0 sc1\n\t" \
    "buffer_load_dwordx4 v[136:139], v8, s[4:7], s48 offen offset:256 sc1\n\t" \
    "buffer_load_dwordx4 v[140:143], v1, s[4:7], s48 offen offset:256 sc1\n\t" \
    "buffer_load_dwordx4 v[152:155], v1, s[4:7], s48 offen offset:1024 sc1\n\t" \
    "buffer_load_dwordx4 v[156:159], v1, s[4:7], s48 offen offset:1280 sc1\n\t" \
    "s_waitcnt vmcnt(5)\n\t" \
    "v_mfma_f32_32x32x16_bf16 v[192:207], v[120:123], v[128:131], 0\n\t" \
    "buffer_load_dwordx4 v[172:175], v8, s[4:7], s49 offen offset:0 sc1\n\t" \
    "s_waitcnt vmcnt(5)\n\t" \
    "v_mfma_f32_32x32x16_bf16 v[192:207], v[124:127], v[132:135], v[192:207]\n\t" \
    "buffer_load_dwordx4 v[176:179], v1, s[4:7], s49 offen offset:0 sc1\n\t" \
    "s_waitcnt vmcnt(5)\n\t" \
    "v_mfma_f32_32x32x16_bf16 v[208:223], v[120:123], v[136:139], 0\n\t" \
    "buffer_load_dwordx4 v[180:183], v8, s[4:7], s49 offen offset:256 sc1\n\t" \
    "s_waitcnt vmcnt(5)\n\t" \
    "v_mfma_f32_32x32x16_bf16 v[208:223], v[124:127], v[140:143], v[208:223]\n\t" \
    "buffer_load_dwordx4 v[184:187], v1, s[4:7], s49 offen offset:256 sc1\n\t" \
    "v_mfma_f32_32x32x16_bf16 v[224:239], v[120:123], v[132:135], 0\n\t" \
    "buffer_load_dwordx4 v[188:191], v1, s[4:7], s49 offen offset:1024 sc1\n\t" \
    "s_waitcnt vmcnt(6)\n\t" \
    "v_mfma_f32_32x32x16_bf16 v[224:239], v[124:127], v[152:155], v[224:239]\n\t" \
    "v_mfma_f32_32x32x16_bf16 v[240:255], v[120:123], v[140:143], 0\n\t" \
    "buffer_load_dwordx4 v[4:7], v1, s[4:7], s49 offen offset:1280 sc1\n\t" \
    "s_waitcnt vmcnt(6)\n\t" \
    "v_mfma_f32_32x32x16_bf16 v[240:255], v[124:127], v[156:159], v[240:255]\n\t" \
    "s_add_u32 m0, s44, 0\n\t" \
    "s_nop 1\n\t" \
    "v_cvt_pk_bf16_f32 v114, v192, v208\n\t" \
    "v_cvt_pk_bf16_f32 v11, v193, v209\n\t" \
    "v_cvt_pk_bf16_f32 v25, v194, v210\n\t" \
    "ds_write_addtid_b32 v114 offset:0\n\t" \
    "v_cvt_pk_bf16_f32 v14, v195, v211\n\t" \
    "ds_write_addtid_b32 v11 offset:256\n\t" \
    "v_cvt_pk_bf16_f32 v24, v196, v212\n\t" \
    "ds_write_addtid_b32 v25 offset:512\n\t" \
    "v_cvt_pk_bf16_f32 v9, v197, v213\n\t" \
    "ds_write_addtid_b32 v14 offset:768\n\t" \
    "v_cvt_pk_bf16_f32 v16, v198, v214\n\t" \
    "ds_write_addtid_b32 v24 offset:1024\n\t" \
    "v_cvt_pk_bf16_f32 v17, v199, v215\n\t" \
    "ds_write_addtid_b32 v9 offset:1280\n\t" \
    "v_cvt_pk_bf16_f32 v18, v200, v216\n\t" \
    "ds_write_addtid_b32 v16 offset:1536\n\t" \
    "v_cvt_pk_bf16_f32 v19, v201, v217\n\t" \
    "ds_write_addtid_b32 v17 offset:1792\n\t" \
    "v_cvt_pk_bf16_f32 v114, v202, v218\n\t" \
    "ds_write_addtid_b32 v18 offset:2048\n\t" \
    "v_cvt_pk_bf16_f32 v11, v203, v219\n\t" \
    "ds_write_addtid_b32 v19 offset:2304\n\t" \
    "v_cvt_pk_bf16_f32 v25, v204, v220\n\t" \
    "ds_write_addtid_b32 v114 offset:2560\n\t" \
    "v_cvt_pk_bf16_f32 v14, v205, v221\n\t" \
    "ds_write_addtid_b32 v11 offset:2816\n\t" \
    "ds_write_addtid_b32 v25 offset:3072\n\t" \
    "ds_write_addtid_b32 v14 offset:3328\n\t" \
    "s_waitcnt vmcnt(5)\n\t" \
    "v_mfma_f32_32x32x16_bf16 v[192:207], v[120:123], v[172:175], 0\n\t" \
    "v_cvt_pk_bf16_f32 v24, v224, v240\n\t" \
    "v_cvt_pk_bf16_f32 v9, v225, v241\n\t" \
    "v_cvt_pk_bf16_f32 v114, v226, v242\n\t" \
    "ds_write_addtid_b32 v24 offset:3616\n\t" \
    "buffer_load_dwordx4 v[28:31], v8, s[4:7], s50 offen offset:0 sc1\n\t" \
    "v_cvt_pk_bf16_f32 v11, v227, v243\n\t" \
    "ds_write_addtid_b32 v9 offset:3872\n\t" \
    "v_cvt_pk_bf16_f32 v25, v228, v244\n\t" \
    "s_waitcnt vmcnt(5)\n\t" \
    "v_mfma_f32_32x32x16_bf16 v[192:207], v[124:127], v[176:179], v[192:207]\n\t" \
    "ds_write_addtid_b32 v114 offset:4128\n\t" \
    "v_cvt_pk_bf16_f32 v14, v229, v245\n\t" \
    "ds_write_addtid_b32 v11 offset:4384\n\t" \
    "v_cvt_pk_bf16_f32 v24, v230, v246\n\t" \
    "buffer_load_dwordx4 v[160:163], v1, s[4:7], s50 offen offset:0 sc1\n\t" \
    "ds_write_addtid_b32 v25 offset:4640\n\t" \
    "v_cvt_pk_bf16_f32 v9, v231, v247\n\t" \
    "ds_write_addtid_b32 v14 offset:4896\n\t" \
    "s_waitcnt vmcnt(5)\n\t" \
    "v_mfma_f32_32x32x16_bf16 v[208:223], v[120:123], v[180:183], 0\n\t" \
    "v_cvt_pk_bf16_f32 v114, v232, v248\n\t" \
    "ds_write_addtid_b32 v24 offset:5152\n\t" \
    "v_cvt_pk_bf16_f32 v11, v233, v249\n\t" \
    "ds_write_addtid_b32 v9 offset:5408\n\t" \
    "buffer_load_dwordx4 v[164:167], v8, s[4:7], s50 offen offset:256 sc1\n\t" \
    "v_cvt_pk_bf16_f32 v25, v234, v250\n\t" \
    "ds_write_addtid_b32 v114 offset:5664\n\t" \
    "v_cvt_pk_bf16_f32 v14, v235, v251\n\t" \
    "s_waitcnt vmcnt(5)\n\t" \
    "v_mfma_f32_32x32x16_bf16 v[208:223], v[124:127], v[184:187], v[208:223]\n\t" \
    "ds_write_addtid_b32 v11 offset:5920\n\t" \
    "v_cvt_pk_bf16_f32 v24, v236, v252\n\t" \
    "ds_write_addtid_b32 v25 offset:6176\n\t" \
    "v_cvt_pk_bf16_f32 v9, v237, v253\n\t" \
    "buffer_load_dwordx4 v[32:35], v1, s[4:7], s50 offen offset:256 sc1\n\t" \
    "ds_write_addtid_b32 v14 offset:6432\n\t" \
    "ds_write_addtid_b32 v24 offset:6688\n\t" \
    "ds_write_addtid_b32 v9 offset:6944\n\t" \
    "s_waitcnt lgkmcnt(0)\n\t" \
    "s_barrier\n\t" \
    "s_buffer_load_dword s12, s[4:7], s12 offset:0xc0000\n\t" \
    "s_buffer_load_dword s13, s[4:7], s13 offset:0xc0000\n\t" \
    "s_buffer_load_dword s14, s[4:7], s14 offset:0xc0000\n\t" \
    "s_buffer_load_dword s15, s[4:7], s15 offset:0xc0000\n\t" \
    "s_buffer_load_dword s16, s[4:7], s16 offset:0xc0000\n\t" \
    "s_buffer_load_dword s17, s[4:7], s17 offset:0xc0000\n\t" \
    "s_buffer_load_dword s18, s[4:7], s18 offset:0xc0000\n\t" \
    "s_buffer_load_dword s19, s[4:7], s19 offset:0xc0000\n\t" \
    "s_buffer_load_dword s3, s[4:7], s3 offset:0xc0000\n\t" \
    "ds_read_b128 v[40:43], v109 offset:0\n\t" \
    "ds_read_b128 v[44:47], v109 offset:128\n\t" \
    "ds_read_b128 v[20:23], v109 offset:29184\n\t" \
    "ds_read_b128 v[36:39], v109 offset:29312\n\t" \
    "ds_read_b128 v[84:87], v110 offset:0\n\t" \
    "ds_read_b128 v[56:59], v110 offset:128\n\t" \
    "ds_read_b128 v[60:63], v110 offset:29184\n\t" \
    "ds_read_b128 v[80:83], v110 offset:29312\n\t" \
    "v_mfma_f32_32x32x16_bf16 v[224:239], v[120:123], v[176:179], 0\n\t" \
    "v_cvt_pk_bf16_f32 v8, v192, v208\n\t" \
    "v_cvt_pk_bf16_f32 v114, v193, v209\n\t" \
    "v_cvt_pk_bf16_f32 v11, v194, v210\n\t" \
    "ds_write_addtid_b32 v8 offset:58368\n\t" \
    "v_cvt_pk_bf16_f32 v25, v195, v211\n\t" \
    "ds_write_addtid_b32 v114 offset:58624\n\t" \
    "v_cvt_pk_bf16_f32 v14, v196, v212\n\t" \
    "s_waitcnt vmcnt(5)\n\t" \
    "v_mfma_f32_32x32x16_bf16 v[224:239], v[124:127], v[188:191], v[224:239]\n\t" \
    "buffer_load_dwordx4 v[64:67], v1, s[4:7], s50 offen offset:1024 sc1\n\t" \
    "ds_write_addtid_b32 v11 offset:58880\n\t" \
    "v_cvt_pk_bf16_f32 v24, v197, v213\n\t" \
    "ds_write_addtid_b32 v25 offset:59136\n\t" \
    "v_cvt_pk_bf16_f32 v9, v198, v214\n\t" \
    "ds_write_addtid_b32 v14 offset:59392\n\t" \
    "v_cvt_pk_bf16_f32 v8, v199, v215\n\t" \
    "ds_write_addtid_b32 v24 offset:59648\n\t" \
    "v_mfma_f32_32x32x16_bf16 v[240:255], v[120:123], v[184:187], 0\n\t" \
    "v_cvt_pk_bf16_f32 v114, v200, v216\n\t" \
    "ds_write_addtid_b32 v9 offset:59904\n\t" \
    "v_cvt_pk_bf16_f32 v11, v201, v217\n\t" \
    "ds_write_addtid_b32 v8 offset:60160\n\t" \
    "v_cvt_pk_bf16_f32 v25, v202, v218\n\t" \
    "ds_write_addtid_b32 v114 offset:60416\n\t" \
    "v_cvt_pk_bf16_f32 v14, v203, v219\n\t" \
    "s_waitcnt vmcnt(5)\n\t" \
    "v_mfma_f32_32x32x16_bf16 v[240:255], v[124:127], v[4:7], v[240:255]\n\t" \
    "buffer_load_dwordx4 v[68:71], v1, s[4:7], s50 offen offset:1280 sc1\n\t" \
    "ds_write_addtid_b32 v11 offset:60672\n\t" \
    "v_cvt_pk_bf16_f32 v24, v204, v220\n\t" \
    "ds_write_addtid_b32 v25 offset:60928\n\t" \
    "v_cvt_pk_bf16_f32 v9, v205, v221\n\t" \
    "ds_write_addtid_b32 v14 offset:61184\n\t" \
    "ds_write_addtid_b32 v24 offset:61440\n\t" \
    "ds_write_addtid_b32 v9 offset:61696\n\t" \
    "s_waitcnt vmcnt(5)\n\t" \
    "v_mfma_f32_32x32x16_bf16 v[192:207], v[120:123], v[28:31], 0\n\t" \
    "s_nop 1\n\t" \
    "v_cvt_pk_bf16_f32 v8, v224, v240\n\t" \
    "v_cvt_pk_bf16_f32 v114, v225, v241\n\t" \
    "v_cvt_pk_bf16_f32 v1, v226, v242\n\t" \
    "ds_write_addtid_b32 v8 offset:61984\n\t" \
    "v_cvt_pk_bf16_f32 v11, v227, v243\n\t" \
    "ds_write_addtid_b32 v114 offset:62240\n\t" \
    "v_cvt_pk_bf16_f32 v25, v228, v244\n\t" \
    "s_waitcnt vmcnt(4)\n\t" \
    "v_mfma_f32_32x32x16_bf16 v[192:207], v[124:127], v[160:163], v[192:207]\n\t" \
    "ds_write_addtid_b32 v1 offset:62496\n\t" \
    "v_cvt_pk_bf16_f32 v14, v229, v245\n\t" \
    "ds_write_addtid_b32 v11 offset:62752\n\t" \
    "v_cvt_pk_bf16_f32 v24, v230, v246\n\t" \
    "ds_write_addtid_b32 v25 offset:63008\n\t" \
    "v_cvt_pk_bf16_f32 v9, v231, v247\n\t" \
    "ds_write_addtid_b32 v14 offset:63264\n\t" \
    "s_waitcnt vmcnt(3)\n\t" \
    "v_mfma_f32_32x32x16_bf16 v[208:223], v[120:123], v[164:167], 0\n\t" \
    "v_cvt_pk_bf16_f32 v8, v232, v248\n\t" \
    "ds_write_addtid_b32 v24 offset:63520\n\t" \
    "v_cvt_pk_bf16_f32 v114, v233, v249\n\t" \
    "ds_write_addtid_b32 v9 offset:63776\n\t" \
    "v_cvt_pk_bf16_f32 v1, v234, v250\n\t" \
    "ds_write_addtid_b32 v8 offset:64032\n\t" \
    "v_cvt_pk_bf16_f32 v11, v235, v251\n\t" \
    "s_waitcnt vmcnt(2)\n\t" \
    "v_mfma_f32_32x32x16_bf16 v[208:223], v[124:127], v[32:35], v[208:223]\n\t" \
    "ds_write_addtid_b32 v114 offset:64288\n\t" \
    "v_cvt_pk_bf16_f32 v25, v236, v252\n\t" \
    "ds_write_addtid_b32 v1 offset:64544\n\t" \
    "v_cvt_pk_bf16_f32 v14, v237, v253\n\t" \
    "ds_write_addtid_b32 v11 offset:64800\n\t" \
    "ds_write_addtid_b32 v25 offset:65056\n\t" \
    "ds_write_addtid_b32 v14 offset:65312\n\t" \
    "s_waitcnt lgkmcnt(0)\n\t" \
    "s_xor_b32 s12, s12, s34\n\t" \
    "s_xor_b32 s13, s13, s34\n\t" \
    "s_xor_b32 s14, s14, s34\n\t" \
    "s_xor_b32 s15, s15, s34\n\t" \
    "s_xor_b32 s16, s16, s34\n\t" \
    "s_xor_b32 s17, s17, s34\n\t" \
    "s_xor_b32 s18, s18, s34\n\t" \
    "s_xor_b32 s19, s19, s34\n\t" \
    "s_xor_b32 s3, s3, s34\n\t" \
    "s_or_b32 s12, s12, s13\n\t" \
    "s_or_b32 s12, s12, s14\n\t" \
    "s_or_b32 s12, s12, s15\n\t" \
    "s_or_b32 s12, s12, s16\n\t" \
    "s_or_b32 s12, s12, s17\n\t" \
    "s_or_b32 s12, s12, s18\n\t" \
    "s_or_b32 s12, s12, s19\n\t" \
    "s_or_b32 s12, s12, s3\n\t" \
    "s_cbranch_scc1 .Lq1021_%=\n\t" \
    ".Lb20_%=:\n\t" \
    "s_waitcnt vmcnt(0)\n\t" \
    "; wait\n\t" \
    "s_waitcnt lgkmcnt(0)\n\t" \
    "s_barrier\n\t" \
    "s_cmp_lg_u32 s24, 7\n\t" \
    "s_cbranch_scc1 .Lar_%=\n\t" \
    "s_add_u32 s37, s32, 1048576\n\t" \
    "s_mov_b64 exec, 0xff\n\t" \
    "buffer_store_dwordx4 v[168:171], v0, s[4:7], s37 offen sc1\n\t" \
    "s_mov_b64 exec, -1\n\t" \
    ".Lar_%=:\n\t" \
    "v_add_u32 v109, 58368, v109\n\t" \
    "v_add_u32 v110, 58368, v110\n\t" \
    "ds_read_b128 v[144:147], v109 offset:0\n\t" \
    "ds_read_b128 v[92:95], v109 offset:128\n\t" \
    "ds_read_b128 v[148:151], v109 offset:29184\n\t" \
    "ds_read_b128 v[72:75], v109 offset:29312\n\t" \
    "ds_read_b128 v[88:91], v110 offset:0\n\t" \
    "ds_read_b128 v[100:103], v110 offset:128\n\t" \
    "ds_read_b128 v[104:107], v110 offset:29184\n\t" \
    "ds_read_b128 v[48:51], v110 offset:29312\n\t" \
    "v_mfma_f32_32x32x16_bf16 v[224:239], v[120:123], v[160:163], 0\n\t" \
    "v_cvt_pk_bf16_f32 v9, v192, v208\n\t" \
    "v_cvt_pk_bf16_f32 v8, v193, v209\n\t" \
    "v_cvt_pk_bf16_f32 v114, v194, v210\n\t" \
    "ds_write_addtid_b32 v9 offset:0\n\t" \
    "s_mov_b32 s45, 0\n\t" \
    "buffer_load_dwordx4 v[52:55], v13, s[4:7], s45 offen offset:0 sc1\n\t" \
    "v_cvt_pk_bf16_f32 v1, v195, v211\n\t" \
    "ds_write_addtid_b32 v8 offset:256\n\t" \
    "v_cvt_pk_bf16_f32 v11, v196, v212\n\t" \
    "v_mfma_f32_32x32x16_bf16 v[224:239], v[124:127], v[64:67], v[224:239]\n\t" \
    "ds_write_addtid_b32 v114 offset:512\n\t" \
    "v_cvt_pk_bf16_f32 v25, v197, v213\n\t" \
    "ds_write_addtid_b32 v1 offset:768\n\t" \
    "v_cvt_pk_bf16_f32 v14, v198, v214\n\t" \
    "buffer_load_dwordx4 v[76:79], v13, s[4:7], s45 offen offset:1024 sc1\n\t" \
    "ds_write_addtid_b32 v11 offset:1024\n\t" \
    "v_cvt_pk_bf16_f32 v112, v199, v215\n\t" \
    "ds_write_addtid_b32 v25 offset:1280\n\t" \
    "v_mfma_f32_32x32x16_bf16 v[240:255], v[120:123], v[32:35], 0\n\t" \
    "v_cvt_pk_bf16_f32 v24, v200, v216\n\t" \
    "ds_write_addtid_b32 v14 offset:1536\n\t" \
    "v_cvt_pk_bf16_f32 v9, v201, v217\n\t" \
    "ds_write_addtid_b32 v112 offset:1792\n\t" \
    "s_mov_b32 s46, 114688\n\t" \
    "buffer_load_dwordx4 v[96:99], v13, s[4:7], s46 offen offset:0 sc1\n\t" \
    "v_cvt_pk_bf16_f32 v8, v202, v218\n\t" \
    "ds_write_addtid_b32 v24 offset:2048\n\t" \
    "v_cvt_pk_bf16_f32 v114, v203, v219\n\t" \
    "v_mfma_f32_32x32x16_bf16 v[240:255], v[124:127], v[68:71], v[240:255]\n\t" \
    "ds_write_addtid_b32 v9 offset:2304\n\t" \
    "v_cvt_pk_bf16_f32 v1, v204, v220\n\t" \
    "ds_write_addtid_b32 v8 offset:2560\n\t" \
    "v_cvt_pk_bf16_f32 v11, v205, v221\n\t" \
    "buffer_load_dwordx4 v[116:119], v13, s[4:7], s46 offen offset:1024 sc1\n\t" \
    "ds_write_addtid_b32 v114 offset:2816\n\t" \
    "ds_write_addtid_b32 v1 offset:3072\n\t" \
    "ds_write_addtid_b32 v11 offset:3328\n\t" \
    "s_nop 3\n\t" \
    "v_cvt_pk_bf16_f32 v25, v224, v240\n\t" \
    "s_mov_b32 s47, 229376\n\t" \
    "buffer_load_dwordx4 v[128:131], v13, s[4:7], s47 offen offset:0 sc1\n\t" \
    "v_cvt_pk_bf16_f32 v14, v225, v241\n\t" \
    "v_cvt_pk_bf16_f32 v112, v226, v242\n\t" \
    "ds_write_addtid_b32 v25 offset:3616\n\t" \
    "v_cvt_pk_bf16_f32 v24, v227, v243\n\t" \
    "ds_write_addtid_b32 v14 offset:3872\n\t" \
    "v_cvt_pk_bf16_f32 v9, v228, v244\n\t" \
    "ds_write_addtid_b32 v112 offset:4128\n\t" \
    "buffer_load_dwordx4 v[136:139], v13, s[4:7], s47 offen offset:1024 sc1\n\t" \
    "v_cvt_pk_bf16_f32 v8, v229, v245\n\t" \
    "ds_write_addtid_b32 v24 offset:4384\n\t" \
    "v_cvt_pk_bf16_f32 v114, v230, v246\n\t" \
    "ds_write_addtid_b32 v9 offset:4640\n\t" \
    "v_cvt_pk_bf16_f32 v1, v231, v247\n\t" \
    "ds_write_addtid_b32 v8 offset:4896\n\t" \
    "v_cvt_pk_bf16_f32 v11, v232, v248\n\t" \
    "buffer_load_dwordx4 v[132:135], v15, s[4:7], s45 offen offset:0 sc1\n\t" \
    "ds_write_addtid_b32 v114 offset:5152\n\t" \
    "v_cvt_pk_bf16_f32 v25, v233, v249\n\t" \
    "ds_write_addtid_b32 v1 offset:5408\n\t" \
    "v_cvt_pk_bf16_f32 v14, v234, v250\n\t" \
    "ds_write_addtid_b32 v11 offset:5664\n\t" \
    "v_cvt_pk_bf16_f32 v112, v235, v251\n\t" \
    "ds_write_addtid_b32 v25 offset:5920\n\t" \
    "buffer_load_dwordx4 v[152:155], v15, s[4:7], s46 offen offset:0 sc1\n\t" \
    "v_cvt_pk_bf16_f32 v24, v236, v252\n\t" \
    "ds_write_addtid_b32 v14 offset:6176\n\t" \
    "v_cvt_pk_bf16_f32 v9, v237, v253\n\t" \
    "ds_write_addtid_b32 v112 offset:6432\n\t" \
    "ds_write_addtid_b32 v24 offset:6688\n\t" \
    "ds_write_addtid_b32 v9 offset:6944\n\t" \
    "s_waitcnt lgkmcnt(0)\n\t" \
    "s_barrier\n\t" \
    "buffer_load_dwordx4 v[140:143], v15, s[4:7], s47 offen offset:0 sc1\n\t" \
    "buffer_load_dwordx4 v[156:159], v27, s[4:7], s45 offen offset:1024 sc1\n\t" \
    "s_cmp_lt_u32 s24, 4\n\t" \
    "s_cbranch_scc1 .Lp33_%=\n\t" \
    "s_setprio 1\n\t" \
    ".Lp33_%=:\n\t" \
    "ds_read_b128 v[16:19], v10 offset:2048\n\t" \
    "v_mov_b32 v8, 0x10000\n\t" \
    "s_waitcnt vmcnt(9)\n\t" \
    "v_mfma_f32_16x16x32_bf16 v[172:175], v[52:55], v[40:43], 0\n\t" \
    "v_subrev_u32 v109, 58368, v109\n\t" \
    "buffer_load_dwordx4 v[180:183], v27, s[4:7], s46 offen offset:1024 sc1\n\t" \
    "v_mfma_f32_16x16x32_bf16 v[176:179], v[52:55], v[44:47], 0\n\t" \
    "v_subrev_u32 v110, 58368, v110\n\t" \
    "v_mfma_f32_16x16x32_bf16 v[188:191], v[52:55], v[20:23], 0\n\t" \
    "ds_read_b128 v[184:187], v109 offset:0\n\t" \
    "buffer_load_dwordx4 v[4:7], v27, s[4:7], s47 offen offset:1024 sc1\n\t" \
    "v_mfma_f32_16x16x32_bf16 v[28:31], v[52:55], v[36:39], 0\n\t" \
    "ds_read_b128 v[164:167], v109 offset:128\n\t" \
    "s_waitcnt vmcnt(10)\n\t" \
    "v_mfma_f32_16x16x32_bf16 v[168:171], v[76:79], v[44:47], 0\n\t" \
    "ds_read_b128 v[160:163], v109 offset:29184\n\t" \
    "v_mfma_f32_16x16x32_bf16 v[176:179], v[76:79], v[40:43], v[176:179]\n\t" \
    "ds_read_b128 v[64:67], v109 offset:29312\n\t" \
    "v_mfma_f32_16x16x32_bf16 v[32:35], v[76:79], v[36:39], 0\n\t" \
    "ds_read_b128 v[120:123], v110 offset:0\n\t" \
    "s_mov_b32 s45, 344064\n\t" \
    "buffer_load_dwordx4 v[68:71], v13, s[4:7], s45 offen offset:0 sc1\n\t" \
    "v_mfma_f32_16x16x32_bf16 v[28:31], v[76:79], v[20:23], v[28:31]\n\t" \
    "ds_read_b128 v[124:127], v110 offset:128\n\t" \
    "s_waitcnt vmcnt(10)\n\t" \
    "v_mfma_f32_16x16x32_bf16 v[172:175], v[96:99], v[144:147], v[172:175]\n\t" \
    "ds_read_b128 v[192:195], v110 offset:29184\n\t" \
    "v_mfma_f32_16x16x32_bf16 v[176:179], v[96:99], v[92:95], v[176:179]\n\t" \
    "ds_read_b128 v[196:199], v110 offset:29312\n\t" \
    "v_mfma_f32_16x16x32_bf16 v[188:191], v[96:99], v[148:151], v[188:191]\n\t" \
    "buffer_load_dwordx4 v[200:203], v13, s[4:7], s45 offen offset:1024 sc1\n\t" \
    "v_mfma_f32_16x16x32_bf16 v[28:31], v[96:99], v[72:75], v[28:31]\n\t" \
    "s_waitcnt vmcnt(10)\n\t" \
    "v_mfma_f32_16x16x32_bf16 v[168:171], v[116:119], v[92:95], v[168:171]\n\t" \
    "v_mfma_f32_16x16x32_bf16 v[176:179], v[116:119], v[144:147], v[176:179]\n\t" \
    "v_mfma_f32_16x16x32_bf16 v[32:35], v[116:119], v[72:75], v[32:35]\n\t" \
    "s_mov_b32 s46, 458752\n\t" \
    "buffer_load_dwordx4 v[204:207], v13, s[4:7], s46 offen offset:0 sc1\n\t" \
    "v_mfma_f32_16x16x32_bf16 v[28:31], v[116:119], v[148:151], v[28:31]\n\t" \
    "s_waitcnt vmcnt(10) lgkmcnt(7)\n\t" \
    "v_mfma_f32_16x16x32_bf16 v[172:175], v[128:131], v[184:187], v[172:175]\n\t" \
    "s_waitcnt lgkmcnt(6)\n\t" \
    "v_mfma_f32_16x16x32_bf16 v[176:179], v[128:131], v[164:167], v[176:179]\n\t" \
    "s_waitcnt lgkmcnt(5)\n\t" \
    "v_mfma_f32_16x16x32_bf16 v[188:191], v[128:131], v[160:163], v[188:191]\n\t" \
    "s_waitcnt lgkmcnt(0)\n\t" \
    "s_add_u32 s12, s55, 34603008\n\t" \
    "s_buffer_load_dword s13, s[4:7], s12\n\t" \
    "s_buffer_load_dword s14, s[4:7], s12 offset:0x400\n\t" \
    "buffer_load_dwordx4 v[208:211], v13, s[4:7], s46 offen offset:1024 sc1\n\t" \
    "v_mfma_f32_16x16x32_bf16 v[28:31], v[128:131], v[64:67], v[28:31]\n\t" \
    "s_waitcnt vmcnt(10)\n\t" \
    "v_mfma_f32_16x16x32_bf16 v[168:171], v[136:139], v[164:167], v[168:171]\n\t" \
    "v_mfma_f32_16x16x32_bf16 v[176:179], v[136:139], v[184:187], v[176:179]\n\t" \
    "v_mfma_f32_16x16x32_bf16 v[32:35], v[136:139], v[64:67], v[32:35]\n\t" \
    "s_mov_b32 s47, 573440\n\t" \
    "buffer_load_dwordx4 v[212:215], v13, s[4:7], s47 offen offset:0 sc1\n\t" \
    "v_mfma_f32_16x16x32_bf16 v[28:31], v[136:139], v[160:163], v[28:31]\n\t" \
    "s_waitcnt vmcnt(10)\n\t" \
    "v_mfma_f32_16x16x32_bf16 v[216:219], v[132:135], v[84:87], 0\n\t" \
    "v_mfma_f32_16x16x32_bf16 v[220:223], v[132:135], v[56:59], 0\n\t" \
    "v_mfma_f32_16x16x32_bf16 v[224:227], v[132:135], v[60:63], 0\n\t" \
    "v_sub_f32 v172, v172, v168\n\t" \
    "v_sub_f32 v173, v173, v169\n\t" \
    "buffer_load_dwordx4 v[228:231], v13, s[4:7], s47 offen offset:1024 sc1\n\t" \
    "v_mfma_f32_16x16x32_bf16 v[232:235], v[132:135], v[80:83], 0\n\t" \
    "v_sub_f32 v174, v174, v170\n\t" \
    "v_sub_f32 v175, v175, v171\n\t" \
    "s_waitcnt vmcnt(10)\n\t" \
    "v_mfma_f32_16x16x32_bf16 v[216:219], v[152:155], v[88:91], v[216:219]\n\t" \
    "v_sub_f32 v188, v188, v32\n\t" \
    "v_sub_f32 v189, v189, v33\n\t" \
    "v_mfma_f32_16x16x32_bf16 v[220:223], v[152:155], v[100:103], v[220:223]\n\t" \
    "v_sub_f32 v190, v190, v34\n\t" \
    "v_sub_f32 v191, v191, v35\n\t" \
    "v_mfma_f32_16x16x32_bf16 v[224:227], v[152:155], v[104:107], v[224:227]\n\t" \
    "v_cvt_pk_bf16_f32 v238, v176, v177\n\t" \
    "v_cvt_pk_bf16_f32 v239, v178, v179\n\t" \
    "buffer_load_dwordx4 v[240:243], v15, s[4:7], s45 offen offset:0 sc1\n\t" \
    "v_mfma_f32_16x16x32_bf16 v[232:235], v[152:155], v[48:51], v[232:235]\n\t" \
    "v_cvt_pk_bf16_f32 v236, v172, v173\n\t" \
    "v_cvt_pk_bf16_f32 v237, v174, v175\n\t" \
    "s_waitcnt vmcnt(10)\n\t" \
    "v_mfma_f32_16x16x32_bf16 v[216:219], v[140:143], v[120:123], v[216:219]\n\t" \
    "v_cvt_pk_bf16_f32 v246, v28, v29\n\t" \
    "v_cvt_pk_bf16_f32 v247, v30, v31\n\t" \
    "v_mfma_f32_16x16x32_bf16 v[220:223], v[140:143], v[124:127], v[220:223]\n\t" \
    "ds_write_b128 v113, v[236:239] offset:58368\n\t" \
    "v_cvt_pk_bf16_f32 v244, v188, v189\n\t" \
    "v_mfma_f32_16x16x32_bf16 v[224:227], v[140:143], v[192:195], v[224:227]\n\t" \
    "v_cvt_pk_bf16_f32 v245, v190, v191\n\t" \
    "ds_write_b128 v113, v[244:247] offset:59392\n\t" \
    "buffer_load_dwordx4 v[248:251], v15, s[4:7], s46 offen offset:0 sc1\n\t" \
    "v_mfma_f32_16x16x32_bf16 v[232:235], v[140:143], v[196:199], v[232:235]\n\t" \
    "s_waitcnt vmcnt(8)\n\t" \
    "s_cmp_ge_u32 s24, 4\n\t" \
    "s_cbranch_scc1 .Lk34_%=\n\t" \
    "v_mfma_f32_16x16x32_bf16 v[252:255], v[156:159], v[56:59], 0\n\t" \
    "v_mfma_f32_16x16x32_bf16 v[252:255], v[180:183], v[100:103], v[252:255]\n\t" \
    "v_mfma_f32_16x16x32_bf16 v[252:255], v[4:7], v[124:127], v[252:255]\n\t" \
    "v_mfma_f32_16x16x32_bf16 v[52:55], v[156:159], v[80:83], 0\n\t" \
    "v_mfma_f32_16x16x32_bf16 v[52:55], v[180:183], v[48:51], v[52:55]\n\t" \
    "v_mfma_f32_16x16x32_bf16 v[52:55], v[4:7], v[196:199], v[52:55]\n\t" \
    "v_mfma_f32_16x16x32_bf16 v[220:223], v[156:159], v[84:87], v[220:223]\n\t" \
    "v_mfma_f32_16x16x32_bf16 v[220:223], v[180:183], v[88:91], v[220:223]\n\t" \
    "v_mfma_f32_16x16x32_bf16 v[220:223], v[4:7], v[120:123], v[220:223]\n\t" \
    "v_mfma_f32_16x16x32_bf16 v[232:235], v[156:159], v[60:63], v[232:235]\n\t" \
    "s_nop 0\n\t" \
    "v_sub_f32 v216, v216, v252\n\t" \
    "v_sub_f32 v217, v217, v253\n\t" \
    "v_mfma_f32_16x16x32_bf16 v[232:235], v[180:183], v[104:107], v[232:235]\n\t" \
    "v_sub_f32 v218, v218, v254\n\t" \
    "v_sub_f32 v219, v219, v255\n\t" \
    "v_mfma_f32_16x16x32_bf16 v[232:235], v[4:7], v[192:195], v[232:235]\n\t" \
    "v_sub_f32 v224, v224, v52\n\t" \
    "v_sub_f32 v225, v225, v53\n\t" \
    "v_sub_f32 v226, v226, v54\n\t" \
    "v_sub_f32 v227, v227, v55\n\t" \
    ".Lk34_%=:\n\t" \
    "s_waitcnt vmcnt(7)\n\t" \
    "v_mfma_f32_16x16x32_bf16 v[76:79], v[68:71], v[40:43], 0\n\t" \
    "buffer_load_dwordx4 v[96:99], v15, s[4:7], s47 offen offset:0 sc1\n\t" \
    "v_mfma_f32_16x16x32_bf16 v[116:119], v[68:71], v[44:47], 0\n\t" \
    "v_cvt_pk_bf16_f32 v130, v220, v221\n\t" \
    "v_cvt_pk_bf16_f32 v131, v222, v223\n\t" \
    "v_mfma_f32_16x16x32_bf16 v[136:139], v[68:71], v[20:23], 0\n\t" \
    "v_cvt_pk_bf16_f32 v128, v216, v217\n\t" \
    "v_cvt_pk_bf16_f32 v129, v218, v219\n\t" \
    "buffer_load_dwordx4 v[132:135], v27, s[4:7], s45 offen offset:1024 sc1\n\t" \
    "v_mfma_f32_16x16x32_bf16 v[168:171], v[68:71], v[36:39], 0\n\t" \
    "v_cvt_pk_bf16_f32 v34, v232, v233\n\t" \
    "v_cvt_pk_bf16_f32 v35, v234, v235\n\t" \
    "s_waitcnt vmcnt(8)\n\t" \
    "v_mfma_f32_16x16x32_bf16 v[176:179], v[200:203], v[44:47], 0\n\t" \
    "ds_write_b128 v113, v[128:131] offset:60432\n\t" \
    "v_cvt_pk_bf16_f32 v32, v224, v225\n\t" \
    "buffer_load_dwordx4 v[152:155], v27, s[4:7], s46 offen offset:1024 sc1\n\t" \
    "v_mfma_f32_16x16x32_bf16 v[116:119], v[200:203], v[40:43], v[116:119]\n\t" \
    "v_cvt_pk_bf16_f32 v33, v226, v227\n\t" \
    "v_mfma_f32_16x16x32_bf16 v[172:175], v[200:203], v[36:39], 0\n\t" \
    "ds_write_b128 v113, v[32:35] offset:61456\n\t" \
    "buffer_load_dwordx4 v[28:31], v27, s[4:7], s47 offen offset:1024 sc1\n\t" \
    "v_mfma_f32_16x16x32_bf16 v[168:171], v[200:203], v[20:23], v[168:171]\n\t" \
    "s_waitcnt vmcnt(9)\n\t" \
    "v_mfma_f32_16x16x32_bf16 v[76:79], v[204:207], v[144:147], v[76:79]\n\t" \
    "s_mov_b32 s45, 688128\n\t" \
    "buffer_load_dwordx4 v[236:239], v13, s[4:7], s45 offen offset:0 sc1\n\t" \
    "v_mfma_f32_16x16x32_bf16 v[116:119], v[204:207], v[92:95], v[116:119]\n\t" \
    "s_waitcnt lgkmcnt(0)\n\t" \
    "s_xor_b32 s13, s13, s34\n\t" \
    "s_xor_b32 s14, s14, s34\n\t" \
    "s_or_b32 s13, s13, s14\n\t" \
    "s_cbranch_scc1 .Lq1024_%=\n\t" \
    ".Lb23_%=:\n\t" \
    "s_waitcnt lgkmcnt(0)\n\t" \
    "s_barrier\n\t" \
    "ds_read_b64_tr_b16 v[188:189], v12 offset:58368\n\t" \
    "ds_read_b64_tr_b16 v[190:191], v12 offset:62496\n\t" \
    "v_mfma_f32_16x16x32_bf16 v[136:139], v[204:207], v[148:151], v[136:139]\n\t" \
    "ds_read_b64_tr_b16 v[244:245], v12 offset:58624\n\t" \
    "ds_read_b64_tr_b16 v[246:247], v12 offset:62752\n\t" \
    "buffer_load_dwordx4 v[140:143], v13, s[4:7], s45 offen offset:1024 sc1\n\t" \
    "v_mfma_f32_16x16x32_bf16 v[168:171], v[204:207], v[72:75], v[168:171]\n\t" \
    "ds_read_b64_tr_b16 v[156:157], v12 offset:58384\n\t" \
    "ds_read_b64_tr_b16 v[158:159], v12 offset:62512\n\t" \
    "s_waitcnt vmcnt(10)\n\t" \
    "v_mfma_f32_16x16x32_bf16 v[176:179], v[208:211], v[92:95], v[176:179]\n\t" \
    "ds_read_b64_tr_b16 v[180:181], v12 offset:58640\n\t" \
    "ds_read_b64_tr_b16 v[182:183], v12 offset:62768\n\t" \
    "v_mfma_f32_16x16x32_bf16 v[116:119], v[208:211], v[144:147], v[116:119]\n\t" \
    "v_mfma_f32_16x16x32_bf16 v[172:175], v[208:211], v[72:75], v[172:175]\n\t" \
    "s_mov_b32 s46, 802816\n\t" \
    "buffer_load_dwordx4 v[252:255], v13, s[4:7], s46 offen offset:0 sc1\n\t" \
    "v_mfma_f32_16x16x32_bf16 v[168:171], v[208:211], v[148:151], v[168:171]\n\t" \
    "s_waitcnt lgkmcnt(6)\n\t" \
    "v_mfma_f32_16x16x32_bf16 v[4:7], v[188:191], v[16:19], 0\n\t" \
    "s_waitcnt lgkmcnt(4)\n\t" \
    "v_mfma_f32_16x16x32_bf16 v[52:55], v[244:247], v[16:19], 0\n\t" \
    "s_waitcnt vmcnt(10)\n\t" \
    "v_mfma_f32_16x16x32_bf16 v[76:79], v[212:215], v[184:187], v[76:79]\n\t" \
    "s_waitcnt lgkmcnt(2)\n\t" \
    "v_mfma_f32_16x16x32_bf16 v[220:223], v[156:159], v[16:19], 0\n\t" \
    "s_waitcnt lgkmcnt(0)\n\t" \
    "v_mfma_f32_16x16x32_bf16 v[216:219], v[180:183], v[16:19], 0\n\t" \
    "v_mfma_f32_16x16x32_bf16 v[116:119], v[212:215], v[164:167], v[116:119]\n\t" \
    "s_memrealtime s[16:17]\n\t" \
    "v_mfma_f32_16x16x32_bf16 v[136:139], v[212:215], v[160:163], v[136:139]\n\t" \
    "buffer_load_dwordx4 v[68:71], v13, s[4:7], s46 offen offset:1024 sc1\n\t" \
    "v_mfma_f32_16x16x32_bf16 v[168:171], v[212:215], v[64:67], v[168:171]\n\t" \
    "v_bfi_b32 v4, s41, v8, v4\n\t" \
    "v_bfi_b32 v52, s41, v8, v52\n\t" \
    "s_waitcnt vmcnt(10)\n\t" \
    "v_mfma_f32_16x16x32_bf16 v[176:179], v[228:231], v[164:167], v[176:179]\n\t" \
    "v_cvt_pk_bf16_f32 v233, v6, v7\n\t" \
    "v_cvt_pk_bf16_f32 v235, v54, v55\n\t" \
    "v_mfma_f32_16x16x32_bf16 v[116:119], v[228:231], v[184:187], v[116:119]\n\t" \
    "v_cvt_pk_bf16_f32 v232, v4, v5\n\t" \
    "v_cvt_pk_bf16_f32 v234, v52, v53\n\t" \
    "v_mfma_f32_16x16x32_bf16 v[172:175], v[228:231], v[64:67], v[172:175]\n\t" \
    "v_bfi_b32 v220, s41, v8, v220\n\t" \
    "v_bfi_b32 v216, s41, v8, v216\n\t" \
    "s_mov_b32 s47, 917504\n\t" \
    "buffer_load_dwordx4 v[128:131], v13, s[4:7], s47 offen offset:0 sc1\n\t" \
    "v_mfma_f32_16x16x32_bf16 v[168:171], v[228:231], v[160:163], v[168:171]\n\t" \
    "buffer_store_dwordx4 v[232:235], v111, s[4:7], s48 offen offset:0 sc1\n\t" \
    "v_cvt_pk_bf16_f32 v225, v222, v223\n\t" \
    "s_waitcnt vmcnt(11)\n\t" \
    "v_mfma_f32_16x16x32_bf16 v[32:35], v[240:243], v[84:87], 0\n\t" \
    "v_cvt_pk_bf16_f32 v227, v218, v219\n\t" \
    "v_cvt_pk_bf16_f32 v224, v220, v221\n\t" \
    "v_mfma_f32_16x16x32_bf16 v[200:203], v[240:243], v[56:59], 0\n\t" \
    "v_cvt_pk_bf16_f32 v226, v216, v217\n\t" \
    "buffer_store_dwordx4 v[224:227], v111, s[4:7], s48 offen offset:1024 sc1\n\t" \
    "v_mfma_f32_16x16x32_bf16 v[204:207], v[240:243], v[60:63], 0\n\t" \
    "s_cmp_lg_u32 s24, 0\n\t" \
    "s_cbranch_scc1 .Lph_%=\n\t" \
    "s_waitcnt lgkmcnt(0)\n\t" \
    "v_mov_b32 v208, s16\n\t" \
    "v_mov_b32 v209, s16\n\t" \
    "v_mov_b32 v210, s16\n\t" \
    "v_mov_b32 v211, s16\n\t" \
    "buffer_store_dwordx4 v[208:211], v2, s[4:7], s32 offen sc1\n\t" \
    "s_nop 0\n\t" \
    ".Lph_%=:\n\t" \
    "v_sub_f32 v76, v76, v176\n\t" \
    "v_sub_f32 v77, v77, v177\n\t" \
    "buffer_load_dwordx4 v[188:191], v13, s[4:7], s47 offen offset:1024 sc1\n\t" \
    "v_mfma_f32_16x16x32_bf16 v[244:247], v[240:243], v[80:83], 0\n\t" \
    "v_sub_f32 v78, v78, v178\n\t" \
    "v_sub_f32 v79, v79, v179\n\t" \
    "s_waitcnt vmcnt(12)\n\t" \
    "v_mfma_f32_16x16x32_bf16 v[32:35], v[248:251], v[88:91], v[32:35]\n\t" \
    "v_sub_f32 v136, v136, v172\n\t" \
    "v_sub_f32 v137, v137, v173\n\t" \
    "v_mfma_f32_16x16x32_bf16 v[200:203], v[248:251], v[100:103], v[200:203]\n\t" \
    "v_sub_f32 v138, v138, v174\n\t" \
    "v_sub_f32 v139, v139, v175\n\t" \
    "v_mfma_f32_16x16x32_bf16 v[204:207], v[248:251], v[104:107], v[204:207]\n\t" \
    "v_cvt_pk_bf16_f32 v158, v116, v117\n\t" \
    "v_cvt_pk_bf16_f32 v159, v118, v119\n\t" \
    "buffer_load_dwordx4 v[180:183], v15, s[4:7], s45 offen offset:0 sc1\n\t" \
    "v_mfma_f32_16x16x32_bf16 v[244:247], v[248:251], v[48:51], v[244:247]\n\t" \
    "v_cvt_pk_bf16_f32 v156, v76, v77\n\t" \
    "v_cvt_pk_bf16_f32 v157, v78, v79\n\t" \
    "s_waitcnt vmcnt(12)\n\t" \
    "v_mfma_f32_16x16x32_bf16 v[32:35], v[96:99], v[120:123], v[32:35]\n\t" \
    "v_cvt_pk_bf16_f32 v214, v168, v169\n\t" \
    "v_cvt_pk_bf16_f32 v215, v170, v171\n\t" \
    "v_mfma_f32_16x16x32_bf16 v[200:203], v[96:99], v[124:127], v[200:203]\n\t" \
    "ds_write_b128 v113, v[156:159] offset:0\n\t" \
    "v_cvt_pk_bf16_f32 v212, v136, v137\n\t" \
    "v_mfma_f32_16x16x32_bf16 v[204:207], v[96:99], v[192:195], v[204:207]\n\t" \
    "v_cvt_pk_bf16_f32 v213, v138, v139\n\t" \
    "ds_write_b128 v113, v[212:215] offset:1024\n\t" \
    "buffer_load_dwordx4 v[4:7], v15, s[4:7], s46 offen offset:0 sc1\n\t" \
    "v_mfma_f32_16x16x32_bf16 v[244:247], v[96:99], v[196:199], v[244:247]\n\t" \
    "s_waitcnt vmcnt(10)\n\t" \
    "s_cmp_ge_u32 s24, 4\n\t" \
    "s_cbranch_scc1 .Lk35_%=\n\t" \
    "v_mfma_f32_16x16x32_bf16 v[52:55], v[132:135], v[56:59], 0\n\t" \
    "v_mfma_f32_16x16x32_bf16 v[52:55], v[152:155], v[100:103], v[52:55]\n\t" \
    "v_mfma_f32_16x16x32_bf16 v[52:55], v[28:31], v[124:127], v[52:55]\n\t" \
    "v_mfma_f32_16x16x32_bf16 v[228:231], v[132:135], v[80:83], 0\n\t" \
    "v_mfma_f32_16x16x32_bf16 v[228:231], v[152:155], v[48:51], v[228:231]\n\t" \
    "v_mfma_f32_16x16x32_bf16 v[228:231], v[28:31], v[196:199], v[228:231]\n\t" \
    "v_mfma_f32_16x16x32_bf16 v[200:203], v[132:135], v[84:87], v[200:203]\n\t" \
    "v_mfma_f32_16x16x32_bf16 v[200:203], v[152:155], v[88:91], v[200:203]\n\t" \
    "v_mfma_f32_16x16x32_bf16 v[200:203], v[28:31], v[120:123], v[200:203]\n\t" \
    "v_mfma_f32_16x16x32_bf16 v[244:247], v[132:135], v[60:63], v[244:247]\n\t" \
    "s_nop 0\n\t" \
    "v_sub_f32 v32, v32, v52\n\t" \
    "v_sub_f32 v33, v33, v53\n\t" \
    "v_mfma_f32_16x16x32_bf16 v[244:247], v[152:155], v[104:107], v[244:247]\n\t" \
    "v_sub_f32 v34, v34, v54\n\t" \
    "v_sub_f32 v35, v35, v55\n\t" \
    "v_mfma_f32_16x16x32_bf16 v[244:247], v[28:31], v[192:195], v[244:247]\n\t" \
    "v_sub_f32 v204, v204, v228\n\t" \
    "v_sub_f32 v205, v205, v229\n\t" \
    "v_sub_f32 v206, v206, v230\n\t" \
    "v_sub_f32 v207, v207, v231\n\t" \
    ".Lk35_%=:\n\t" \
    "s_waitcnt vmcnt(9)\n\t" \
    "v_mfma_f32_16x16x32_bf16 v[232:235], v[236:239], v[40:43], 0\n\t" \
    "buffer_load_dwordx4 v[220:223], v15, s[4:7], s47 offen offset:0 sc1\n\t" \
    "v_mfma_f32_16x16x32_bf16 v[216:219], v[236:239], v[44:47], 0\n\t" \
    "v_cvt_pk_bf16_f32 v226, v200, v201\n\t" \
    "v_cvt_pk_bf16_f32 v227, v202, v203\n\t" \
    "v_mfma_f32_16x16x32_bf16 v[208:211], v[236:239], v[20:23], 0\n\t" \
    "v_cvt_pk_bf16_f32 v224, v32, v33\n\t" \
    "v_cvt_pk_bf16_f32 v225, v34, v35\n\t" \
    "buffer_load_dwordx4 v[240:243], v27, s[4:7], s45 offen offset:1024 sc1\n\t" \
    "v_mfma_f32_16x16x32_bf16 v[176:179], v[236:239], v[36:39], 0\n\t" \
    "v_cvt_pk_bf16_f32 v174, v244, v245\n\t" \
    "v_cvt_pk_bf16_f32 v175, v246, v247\n\t" \
    "s_waitcnt vmcnt(10)\n\t" \
    "v_mfma_f32_16x16x32_bf16 v[116:119], v[140:143], v[44:47], 0\n\t" \
    "ds_write_b128 v113, v[224:227] offset:2064\n\t" \
    "v_cvt_pk_bf16_f32 v172, v204, v205\n\t" \
    "buffer_load_dwordx4 v[248:251], v27, s[4:7], s46 offen offset:1024 sc1\n\t" \
    "v_mfma_f32_16x16x32_bf16 v[216:219], v[140:143], v[40:43], v[216:219]\n\t" \
    "v_cvt_pk_bf16_f32 v173, v206, v207\n\t" \
    "v_mfma_f32_16x16x32_bf16 v[76:79], v[140:143], v[36:39], 0\n\t" \
    "ds_write_b128 v113, v[172:175] offset:3088\n\t" \
    "buffer_load_dwordx4 v[168:171], v27, s[4:7], s47 offen offset:1024 sc1\n\t" \
    "v_mfma_f32_16x16x32_bf16 v[176:179], v[140:143], v[20:23], v[176:179]\n\t" \
    "s_waitcnt vmcnt(11)\n\t" \
    "v_mfma_f32_16x16x32_bf16 v[232:235], v[252:255], v[144:147], v[232:235]\n\t" \
    "v_mfma_f32_16x16x32_bf16 v[216:219], v[252:255], v[92:95], v[216:219]\n\t" \
    "s_waitcnt lgkmcnt(0)\n\t" \
    "s_barrier\n\t" \
    "ds_read_b64_tr_b16 v[156:157], v12 offset:0\n\t" \
    "ds_read_b64_tr_b16 v[158:159], v12 offset:4128\n\t" \
    "v_mfma_f32_16x16x32_bf16 v[208:211], v[252:255], v[148:151], v[208:211]\n\t" \
    "ds_read_b64_tr_b16 v[136:137], v12 offset:256\n\t" \
    "ds_read_b64_tr_b16 v[138:139], v12 offset:4384\n\t" \
    "v_mfma_f32_16x16x32_bf16 v[176:179], v[252:255], v[72:75], v[176:179]\n\t" \
    "ds_read_b64_tr_b16 v[212:213], v12 offset:16\n\t" \
    "ds_read_b64_tr_b16 v[214:215], v12 offset:4144\n\t" \
    "s_waitcnt vmcnt(10)\n\t" \
    "v_mfma_f32_16x16x32_bf16 v[116:119], v[68:71], v[92:95], v[116:119]\n\t" \
    "ds_read_b64_tr_b16 v[96:97], v12 offset:272\n\t" \
    "ds_read_b64_tr_b16 v[98:99], v12 offset:4400\n\t" \
    "v_mfma_f32_16x16x32_bf16 v[216:219], v[68:71], v[144:147], v[216:219]\n\t" \
    "v_mfma_f32_16x16x32_bf16 v[76:79], v[68:71], v[72:75], v[76:79]\n\t" \
    "v_mfma_f32_16x16x32_bf16 v[176:179], v[68:71], v[148:151], v[176:179]\n\t" \
    "s_waitcnt lgkmcnt(6)\n\t" \
    "v_mfma_f32_16x16x32_bf16 v[132:135], v[156:159], v[16:19], 0\n\t" \
    "s_waitcnt lgkmcnt(4)\n\t" \
    "v_mfma_f32_16x16x32_bf16 v[152:155], v[136:139], v[16:19], 0\n\t" \
    "s_waitcnt vmcnt(9)\n\t" \
    "v_mfma_f32_16x16x32_bf16 v[232:235], v[128:131], v[184:187], v[232:235]\n\t" \
    "s_waitcnt lgkmcnt(2)\n\t" \
    "v_mfma_f32_16x16x32_bf16 v[52:55], v[212:215], v[16:19], 0\n\t" \
    "s_waitcnt lgkmcnt(0)\n\t" \
    "v_mfma_f32_16x16x32_bf16 v[28:31], v[96:99], v[16:19], 0\n\t" \
    "v_mfma_f32_16x16x32_bf16 v[216:219], v[128:131], v[164:167], v[216:219]\n\t" \
    "s_memrealtime s[18:19]\n\t" \
    "v_mfma_f32_16x16x32_bf16 v[208:211], v[128:131], v[160:163], v[208:211]\n\t" \
    "v_mfma_f32_16x16x32_bf16 v[176:179], v[128:131], v[64:67], v[176:179]\n\t" \
    "v_bfi_b32 v132, s41, v8, v132\n\t" \
    "v_bfi_b32 v152, s41, v8, v152\n\t" \
    "s_waitcnt vmcnt(6)\n\t" \
    "v_mfma_f32_16x16x32_bf16 v[116:119], v[188:191], v[164:167], v[116:119]\n\t" \
    "v_cvt_pk_bf16_f32 v229, v134, v135\n\t" \
    "v_cvt_pk_bf16_f32 v231, v154, v155\n\t" \
    "v_add_u32 v113, 91392, v113\n\t" \
    "v_mfma_f32_16x16x32_bf16 v[216:219], v[188:191], v[184:187], v[216:219]\n\t" \
    "v_cvt_pk_bf16_f32 v228, v132, v133\n\t" \
    "v_cvt_pk_bf16_f32 v230, v152, v153\n\t" \
    "v_mfma_f32_16x16x32_bf16 v[76:79], v[188:191], v[64:67], v[76:79]\n\t" \
    "v_bfi_b32 v52, s41, v8, v52\n\t" \
    "v_bfi_b32 v28, s41, v8, v28\n\t" \
    "v_mfma_f32_16x16x32_bf16 v[176:179], v[188:191], v[160:163], v[176:179]\n\t" \
    "buffer_store_dwordx4 v[228:231], v111, s[4:7], s49 offen offset:0 sc1\n\t" \
    "v_cvt_pk_bf16_f32 v201, v54, v55\n\t" \
    "s_waitcnt vmcnt(6)\n\t" \
    "v_mfma_f32_16x16x32_bf16 v[32:35], v[180:183], v[84:87], 0\n\t" \
    "v_cvt_pk_bf16_f32 v203, v30, v31\n\t" \
    "v_cvt_pk_bf16_f32 v200, v52, v53\n\t" \
    "v_mfma_f32_16x16x32_bf16 v[236:239], v[180:183], v[56:59], 0\n\t" \
    "v_cvt_pk_bf16_f32 v202, v28, v29\n\t" \
    "buffer_store_dwordx4 v[200:203], v111, s[4:7], s49 offen offset:1024 sc1\n\t" \
    "s_sub_u32 s37, s24, s36\n\t" \
    "s_and_b32 s37, s37, 7\n\t" \
    "s_mul_i32 s39, s27, 28\n\t" \
    "s_add_u32 s39, s39, s26\n\t" \
    "s_lshr_b32 s39, s39, 5\n\t" \
    "s_lshl4_add_u32 s37, s39, s37\n\t" \
    "s_lshl_b32 s37, s37, 7\n\t" \
    "s_add_u32 s37, s37, s33\n\t" \
    "s_buffer_load_dword s58, s[4:7], s37 offset:0x0\n\t" \
    "s_buffer_load_dword s59, s[4:7], s37 offset:0x400\n\t" \
    "s_buffer_load_dword s60, s[4:7], s37 offset:0x4000\n\t" \
    "s_buffer_load_dword s61, s[4:7], s37 offset:0x4400\n\t" \
    "v_mfma_f32_16x16x32_bf16 v[244:247], v[180:183], v[60:63], 0\n\t" \
    "v_sub_f32 v232, v232, v116\n\t" \
    "v_sub_f32 v233, v233, v117\n\t" \
    "v_mfma_f32_16x16x32_bf16 v[44:47], v[180:183], v[80:83], 0\n\t" \
    "v_sub_f32 v234, v234, v118\n\t" \
    "v_sub_f32 v235, v235, v119\n\t" \
    "s_waitcnt vmcnt(6)\n\t" \
    "v_mfma_f32_16x16x32_bf16 v[32:35], v[4:7], v[88:91], v[32:35]\n\t" \
    "v_sub_f32 v208, v208, v76\n\t" \
    "v_sub_f32 v209, v209, v77\n\t" \
    "v_mfma_f32_16x16x32_bf16 v[236:239], v[4:7], v[100:103], v[236:239]\n\t" \
    "v_sub_f32 v210, v210, v78\n\t" \
    "v_sub_f32 v211, v211, v79\n\t" \
    "v_mfma_f32_16x16x32_bf16 v[244:247], v[4:7], v[104:107], v[244:247]\n\t" \
    "v_cvt_pk_bf16_f32 v226, v216, v217\n\t" \
    "v_cvt_pk_bf16_f32 v227, v218, v219\n\t" \
    "v_mfma_f32_16x16x32_bf16 v[44:47], v[4:7], v[48:51], v[44:47]\n\t" \
    "v_cvt_pk_bf16_f32 v224, v232, v233\n\t" \
    "v_cvt_pk_bf16_f32 v225, v234, v235\n\t" \
    "s_waitcnt vmcnt(5)\n\t" \
    "v_mfma_f32_16x16x32_bf16 v[32:35], v[220:223], v[120:123], v[32:35]\n\t" \
    "v_cvt_pk_bf16_f32 v42, v176, v177\n\t" \
    "v_cvt_pk_bf16_f32 v43, v178, v179\n\t" \
    "v_mfma_f32_16x16x32_bf16 v[236:239], v[220:223], v[124:127], v[236:239]\n\t" \
    "ds_write_b128 v113, v[224:227] offset:0\n\t" \
    "v_cvt_pk_bf16_f32 v40, v208, v209\n\t" \
    "v_mfma_f32_16x16x32_bf16 v[244:247], v[220:223], v[192:195], v[244:247]\n\t" \
    "v_cvt_pk_bf16_f32 v41, v210, v211\n\t" \
    "ds_write_b128 v113, v[40:43] offset:1024\n\t" \
    "v_mfma_f32_16x16x32_bf16 v[44:47], v[220:223], v[196:199], v[44:47]\n\t" \
    "s_waitcnt vmcnt(2)\n\t" \
    "s_cmp_ge_u32 s24, 4\n\t" \
    "s_cbranch_scc1 .Lk36_%=\n\t" \
    "v_mfma_f32_16x16x32_bf16 v[204:207], v[240:243], v[56:59], 0\n\t" \
    "v_mfma_f32_16x16x32_bf16 v[204:207], v[248:251], v[100:103], v[204:207]\n\t" \
    "v_mfma_f32_16x16x32_bf16 v[204:207], v[168:171], v[124:127], v[204:207]\n\t" \
    "v_mfma_f32_16x16x32_bf16 v[36:39], v[240:243], v[80:83], 0\n\t" \
    "v_mfma_f32_16x16x32_bf16 v[36:39], v[248:251], v[48:51], v[36:39]\n\t" \
    "v_mfma_f32_16x16x32_bf16 v[36:39], v[168:171], v[196:199], v[36:39]\n\t" \
    "v_mfma_f32_16x16x32_bf16 v[236:239], v[240:243], v[84:87], v[236:239]\n\t" \
    "v_mfma_f32_16x16x32_bf16 v[236:239], v[248:251], v[88:91], v[236:239]\n\t" \
    "v_mfma_f32_16x16x32_bf16 v[236:239], v[168:171], v[120:123], v[236:239]\n\t" \
    "v_mfma_f32_16x16x32_bf16 v[44:47], v[240:243], v[60:63], v[44:47]\n\t" \
    "s_nop 0\n\t" \
    "v_sub_f32 v32, v32, v204\n\t" \
    "v_sub_f32 v33, v33, v205\n\t" \
    "v_mfma_f32_16x16x32_bf16 v[44:47], v[248:251], v[104:107], v[44:47]\n\t" \
    "v_sub_f32 v34, v34, v206\n\t" \
    "v_sub_f32 v35, v35, v207\n\t" \
    "v_mfma_f32_16x16x32_bf16 v[44:47], v[168:171], v[192:195], v[44:47]\n\t" \
    "v_sub_f32 v244, v244, v36\n\t" \
    "v_sub_f32 v245, v245, v37\n\t" \
    "v_sub_f32 v246, v246, v38\n\t" \
    "v_sub_f32 v247, v247, v39\n\t" \
    ".Lk36_%=:\n\t" \
    "ds_read_b128 v[172:175], v10 offset:3072\n\t" \
    "ds_read_b128 v[20:23], v10 offset:4096\n\t" \
    "v_cvt_pk_bf16_f32 v142, v236, v237\n\t" \
    "v_cvt_pk_bf16_f32 v143, v238, v239\n\t" \
    "v_cvt_pk_bf16_f32 v140, v32, v33\n\t" \
    "v_cvt_pk_bf16_f32 v141, v34, v35\n\t" \
    "v_cvt_pk_bf16_f32 v254, v44, v45\n\t" \
    "v_cvt_pk_bf16_f32 v255, v46, v47\n\t" \
    "ds_write_b128 v113, v[140:143] offset:2064\n\t" \
    "v_cvt_pk_bf16_f32 v252, v244, v245\n\t" \
    "v_cvt_pk_bf16_f32 v253, v246, v247\n\t" \
    "v_add_u32 v12, 91392, v12\n\t" \
    "ds_write_b128 v113, v[252:255] offset:3088\n\t" \
    "s_waitcnt lgkmcnt(0)\n\t" \
    "s_sub_u32 s37, s16, 300\n\t" \
    "s_mov_b32 s62, 300\n\t" \
    "s_sub_u32 s39, s58, s37\n\t" \
    "s_cmp_gt_u32 s39, 600\n\t" \
    "s_cselect_b32 s39, 400, s39\n\t" \
    "s_max_u32 s62, s62, s39\n\t" \
    "s_sub_u32 s39, s59, s37\n\t" \
    "s_cmp_gt_u32 s39, 600\n\t" \
    "s_cselect_b32 s39, 400, s39\n\t" \
    "s_max_u32 s62, s62, s39\n\t" \
    "s_sub_u32 s39, s60, s37\n\t" \
    "s_cmp_gt_u32 s39, 600\n\t" \
    "s_cselect_b32 s39, 400, s39\n\t" \
    "s_max_u32 s62, s62, s39\n\t" \
    "s_sub_u32 s39, s61, s37\n\t" \
    "s_cmp_gt_u32 s39, 600\n\t" \
    "s_cselect_b32 s39, 400, s39\n\t" \
    "s_max_u32 s62, s62, s39\n\t" \
    "s_sub_u32 s62, s62, 300\n\t" \
    "s_mul_i32 s62, s62, 5\n\t" \
    "s_lshr_b32 s62, s62, 2\n\t" \
    "s_waitcnt lgkmcnt(0)\n\t" \
    "s_barrier\n\t" \
    "s_memrealtime s[56:57]\n\t" \
    "s_add_u32 s54, s53, 0\n\t" \
    "buffer_load_dwordx4 v[92:95], v0, s[4:7], s54 offen offset:0 sc1\n\t" \
    "s_add_u32 s54, s53, 4243456\n\t" \
    "buffer_load_dwordx4 v[144:147], v0, s[4:7], s54 offen offset:0 sc1\n\t" \
    "ds_read_b64_tr_b16 v[72:73], v12 offset:0\n\t" \
    "ds_read_b64_tr_b16 v[74:75], v12 offset:4128\n\t" \
    "ds_read_b64_tr_b16 v[68:69], v12 offset:256\n\t" \
    "ds_read_b64_tr_b16 v[70:71], v12 offset:4384\n\t" \
    "ds_read_b64_tr_b16 v[148:149], v12 offset:16\n\t" \
    "ds_read_b64_tr_b16 v[150:151], v12 offset:4144\n\t" \
    "ds_read_b64_tr_b16 v[156:157], v12 offset:272\n\t" \
    "ds_read_b64_tr_b16 v[158:159], v12 offset:4400\n\t" \
    "s_waitcnt lgkmcnt(6)\n\t" \
    "v_mfma_f32_16x16x32_bf16 v[136:139], v[72:75], v[16:19], 0\n\t" \
    "s_waitcnt lgkmcnt(4)\n\t" \
    "v_mfma_f32_16x16x32_bf16 v[212:215], v[68:71], v[16:19], 0\n\t" \
    "s_waitcnt lgkmcnt(2)\n\t" \
    "v_mfma_f32_16x16x32_bf16 v[96:99], v[148:151], v[16:19], 0\n\t" \
    "s_waitcnt lgkmcnt(0)\n\t" \
    "v_mfma_f32_16x16x32_bf16 v[128:131], v[156:159], v[16:19], 0\n\t" \
    "s_nop 1\n\t" \
    "v_bfi_b32 v136, s41, v8, v136\n\t" \
    "s_nop 0\n\t" \
    "v_bfi_b32 v212, s41, v8, v212\n\t" \
    "v_cvt_pk_bf16_f32 v165, v138, v139\n\t" \
    "v_cvt_pk_bf16_f32 v167, v214, v215\n\t" \
    "v_cvt_pk_bf16_f32 v164, v136, v137\n\t" \
    "v_cvt_pk_bf16_f32 v166, v212, v213\n\t" \
    "v_bfi_b32 v96, s41, v8, v96\n\t" \
    "v_bfi_b32 v128, s41, v8, v128\n\t" \
    "buffer_store_dwordx4 v[164:167], v111, s[4:7], s50 offen offset:0 sc1\n\t" \
    "v_cvt_pk_bf16_f32 v185, v98, v99\n\t" \
    "v_cvt_pk_bf16_f32 v187, v130, v131\n\t" \
    "v_cvt_pk_bf16_f32 v184, v96, v97\n\t" \
    "v_cvt_pk_bf16_f32 v186, v128, v129\n\t" \
    "buffer_store_dwordx4 v[184:187], v111, s[4:7], s50 offen offset:1024 sc1\n\t" \
    "s_cmp_ge_u32 s25, 252\n\t" \
    "s_cbranch_scc1 .Lend_%=\n\t" \
    "s_add_u32 s37, s62, 90\n\t" \
    ".Lh40_%=:\n\t" \
    "s_memrealtime s[22:23]\n\t" \
    "s_waitcnt lgkmcnt(0)\n\t" \
    "s_sub_u32 s22, s22, s18\n\t" \
    "s_cmp_lt_u32 s22, s37\n\t" \
    "s_cbranch_scc1 .Lh40_%=\n\t" \
    "s_add_u32 s54, s53, 524288\n\t" \
    "buffer_load_dwordx4 v[132:135], v0, s[4:7], s54 offen offset:0 sc1\n\t" \
    "s_add_u32 s54, s53, 4767744\n\t" \
    "buffer_load_dwordx4 v[152:155], v0, s[4:7], s54 offen offset:0 sc1\n\t" \
    "s_waitcnt vmcnt(4)\n\t" \
    "v_and_b32 v1, v92, v94\n\t" \
    "v_bitop3_b32 v1, v1, v144, v146 bitop3:0x80\n\t" \
    "v_and_b32 v1, 1, v1\n\t" \
    "v_cmp_eq_u32 vcc, 0, v1\n\t" \
    "s_nop 1\n\t" \
    "s_cbranch_vccnz .Ls37_%=\n\t" \
    ".Lb37_%=:\n\t" \
    "ds_write_b128 v3, v[92:95] offset:33024\n\t" \
    "ds_write_b128 v3, v[144:147] offset:33152\n\t" \
    "s_waitcnt vmcnt(0)\n\t" \
    "v_and_b32 v25, v132, v134\n\t" \
    "v_bitop3_b32 v25, v25, v152, v154 bitop3:0x80\n\t" \
    "v_and_b32 v25, 1, v25\n\t" \
    "v_cmp_eq_u32 vcc, 0, v25\n\t" \
    "s_nop 1\n\t" \
    "s_cbranch_vccnz .Ls38_%=\n\t" \
    ".Lb38_%=:\n\t" \
    "ds_write_b128 v3, v[132:135] offset:0\n\t" \
    "ds_write_b128 v3, v[152:155] offset:128\n\t" \
    "s_waitcnt lgkmcnt(0)\n\t" \
    "s_barrier\n\t" \
    "ds_read_b64_tr_b16 v[64:65], v26 offset:33024\n\t" \
    "ds_read_b64_tr_b16 v[66:67], v26 offset:33280\n\t" \
    "ds_read_b64_tr_b16 v[160:161], v26 offset:33032\n\t" \
    "ds_read_b64_tr_b16 v[162:163], v26 offset:33288\n\t" \
    "ds_read_b64_tr_b16 v[188:189], v26 offset:0\n\t" \
    "ds_read_b64_tr_b16 v[190:191], v26 offset:256\n\t" \
    "ds_read_b64_tr_b16 v[228:229], v26 offset:8\n\t" \
    "ds_read_b64_tr_b16 v[230:231], v26 offset:264\n\t" \
    "s_waitcnt lgkmcnt(6)\n\t" \
    "v_mfma_f32_16x16x32_bf16 v[52:55], v[64:67], v[172:175], 0\n\t" \
    "s_waitcnt lgkmcnt(4)\n\t" \
    "v_mfma_f32_16x16x32_bf16 v[28:31], v[160:163], v[172:175], 0\n\t" \
    "v_mfma_f32_16x16x32_bf16 v[200:203], v[64:67], v[20:23], 0\n\t" \
    "v_mfma_f32_16x16x32_bf16 v[180:183], v[160:163], v[20:23], 0\n\t" \
    "s_nop 3\n\t" \
    "v_cvt_pk_bf16_f32 v116, v52, v53\n\t" \
    "v_cvt_pk_bf16_f32 v117, v54, v55\n\t" \
    "v_cvt_pk_bf16_f32 v118, v28, v29\n\t" \
    "v_cvt_pk_bf16_f32 v119, v30, v31\n\t" \
    "v_cvt_pk_bf16_f32 v76, v200, v201\n\t" \
    "v_cvt_pk_bf16_f32 v77, v202, v203\n\t" \
    "v_cvt_pk_bf16_f32 v78, v180, v181\n\t" \
    "v_cvt_pk_bf16_f32 v79, v182, v183\n\t" \
    "s_waitcnt lgkmcnt(2)\n\t" \
    "v_mfma_f32_16x16x32_bf16 v[216:219], v[188:191], v[172:175], 0\n\t" \
    "s_waitcnt lgkmcnt(0)\n\t" \
    "v_mfma_f32_16x16x32_bf16 v[4:7], v[228:231], v[172:175], 0\n\t" \
    "v_mfma_f32_16x16x32_bf16 v[232:235], v[188:191], v[20:23], 0\n\t" \
    "v_mfma_f32_16x16x32_bf16 v[176:179], v[228:231], v[20:23], 0\n\t" \
    "s_nop 3\n\t" \
    "v_cvt_pk_bf16_f32 v224, v216, v217\n\t" \
    "v_cvt_pk_bf16_f32 v225, v218, v219\n\t" \
    "v_cvt_pk_bf16_f32 v226, v4, v5\n\t" \
    "v_cvt_pk_bf16_f32 v227, v6, v7\n\t" \
    "v_cvt_pk_bf16_f32 v208, v232, v233\n\t" \
    "v_cvt_pk_bf16_f32 v209, v234, v235\n\t" \
    "v_cvt_pk_bf16_f32 v210, v176, v177\n\t" \
    "v_cvt_pk_bf16_f32 v211, v178, v179\n\t" \
    "s_add_u32 s37, s62, 85\n\t" \
    ".Lh41_%=:\n\t" \
    "s_memrealtime s[22:23]\n\t" \
    "s_waitcnt lgkmcnt(0)\n\t" \
    "s_sub_u32 s22, s22, s56\n\t" \
    "s_cmp_lt_u32 s22, s37\n\t" \
    "s_cbranch_scc1 .Lh41_%=\n\t" \
    "s_add_u32 s54, s53, 1048576\n\t" \
    "buffer_load_dwordx4 v[40:43], v0, s[4:7], s54 offen offset:0 sc1\n\t" \
    "s_add_u32 s54, s53, 5292032\n\t" \
    "buffer_load_dwordx4 v[220:223], v0, s[4:7], s54 offen offset:0 sc1\n\t" \
    "s_waitcnt lgkmcnt(0)\n\t" \
    "s_barrier\n\t" \
    "buffer_store_dwordx4 v[116:119], v108, s[8:11], 0 offen offset:0 sc1\n\t" \
    "buffer_store_dwordx4 v[76:79], v108, s[8:11], 0 offen offset:3072 sc1\n\t" \
    "buffer_store_dwordx4 v[224:227], v108, s[8:11], 0 offen offset:64 sc1\n\t" \
    "buffer_store_dwordx4 v[208:211], v108, s[8:11], 0 offen offset:3136 sc1\n\t" \
    "s_waitcnt vmcnt(4)\n\t" \
    "v_and_b32 v24, v40, v42\n\t" \
    "v_bitop3_b32 v24, v24, v220, v222 bitop3:0x80\n\t" \
    "v_and_b32 v24, 1, v24\n\t" \
    "v_cmp_eq_u32 vcc, 0, v24\n\t" \
    "s_nop 1\n\t" \
    "s_cbranch_vccnz .Ls39_%=\n\t" \
    ".Lb39_%=:\n\t" \
    "ds_write_b128 v3, v[40:43] offset:16512\n\t" \
    "ds_write_b128 v3, v[220:223] offset:16640\n\t" \
    "s_waitcnt lgkmcnt(0)\n\t" \
    "s_barrier\n\t" \
    "ds_read_b64_tr_b16 v[56:57], v26 offset:16512\n\t" \
    "ds_read_b64_tr_b16 v[58:59], v26 offset:16768\n\t" \
    "ds_read_b64_tr_b16 v[100:101], v26 offset:16520\n\t" \
    "ds_read_b64_tr_b16 v[102:103], v26 offset:16776\n\t" \
    "s_waitcnt lgkmcnt(2)\n\t" \
    "v_mfma_f32_16x16x32_bf16 v[124:127], v[56:59], v[172:175], 0\n\t" \
    "s_waitcnt lgkmcnt(0)\n\t" \
    "v_mfma_f32_16x16x32_bf16 v[80:83], v[100:103], v[172:175], 0\n\t" \
    "v_mfma_f32_16x16x32_bf16 v[48:51], v[56:59], v[20:23], 0\n\t" \
    "v_mfma_f32_16x16x32_bf16 v[196:199], v[100:103], v[20:23], 0\n\t" \
    "s_nop 3\n\t" \
    "v_cvt_pk_bf16_f32 v84, v124, v125\n\t" \
    "v_cvt_pk_bf16_f32 v85, v126, v127\n\t" \
    "v_cvt_pk_bf16_f32 v86, v80, v81\n\t" \
    "v_cvt_pk_bf16_f32 v87, v82, v83\n\t" \
    "buffer_store_dwordx4 v[84:87], v108, s[8:11], 0 offen offset:128 sc1\n\t" \
    "v_cvt_pk_bf16_f32 v88, v48, v49\n\t" \
    "v_cvt_pk_bf16_f32 v89, v50, v51\n\t" \
    "v_cvt_pk_bf16_f32 v90, v196, v197\n\t" \
    "v_cvt_pk_bf16_f32 v91, v198, v199\n\t" \
    "buffer_store_dwordx4 v[88:91], v108, s[8:11], 0 offen offset:3200 sc1\n\t" \
    "s_or_b32 s37, s2, s24\n\t" \
    "s_cbranch_scc1 .Lend_%=\n\t" \
    "s_mov_b32 s37, 34635776\n\t" \
    "v_mov_b32 v109, s34\n\t" \
    "s_mov_b64 exec, 1\n\t" \
    "buffer_store_dword v109, v0, s[4:7], s37 offen sc1\n\t" \
    "s_mov_b64 exec, -1\n\t" \
    ".Lend_%=:\n\t" \
    "s_endpgm\n\t" \
    ".Lq1024_%=:\n\t" \
    "s_mov_b32 s40, 0\n\t" \
    ".Lr1024_%=:\n\t" \
    "s_add_u32 s12, s55, 34603008\n\t" \
    "s_buffer_load_dword s13, s[4:7], s12 glc\n\t" \
    "s_buffer_load_dword s14, s[4:7], s12 offset:0x400 glc\n\t" \
    "s_waitcnt lgkmcnt(0)\n\t" \
    "s_xor_b32 s13, s13, s34\n\t" \
    "s_xor_b32 s14, s14, s34\n\t" \
    "s_or_b32 s13, s13, s14\n\t" \
    "s_cbranch_scc0 .Lb23_%=\n\t" \
    "s_add_u32 s40, s40, 1\n\t" \
    "s_cmp_lt_u32 s40, 1\n\t" \
    "s_cbranch_scc1 .Lr1024_%=\n\t" \
    "s_branch .Ls23_%=\n\t" \
    ".Lq1021_%=:\n\t" \
    "s_mov_b32 s40, 0\n\t" \
    ".Lr1021_%=:\n\t" \
    "s_nop 0\n\t" \
    "v_readlane_b32 s12, v112, 0\n\t" \
    "v_readlane_b32 s13, v112, 1\n\t" \
    "v_readlane_b32 s14, v112, 2\n\t" \
    "v_readlane_b32 s15, v112, 3\n\t" \
    "v_readlane_b32 s16, v112, 4\n\t" \
    "v_readlane_b32 s17, v112, 5\n\t" \
    "v_readlane_b32 s18, v112, 6\n\t" \
    "v_readlane_b32 s19, v112, 7\n\t" \
    "v_readlane_b32 s3, v112, 8\n\t" \
    "s_add_u32 s12, s12, s33\n\t" \
    "s_add_u32 s13, s13, s33\n\t" \
    "s_add_u32 s14, s14, s33\n\t" \
    "s_add_u32 s15, s15, s33\n\t" \
    "s_add_u32 s16, s16, s33\n\t" \
    "s_add_u32 s17, s17, s33\n\t" \
    "s_add_u32 s18, s18, s33\n\t" \
    "s_add_u32 s19, s19, s33\n\t" \
    "s_add_u32 s3, s3, s33\n\t" \
    "s_buffer_load_dword s12, s[4:7], s12 offset:0xc0000 glc\n\t" \
    "s_buffer_load_dword s13, s[4:7], s13 offset:0xc0000 glc\n\t" \
    "s_buffer_load_dword s14, s[4:7], s14 offset:0xc0000 glc\n\t" \
    "s_buffer_load_dword s15, s[4:7], s15 offset:0xc0000 glc\n\t" \
    "s_buffer_load_dword s16, s[4:7], s16 offset:0xc0000 glc\n\t" \
    "s_buffer_load_dword s17, s[4:7], s17 offset:0xc0000 glc\n\t" \
    "s_buffer_load_dword s18, s[4:7], s18 offset:0xc0000 glc\n\t" \
    "s_buffer_load_dword s19, s[4:7], s19 offset:0xc0000 glc\n\t" \
    "s_buffer_load_dword s3, s[4:7], s3 offset:0xc0000 glc\n\t" \
    "s_waitcnt lgkmcnt(0)\n\t" \
    "s_xor_b32 s12, s12, s34\n\t" \
    "s_xor_b32 s13, s13, s34\n\t" \
    "s_xor_b32 s14, s14, s34\n\t" \
    "s_xor_b32 s15, s15, s34\n\t" \
    "s_xor_b32 s16, s16, s34\n\t" \
    "s_xor_b32 s17, s17, s34\n\t" \
    "s_xor_b32 s18, s18, s34\n\t" \
    "s_xor_b32 s19, s19, s34\n\t" \
    "s_xor_b32 s3, s3, s34\n\t" \
    "s_or_b32 s12, s12, s13\n\t" \
    "s_or_b32 s12, s12, s14\n\t" \
    "s_or_b32 s12, s12, s15\n\t" \
    "s_or_b32 s12, s12, s16\n\t" \
    "s_or_b32 s12, s12, s17\n\t" \
    "s_or_b32 s12, s12, s18\n\t" \
    "s_or_b32 s12, s12, s19\n\t" \
    "s_or_b32 s12, s12, s3\n\t" \
    "s_cbranch_scc0 .Lb20_%=\n\t" \
    "s_add_u32 s40, s40, 1\n\t" \
    "s_cmp_lt_u32 s40, 1\n\t" \
    "s_cbranch_scc1 .Lr1021_%=\n\t" \
    "s_branch .Ls20_%=\n\t" \
    ".Lpb_%=:\n\t" \
    "s_cmp_lg_u32 s2, 255\n\t" \
    "s_cbranch_scc1 .Le2_%=\n\t" \
    "s_mov_b32 s6, 34668544\n\t" \
    "s_mov_b32 s7, 0x20000\n\t" \
    "s_mul_i32 s38, s37, 0x2100\n\t" \
    "s_and_b32 s38, s38, 0x2300\n\t" \
    "s_add_u32 s38, s38, 34652160\n\t" \
    "s_mov_b32 s60, -1\n\t" \
    "s_mov_b32 s61, -1\n\t" \
    "s_memrealtime s[62:63]\n\t" \
    "s_mov_b64 exec, 0xff\n\t" \
    ".Lpc1_%=:\n\t" \
    "s_memtime s[56:57]\n\t" \
    "s_waitcnt lgkmcnt(0)\n\t" \
    "buffer_store_dwordx4 v[12:15], v0, s[4:7], s38 offen sc1\n\t" \
    "s_waitcnt vmcnt(0)\n\t" \
    "s_memtime s[58:59]\n\t" \
    "s_memrealtime s[22:23]\n\t" \
    "s_waitcnt lgkmcnt(0)\n\t" \
    "s_sub_u32 s56, s58, s56\n\t" \
    "s_min_u32 s56, s56, s60\n\t" \
    "s_mov_b32 s60, s61\n\t" \
    "s_mov_b32 s61, s56\n\t" \
    "s_xor_b32 s38, s38, 0x2000\n\t" \
    "s_add_u32 s38, s38, 0x400\n\t" \
    "s_andn2_b32 s38, s38, 0x1000\n\t" \
    "s_sub_u32 s22, s22, s62\n\t" \
    "s_cmp_lt_u32 s22, 200\n\t" \
    "s_cbranch_scc1 .Lpc1_%=\n\t" \
    "s_mov_b64 exec, -1\n\t" \
    "s_bitcmp1_b32 s38, 13\n\t" \
    "s_cselect_b32 s56, s61, s60\n\t" \
    "s_cselect_b32 s57, s60, s61\n\t" \
    "v_mov_b32 v8, s56\n\t" \
    "v_mov_b32 v9, s57\n\t" \
    "s_lshl3_add_u32 s37, s37, 156992\n\t" \
    "v_mov_b32 v10, s37\n\t" \
    "ds_write_b64 v10, v[8:9]\n\t" \
    "s_branch .Le2_%=\n\t" \
    ".Lr2_%=:\n\t" \
    "s_add_u32 s40, s40, 1\n\t" \
    "s_cmp_gt_u32 s40, 32\n\t" \
    "s_cbranch_scc1 .Lr3_%=\n\t" \
    "s_sleep 2\n\t" \
    "s_and_b32 s37, s2, 7\n\t" \
    "s_lshl_b32 s37, s37, 10\n\t" \
    "s_add_u32 s37, s37, 34643968\n\t" \
    "v_and_b32 v17, 0x70, v0\n\t" \
    "v_lshlrev_b32 v17, 3, v17\n\t" \
    "buffer_load_dword v16, v17, s[4:7], s37 offen sc1\n\t" \
    "s_waitcnt vmcnt(0)\n\t" \
    "v_readlane_b32 s56, v16, 0\n\t" \
    "v_readlane_b32 s57, v16, 1\n\t" \
    "v_readlane_b32 s58, v16, 2\n\t" \
    "v_readlane_b32 s59, v16, 3\n\t" \
    "v_readlane_b32 s60, v16, 4\n\t" \
    "v_readlane_b32 s61, v16, 5\n\t" \
    "v_readlane_b32 s62, v16, 6\n\t" \
    "v_readlane_b32 s63, v16, 7\n\t" \
    "s_branch .Lr1_%=\n\t" \
    ".Lpp_%=:\n\t" \
    "ds_read_b128 v[80:83], v9 offset:64\n\t" \
    "ds_read_b128 v[84:87], v9 offset:80\n\t" \
    "s_getreg_b32 s37, hwreg(HW_REG_XCC_ID, 0, 2)\n\t" \
    "s_waitcnt lgkmcnt(0)\n\t" \
    "v_min3_u32 v80, v80, v82, v84\n\t" \
    "v_min3_u32 v81, v81, v83, v85\n\t" \
    "v_min_u32 v80, v80, v86\n\t" \
    "v_min_u32 v81, v81, v87\n\t" \
    "v_cmp_gt_u32 vcc, v80, v81\n\t" \
    "s_nop 4\n\t" \
    "s_and_b32 s38, vcc_lo, 4\n\t" \
    "s_add_u32 s37, s37, s38\n\t" \
    "s_add_u32 s37, s37, 1\n\t" \
    "s_and_b32 s37, s37, 7\n\t" \
    "s_lshl_b32 s37, s37, 1\n\t" \
    "s_xor_b32 s37, s37, s34\n\t" \
    "v_mov_b32 v80, s37\n\t" \
    "v_mov_b32 v81, s37\n\t" \
    "v_mov_b32 v82, s37\n\t" \
    "v_mov_b32 v83, s37\n\t" \
    "s_mov_b32 s39, 34636800\n\t" \
    "buffer_store_dwordx4 v[80:83], v0, s[4:7], s39 offen sc1\n\t" \
    "s_nop 1\n\t" \
    "s_branch .Lpq_%=\n\t" \
    ".Lr5_%=:\n\t" \
    "s_add_u32 s40, s40, 1\n\t" \
    "s_cmp_gt_u32 s40, 8192\n\t" \
    "s_cbranch_scc1 .Lr6_%=\n\t" \
    "s_sleep 4\n\t" \
    "v_mov_b32 v15, 0\n\t" \
    "buffer_load_dword v13, v15, s[4:7], s52 offen sc1\n\t" \
    "s_waitcnt vmcnt(0)\n\t" \
    "v_readfirstlane_b32 s36, v13\n\t" \
    "s_branch .Lr4_%=\n\t" \
    ".Ls20_%=:\n\t" \
    "s_mov_b32 s40, 0\n\t" \
    "s_add_u32 s38, s33, 786432\n\t" \
    ".Ll20_%=:\n\t" \
    "s_sleep 2\n\t" \
    "buffer_load_dword v24, v112, s[4:7], s38 offen sc1\n\t" \
    "s_waitcnt vmcnt(0)\n\t" \
    "v_cmp_ne_u32 vcc, s34, v24\n\t" \
    "s_add_u32 s40, s40, 1\n\t" \
    "s_cmp_ge_u32 s40, 8192\n\t" \
    "s_cbranch_scc1 .Lb20_%=\n\t" \
    "s_cbranch_vccnz .Ll20_%=\n\t" \
    "s_branch .Lb20_%=\n\t" \
    ".Ls23_%=:\n\t" \
    "s_mov_b32 s40, 0\n\t" \
    "s_add_u32 s38, s55, 34603008\n\t" \
    ".Ll23_%=:\n\t" \
    "s_sleep 2\n\t" \
    "buffer_load_dword v114, v115, s[4:7], s38 offen sc1\n\t" \
    "s_waitcnt vmcnt(0)\n\t" \
    "v_cmp_ne_u32 vcc, s34, v114\n\t" \
    "s_add_u32 s40, s40, 1\n\t" \
    "s_cmp_ge_u32 s40, 8192\n\t" \
    "s_cbranch_scc1 .Lb23_%=\n\t" \
    "s_cbranch_vccnz .Ll23_%=\n\t" \
    "s_branch .Lb23_%=\n\t" \
    ".Ls37_%=:\n\t" \
    "s_mov_b32 s40, 0\n\t" \
    ".Ll37_%=:\n\t" \
    "s_add_u32 s54, s53, 0\n\t" \
    "buffer_load_dwordx4 v[92:95], v0, s[4:7], s54 offen offset:0 sc1\n\t" \
    "s_add_u32 s54, s53, 4243456\n\t" \
    "buffer_load_dwordx4 v[144:147], v0, s[4:7], s54 offen offset:0 sc1\n\t" \
    "s_waitcnt vmcnt(0)\n\t" \
    "v_and_b32 v1, v92, v94\n\t" \
    "v_bitop3_b32 v1, v1, v144, v146 bitop3:0x80\n\t" \
    "v_and_b32 v1, 1, v1\n\t" \
    "v_cmp_eq_u32 vcc, 0, v1\n\t" \
    "s_add_u32 s40, s40, 1\n\t" \
    "s_cmp_ge_u32 s40, 8192\n\t" \
    "s_cbranch_scc1 .Lb37_%=\n\t" \
    "s_cbranch_vccnz .Ll37_%=\n\t" \
    "s_branch .Lb37_%=\n\t" \
    ".Ls38_%=:\n\t" \
    "s_mov_b32 s40, 0\n\t" \
    ".Ll38_%=:\n\t" \
    "s_add_u32 s54, s53, 524288\n\t" \
    "buffer_load_dwordx4 v[132:135], v0, s[4:7], s54 offen offset:0 sc1\n\t" \
    "s_add_u32 s54, s53, 4767744\n\t" \
    "buffer_load_dwordx4 v[152:155], v0, s[4:7], s54 offen offset:0 sc1\n\t" \
    "s_waitcnt vmcnt(0)\n\t" \
    "v_and_b32 v25, v132, v134\n\t" \
    "v_bitop3_b32 v25, v25, v152, v154 bitop3:0x80\n\t" \
    "v_and_b32 v25, 1, v25\n\t" \
    "v_cmp_eq_u32 vcc, 0, v25\n\t" \
    "s_add_u32 s40, s40, 1\n\t" \
    "s_cmp_ge_u32 s40, 8192\n\t" \
    "s_cbranch_scc1 .Lb38_%=\n\t" \
    "s_cbranch_vccnz .Ll38_%=\n\t" \
    "s_branch .Lb38_%=\n\t" \
    ".Ls39_%=:\n\t" \
    "s_mov_b32 s40, 0\n\t" \
    ".Ll39_%=:\n\t" \
    "s_add_u32 s54, s53, 1048576\n\t" \
    "buffer_load_dwordx4 v[40:43], v0, s[4:7], s54 offen offset:0 sc1\n\t" \
    "s_add_u32 s54, s53, 5292032\n\t" \
    "buffer_load_dwordx4 v[220:223], v0, s[4:7], s54 offen offset:0 sc1\n\t" \
    "s_waitcnt vmcnt(0)\n\t" \
    "v_and_b32 v24, v40, v42\n\t" \
    "v_bitop3_b32 v24, v24, v220, v222 bitop3:0x80\n\t" \
    "v_and_b32 v24, 1, v24\n\t" \
    "v_cmp_eq_u32 vcc, 0, v24\n\t" \
    "s_add_u32 s40, s40, 1\n\t" \
    "s_cmp_ge_u32 s40, 8192\n\t" \
    "s_cbranch_scc1 .Lb39_%=\n\t" \
    "s_cbranch_vccnz .Ll39_%=\n\t" \
    "s_branch .Lb39_%=\n\t" \
    ".Lfin_%=:\n\t"
#define K_CLOBBERS "v1", "v2", "v3", "v4", "v5", "v6", "v7", "v8", "v9", "v10", "v11", "v12", "v13", "v14", "v15", "v16", "v17", "v18", "v19", "v20", "v21", "v22", "v23", "v24", "v25", "v26", "v27", "v28", "v29", "v30", "v31", "v32", "v33", "v34", "v35", "v36", "v37", "v38", "v39", "v40", "v41", "v42", "v43", "v44", "v45", "v46", "v47", "v48", "v49", "v50", "v51", "v52", "v53", "v54", "v55", "v56", "v57", "v58", "v59", "v60", "v61", "v62", "v63", "v64", "v65", "v66", "v67", "v68", "v69", "v70", "v71", "v72", "v73", "v74", "v75", "v76", "v77", "v78", "v79", "v80", "v81", "v82", "v83", "v84", "v85", "v86", "v87", "v88", "v89", "v90", "v91", "v92", "v93", "v94", "v95", "v96", "v97", "v98", "v99", "v100", "v101", "v102", "v103", "v104", "v105", "v106", "v107", "v108", "v109", "v110", "v111", "v112", "v113", "v114", "v115", "v116", "v117", "v118", "v119", "v120", "v121", "v122", "v123", "v124", "v125", "v126", "v127", "v128", "v129", "v130", "v131", "v132", "v133", "v134", "v135", "v136", "v137", "v138", "v139", "v140", "v141", "v142", "v143", "v144", "v145", "v146", "v147", "v148", "v149", "v150", "v151", "v152", "v153", "v154", "v155", "v156", "v157", "v158", "v159", "v160", "v161", "v162", "v163", "v164", "v165", "v166", "v167", "v168", "v169", "v170", "v171", "v172", "v173", "v174", "v175", "v176", "v177", "v178", "v179", "v180", "v181", "v182", "v183", "v184", "v185", "v186", "v187", "v188", "v189", "v190", "v191", "v192", "v193", "v194", "v195", "v196", "v197", "v198", "v199", "v200", "v201", "v202", "v203", "v204", "v205", "v206", "v207", "v208", "v209", "v210", "v211", "v212", "v213", "v214", "v215", "v216", "v217", "v218", "v219", "v220", "v221", "v222", "v223", "v224", "v225", "v226", "v227", "v228", "v229", "v230", "v231", "v232", "v233", "v234", "v235", "v236", "v237", "v238", "v239", "v240", "v241", "v242", "v243", "v244", "v245", "v246", "v247", "v248", "v249", "v250", "v251", "v252", "v253", "v254", "v255", "s3", "s4", "s5", "s6", "s7", "s8", "s9", "s10", "s11", "s12", "s13", "s14", "s15", "s16", "s17", "s18", "s19", "s20", "s21", "s22", "s23", "s24", "s25", "s26", "s27", "s28", "s29", "s30", "s31", "s32", "s33", "s34", "s35", "s36", "s37", "s38", "s39", "s40", "s41", "s42", "s43", "s44", "s45", "s46", "s47", "s48", "s49", "s50", "s51", "s52", "s53", "s54", "s55", "s56", "s57", "s58", "s59", "s60", "s61", "s62", "s63", "s64", "s65"

extern "C" __global__ __launch_bounds__(512, 2)
void k_conv(const char* __restrict__ x, const char* __restrict__ wgt, char* __restrict__ ws, char* __restrict__ out)
{
  __shared__ __attribute__((aligned(256))) char smem[157568];
  unsigned tid = threadIdx.x;
  auto kp = __builtin_amdgcn_kernarg_segment_ptr();
  asm volatile(K_BODY : "+{v0}"(tid), "+{s[0:1]}"(kp)
               : "{s2}"(blockIdx.x), "i"((__attribute__((address_space(3))) char*)smem)
               : "memory", "scc", "vcc", K_CLOBBERS);
}

extern "C" void kernel_launch(void* const* d_in, const int* in_sizes, int n_in,
                              void* d_out, int out_size, void* d_ws, size_t ws_size,
                              hipStream_t stream) {
  k_conv<<<dim3(256), dim3(512), 0, stream>>>((const char*)d_in[0], (const char*)d_in[1], (char*)d_ws, (char*)d_out);
}
```

```cpp
#include <hip/hip_runtime.h>
#include <hip/hip_bf16.h>


#define K_BODY \
    "s_load_dwordx2 s[16:17], s[0:1], 0x0\n\t" \
    "s_load_dwordx2 s[4:5], s[0:1], 0x10\n\t" \
    "s_load_dwordx2 s[12:13], s[0:1], 0x8\n\t" \
    "s_load_dwordx2 s[8:9], s[0:1], 0x18\n\t" \
    "s_getpc_b64 s[22:23]\n\t" \
    ".Lpc_%=:\n\t" \
    "s_cmp_ge_u32 s2, 16\n\t" \
    "s_cbranch_scc1 .Le1_%=\n\t" \
    "s_mov_b32 m0, 157312\n\t" \
    "v_lshlrev_b32 v8, 6, v0\n\t" \
    "v_min_u32 v8, .Lfin_%=-.Lpc_%=, v8\n\t" \
    "global_load_lds_dword v8, s[22:23]\n\t" \
    ".Le1_%=:\n\t" \
    "v_readfirstlane_b32 s24, v0\n\t" \
    "s_and_b32 s25, s2, 7\n\t" \
    "s_lshl_b32 s25, s25, 5\n\t" \
    "s_lshr_b32 s37, s2, 3\n\t" \
    "s_add_u32 s25, s25, s37\n\t" \
    "s_min_u32 s28, s25, 251\n\t" \
    "s_mul_i32 s26, s28, 57\n\t" \
    "s_lshr_b32 s26, s26, 9\n\t" \
    "s_mul_i32 s37, s26, 9\n\t" \
    "s_sub_u32 s27, s28, s37\n\t" \
    "v_and_b32 v0, 63, v0\n\t" \
    "v_lshlrev_b32 v0, 4, v0\n\t" \
    "s_lshr_b32 s24, s24, 6\n\t" \
    "s_sub_u32 s37, s24, 4\n\t" \
    "s_cbranch_scc1 .Le2_%=\n\t" \
    "s_cmp_ge_u32 s25, 252\n\t" \
    "s_cbranch_scc1 .Le2_%=\n\t" \
    "s_mul_i32 m0, s37, 12416\n\t" \
    "s_lshl2_add_u32 s37, s26, s37\n\t" \
    "s_mul_i32 s37, s37, 86016\n\t" \
    "s_mul_i32 s38, s27, 4992\n\t" \
    "s_add_u32 s37, s37, s38\n\t" \
    "s_addk_i32 s37, 896\n\t" \
    "v_add_u32 v9, s37, v0\n\t" \
    "s_cmp_eq_u32 s27, 0\n\t" \
    "s_cselect_b32 s38, 43008, 0\n\t" \
    "s_sub_u32 s39, s38, 2048\n\t" \
    "v_add_u32 v7, s39, v9\n\t" \
    "v_subrev_u32 v8, 0x400, v9\n\t" \
    "s_mov_b64 exec, 0xff\n\t" \
    "v_add_u32 v8, s38, v8\n\t" \
    "s_mov_b64 exec, -1\n\t" \
    "s_mov_b32 s18, 9633792\n\t" \
    "s_mov_b32 s19, 0x20000\n\t" \
    "s_mov_b32 s39, 43008\n\t" \
    "s_waitcnt lgkmcnt(0)\n\t" \
    "buffer_load_dwordx4 v7, s[16:19], 0 offen offset:0 nt lds\n\t" \
    "s_add_u32 m0, m0, 0x400\n\t" \
    "s_nop 0\n\t" \
    "buffer_load_dwordx4 v8, s[16:19], 0 offen offset:0 nt lds\n\t" \
    "s_add_u32 m0, m0, 0x400\n\t" \
    "s_nop 0\n\t" \
    "buffer_load_dwordx4 v9, s[16:19], 0 offen offset:0 nt lds\n\t" \
    "s_add_u32 m0, m0, 32\n\t" \
    "s_nop 0\n\t" \
    "buffer_load_dwordx4 v9, s[16:19], 0 offen offset:1024 nt lds\n\t" \
    "buffer_load_dwordx4 v9, s[16:19], 0 offen offset:2048 nt lds\n\t" \
    "buffer_load_dwordx4 v9, s[16:19], 0 offen offset:3072 nt lds\n\t" \
    "s_add_u32 m0, m0, 0x1020\n\t" \
    "s_nop 0\n\t" \
    "buffer_load_dwordx4 v7, s[16:19], s39 offen offset:0 nt lds\n\t" \
    "s_add_u32 m0, m0, 0x400\n\t" \
    "s_nop 0\n\t" \
    "buffer_load_dwordx4 v8, s[16:19], s39 offen offset:0 nt lds\n\t" \
    "s_add_u32 m0, m0, 0x400\n\t" \
    "s_nop 0\n\t" \
    "buffer_load_dwordx4 v9, s[16:19], s39 offen offset:0 nt lds\n\t" \
    "s_add_u32 m0, m0, 32\n\t" \
    "s_nop 0\n\t" \
    "buffer_load_dwordx4 v9, s[16:19], s39 offen offset:1024 nt lds\n\t" \
    "buffer_load_dwordx4 v9, s[16:19], s39 offen offset:2048 nt lds\n\t" \
    "buffer_load_dwordx4 v9, s[16:19], s39 offen offset:3072 nt lds\n\t" \
    ".Le2_%=:\n\t" \
    "s_mov_b32 s6, 34668544\n\t" \
    "s_mov_b32 s7, 0x20000\n\t" \
    "v_lshrrev_b32 v8, 7, v0\n\t" \
    "v_and_b32 v9, 0x70, v0\n\t" \
    "v_lshl_or_b32 v2, v8, 15, v9\n\t" \
    "s_lshl_b32 s32, s2, 7\n\t" \
    "s_add_u32 s32, s32, 33554432\n\t" \
    "s_cmp_ge_u32 s24, 4\n\t" \
    "s_cbranch_scc1 .Le3_%=\n\t" \
    "v_mov_b32 v12, 0\n\t" \
    "v_mov_b32 v13, 0\n\t" \
    "v_mov_b32 v14, 0\n\t" \
    "v_mov_b32 v15, 0\n\t" \
    "s_lshl_b32 s37, s24, 18\n\t" \
    "s_add_u32 s37, s37, s32\n\t" \
    "s_xor_b32 s38, s24, 2\n\t" \
    "s_lshr_b32 s39, s2, 3\n\t" \
    "s_or_b32 s38, s38, s39\n\t" \
    "s_memrealtime s[22:23]\n\t" \
    "s_waitcnt lgkmcnt(0)\n\t" \
    "s_cbranch_scc1 .Le4_%=\n\t" \
    "v_lshl_or_b32 v8, v8, 10, v9\n\t" \
    "v_mov_b32 v16, s22\n\t" \
    "v_mov_b32 v17, s22\n\t" \
    "v_mov_b32 v18, s22\n\t" \
    "v_mov_b32 v19, s22\n\t" \
    "s_add_u32 s38, s32, 1089536\n\t" \
    "buffer_store_dwordx4 v[16:19], v8, s[4:7], s38 offen sc1\n\t" \
    ".Le4_%=:\n\t" \
    "\n\t" \
    ".Le3_%=:\n\t" \
    "s_mul_i32 s30, s28, 293\n\t" \
    "s_lshr_b32 s30, s30, 11\n\t" \
    "s_mul_i32 s37, s30, 7\n\t" \
    "s_sub_u32 s28, s28, s37\n\t" \
    "s_mul_i32 s29, s30, 43\n\t" \
    "s_lshr_b32 s29, s29, 7\n\t" \
    "s_mul_i32 s37, s29, 3\n\t" \
    "s_sub_u32 s30, s30, s37\n\t" \
    "s_and_b32 s37, s29, 12\n\t" \
    "s_add_u32 s37, s37, s29\n\t" \
    "s_mul_i32 s31, s37, 768\n\t" \
    "s_lshl_b32 s37, s30, 6\n\t" \
    "s_add_u32 s31, s31, s37\n\t" \
    "s_add_u32 s41, s29, s30\n\t" \
    "s_add_u32 s41, s41, s28\n\t" \
    "s_and_b32 s41, s41, 3\n\t" \
    "s_cmp_lt_u32 s25, 252\n\t" \
    "s_cselect_b32 s14, 903168, 0\n\t" \
    "s_mov_b32 s15, 0x20000\n\t" \
    "v_lshrrev_b32 v10, 6, v0\n\t" \
    "v_lshrrev_b32 v11, 9, v0\n\t" \
    "v_sub_u32 v8, v10, v11\n\t" \
    "v_mul_u32_u24 v8, 18432, v8\n\t" \
    "v_and_b32 v9, 0x30, v0\n\t" \
    "v_lshlrev_b32 v11, 5, v11\n\t" \
    "v_bitop3_b32 v1, v8, v9, v11 bitop3:0xf6\n\t" \
    "s_mov_b32 exec_lo, 0xf0000000\n\t" \
    "s_mov_b32 exec_hi, 0xf0000000\n\t" \
    "v_bfrev_b32 v1, 1\n\t" \
    "s_mov_b64 exec, -1\n\t" \
    "s_cmp_ge_u32 s24, 4\n\t" \
    "s_cbranch_scc1 .Le9_%=\n\t" \
    "v_bfe_u32 v20, v0, 4, 4\n\t" \
    "v_and_b32 v10, 12, v10\n\t" \
    "v_cvt_f32_ubyte0 v21, v10\n\t" \
    "v_and_b32 v22, 1, v20\n\t" \
    "v_cvt_f32_ubyte0 v22, v22\n\t" \
    "v_mul_f32 v22, 0x3e800000, v22\n\t" \
    "v_lshrrev_b32 v23, 1, v20\n\t" \
    "v_mov_b32 v32, 0\n\t" \
    "v_mov_b32 v33, -1\n\t" \
    "v_mov_b32 v31, 0x3e800000\n\t" \
    "s_mov_b32 s65, 0\n\t" \
    "s_mov_b32 s40, 0\n\t" \
    "s_cmp_eq_u32 s24, 1\n\t" \
    "s_cbranch_scc1 .Lm1_%=\n\t" \
    "s_cmp_eq_u32 s24, 2\n\t" \
    "s_cbranch_scc1 .Lm2_%=\n\t" \
    "s_cmp_eq_u32 s24, 3\n\t" \
    "s_cbranch_scc1 .Lm3_%=\n\t" \
    "s_add_u32 s37, s32, 1048576\n\t" \
    "s_mov_b64 exec, 0xff\n\t" \
    "\n\t" \
    "s_mov_b64 exec, -1\n\t" \
    "v_or_b32 v24, 1, v20\n\t" \
    "v_cvt_f32_ubyte0 v24, v24\n\t" \
    "v_mul_f32 v24, 0x3c800000, v24\n\t" \
    "v_lshrrev_b32 v25, 4, v0\n\t" \
    "v_and_b32 v25, 16, v25\n\t" \
    "v_lshrrev_b32 v26, 2, v11\n\t" \
    "v_or_b32 v25, v25, v26\n\t" \
    "v_cvt_f32_ubyte0 v25, v25\n\t" \
    "v_add_f32 v26, 2.0, v25\n\t" \
    "v_add_f32 v27, 4.0, v25\n\t" \
    "v_add_f32 v28, 4.0, v26\n\t" \
    "v_mov_b32 v29, v22\n\t" \
    "v_mov_b32 v30, v22\n\t" \
    "v_mov_b32 v31, v24\n\t" \
    "s_mov_b32 s64, 0x3e800000\n\t" \
    "s_mov_b32 s65, 0x3e800000\n\t" \
    "v_add_u32 v34, 154880, v0\n\t" \
    "s_branch .Ltl_%=\n\t" \
    ".Lm1_%=:\n\t" \
    "v_lshrrev_b32 v24, 5, v0\n\t" \
    "v_and_b32 v24, 12, v24\n\t" \
    "v_and_or_b32 v24, v20, 3, v24\n\t" \
    "v_cvt_f32_ubyte0 v24, v24\n\t" \
    "v_mul_f32 v24, 0x3d924925, v24\n\t" \
    "v_mul_f32 v32, 0x41000000, v24\n\t" \
    "v_lshrrev_b32 v27, 3, v11\n\t" \
    "v_cvt_f32_ubyte0 v27, v27\n\t" \
    "v_add_f32 v25, -2.0, v27\n\t" \
    "v_add_f32 v26, -1.0, v27\n\t" \
    "v_add_f32 v28, 1.0, v27\n\t" \
    "v_and_b32 v29, 4, v20\n\t" \
    "v_cvt_f32_ubyte0 v29, v29\n\t" \
    "v_mul_f32 v29, 0x3d800000, v29\n\t" \
    "v_mov_b32 v30, v29\n\t" \
    "v_mov_b32 v31, 0xbe800000\n\t" \
    "s_mov_b32 s64, 0\n\t" \
    "s_mov_b32 exec_hi, 0\n\t" \
    "v_mov_b32 v33, 0\n\t" \
    "s_mov_b64 exec, -1\n\t" \
    "v_add_u32 v34, 149760, v0\n\t" \
    "s_branch .Ltl_%=\n\t" \
    ".Lm2_%=:\n\t" \
    "v_add_u32 v24, 6, v20\n\t" \
    "v_cvt_f32_ubyte0 v24, v24\n\t" \
    "v_mul_f32 v24, 0x3d000000, v24\n\t" \
    "v_add_f32 v25, 0.5, v21\n\t" \
    "v_add_f32 v26, 1.0, v25\n\t" \
    "v_add_f32 v27, 2.0, v25\n\t" \
    "v_add_f32 v28, 2.0, v26\n\t" \
    "v_mov_b32 v29, 0\n\t" \
    "v_mov_b32 v30, 0\n\t" \
    "s_mov_b32 s64, 0.5\n\t" \
    "v_add_u32 v34, 152832, v0\n\t" \
    "s_branch .Ltl_%=\n\t" \
    ".Lm3_%=:\n\t" \
    "s_mov_b32 s37, 34635776\n\t" \
    "buffer_load_dword v3, v12, s[4:7], s37 offen sc1\n\t" \
    "v_lshrrev_b32 v8, 7, v0\n\t" \
    "v_mul_u32_u24 v8, 96, v8\n\t" \
    "v_and_b32 v9, 16, v0\n\t" \
    "v_bfe_u32 v10, v0, 5, 2\n\t" \
    "v_add3_u32 v8, v8, v9, v10\n\t" \
    "v_mul_u32_u24 v8, 192, v8\n\t" \
    "s_mul_i32 s37, s28, 129024\n\t" \
    "s_add_u32 s37, s37, s31\n\t" \
    "s_mov_b32 m0, 157312\n\t" \
    "s_mov_b32 exec_hi, 0xffffff\n\t" \
    "buffer_load_dword v8, s[12:15], s37 offen  lds\n\t" \
    "s_mov_b64 exec, -1\n\t" \
    "v_lshrrev_b32 v8, 2, v0\n\t" \
    "v_and_b32 v8, 28, v8\n\t" \
    "v_add_u32 v8, 156928, v8\n\t" \
    "ds_write_b32 v8, v12\n\t" \
    "v_add_u32 v24, 6, v23\n\t" \
    "v_cvt_f32_ubyte0 v24, v24\n\t" \
    "v_mul_f32 v24, 0x3d924925, v24\n\t" \
    "v_mov_b32 v25, v21\n\t" \
    "v_add_f32 v26, 1.0, v21\n\t" \
    "v_add_f32 v27, 2.0, v21\n\t" \
    "v_add_f32 v28, 2.0, v26\n\t" \
    "v_sub_f32 v29, 0, v22\n\t" \
    "v_sub_f32 v30, 0, v22\n\t" \
    "s_mov_b32 exec_lo, 0\n\t" \
    "v_fma_f32 v25, v21, 0.5, 4.0\n\t" \
    "v_add_f32 v27, 1.0, v25\n\t" \
    "v_fmaak_f32 v26, v21, v31, 0x41200000\n\t" \
    "v_mov_b32 v28, v26\n\t" \
    "v_add_f32 v30, v31, v30\n\t" \
    "s_mov_b64 exec, -1\n\t" \
    "s_mov_b32 s40, 0x11\n\t" \
    "v_add_u32 v34, 151808, v0\n\t" \
    ".Ltl_%=:\n\t" \
    "v_fma_f32 v36, v24, v25, v29\n\t" \
    "v_fma_f32 v38, v24, v26, v29\n\t" \
    "v_fma_f32 v40, v24, v27, v29\n\t" \
    "v_fma_f32 v42, v24, v28, v30\n\t" \
    "v_add_f32 v37, v36, v31\n\t" \
    "v_add_f32 v39, v38, v31\n\t" \
    "v_add_f32 v41, v40, v31\n\t" \
    "v_add_f32 v43, v42, v31\n\t" \
    "v_cos_f32 v36, v36\n\t" \
    "v_cos_f32 v37, v37\n\t" \
    "v_cos_f32 v38, v38\n\t" \
    "v_cos_f32 v39, v39\n\t" \
    "v_cos_f32 v40, v40\n\t" \
    "v_cos_f32 v41, v41\n\t" \
    "v_cos_f32 v42, v42\n\t" \
    "v_cos_f32 v43, v43\n\t" \
    "s_bitcmp0_b32 s40, 4\n\t" \
    "s_cbranch_scc1 .Lt2_%=\n\t" \
    "v_mul_f32 v36, 0x3b924925, v36\n\t" \
    "v_mul_f32 v37, 0x3b924925, v37\n\t" \
    "v_mul_f32 v38, 0x3b924925, v38\n\t" \
    "v_mul_f32 v39, 0x3b924925, v39\n\t" \
    "v_mul_f32 v40, 0x3b924925, v40\n\t" \
    "v_mul_f32 v41, 0x3b924925, v41\n\t" \
    "v_mul_f32 v42, 0x3b924925, v42\n\t" \
    "v_mul_f32 v43, 0x3b924925, v43\n\t" \
    ".Lt2_%=:\n\t" \
    "v_cvt_pk_bf16_f32 v44, v36, v37\n\t" \
    "v_cvt_pk_bf16_f32 v45, v38, v39\n\t" \
    "v_cvt_pk_bf16_f32 v46, v40, v41\n\t" \
    "v_cvt_pk_bf16_f32 v47, v42, v43\n\t" \
    "v_and_b32 v44, v33, v44\n\t" \
    "v_and_b32 v45, v33, v45\n\t" \
    "v_mov_b32 v33, -1\n\t" \
    "v_add_f32 v24, s64, v24\n\t" \
    "v_add_f32 v31, s65, v31\n\t" \
    "v_add_f32 v29, v32, v29\n\t" \
    "v_add_f32 v30, v32, v30\n\t" \
    "ds_write_b128 v34, v[44:47]\n\t" \
    "s_add_u32 s40, s40, 1\n\t" \
    "s_bitcmp1_b32 s40, 0\n\t" \
    "v_add_u32 v34, 0x400, v34\n\t" \
    "s_cbranch_scc1 .Ltl_%=\n\t" \
    "s_bitcmp1_b32 s40, 2\n\t" \
    "s_cbranch_scc1 .Lt3_%=\n\t" \
    "s_mov_b32 s40, 2\n\t" \
    "s_lshl2_add_u32 s37, s28, s24\n\t" \
    "s_lshl_b32 s37, s37, 2\n\t" \
    "v_lshrrev_b32 v25, 2, v20\n\t" \
    "v_add_u32 v25, s37, v25\n\t" \
    "v_mul_u32_u24 v26, 147, v25\n\t" \
    "v_lshrrev_b32 v26, 10, v26\n\t" \
    "v_lshl_or_b32 v24, v26, 1, 1\n\t" \
    "v_cvt_f32_ubyte0 v24, v24\n\t" \
    "v_mul_f32 v24, 0x3c800000, v24\n\t" \
    "v_mul_u32_u24 v26, 7, v26\n\t" \
    "v_sub_u32 v25, v25, v26\n\t" \
    "v_xor_b32 v22, s41, v20\n\t" \
    "v_bfe_u32 v32, v22, 1, 1\n\t" \
    "v_and_b32 v22, 1, v22\n\t" \
    "v_cvt_f32_ubyte0 v22, v22\n\t" \
    "v_mul_f32 v22, 0x3e800000, v22\n\t" \
    "v_lshl_or_b32 v32, v25, 1, v32\n\t" \
    "v_cvt_f32_ubyte0 v32, v32\n\t" \
    "v_mul_f32 v32, 0x3e924925, v32\n\t" \
    "v_mul_f32 v29, 0x3d800000, v21\n\t" \
    "v_fma_f32 v29, v29, v32, v22\n\t" \
    "v_mov_b32 v30, v29\n\t" \
    "v_mov_b32 v31, v24\n\t" \
    "v_mov_b32 v25, 0\n\t" \
    "v_mov_b32 v26, 2.0\n\t" \
    "v_mov_b32 v27, 4.0\n\t" \
    "v_mov_b32 v28, 0x40c00000\n\t" \
    "s_mov_b32 s64, 0\n\t" \
    "s_mov_b32 s65, 0\n\t" \
    "s_lshl_b32 s37, s24, 11\n\t" \
    "s_add_u32 s37, s37, 49664\n\t" \
    "v_add_u32 v34, s37, v0\n\t" \
    "s_branch .Ltl_%=\n\t" \
    ".Lt3_%=:\n\t" \
    ".Le9_%=:\n\t" \
    "s_lshr_b32 s37, s24, 2\n\t" \
    "s_and_b32 s38, s24, 2\n\t" \
    "s_mul_i32 s39, s37, 24832\n\t" \
    "s_lshl4_add_u32 s39, s38, s39\n\t" \
    "v_lshrrev_b32 v4, 6, v0\n\t" \
    "v_and_b32 v5, 12, v4\n\t" \
    "v_add_u32 v6, v4, v5\n\t" \
    "v_lshrrev_b32 v7, 1, v0\n\t" \
    "v_and_b32 v7, 24, v7\n\t" \
    "v_and_b32 v4, 11, v4\n\t" \
    "v_mul_u32_u24 v4, 192, v4\n\t" \
    "v_bfe_u32 v5, v0, 8, 1\n\t" \
    "v_mul_u32_u24 v5, 3104, v5\n\t" \
    "v_add3_u32 v4, v4, v5, v7\n\t" \
    "v_add_u32 v8, s39, v4\n\t" \
    "v_mov_b32 v9, 156928\n\t" \
    "v_add_u32 v10, 149760, v0\n\t" \
    "s_lshl2_add_u32 s37, s37, s38\n\t" \
    "s_lshl_b32 s37, s37, 7\n\t" \
    "s_mul_i32 s53, s27, 28\n\t" \
    "s_add_u32 s53, s53, s26\n\t" \
    "s_andn2_b32 s39, s53, 7\n\t" \
    "s_add_u32 s53, s53, s39\n\t" \
    "s_lshl_b32 s53, s53, 10\n\t" \
    "s_add_u32 s37, s37, s53\n\t" \
    "s_and_b32 s38, s24, 1\n\t" \
    "s_lshl_b32 s39, s38, 13\n\t" \
    "s_add_u32 s37, s37, s39\n\t" \
    "s_cmp_lt_u32 s25, 252\n\t" \
    "s_cselect_b32 s43, 0, 0x80000000\n\t" \
    "s_add_u32 s37, s37, s43\n\t" \
    "v_lshrrev_b32 v11, 8, v0\n\t" \
    "v_and_b32 v12, 0xf0, v0\n\t" \
    "v_mul_u32_u24 v4, 8486912, v11\n\t" \
    "v_add3_u32 v13, v4, v12, s37\n\t" \
    "s_lshl_b32 s39, s38, 10\n\t" \
    "v_add_u32 v14, s39, v10\n\t" \
    "s_lshl_b32 s39, s38, 5\n\t" \
    "s_add_u32 s39, s39, 116736\n\t" \
    "v_lshl_add_u32 v4, v6, 6, v7\n\t" \
    "v_lshrrev_b32 v5, 3, v0\n\t" \
    "v_and_b32 v5, 32, v5\n\t" \
    "v_xor_b32 v5, s39, v5\n\t" \
    "v_add_u32 v15, v4, v5\n\t" \
    "s_and_b32 s37, s24, 6\n\t" \
    "s_lshl_b32 s37, s37, 10\n\t" \
    "v_add_u32 v16, s37, v0\n\t" \
    "s_waitcnt lgkmcnt(0)\n\t" \
    "s_barrier\n\t" \
    "ds_read_b128 v[20:23], v14 offset:5120\n\t" \
    "ds_read_b128 v[24:27], v16 offset:49664\n\t" \
    "ds_read_b128 v[28:31], v16 offset:50688\n\t" \
    "s_and_b32 s33, s2, 7\n\t" \
    "s_lshl_b32 s33, s33, 15\n\t" \
    "s_add_u32 s33, s33, 33554432\n\t" \
    "s_lshr_b32 s37, s29, 2\n\t" \
    "s_mul_i32 s39, s30, 3\n\t" \
    "s_add_u32 s37, s37, s39\n\t" \
    "s_mul_i32 s37, s37, 114688\n\t" \
    "s_and_b32 s39, s29, 3\n\t" \
    "s_lshl_b32 s39, s39, 8\n\t" \
    "s_add_u32 s37, s37, s39\n\t" \
    "s_add_u32 s37, s37, s43\n\t" \
    "s_add_u32 s37, s37, 2097152\n\t" \
    "s_and_b32 s39, s24, 6\n\t" \
    "s_lshl3_add_u32 s39, s28, s39\n\t" \
    "s_lshl_b32 s39, s39, 1\n\t" \
    "v_add_u32 v14, s39, v11\n\t" \
    "v_mul_u32_u24 v17, 147, v14\n\t" \
    "v_lshrrev_b32 v17, 10, v17\n\t" \
    "s_and_b32 s39, s24, 1\n\t" \
    "v_subrev_u32 v18, s39, v17\n\t" \
    "v_mul_i32_i24 v18, 7, v18\n\t" \
    "v_sub_u32 v14, v14, v18\n\t" \
    "v_and_b32 v18, 14, v14\n\t" \
    "v_add_lshl_u32 v14, v14, v18, 12\n\t" \
    "v_and_b32 v18, 7, v17\n\t" \
    "v_mul_u32_u24 v18, 4243456, v18\n\t" \
    "v_add3_u32 v14, v14, v18, v12\n\t" \
    "v_and_b32 v17, 8, v17\n\t" \
    "v_xor_b32 v17, 8, v17\n\t" \
    "v_lshl_add_u32 v14, v17, 10, v14\n\t" \
    "v_add_u32 v19, s37, v14\n\t" \
    "s_lshl_b32 s43, s41, 10\n\t" \
    "s_cmp_lt_u32 s30, 3\n\t" \
    "s_cselect_b64 s[20:21], -1, 0\n\t" \
    "s_and_b32 s37, s2, 0x70\n\t" \
    "s_lshl_b32 s37, s37, 1\n\t" \
    "s_lshl2_add_u32 s57, s24, s37\n\t" \
    "s_min_u32 s58, s57, 248\n\t" \
    "s_mul_i32 s59, s58, 147\n\t" \
    "s_lshr_b32 s59, s59, 12\n\t" \
    "s_mul_i32 s37, s59, 28\n\t" \
    "s_sub_u32 s60, s58, s37\n\t" \
    "s_andn2_b32 s39, s58, 7\n\t" \
    "s_add_u32 s51, s58, s39\n\t" \
    "s_lshl_b32 s51, s51, 10\n\t" \
    "s_sub_u32 s37, s60, 1\n\t" \
    "s_cselect_b32 s52, 27, -1\n\t" \
    "s_add_u32 s52, s52, s58\n\t" \
    "s_andn2_b32 s39, s52, 7\n\t" \
    "s_add_u32 s52, s52, s39\n\t" \
    "s_lshl_b32 s52, s52, 10\n\t" \
    "v_lshrrev_b32 v16, 9, v0\n\t" \
    "v_and_b32 v18, 0x2f0, v0\n\t" \
    "v_and_b32 v17, 1, v11\n\t" \
    "v_lshl_add_u32 v14, v17, 11, v18\n\t" \
    "v_add_u32 v32, s51, v14\n\t" \
    "s_sub_u32 s37, s51, s52\n\t" \
    "s_add_u32 s37, s37, 1024\n\t" \
    "v_mul_i32_i24 v17, s37, v17\n\t" \
    "v_add3_u32 v33, v17, v18, s52\n\t" \
    "s_mul_i32 s44, s24, 7296\n\t" \
    "s_lshl_b32 s37, s24, 2\n\t" \
    "s_lshl1_add_u32 s39, s24, 8\n\t" \
    "s_min_u32 s61, s37, s39\n\t" \
    "s_add_u32 s37, s37, 2\n\t" \
    "s_add_u32 s39, s24, 20\n\t" \
    "s_cmp_lt_u32 s24, 4\n\t" \
    "s_cselect_b32 s62, s37, s39\n\t" \
    "s_bfe_u32 s63, s2, 0x10003\n\t" \
    "s_mul_i32 s47, s63, 28\n\t" \
    "s_add_u32 s45, s47, s61\n\t" \
    "s_add_u32 s46, s47, s62\n\t" \
    "s_andn2_b32 s39, s45, 7\n\t" \
    "s_add_u32 s45, s45, s39\n\t" \
    "s_lshl_b32 s45, s45, 10\n\t" \
    "s_andn2_b32 s39, s46, 7\n\t" \
    "s_add_u32 s46, s46, s39\n\t" \
    "s_lshl_b32 s46, s46, 10\n\t" \
    "v_lshlrev_b32 v34, 4, v11\n\t" \
    "v_lshrrev_b32 v35, 6, v12\n\t" \
    "v_mul_u32_u24 v35, 7296, v35\n\t" \
    "v_and_b32 v14, 16, v0\n\t" \
    "v_mul_u32_u24 v14, 226, v14\n\t" \
    "v_and_b32 v18, 32, v0\n\t" \
    "v_lshl_add_u32 v14, v18, 1, v14\n\t" \
    "v_add3_u32 v35, v35, v14, v34\n\t" \
    "s_lshl_b32 s37, s61, 7\n\t" \
    "v_add_u32 v17, s37, v35\n\t" \
    "s_and_b32 s37, s62, 30\n\t" \
    "s_lshl_b32 s37, s37, 7\n\t" \
    "v_add_u32 v36, s37, v35\n\t" \
    "s_mov_b32 s1, 0\n\t" \
    "s_mov_b32 s41, 0x18000\n\t" \
    "s_lshl_b32 s37, s63, 9\n\t" \
    "s_add_u32 s51, s51, s37\n\t" \
    "s_cmp_lt_u32 s57, 252\n\t" \
    "s_cselect_b32 s51, s51, 0x80000000\n\t" \
    "s_and_b32 s37, s24, 3\n\t" \
    "s_mul_i32 s37, s37, 8486912\n\t" \
    "s_add_u32 s53, s53, s37\n\t" \
    "s_and_b32 s37, s24, 4\n\t" \
    "s_lshl_b32 s37, s37, 11\n\t" \
    "s_add_u32 s53, s53, s37\n\t" \
    "s_and_b32 s37, s2, 0x87\n\t" \
    "s_lshl4_add_u32 s37, s24, s37\n\t" \
    "s_lshl_b32 s55, s37, 7\n\t" \
    "v_and_b32 v37, 16, v0\n\t" \
    "v_lshlrev_b32 v38, 6, v37\n\t" \
    "s_lshl3_add_u32 s37, s26, s24\n\t" \
    "s_mul_i32 s37, s37, 43008\n\t" \
    "s_mul_i32 s39, s27, 4992\n\t" \
    "s_add_u32 s37, s37, s39\n\t" \
    "s_add_u32 s8, s8, s37\n\t" \
    "s_addc_u32 s9, s9, 0\n\t" \
    "s_cmp_eq_u32 s27, 8\n\t" \
    "s_movk_i32 s10, 4992\n\t" \
    "s_cselect_b32 s10, 3072, s10\n\t" \
    "s_mov_b32 s11, 0x20000\n\t" \
    "v_mad_u32_u24 v39, v12, 12, v34\n\t" \
    "v_and_b32 v18, 0x1f0, v0\n\t" \
    "v_lshl_add_u32 v18, v16, 11, v18\n\t" \
    "v_add_u32 v14, s51, v18\n\t" \
    "s_mul_i32 s37, s24, 2064\n\t" \
    "v_and_b32 v18, 0x300, v0\n\t" \
    "v_add3_u32 v35, v18, v0, s37\n\t" \
    "s_lshl_b32 s37, s37, 1\n\t" \
    "v_lshlrev_b32 v18, 7, v16\n\t" \
    "v_xor_b32 v18, v18, v0\n\t" \
    "v_add_u32 v40, s37, v18\n\t" \
    "v_lshrrev_b32 v18, 5, v12\n\t" \
    "v_mul_u32_u24 v18, 2064, v18\n\t" \
    "v_lshl_add_u32 v18, v37, 2, v18\n\t" \
    "s_lshl_b32 s37, s24, 8\n\t" \
    "v_add3_u32 v41, v18, v34, s37\n\t" \
    "v_bfe_u32 v18, v0, 7, 1\n\t" \
    "v_lshl_add_u32 v18, v11, 2, v18\n\t" \
    "v_mul_u32_u24 v18, 2064, v18\n\t" \
    "v_lshrrev_b32 v42, 3, v0\n\t" \
    "v_and_or_b32 v18, v42, 8, v18\n\t" \
    "s_and_b32 s37, s24, 3\n\t" \
    "s_lshl_b32 s37, s37, 6\n\t" \
    "s_and_b32 s39, s24, 4\n\t" \
    "s_lshl_b32 s39, s39, 8\n\t" \
    "s_or_b32 s37, s37, s39\n\t" \
    "v_and_or_b32 v42, v0, 32, s37\n\t" \
    "v_mul_u32_u24 v37, 40, v37\n\t" \
    "v_xor_b32 v42, v42, v37\n\t" \
    "v_add_u32 v43, v18, v42\n\t" \
    "s_and_b32 s37, s2, 0x70\n\t" \
    "s_lshl_b32 s37, s37, 1\n\t" \
    "v_lshrrev_b32 v16, 7, v0\n\t" \
    "v_lshl_add_u32 v16, v16, 2, s37\n\t" \
    "v_min_u32 v16, 248, v16\n\t" \
    "v_mul_u32_u24 v12, 147, v16\n\t" \
    "v_lshrrev_b32 v12, 12, v12\n\t" \
    "v_mul_u32_u24 v34, 28, v12\n\t" \
    "v_sub_u32 v16, v16, v34\n\t" \
    "v_bfe_u32 v34, v0, 4, 3\n\t" \
    "v_min_u32 v34, 4, v34\n\t" \
    "v_add3_u32 v16, v16, v34, -1\n\t" \
    "v_add_u32 v34, 28, v16\n\t" \
    "v_min_u32 v16, v16, v34\n\t" \
    "v_mad_u32_u24 v16, v16, 9, v12\n\t" \
    "v_and_b32 v11, 31, v16\n\t" \
    "v_lshrrev_b32 v37, 5, v16\n\t" \
    "v_lshlrev_b32 v37, 7, v37\n\t" \
    "v_lshl_or_b32 v18, v11, 10, v37\n\t" \
    "ds_read_b128 v[44:47], v10 offset:0\n\t" \
    "ds_read_b128 v[48:51], v10 offset:1024\n\t" \
    "s_and_b32 s37, s24, 1\n\t" \
    "s_lshl_b32 s37, s37, 4\n\t" \
    "s_lshr_b32 s39, s24, 1\n\t" \
    "s_add_u32 s37, s37, s39\n\t" \
    "s_mul_i32 s37, s37, 192\n\t" \
    "s_add_u32 s37, s37, s31\n\t" \
    "s_lshl_b32 s39, s24, 12\n\t" \
    "s_add_u32 m0, s39, 116736\n\t" \
    "s_add_u32 s38, s37, 0\n\t" \
    "buffer_load_dwordx4 v1, s[12:15], s38 offen  lds\n\t" \
    "s_add_u32 m0, s39, 117760\n\t" \
    "s_add_u32 s38, s37, 258048\n\t" \
    "buffer_load_dwordx4 v1, s[12:15], s38 offen  lds\n\t" \
    "s_add_u32 m0, s39, 118784\n\t" \
    "s_add_u32 s38, s37, 516096\n\t" \
    "buffer_load_dwordx4 v1, s[12:15], s38 offen  lds\n\t" \
    "s_add_u32 m0, s39, 119808\n\t" \
    "s_add_u32 s38, s37, 774144\n\t" \
    "buffer_load_dwordx4 v1, s[12:15], s38 offen  lds\n\t" \
    "s_cmp_lg_u32 s24, 3\n\t" \
    "s_cbranch_scc1 .Lr9_%=\n\t" \
    "s_and_b32 s37, s2, 7\n\t" \
    "s_lshl_b32 s37, s37, 10\n\t" \
    "s_add_u32 s37, s37, 34643968\n\t" \
    "s_buffer_load_dword s56, s[4:7], s37 offset:0x0\n\t" \
    "s_buffer_load_dword s57, s[4:7], s37 offset:0x80\n\t" \
    "s_buffer_load_dword s58, s[4:7], s37 offset:0x100\n\t" \
    "s_buffer_load_dword s59, s[4:7], s37 offset:0x180\n\t" \
    "s_buffer_load_dword s60, s[4:7], s37 offset:0x200\n\t" \
    "s_buffer_load_dword s61, s[4:7], s37 offset:0x280\n\t" \
    "s_buffer_load_dword s62, s[4:7], s37 offset:0x300\n\t" \
    "s_buffer_load_dword s63, s[4:7], s37 offset:0x380\n\t" \
    "s_mov_b32 s40, 0\n\t" \
    ".Lr1_%=:\n\t" \
    "s_waitcnt lgkmcnt(0)\n\t" \
    "s_memrealtime s[22:23]\n\t" \
    "s_waitcnt lgkmcnt(0)\n\t" \
    "s_mov_b32 s37, 1000\n\t" \
    "s_mov_b32 s38, 0\n\t" \
    "s_sub_u32 s39, s22, s56\n\t" \
    "s_min_u32 s37, s37, s39\n\t" \
    "s_max_u32 s38, s38, s39\n\t" \
    "s_sub_u32 s39, s22, s57\n\t" \
    "s_min_u32 s37, s37, s39\n\t" \
    "s_max_u32 s38, s38, s39\n\t" \
    "s_sub_u32 s39, s22, s58\n\t" \
    "s_min_u32 s37, s37, s39\n\t" \
    "s_max_u32 s38, s38, s39\n\t" \
    "s_sub_u32 s39, s22, s59\n\t" \
    "s_min_u32 s37, s37, s39\n\t" \
    "s_max_u32 s38, s38, s39\n\t" \
    "s_sub_u32 s39, s22, s60\n\t" \
    "s_min_u32 s37, s37, s39\n\t" \
    "s_max_u32 s38, s38, s39\n\t" \
    "s_sub_u32 s39, s22, s61\n\t" \
    "s_min_u32 s37, s37, s39\n\t" \
    "s_max_u32 s38, s38, s39\n\t" \
    "s_sub_u32 s39, s22, s62\n\t" \
    "s_min_u32 s37, s37, s39\n\t" \
    "s_max_u32 s38, s38, s39\n\t" \
    "s_sub_u32 s39, s22, s63\n\t" \
    "s_min_u32 s37, s37, s39\n\t" \
    "s_max_u32 s38, s38, s39\n\t" \
    "s_cmp_gt_u32 s38, 1000\n\t" \
    "s_cbranch_scc1 .Lr2_%=\n\t" \
    ".Lr3_%=:\n\t" \
    "s_sub_u32 s35, s22, s37\n\t" \
    "s_waitcnt vmcnt(5)\n\t" \
    "v_readfirstlane_b32 s34, v3\n\t" \
    "s_mul_i32 s34, s34, 1664525\n\t" \
    "s_add_u32 s34, s34, 1013904223\n\t" \
    "s_or_b32 s34, s34, 1\n\t" \
    "v_mov_b32 v52, s34\n\t" \
    "v_mov_b32 v53, s35\n\t" \
    "ds_write_b64 v9, v[52:53] offset:32\n\t" \
    ".Lr9_%=:\n\t" \
    "s_cmp_eq_u32 s2, 255\n\t" \
    "s_cbranch_scc1 .Lpb_%=\n\t" \
    ".Lpe_%=:\n\t" \
    "s_waitcnt vmcnt(4)\n\t" \
    "s_waitcnt lgkmcnt(0)\n\t" \
    "s_barrier\n\t" \
    "ds_read_b64_tr_b16 v[60:61], v8 offset:0\n\t" \
    "ds_read_b64_tr_b16 v[62:63], v8 offset:768\n\t" \
    "ds_read_b64_tr_b16 v[64:65], v8 offset:12416\n\t" \
    "ds_read_b64_tr_b16 v[66:67], v8 offset:13184\n\t" \
    "ds_read_b64_tr_b16 v[68:69], v8 offset:6208\n\t" \
    "ds_read_b64_tr_b16 v[70:71], v8 offset:6976\n\t" \
    "ds_read_b64_tr_b16 v[72:73], v8 offset:18624\n\t" \
    "ds_read_b64_tr_b16 v[74:75], v8 offset:19392\n\t" \
    "ds_read_b128 v[76:79], v9 offset:32\n\t" \
    "s_waitcnt lgkmcnt(7)\n\t" \
    "v_mfma_f32_16x16x32_bf16 v[80:83], v[20:23], v[60:63], 0\n\t" \
    "s_waitcnt lgkmcnt(5)\n\t" \
    "v_mfma_f32_16x16x32_bf16 v[84:87], v[20:23], v[64:67], 0\n\t" \
    "s_waitcnt lgkmcnt(3)\n\t" \
    "v_mfma_f32_16x16x32_bf16 v[88:91], v[20:23], v[68:71], 0\n\t" \
    "s_waitcnt lgkmcnt(1)\n\t" \
    "v_mfma_f32_16x16x32_bf16 v[92:95], v[20:23], v[72:75], 0\n\t" \
    "ds_read_b64_tr_b16 v[96:97], v8 offset:64\n\t" \
    "ds_read_b64_tr_b16 v[98:99], v8 offset:832\n\t" \
    "ds_read_b64_tr_b16 v[100:101], v8 offset:12480\n\t" \
    "ds_read_b64_tr_b16 v[102:103], v8 offset:13248\n\t" \
    "ds_read_b64_tr_b16 v[104:105], v8 offset:6272\n\t" \
    "ds_read_b64_tr_b16 v[106:107], v8 offset:7040\n\t" \
    "ds_read_b64_tr_b16 v[108:109], v8 offset:18688\n\t" \
    "ds_read_b64_tr_b16 v[110:111], v8 offset:19456\n\t" \
    "s_waitcnt lgkmcnt(8)\n\t" \
    "v_readfirstlane_b32 s34, v76\n\t" \
    "v_readfirstlane_b32 s35, v77\n\t" \
    "s_lshl_b32 s37, s24, 8\n\t" \
    "s_add_u32 s37, s37, s2\n\t" \
    "s_cmp_eq_u32 s37, 255\n\t" \
    "s_cbranch_scc1 .Lpp_%=\n\t" \
    ".Lpq_%=:\n\t" \
    "v_add_u32 v80, 0x8000, v80\n\t" \
    "v_add_u32 v82, 0x8000, v82\n\t" \
    "v_add_u32 v84, 0x8000, v84\n\t" \
    "v_add_u32 v86, 0x8000, v86\n\t" \
    "v_cvt_pk_bf16_f32 v120, v80, v81\n\t" \
    "v_cvt_pk_bf16_f32 v122, v84, v85\n\t" \
    "v_cvt_pk_bf16_f32 v121, v88, v89\n\t" \
    "v_cvt_pk_bf16_f32 v123, v92, v93\n\t" \
    "v_and_b32 v120, -2, v120\n\t" \
    "v_and_b32 v122, -2, v122\n\t" \
    "v_cvt_pk_bf16_f32 v124, v82, v83\n\t" \
    "v_cvt_pk_bf16_f32 v126, v86, v87\n\t" \
    "v_cvt_pk_bf16_f32 v125, v90, v91\n\t" \
    "v_cvt_pk_bf16_f32 v127, v94, v95\n\t" \
    "v_and_b32 v124, -2, v124\n\t" \
    "v_and_b32 v126, -2, v126\n\t" \
    "ds_read_b64_tr_b16 v[128:129], v8 offset:128\n\t" \
    "ds_read_b64_tr_b16 v[130:131], v8 offset:896\n\t" \
    "ds_read_b64_tr_b16 v[132:133], v8 offset:12544\n\t" \
    "ds_read_b64_tr_b16 v[134:135], v8 offset:13312\n\t" \
    "ds_read_b64_tr_b16 v[136:137], v8 offset:6336\n\t" \
    "ds_read_b64_tr_b16 v[138:139], v8 offset:7104\n\t" \
    "ds_read_b64_tr_b16 v[140:141], v8 offset:18752\n\t" \
    "ds_read_b64_tr_b16 v[142:143], v8 offset:19520\n\t" \
    ".Lh25_%=:\n\t" \
    "s_memrealtime s[22:23]\n\t" \
    "s_waitcnt lgkmcnt(0)\n\t" \
    "s_sub_u32 s22, s22, s35\n\t" \
    "s_cmp_lt_u32 s22, 200\n\t" \
    "s_cbranch_scc1 .Lh25_%=\n\t" \
    "buffer_store_dwordx4 v[120:123], v13, s[4:7], 0 offen offset:0 sc1\n\t" \
    "s_mov_b32 s42, 4243456\n\t" \
    "buffer_store_dwordx4 v[124:127], v13, s[4:7], s42 offen offset:0 sc1\n\t" \
    "s_lshl_b32 s37, s24, 18\n\t" \
    "s_add_u32 s37, s37, s32\n\t" \
    "s_bitcmp1_b32 29, s24\n\t" \
    "s_cselect_b32 s39, 0, 0x80000000\n\t" \
    "v_add_u32 v12, s39, v2\n\t" \
    "v_mov_b32 v144, 0\n\t" \
    "v_mov_b32 v145, 0\n\t" \
    "v_mov_b32 v146, 0\n\t" \
    "v_mov_b32 v147, 0\n\t" \
    "s_cmp_eq_u32 s24, 4\n\t" \
    "s_cselect_b64 exec, 0xff, -1\n\t" \
    "buffer_store_dwordx4 v[144:147], v12, s[4:7], s37 offen sc1\n\t" \
    "s_mov_b64 exec, -1\n\t" \
    "v_mfma_f32_16x16x32_bf16 v[148:151], v[20:23], v[96:99], 0\n\t" \
    "v_mfma_f32_16x16x32_bf16 v[152:155], v[20:23], v[100:103], 0\n\t" \
    "v_mfma_f32_16x16x32_bf16 v[156:159], v[20:23], v[104:107], 0\n\t" \
    "v_mfma_f32_16x16x32_bf16 v[160:163], v[20:23], v[108:111], 0\n\t" \
    "v_mfma_f32_16x16x32_bf16 v[164:167], v[20:23], v[128:131], 0\n\t" \
    "v_mfma_f32_16x16x32_bf16 v[168:171], v[20:23], v[132:135], 0\n\t" \
    "v_mfma_f32_16x16x32_bf16 v[172:175], v[20:23], v[136:139], 0\n\t" \
    "v_mfma_f32_16x16x32_bf16 v[176:179], v[20:23], v[140:143], 0\n\t" \
    "s_nop 0\n\t" \
    "v_add_u32 v148, 0x8000, v148\n\t" \
    "v_add_u32 v150, 0x8000, v150\n\t" \
    "v_add_u32 v152, 0x8000, v152\n\t" \
    "v_add_u32 v154, 0x8000, v154\n\t" \
    "v_cvt_pk_bf16_f32 v180, v148, v149\n\t" \
    "v_cvt_pk_bf16_f32 v182, v152, v153\n\t" \
    "v_cvt_pk_bf16_f32 v181, v156, v157\n\t" \
    "v_cvt_pk_bf16_f32 v183, v160, v161\n\t" \
    "v_and_b32 v180, -2, v180\n\t" \
    "v_and_b32 v182, -2, v182\n\t" \
    "v_cvt_pk_bf16_f32 v184, v150, v151\n\t" \
    "v_cvt_pk_bf16_f32 v186, v154, v155\n\t" \
    "v_cvt_pk_bf16_f32 v185, v158, v159\n\t" \
    "v_cvt_pk_bf16_f32 v187, v162, v163\n\t" \
    "v_and_b32 v184, -2, v184\n\t" \
    "v_and_b32 v186, -2, v186\n\t" \
    "v_add_u32 v164, 0x8000, v164\n\t" \
    "v_add_u32 v166, 0x8000, v166\n\t" \
    "v_add_u32 v168, 0x8000, v168\n\t" \
    "v_add_u32 v170, 0x8000, v170\n\t" \
    "v_cvt_pk_bf16_f32 v188, v164, v165\n\t" \
    "v_cvt_pk_bf16_f32 v190, v168, v169\n\t" \
    "v_cvt_pk_bf16_f32 v189, v172, v173\n\t" \
    "v_cvt_pk_bf16_f32 v191, v176, v177\n\t" \
    "v_and_b32 v188, -2, v188\n\t" \
    "v_and_b32 v190, -2, v190\n\t" \
    "v_cvt_pk_bf16_f32 v4, v166, v167\n\t" \
    "v_cvt_pk_bf16_f32 v6, v170, v171\n\t" \
    "v_cvt_pk_bf16_f32 v5, v174, v175\n\t" \
    "v_cvt_pk_bf16_f32 v7, v178, v179\n\t" \
    "v_and_b32 v4, -2, v4\n\t" \
    "v_and_b32 v6, -2, v6\n\t" \
    "v_mov_b32 v52, s34\n\t" \
    "v_mov_b32 v53, s34\n\t" \
    "v_mov_b32 v54, s34\n\t" \
    "v_mov_b32 v55, s34\n\t" \
    "s_waitcnt vmcnt(3)\n\t" \
    "; wait\n\t" \
    "s_waitcnt lgkmcnt(0)\n\t" \
    "s_barrier\n\t" \
    "ds_read_b64_tr_b16 v[56:57], v15 offset:0\n\t" \
    "ds_read_b64_tr_b16 v[58:59], v15 offset:256\n\t" \
    "ds_read_b64_tr_b16 v[60:61], v15 offset:2048\n\t" \
    "ds_read_b64_tr_b16 v[62:63], v15 offset:2304\n\t" \
    "ds_read_b64_tr_b16 v[64:65], v15 offset:4096\n\t" \
    "ds_read_b64_tr_b16 v[66:67], v15 offset:4352\n\t" \
    "ds_read_b64_tr_b16 v[68:69], v15 offset:6144\n\t" \
    "ds_read_b64_tr_b16 v[70:71], v15 offset:6400\n\t" \
    "ds_read_b64_tr_b16 v[72:73], v15 offset:8192\n\t" \
    "ds_read_b64_tr_b16 v[74:75], v15 offset:8448\n\t" \
    "ds_read_b64_tr_b16 v[76:77], v15 offset:10240\n\t" \
    "ds_read_b64_tr_b16 v[78:79], v15 offset:10496\n\t" \
    "ds_read_b64_tr_b16 v[112:113], v15 offset:12288\n\t" \
    "ds_read_b64_tr_b16 v[114:115], v15 offset:12544\n\t" \
    "s_waitcnt lgkmcnt(12)\n\t" \
    "v_mfma_f32_16x16x32_bf16 v[116:119], v[24:27], v[56:59], 0\n\t" \
    "ds_read_b64_tr_b16 v[80:81], v15 offset:14336\n\t" \
    "ds_read_b64_tr_b16 v[82:83], v15 offset:14592\n\t" \
    "s_waitcnt lgkmcnt(12)\n\t" \
    "v_mfma_f32_16x16x32_bf16 v[116:119], v[28:31], v[60:63], v[116:119]\n\t" \
    "ds_read_b64_tr_b16 v[84:85], v15 offset:16384\n\t" \
    "ds_read_b64_tr_b16 v[86:87], v15 offset:16640\n\t" \
    "s_waitcnt lgkmcnt(12)\n\t" \
    "v_mfma_f32_16x16x32_bf16 v[88:91], v[24:27], v[64:67], 0\n\t" \
    "ds_read_b64_tr_b16 v[92:93], v15 offset:18432\n\t" \
    "ds_read_b64_tr_b16 v[94:95], v15 offset:18688\n\t" \
    "s_waitcnt lgkmcnt(12)\n\t" \
    "v_mfma_f32_16x16x32_bf16 v[88:91], v[28:31], v[68:71], v[88:91]\n\t" \
    "ds_read_b64_tr_b16 v[120:121], v15 offset:20480\n\t" \
    "ds_read_b64_tr_b16 v[122:123], v15 offset:20736\n\t" \
    "s_waitcnt lgkmcnt(12)\n\t" \
    "v_mfma_f32_16x16x32_bf16 v[124:127], v[24:27], v[72:75], 0\n\t" \
    "ds_read_b64_tr_b16 v[144:145], v15 offset:22528\n\t" \
    "ds_read_b64_tr_b16 v[146:147], v15 offset:22784\n\t" \
    "s_waitcnt lgkmcnt(12)\n\t" \
    "v_mfma_f32_16x16x32_bf16 v[124:127], v[28:31], v[76:79], v[124:127]\n\t" \
    "ds_read_b64_tr_b16 v[96:97], v15 offset:24576\n\t" \
    "ds_read_b64_tr_b16 v[98:99], v15 offset:24832\n\t" \
    "s_waitcnt lgkmcnt(12)\n\t" \
    "v_mfma_f32_16x16x32_bf16 v[100:103], v[24:27], v[112:115], 0\n\t" \
    "ds_read_b64_tr_b16 v[104:105], v15 offset:26624\n\t" \
    "ds_read_b64_tr_b16 v[106:107], v15 offset:26880\n\t" \
    "s_waitcnt lgkmcnt(12)\n\t" \
    "v_mfma_f32_16x16x32_bf16 v[100:103], v[28:31], v[80:83], v[100:103]\n\t" \
    "ds_read_b64_tr_b16 v[108:109], v15 offset:28672\n\t" \
    "ds_read_b64_tr_b16 v[110:111], v15 offset:28928\n\t" \
    ".Lh26_%=:\n\t" \
    "s_memrealtime s[22:23]\n\t" \
    "s_waitcnt lgkmcnt(0)\n\t" \
    "s_sub_u32 s22, s22, s35\n\t" \
    "s_cmp_lt_u32 s22, 255\n\t" \
    "s_cbranch_scc1 .Lh26_%=\n\t" \
    "s_mov_b32 s42, 524288\n\t" \
    "buffer_store_dwordx4 v[180:183], v13, s[4:7], s42 offen offset:0 sc1\n\t" \
    "s_mov_b32 s42, 4767744\n\t" \
    "buffer_store_dwordx4 v[184:187], v13, s[4:7], s42 offen offset:0 sc1\n\t" \
    "v_mfma_f32_16x16x32_bf16 v[128:131], v[24:27], v[84:87], 0\n\t" \
    "ds_read_b64_tr_b16 v[132:133], v15 offset:30720\n\t" \
    "ds_read_b64_tr_b16 v[134:135], v15 offset:30976\n\t" \
    "v_mfma_f32_16x16x32_bf16 v[128:131], v[28:31], v[92:95], v[128:131]\n\t" \
    "v_cvt_pk_bf16_f32 v136, v116, v88\n\t" \
    "v_cvt_pk_bf16_f32 v20, v117, v89\n\t" \
    "v_mfma_f32_16x16x32_bf16 v[140:143], v[24:27], v[120:123], 0\n\t" \
    "v_cvt_pk_bf16_f32 v148, v118, v90\n\t" \
    "v_cvt_pk_bf16_f32 v152, v119, v91\n\t" \
    "v_mfma_f32_16x16x32_bf16 v[140:143], v[28:31], v[144:147], v[140:143]\n\t" \
    "v_cvt_pk_bf16_f32 v137, v124, v100\n\t" \
    "v_cvt_pk_bf16_f32 v21, v125, v101\n\t" \
    "v_mfma_f32_16x16x32_bf16 v[156:159], v[24:27], v[96:99], 0\n\t" \
    "v_cvt_pk_bf16_f32 v149, v126, v102\n\t" \
    "v_cvt_pk_bf16_f32 v153, v127, v103\n\t" \
    "v_mfma_f32_16x16x32_bf16 v[156:159], v[28:31], v[104:107], v[156:159]\n\t" \
    "v_mfma_f32_16x16x32_bf16 v[160:163], v[24:27], v[108:111], 0\n\t" \
    "s_waitcnt lgkmcnt(0)\n\t" \
    "v_mfma_f32_16x16x32_bf16 v[160:163], v[28:31], v[132:135], v[160:163]\n\t" \
    "v_cvt_pk_bf16_f32 v138, v128, v140\n\t" \
    "v_cvt_pk_bf16_f32 v22, v129, v141\n\t" \
    "v_cvt_pk_bf16_f32 v150, v130, v142\n\t" \
    "v_cvt_pk_bf16_f32 v154, v131, v143\n\t" \
    "s_nop 3\n\t" \
    "v_cvt_pk_bf16_f32 v139, v156, v160\n\t" \
    "v_cvt_pk_bf16_f32 v23, v157, v161\n\t" \
    "v_cvt_pk_bf16_f32 v151, v158, v162\n\t" \
    "v_cvt_pk_bf16_f32 v155, v159, v163\n\t" \
    ".Lh27_%=:\n\t" \
    "s_memrealtime s[22:23]\n\t" \
    "s_waitcnt lgkmcnt(0)\n\t" \
    "s_sub_u32 s22, s22, s35\n\t" \
    "s_cmp_lt_u32 s22, 310\n\t" \
    "s_cbranch_scc1 .Lh27_%=\n\t" \
    "s_mov_b32 s42, 1048576\n\t" \
    "buffer_store_dwordx4 v[188:191], v13, s[4:7], s42 offen offset:0 sc1\n\t" \
    "s_mov_b32 s42, 5292032\n\t" \
    "buffer_store_dwordx4 v[4:7], v13, s[4:7], s42 offen offset:0 sc1\n\t" \
    ".Lh28_%=:\n\t" \
    "s_memrealtime s[22:23]\n\t" \
    "s_waitcnt lgkmcnt(0)\n\t" \
    "s_sub_u32 s22, s22, s35\n\t" \
    "s_cmp_lt_u32 s22, 350\n\t" \
    "s_cbranch_scc1 .Lh28_%=\n\t" \
    "s_cmp_eq_u32 s24, 0\n\t" \
    "s_cbranch_scc1 .Lj30_%=\n\t" \
    ".Lh29_%=:\n\t" \
    "s_memrealtime s[22:23]\n\t" \
    "s_waitcnt lgkmcnt(0)\n\t" \
    "s_sub_u32 s22, s22, s35\n\t" \
    "s_cmp_lt_u32 s22, 370\n\t" \
    "s_cbranch_scc1 .Lh29_%=\n\t" \
    ".Lj30_%=:\n\t" \
    "s_waitcnt lgkmcnt(0)\n\t" \
    "s_xor_b32 s42, s43, 0\n\t" \
    "buffer_store_dwordx4 v[136:139], v19, s[4:7], s42 offen offset:0 sc1\n\t" \
    "s_xor_b32 s42, s43, 1024\n\t" \
    "buffer_store_dwordx4 v[20:23], v19, s[4:7], s42 offen offset:0 sc1\n\t" \
    "s_waitcnt vmcnt(2)\n\t" \
    "s_mov_b64 exec, 1\n\t" \
    "ds_add_rtn_u32 v16, v9, v9 offset:8\n\t" \
    "s_add_u32 s37, s32, 524288\n\t" \
    "s_waitcnt lgkmcnt(0)\n\t" \
    "v_cmp_eq_u32 vcc, 1098496, v16\n\t" \
    "s_mov_b64 exec, -1\n\t" \
    "s_nop 0\n\t" \
    "s_cbranch_vccz .La13_%=\n\t" \
    "buffer_store_dwordx4 v[52:55], v2, s[4:7], s37 offen sc1\n\t" \
    ".La13_%=:\n\t" \
    "s_mov_b64 exec, -1\n\t" \
    "s_cmp_eq_u32 s24, 0\n\t" \
    "s_cbranch_scc1 .Lj32_%=\n\t" \
    ".Lh31_%=:\n\t" \
    "s_memrealtime s[22:23]\n\t" \
    "s_waitcnt lgkmcnt(0)\n\t" \
    "s_sub_u32 s22, s22, s35\n\t" \
    "s_cmp_lt_u32 s22, 425\n\t" \
    "s_cbranch_scc1 .Lh31_%=\n\t" \
    ".Lj32_%=:\n\t" \
    "s_waitcnt lgkmcnt(0)\n\t" \
    "s_xor_b32 s42, s43, 2048\n\t" \
    "buffer_store_dwordx4 v[148:151], v19, s[4:7], s42 offen offset:0 sc1\n\t" \
    "s_xor_b32 s42, s43, 3072\n\t" \
    "buffer_store_dwordx4 v[152:155], v19, s[4:7], s42 offen offset:0 sc1\n\t" \
    ".Lh33_%=:\n\t" \
    "s_memrealtime s[22:23]\n\t" \
    "s_waitcnt lgkmcnt(0)\n\t" \
    "s_sub_u32 s22, s22, s35\n\t" \
    "s_cmp_lt_u32 s22, 390\n\t" \
    "s_cbranch_scc1 .Lh33_%=\n\t" \
    "s_and_b32 s52, s2, 7\n\t" \
    "s_lshl_b32 s52, s52, 7\n\t" \
    "s_add_u32 s52, s52, 34636800\n\t" \
    "s_buffer_load_dword s36, s[4:7], s52\n\t" \
    "s_mov_b32 s40, 0\n\t" \
    "s_waitcnt lgkmcnt(0)\n\t" \
    ".Lr4_%=:\n\t" \
    "s_xor_b32 s36, s36, s34\n\t" \
    "s_andn2_b32 s37, s36, 14\n\t" \
    "s_cbranch_scc1 .Lr5_%=\n\t" \
    ".Lr6_%=:\n\t" \
    "s_bfe_u32 s36, s36, 0x30001\n\t" \
    "s_add_u32 s56, s2, s36\n\t" \
    "s_and_b32 s56, s56, 7\n\t" \
    "s_lshr_b32 s37, s2, 7\n\t" \
    "s_lshl1_add_u32 s56, s56, s37\n\t" \
    "s_and_b32 s37, s56, 7\n\t" \
    "s_mul_i32 s48, s37, 4243456\n\t" \
    "s_and_b32 s37, s56, 8\n\t" \
    "s_lshl_b32 s37, s37, 10\n\t" \
    "s_add_u32 s48, s48, s37\n\t" \
    "s_add_u32 s49, s48, 524288\n\t" \
    "s_add_u32 s50, s48, 1048576\n\t" \
    "s_xor_b32 s37, s48, 0x2000\n\t" \
    "s_add_u32 s37, s37, 2097152\n\t" \
    "s_add_u32 s45, s45, s37\n\t" \
    "s_add_u32 s46, s46, s37\n\t" \
    "s_cmp_lt_u32 s24, 4\n\t" \
    "s_cselect_b32 s47, s46, 0x80000000\n\t" \
    "v_add_u32 v1, s45, v0\n\t" \
    "v_add_u32 v3, s46, v0\n\t" \
    "v_add_u32 v42, s47, v0\n\t" \
    "s_lshr_b32 s37, s24, 2\n\t" \
    "s_mul_i32 s37, s37, 6\n\t" \
    "s_mul_i32 s39, s56, 7\n\t" \
    "s_add_u32 s37, s37, s39\n\t" \
    "s_lshr_b32 s37, s37, 4\n\t" \
    "s_and_b32 s39, s24, 3\n\t" \
    "s_mul_i32 s39, s39, 63\n\t" \
    "s_add_u32 s37, s37, s39\n\t" \
    "v_lshrrev_b32 v34, 4, v0\n\t" \
    "v_min_u32 v34, 8, v34\n\t" \
    "v_mad_u32_u24 v34, v34, 7, s37\n\t" \
    "v_and_b32 v8, 31, v34\n\t" \
    "v_lshrrev_b32 v12, 5, v34\n\t" \
    "v_lshlrev_b32 v12, 7, v12\n\t" \
    "v_lshl_or_b32 v15, v8, 10, v12\n\t" \
    "s_nop 0\n\t" \
    "v_readlane_b32 s12, v15, 0\n\t" \
    "v_readlane_b32 s13, v15, 1\n\t" \
    "v_readlane_b32 s14, v15, 2\n\t" \
    "v_readlane_b32 s15, v15, 3\n\t" \
    "v_readlane_b32 s16, v15, 4\n\t" \
    "v_readlane_b32 s17, v15, 5\n\t" \
    "v_readlane_b32 s18, v15, 6\n\t" \
    "v_readlane_b32 s19, v15, 7\n\t" \
    "v_readlane_b32 s3, v15, 8\n\t" \
    "s_add_u32 s12, s12, s33\n\t" \
    "s_add_u32 s13, s13, s33\n\t" \
    "s_add_u32 s14, s14, s33\n\t" \
    "s_add_u32 s15, s15, s33\n\t" \
    "s_add_u32 s16, s16, s33\n\t" \
    "s_add_u32 s17, s17, s33\n\t" \
    "s_add_u32 s18, s18, s33\n\t" \
    "s_add_u32 s19, s19, s33\n\t" \
    "s_add_u32 s3, s3, s33\n\t" \
    "s_waitcnt vmcnt(0)\n\t" \
    "s_mov_b64 exec, 1\n\t" \
    "ds_add_rtn_u32 v13, v9, v9 offset:12\n\t" \
    "s_add_u32 s37, s32, 786432\n\t" \
    "s_waitcnt lgkmcnt(0)\n\t" \
    "v_cmp_eq_u32 vcc, 1098496, v13\n\t" \
    "s_mov_b64 exec, -1\n\t" \
    "s_nop 0\n\t" \
    "s_cbranch_vccz .La18_%=\n\t" \
    "buffer_store_dwordx4 v[52:55], v2, s[4:7], s37 offen sc1\n\t" \
    ".La18_%=:\n\t" \
    "s_mov_b64 exec, -1\n\t" \
    ".Lh34_%=:\n\t" \
    "s_memrealtime s[22:23]\n\t" \
    "s_waitcnt lgkmcnt(0)\n\t" \
    "s_sub_u32 s22, s22, s35\n\t" \
    "s_cmp_lt_u32 s22, 470\n\t" \
    "s_cbranch_scc1 .Lh34_%=\n\t" \
    "s_cmp_lg_u32 s24, 0\n\t" \
    "s_cbranch_scc1 .Lb15_%=\n\t" \
    "s_mov_b32 s40, 0\n\t" \
    "s_add_u32 s38, s33, 524288\n\t" \
    ".Ll15_%=:\n\t" \
    "buffer_load_dword v16, v18, s[4:7], s38 offen sc1\n\t" \
    "s_waitcnt vmcnt(0)\n\t" \
    "v_cmp_ne_u32 vcc, s34, v16\n\t" \
    "s_add_u32 s40, s40, 1\n\t" \
    "s_cmp_ge_u32 s40, 8192\n\t" \
    "s_cbranch_scc1 .Lb15_%=\n\t" \
    "s_cbranch_vccz .Lb15_%=\n\t" \
    "s_sleep 2\n\t" \
    "s_branch .Ll15_%=\n\t" \
    ".Lb15_%=:\n\t" \
    "s_waitcnt lgkmcnt(0)\n\t" \
    "s_barrier\n\t" \
    "buffer_load_dwordx4 v[164:167], v33, s[4:7], s48 offen offset:0 sc1\n\t" \
    "buffer_load_dwordx4 v[168:171], v32, s[4:7], s48 offen offset:0 sc1\n\t" \
    "buffer_load_dwordx4 v[172:175], v33, s[4:7], s48 offen offset:256 sc1\n\t" \
    "buffer_load_dwordx4 v[176:179], v32, s[4:7], s48 offen offset:256 sc1\n\t" \
    "buffer_load_dwordx4 v[56:59], v32, s[4:7], s48 offen offset:1024 sc1\n\t" \
    "buffer_load_dwordx4 v[60:63], v32, s[4:7], s48 offen offset:1280 sc1\n\t" \
    "s_waitcnt vmcnt(5)\n\t" \
    "v_mfma_f32_32x32x16_bf16 v[192:207], v[44:47], v[164:167], 0\n\t" \
    "buffer_load_dwordx4 v[64:67], v33, s[4:7], s49 offen offset:0 sc1\n\t" \
    "s_waitcnt vmcnt(5)\n\t" \
    "v_mfma_f32_32x32x16_bf16 v[192:207], v[48:51], v[168:171], v[192:207]\n\t" \
    "buffer_load_dwordx4 v[68:71], v32, s[4:7], s49 offen offset:0 sc1\n\t" \
    "s_waitcnt vmcnt(5)\n\t" \
    "v_mfma_f32_32x32x16_bf16 v[208:223], v[44:47], v[172:175], 0\n\t" \
    "buffer_load_dwordx4 v[72:75], v33, s[4:7], s49 offen offset:256 sc1\n\t" \
    "s_waitcnt vmcnt(5)\n\t" \
    "v_mfma_f32_32x32x16_bf16 v[208:223], v[48:51], v[176:179], v[208:223]\n\t" \
    "buffer_load_dwordx4 v[76:79], v32, s[4:7], s49 offen offset:256 sc1\n\t" \
    "v_mfma_f32_32x32x16_bf16 v[224:239], v[44:47], v[168:171], 0\n\t" \
    "buffer_load_dwordx4 v[112:115], v32, s[4:7], s49 offen offset:1024 sc1\n\t" \
    "s_waitcnt vmcnt(6)\n\t" \
    "v_mfma_f32_32x32x16_bf16 v[224:239], v[48:51], v[56:59], v[224:239]\n\t" \
    "v_mfma_f32_32x32x16_bf16 v[240:255], v[44:47], v[176:179], 0\n\t" \
    "buffer_load_dwordx4 v[80:83], v32, s[4:7], s49 offen offset:1280 sc1\n\t" \
    "s_waitcnt vmcnt(6)\n\t" \
    "v_mfma_f32_32x32x16_bf16 v[240:255], v[48:51], v[60:63], v[240:255]\n\t" \
    "s_add_u32 m0, s44, 0\n\t" \
    "s_nop 1\n\t" \
    "v_cvt_pk_bf16_f32 v19, v192, v208\n\t" \
    "v_cvt_pk_bf16_f32 v11, v193, v209\n\t" \
    "v_cvt_pk_bf16_f32 v37, v194, v210\n\t" \
    "ds_write_addtid_b32 v19 offset:0\n\t" \
    "v_cvt_pk_bf16_f32 v34, v195, v211\n\t" \
    "ds_write_addtid_b32 v11 offset:256\n\t" \
    "v_cvt_pk_bf16_f32 v8, v196, v212\n\t" \
    "ds_write_addtid_b32 v37 offset:512\n\t" \
    "v_cvt_pk_bf16_f32 v12, v197, v213\n\t" \
    "ds_write_addtid_b32 v34 offset:768\n\t" \
    "v_cvt_pk_bf16_f32 v9, v198, v214\n\t" \
    "ds_write_addtid_b32 v8 offset:1024\n\t" \
    "v_cvt_pk_bf16_f32 v13, v199, v215\n\t" \
    "ds_write_addtid_b32 v12 offset:1280\n\t" \
    "v_cvt_pk_bf16_f32 v18, v200, v216\n\t" \
    "ds_write_addtid_b32 v9 offset:1536\n\t" \
    "v_cvt_pk_bf16_f32 v16, v201, v217\n\t" \
    "ds_write_addtid_b32 v13 offset:1792\n\t" \
    "v_cvt_pk_bf16_f32 v19, v202, v218\n\t" \
    "ds_write_addtid_b32 v18 offset:2048\n\t" \
    "v_cvt_pk_bf16_f32 v11, v203, v219\n\t" \
    "ds_write_addtid_b32 v16 offset:2304\n\t" \
    "v_cvt_pk_bf16_f32 v37, v204, v220\n\t" \
    "ds_write_addtid_b32 v19 offset:2560\n\t" \
    "v_cvt_pk_bf16_f32 v34, v205, v221\n\t" \
    "ds_write_addtid_b32 v11 offset:2816\n\t" \
    "ds_write_addtid_b32 v37 offset:3072\n\t" \
    "ds_write_addtid_b32 v34 offset:3328\n\t" \
    ".Lh35_%=:\n\t" \
    "s_memrealtime s[22:23]\n\t" \
    "s_waitcnt lgkmcnt(0)\n\t" \
    "s_sub_u32 s22, s22, s35\n\t" \
    "s_cmp_lt_u32 s22, 595\n\t" \
    "s_cbranch_scc1 .Lh35_%=\n\t" \
    "s_buffer_load_dword s12, s[4:7], s12 offset:0xc0000\n\t" \
    "s_buffer_load_dword s13, s[4:7], s13 offset:0xc0000\n\t" \
    "s_buffer_load_dword s14, s[4:7], s14 offset:0xc0000\n\t" \
    "s_buffer_load_dword s15, s[4:7], s15 offset:0xc0000\n\t" \
    "s_buffer_load_dword s16, s[4:7], s16 offset:0xc0000\n\t" \
    "s_buffer_load_dword s17, s[4:7], s17 offset:0xc0000\n\t" \
    "s_buffer_load_dword s18, s[4:7], s18 offset:0xc0000\n\t" \
    "s_buffer_load_dword s19, s[4:7], s19 offset:0xc0000\n\t" \
    "s_buffer_load_dword s3, s[4:7], s3 offset:0xc0000\n\t" \
    "s_waitcnt vmcnt(5)\n\t" \
    "v_mfma_f32_32x32x16_bf16 v[192:207], v[44:47], v[64:67], 0\n\t" \
    "v_cvt_pk_bf16_f32 v8, v224, v240\n\t" \
    "v_cvt_pk_bf16_f32 v12, v225, v241\n\t" \
    "v_cvt_pk_bf16_f32 v9, v226, v242\n\t" \
    "ds_write_addtid_b32 v8 offset:3616\n\t" \
    "buffer_load_dwordx4 v[180:183], v33, s[4:7], s50 offen offset:0 sc1\n\t" \
    "v_cvt_pk_bf16_f32 v13, v227, v243\n\t" \
    "ds_write_addtid_b32 v12 offset:3872\n\t" \
    "v_cvt_pk_bf16_f32 v18, v228, v244\n\t" \
    "s_waitcnt vmcnt(5)\n\t" \
    "v_mfma_f32_32x32x16_bf16 v[192:207], v[48:51], v[68:71], v[192:207]\n\t" \
    "ds_write_addtid_b32 v9 offset:4128\n\t" \
    "v_cvt_pk_bf16_f32 v16, v229, v245\n\t" \
    "ds_write_addtid_b32 v13 offset:4384\n\t" \
    "v_cvt_pk_bf16_f32 v19, v230, v246\n\t" \
    "buffer_load_dwordx4 v[184:187], v32, s[4:7], s50 offen offset:0 sc1\n\t" \
    "ds_write_addtid_b32 v18 offset:4640\n\t" \
    "v_cvt_pk_bf16_f32 v11, v231, v247\n\t" \
    "ds_write_addtid_b32 v16 offset:4896\n\t" \
    "s_waitcnt vmcnt(5)\n\t" \
    "v_mfma_f32_32x32x16_bf16 v[208:223], v[44:47], v[72:75], 0\n\t" \
    "v_cvt_pk_bf16_f32 v37, v232, v248\n\t" \
    "ds_write_addtid_b32 v19 offset:5152\n\t" \
    "v_cvt_pk_bf16_f32 v34, v233, v249\n\t" \
    "ds_write_addtid_b32 v11 offset:5408\n\t" \
    "buffer_load_dwordx4 v[84:87], v33, s[4:7], s50 offen offset:256 sc1\n\t" \
    "v_cvt_pk_bf16_f32 v8, v234, v250\n\t" \
    "ds_write_addtid_b32 v37 offset:5664\n\t" \
    "v_cvt_pk_bf16_f32 v12, v235, v251\n\t" \
    "s_waitcnt vmcnt(5)\n\t" \
    "v_mfma_f32_32x32x16_bf16 v[208:223], v[48:51], v[76:79], v[208:223]\n\t" \
    "ds_write_addtid_b32 v34 offset:5920\n\t" \
    "v_cvt_pk_bf16_f32 v9, v236, v252\n\t" \
    "ds_write_addtid_b32 v8 offset:6176\n\t" \
    "v_cvt_pk_bf16_f32 v13, v237, v253\n\t" \
    "buffer_load_dwordx4 v[92:95], v32, s[4:7], s50 offen offset:256 sc1\n\t" \
    "ds_write_addtid_b32 v12 offset:6432\n\t" \
    "ds_write_addtid_b32 v9 offset:6688\n\t" \
    "ds_write_addtid_b32 v13 offset:6944\n\t" \
    "s_waitcnt lgkmcnt(0)\n\t" \
    "s_xor_b32 s12, s12, s34\n\t" \
    "s_xor_b32 s13, s13, s34\n\t" \
    "s_xor_b32 s14, s14, s34\n\t" \
    "s_xor_b32 s15, s15, s34\n\t" \
    "s_xor_b32 s16, s16, s34\n\t" \
    "s_xor_b32 s17, s17, s34\n\t" \
    "s_xor_b32 s18, s18, s34\n\t" \
    "s_xor_b32 s19, s19, s34\n\t" \
    "s_xor_b32 s3, s3, s34\n\t" \
    "s_or_b32 s12, s12, s13\n\t" \
    "s_or_b32 s12, s12, s14\n\t" \
    "s_or_b32 s12, s12, s15\n\t" \
    "s_or_b32 s12, s12, s16\n\t" \
    "s_or_b32 s12, s12, s17\n\t" \
    "s_or_b32 s12, s12, s18\n\t" \
    "s_or_b32 s12, s12, s19\n\t" \
    "s_or_b32 s12, s12, s3\n\t" \
    "s_cbranch_scc1 .Lq1021_%=\n\t" \
    ".Lb20_%=:\n\t" \
    "s_waitcnt lgkmcnt(0)\n\t" \
    "s_barrier\n\t" \
    "ds_read_b128 v[120:123], v17 offset:0\n\t" \
    "ds_read_b128 v[88:91], v17 offset:128\n\t" \
    "ds_read_b128 v[116:119], v17 offset:29184\n\t" \
    "ds_read_b128 v[144:147], v17 offset:29312\n\t" \
    "ds_read_b128 v[96:99], v36 offset:0\n\t" \
    "ds_read_b128 v[100:103], v36 offset:128\n\t" \
    "ds_read_b128 v[124:127], v36 offset:29184\n\t" \
    "ds_read_b128 v[104:107], v36 offset:29312\n\t" \
    "v_mfma_f32_32x32x16_bf16 v[224:239], v[44:47], v[68:71], 0\n\t" \
    "v_cvt_pk_bf16_f32 v16, v192, v208\n\t" \
    "v_cvt_pk_bf16_f32 v19, v193, v209\n\t" \
    "v_cvt_pk_bf16_f32 v11, v194, v210\n\t" \
    "ds_write_addtid_b32 v16 offset:58368\n\t" \
    "v_cvt_pk_bf16_f32 v33, v195, v211\n\t" \
    "s_mov_b32 s45, 0\n\t" \
    "buffer_load_dwordx4 v[24:27], v1, s[4:7], s45 offen offset:0 sc1\n\t" \
    "ds_write_addtid_b32 v19 offset:58624\n\t" \
    "v_cvt_pk_bf16_f32 v37, v196, v212\n\t" \
    "s_waitcnt vmcnt(6)\n\t" \
    "v_mfma_f32_32x32x16_bf16 v[224:239], v[48:51], v[112:115], v[224:239]\n\t" \
    "buffer_load_dwordx4 v[108:111], v32, s[4:7], s50 offen offset:1024 sc1\n\t" \
    "ds_write_addtid_b32 v11 offset:58880\n\t" \
    "v_cvt_pk_bf16_f32 v34, v197, v213\n\t" \
    "ds_write_addtid_b32 v33 offset:59136\n\t" \
    "v_cvt_pk_bf16_f32 v8, v198, v214\n\t" \
    "ds_write_addtid_b32 v37 offset:59392\n\t" \
    "v_cvt_pk_bf16_f32 v12, v199, v215\n\t" \
    "ds_write_addtid_b32 v34 offset:59648\n\t" \
    "v_mfma_f32_32x32x16_bf16 v[240:255], v[44:47], v[76:79], 0\n\t" \
    "buffer_load_dwordx4 v[28:31], v1, s[4:7], s45 offen offset:1024 sc1\n\t" \
    "v_cvt_pk_bf16_f32 v9, v200, v216\n\t" \
    "ds_write_addtid_b32 v8 offset:59904\n\t" \
    "v_cvt_pk_bf16_f32 v13, v201, v217\n\t" \
    "ds_write_addtid_b32 v12 offset:60160\n\t" \
    "v_cvt_pk_bf16_f32 v15, v202, v218\n\t" \
    "ds_write_addtid_b32 v9 offset:60416\n\t" \
    "v_cvt_pk_bf16_f32 v18, v203, v219\n\t" \
    "s_waitcnt vmcnt(7)\n\t" \
    "v_mfma_f32_32x32x16_bf16 v[240:255], v[48:51], v[80:83], v[240:255]\n\t" \
    "buffer_load_dwordx4 v[132:135], v32, s[4:7], s50 offen offset:1280 sc1\n\t" \
    "ds_write_addtid_b32 v13 offset:60672\n\t" \
    "v_cvt_pk_bf16_f32 v16, v204, v220\n\t" \
    "ds_write_addtid_b32 v15 offset:60928\n\t" \
    "s_mov_b32 s46, 114688\n\t" \
    "buffer_load_dwordx4 v[128:131], v1, s[4:7], s46 offen offset:0 sc1\n\t" \
    "v_cvt_pk_bf16_f32 v19, v205, v221\n\t" \
    "ds_write_addtid_b32 v18 offset:61184\n\t" \
    "ds_write_addtid_b32 v16 offset:61440\n\t" \
    "ds_write_addtid_b32 v19 offset:61696\n\t" \
    "s_waitcnt vmcnt(8)\n\t" \
    "v_mfma_f32_32x32x16_bf16 v[192:207], v[44:47], v[180:183], 0\n\t" \
    "v_cvt_pk_bf16_f32 v11, v224, v240\n\t" \
    "v_cvt_pk_bf16_f32 v33, v225, v241\n\t" \
    "v_cvt_pk_bf16_f32 v37, v226, v242\n\t" \
    "ds_write_addtid_b32 v11 offset:61984\n\t" \
    "buffer_load_dwordx4 v[140:143], v1, s[4:7], s46 offen offset:1024 sc1\n\t" \
    "v_cvt_pk_bf16_f32 v34, v227, v243\n\t" \
    "ds_write_addtid_b32 v33 offset:62240\n\t" \
    "v_cvt_pk_bf16_f32 v8, v228, v244\n\t" \
    "s_waitcnt vmcnt(8)\n\t" \
    "v_mfma_f32_32x32x16_bf16 v[192:207], v[48:51], v[184:187], v[192:207]\n\t" \
    "ds_write_addtid_b32 v37 offset:62496\n\t" \
    "v_cvt_pk_bf16_f32 v12, v229, v245\n\t" \
    "ds_write_addtid_b32 v34 offset:62752\n\t" \
    "v_cvt_pk_bf16_f32 v9, v230, v246\n\t" \
    "buffer_load_dwordx4 v[156:159], v3, s[4:7], s45 offen offset:0 sc1\n\t" \
    "ds_write_addtid_b32 v8 offset:63008\n\t" \
    "v_cvt_pk_bf16_f32 v32, v231, v247\n\t" \
    "ds_write_addtid_b32 v12 offset:63264\n\t" \
    "s_waitcnt vmcnt(8)\n\t" \
    "v_mfma_f32_32x32x16_bf16 v[208:223], v[44:47], v[84:87], 0\n\t" \
    "v_cvt_pk_bf16_f32 v13, v232, v248\n\t" \
    "ds_write_addtid_b32 v9 offset:63520\n\t" \
    "v_cvt_pk_bf16_f32 v15, v233, v249\n\t" \
    "ds_write_addtid_b32 v32 offset:63776\n\t" \
    "buffer_load_dwordx4 v[160:163], v3, s[4:7], s46 offen offset:0 sc1\n\t" \
    "v_cvt_pk_bf16_f32 v18, v234, v250\n\t" \
    "ds_write_addtid_b32 v13 offset:64032\n\t" \
    "v_cvt_pk_bf16_f32 v16, v235, v251\n\t" \
    "s_waitcnt vmcnt(8)\n\t" \
    "v_mfma_f32_32x32x16_bf16 v[208:223], v[48:51], v[92:95], v[208:223]\n\t" \
    "ds_write_addtid_b32 v15 offset:64288\n\t" \
    "v_cvt_pk_bf16_f32 v19, v236, v252\n\t" \
    "ds_write_addtid_b32 v18 offset:64544\n\t" \
    "v_cvt_pk_bf16_f32 v11, v237, v253\n\t" \
    "s_mov_b32 s47, 229376\n\t" \
    "buffer_load_dwordx4 v[188:191], v1, s[4:7], s47 offen offset:0 sc1\n\t" \
    "ds_write_addtid_b32 v16 offset:64800\n\t" \
    "ds_write_addtid_b32 v19 offset:65056\n\t" \
    "ds_write_addtid_b32 v11 offset:65312\n\t" \
    "s_waitcnt vmcnt(5)\n\t" \
    "; wait\n\t" \
    "s_waitcnt lgkmcnt(0)\n\t" \
    "s_barrier\n\t" \
    "s_memrealtime s[64:65]\n\t" \
    "s_cmp_lg_u32 s24, 7\n\t" \
    "s_cbranch_scc1 .Lar_%=\n\t" \
    "s_waitcnt lgkmcnt(0)\n\t" \
    "v_mov_b32 v53, s64\n\t" \
    "s_add_u32 s37, s32, 1048576\n\t" \
    "s_mov_b64 exec, 0xff\n\t" \
    "buffer_store_dwordx4 v[52:55], v0, s[4:7], s37 offen sc1\n\t" \
    "s_mov_b64 exec, -1\n\t" \
    ".Lar_%=:\n\t" \
    "v_add_u32 v17, 58368, v17\n\t" \
    "v_add_u32 v36, 58368, v36\n\t" \
    "ds_read_b128 v[4:7], v17 offset:0\n\t" \
    "ds_read_b128 v[136:139], v17 offset:128\n\t" \
    "ds_read_b128 v[20:23], v17 offset:29184\n\t" \
    "ds_read_b128 v[148:151], v17 offset:29312\n\t" \
    "ds_read_b128 v[152:155], v36 offset:0\n\t" \
    "ds_read_b128 v[164:167], v36 offset:128\n\t" \
    "ds_read_b128 v[172:175], v36 offset:29184\n\t" \
    "ds_read_b128 v[168:171], v36 offset:29312\n\t" \
    "v_mfma_f32_32x32x16_bf16 v[224:239], v[44:47], v[184:187], 0\n\t" \
    "v_cvt_pk_bf16_f32 v33, v192, v208\n\t" \
    "v_cvt_pk_bf16_f32 v37, v193, v209\n\t" \
    "v_mfma_f32_16x16x32_bf16 v[56:59], v[24:27], v[120:123], 0\n\t" \
    "v_cvt_pk_bf16_f32 v34, v194, v210\n\t" \
    "ds_write_addtid_b32 v33 offset:0\n\t" \
    "buffer_load_dwordx4 v[176:179], v1, s[4:7], s47 offen offset:1024 sc1\n\t" \
    "v_cvt_pk_bf16_f32 v8, v195, v211\n\t" \
    "ds_write_addtid_b32 v37 offset:256\n\t" \
    "v_mfma_f32_16x16x32_bf16 v[60:63], v[24:27], v[88:91], 0\n\t" \
    "v_cvt_pk_bf16_f32 v12, v196, v212\n\t" \
    "v_mfma_f32_32x32x16_bf16 v[224:239], v[48:51], v[108:111], v[224:239]\n\t" \
    "ds_write_addtid_b32 v34 offset:512\n\t" \
    "v_cvt_pk_bf16_f32 v9, v197, v213\n\t" \
    "v_mfma_f32_16x16x32_bf16 v[64:67], v[24:27], v[116:119], 0\n\t" \
    "ds_write_addtid_b32 v8 offset:768\n\t" \
    "v_cvt_pk_bf16_f32 v32, v198, v214\n\t" \
    "buffer_load_dwordx4 v[72:75], v3, s[4:7], s47 offen offset:0 sc1\n\t" \
    "ds_write_addtid_b32 v12 offset:1024\n\t" \
    "v_cvt_pk_bf16_f32 v13, v199, v215\n\t" \
    "v_mfma_f32_16x16x32_bf16 v[68:71], v[24:27], v[144:147], 0\n\t" \
    "ds_write_addtid_b32 v9 offset:1280\n\t" \
    "v_mfma_f32_32x32x16_bf16 v[240:255], v[44:47], v[92:95], 0\n\t" \
    "v_cvt_pk_bf16_f32 v15, v200, v216\n\t" \
    "ds_write_addtid_b32 v32 offset:1536\n\t" \
    "v_mfma_f32_16x16x32_bf16 v[112:115], v[28:31], v[88:91], 0\n\t" \
    "v_cvt_pk_bf16_f32 v18, v201, v217\n\t" \
    "ds_write_addtid_b32 v13 offset:1792\n\t" \
    "buffer_load_dwordx4 v[76:79], v42, s[4:7], s45 offen offset:1024 sc1\n\t" \
    "v_cvt_pk_bf16_f32 v16, v202, v218\n\t" \
    "ds_write_addtid_b32 v15 offset:2048\n\t" \
    "v_mfma_f32_16x16x32_bf16 v[60:63], v[28:31], v[120:123], v[60:63]\n\t" \
    "v_cvt_pk_bf16_f32 v19, v203, v219\n\t" \
    "v_mfma_f32_32x32x16_bf16 v[240:255], v[48:51], v[132:135], v[240:255]\n\t" \
    "ds_write_addtid_b32 v18 offset:2304\n\t" \
    "v_cvt_pk_bf16_f32 v11, v204, v220\n\t" \
    "v_mfma_f32_16x16x32_bf16 v[80:83], v[28:31], v[144:147], 0\n\t" \
    "ds_write_addtid_b32 v16 offset:2560\n\t" \
    "v_cvt_pk_bf16_f32 v33, v205, v221\n\t" \
    "buffer_load_dwordx4 v[180:183], v42, s[4:7], s46 offen offset:1024 sc1\n\t" \
    "ds_write_addtid_b32 v19 offset:2816\n\t" \
    "ds_write_addtid_b32 v11 offset:3072\n\t" \
    "v_mfma_f32_16x16x32_bf16 v[68:71], v[28:31], v[116:119], v[68:71]\n\t" \
    "ds_write_addtid_b32 v33 offset:3328\n\t" \
    "s_nop 1\n\t" \
    "v_cvt_pk_bf16_f32 v37, v224, v240\n\t" \
    "s_waitcnt vmcnt(8) lgkmcnt(15)\n\t" \
    "v_mfma_f32_16x16x32_bf16 v[56:59], v[128:131], v[4:7], v[56:59]\n\t" \
    "v_cvt_pk_bf16_f32 v34, v225, v241\n\t" \
    "v_cvt_pk_bf16_f32 v8, v226, v242\n\t" \
    "buffer_load_dwordx4 v[84:87], v42, s[4:7], s47 offen offset:1024 sc1\n\t" \
    "ds_write_addtid_b32 v37 offset:3616\n\t" \
    "v_mfma_f32_16x16x32_bf16 v[60:63], v[128:131], v[136:139], v[60:63]\n\t" \
    "v_cvt_pk_bf16_f32 v12, v227, v243\n\t" \
    "ds_write_addtid_b32 v34 offset:3872\n\t" \
    "v_cvt_pk_bf16_f32 v9, v228, v244\n\t" \
    "ds_write_addtid_b32 v8 offset:4128\n\t" \
    "v_mfma_f32_16x16x32_bf16 v[64:67], v[128:131], v[20:23], v[64:67]\n\t" \
    "v_cvt_pk_bf16_f32 v32, v229, v245\n\t" \
    "s_mov_b32 s45, 344064\n\t" \
    "buffer_load_dwordx4 v[52:55], v1, s[4:7], s45 offen offset:0 sc1\n\t" \
    "ds_write_addtid_b32 v12 offset:4384\n\t" \
    "v_cvt_pk_bf16_f32 v13, v230, v246\n\t" \
    "v_mfma_f32_16x16x32_bf16 v[68:71], v[128:131], v[148:151], v[68:71]\n\t" \
    "ds_write_addtid_b32 v9 offset:4640\n\t" \
    "v_cvt_pk_bf16_f32 v15, v231, v247\n\t" \
    "ds_write_addtid_b32 v32 offset:4896\n\t" \
    "v_cvt_pk_bf16_f32 v18, v232, v248\n\t" \
    "buffer_load_dwordx4 v[184:187], v1, s[4:7], s45 offen offset:1024 sc1\n\t" \
    "s_waitcnt vmcnt(10)\n\t" \
    "v_mfma_f32_16x16x32_bf16 v[112:115], v[140:143], v[136:139], v[112:115]\n\t" \
    "ds_write_addtid_b32 v13 offset:5152\n\t" \
    "v_cvt_pk_bf16_f32 v16, v233, v249\n\t" \
    "ds_write_addtid_b32 v15 offset:5408\n\t" \
    "v_mfma_f32_16x16x32_bf16 v[60:63], v[140:143], v[4:7], v[60:63]\n\t" \
    "v_cvt_pk_bf16_f32 v19, v234, v250\n\t" \
    "ds_write_addtid_b32 v18 offset:5664\n\t" \
    "s_mov_b32 s46, 458752\n\t" \
    "buffer_load_dwordx4 v[108:111], v1, s[4:7], s46 offen offset:0 sc1\n\t" \
    "v_cvt_pk_bf16_f32 v11, v235, v251\n\t" \
    "ds_write_addtid_b32 v16 offset:5920\n\t" \
    "v_mfma_f32_16x16x32_bf16 v[80:83], v[140:143], v[148:151], v[80:83]\n\t" \
    "v_cvt_pk_bf16_f32 v33, v236, v252\n\t" \
    "ds_write_addtid_b32 v19 offset:6176\n\t" \
    "v_cvt_pk_bf16_f32 v37, v237, v253\n\t" \
    "v_mfma_f32_16x16x32_bf16 v[68:71], v[140:143], v[20:23], v[68:71]\n\t" \
    "ds_write_addtid_b32 v11 offset:6432\n\t" \
    "buffer_load_dwordx4 v[24:27], v1, s[4:7], s46 offen offset:1024 sc1\n\t" \
    "ds_write_addtid_b32 v33 offset:6688\n\t" \
    "ds_write_addtid_b32 v37 offset:6944\n\t" \
    "s_waitcnt lgkmcnt(0)\n\t" \
    "s_barrier\n\t" \
    "s_cmp_lt_u32 s24, 4\n\t" \
    "s_cbranch_scc1 .Lp36_%=\n\t" \
    "s_setprio 1\n\t" \
    ".Lp36_%=:\n\t" \
    "ds_read_b128 v[44:47], v10 offset:2048\n\t" \
    "v_mov_b32 v34, 0x10000\n\t" \
    "s_waitcnt vmcnt(11)\n\t" \
    "v_mfma_f32_16x16x32_bf16 v[92:95], v[156:159], v[96:99], 0\n\t" \
    "v_subrev_u32 v17, 58368, v17\n\t" \
    "v_mfma_f32_16x16x32_bf16 v[48:51], v[156:159], v[100:103], 0\n\t" \
    "v_subrev_u32 v36, 58368, v36\n\t" \
    "v_mfma_f32_16x16x32_bf16 v[132:135], v[156:159], v[124:127], 0\n\t" \
    "ds_read_b128 v[192:195], v17 offset:0\n\t" \
    "s_mov_b32 s47, 573440\n\t" \
    "buffer_load_dwordx4 v[196:199], v1, s[4:7], s47 offen offset:0 sc1\n\t" \
    "v_mfma_f32_16x16x32_bf16 v[200:203], v[156:159], v[104:107], 0\n\t" \
    "ds_read_b128 v[204:207], v17 offset:128\n\t" \
    "s_waitcnt vmcnt(11)\n\t" \
    "v_mfma_f32_16x16x32_bf16 v[92:95], v[160:163], v[152:155], v[92:95]\n\t" \
    "ds_read_b128 v[208:211], v17 offset:29184\n\t" \
    "v_mfma_f32_16x16x32_bf16 v[48:51], v[160:163], v[164:167], v[48:51]\n\t" \
    "ds_read_b128 v[212:215], v17 offset:29312\n\t" \
    "buffer_load_dwordx4 v[216:219], v1, s[4:7], s47 offen offset:1024 sc1\n\t" \
    "v_mfma_f32_16x16x32_bf16 v[132:135], v[160:163], v[172:175], v[132:135]\n\t" \
    "ds_read_b128 v[220:223], v36 offset:0\n\t" \
    "v_mfma_f32_16x16x32_bf16 v[200:203], v[160:163], v[168:171], v[200:203]\n\t" \
    "ds_read_b128 v[28:31], v36 offset:128\n\t" \
    "s_waitcnt vmcnt(11) lgkmcnt(5)\n\t" \
    "v_mfma_f32_16x16x32_bf16 v[56:59], v[188:191], v[192:195], v[56:59]\n\t" \
    "ds_read_b128 v[128:131], v36 offset:29184\n\t" \
    "buffer_load_dwordx4 v[224:227], v3, s[4:7], s45 offen offset:0 sc1\n\t" \
    "s_waitcnt lgkmcnt(5)\n\t" \
    "v_mfma_f32_16x16x32_bf16 v[60:63], v[188:191], v[204:207], v[60:63]\n\t" \
    "ds_read_b128 v[228:231], v36 offset:29312\n\t" \
    "s_waitcnt lgkmcnt(5)\n\t" \
    "v_mfma_f32_16x16x32_bf16 v[64:67], v[188:191], v[208:211], v[64:67]\n\t" \
    "s_waitcnt lgkmcnt(4)\n\t" \
    "v_mfma_f32_16x16x32_bf16 v[68:71], v[188:191], v[212:215], v[68:71]\n\t" \
    "buffer_load_dwordx4 v[232:235], v3, s[4:7], s46 offen offset:0 sc1\n\t" \
    "s_waitcnt vmcnt(12)\n\t" \
    "v_mfma_f32_16x16x32_bf16 v[112:115], v[176:179], v[204:207], v[112:115]\n\t" \
    "s_waitcnt lgkmcnt(0)\n\t" \
    "s_add_u32 s12, s55, 34603008\n\t" \
    "s_buffer_load_dword s13, s[4:7], s12\n\t" \
    "s_buffer_load_dword s14, s[4:7], s12 offset:0x400\n\t" \
    "v_mfma_f32_16x16x32_bf16 v[60:63], v[176:179], v[192:195], v[60:63]\n\t" \
    "v_mfma_f32_16x16x32_bf16 v[80:83], v[176:179], v[212:215], v[80:83]\n\t" \
    "buffer_load_dwordx4 v[236:239], v3, s[4:7], s47 offen offset:0 sc1\n\t" \
    "v_mfma_f32_16x16x32_bf16 v[68:71], v[176:179], v[208:211], v[68:71]\n\t" \
    "s_waitcnt vmcnt(12)\n\t" \
    "v_mfma_f32_16x16x32_bf16 v[92:95], v[72:75], v[220:223], v[92:95]\n\t" \
    "v_mfma_f32_16x16x32_bf16 v[48:51], v[72:75], v[28:31], v[48:51]\n\t" \
    "buffer_load_dwordx4 v[240:243], v42, s[4:7], s45 offen offset:1024 sc1\n\t" \
    "v_mfma_f32_16x16x32_bf16 v[132:135], v[72:75], v[128:131], v[132:135]\n\t" \
    "v_sub_f32 v56, v56, v112\n\t" \
    "v_sub_f32 v57, v57, v113\n\t" \
    "v_sub_f32 v58, v58, v114\n\t" \
    "v_mfma_f32_16x16x32_bf16 v[200:203], v[72:75], v[228:231], v[200:203]\n\t" \
    "v_sub_f32 v59, v59, v115\n\t" \
    "v_sub_f32 v64, v64, v80\n\t" \
    "v_sub_f32 v65, v65, v81\n\t" \
    "v_sub_f32 v66, v66, v82\n\t" \
    "v_sub_f32 v67, v67, v83\n\t" \
    "v_cvt_pk_bf16_f32 v246, v60, v61\n\t" \
    "v_cvt_pk_bf16_f32 v247, v62, v63\n\t" \
    "v_cvt_pk_bf16_f32 v244, v56, v57\n\t" \
    "v_cvt_pk_bf16_f32 v245, v58, v59\n\t" \
    "v_cvt_pk_bf16_f32 v250, v68, v69\n\t" \
    "v_cvt_pk_bf16_f32 v251, v70, v71\n\t" \
    "ds_write_b128 v40, v[244:247] offset:58368\n\t" \
    "v_cvt_pk_bf16_f32 v248, v64, v65\n\t" \
    "v_cvt_pk_bf16_f32 v249, v66, v67\n\t" \
    "ds_write_b128 v40, v[248:251] offset:59392\n\t" \
    "s_waitcnt vmcnt(10)\n\t" \
    "s_cmp_ge_u32 s24, 4\n\t" \
    "s_cbranch_scc1 .Lk37_%=\n\t" \
    "v_mfma_f32_16x16x32_bf16 v[252:255], v[76:79], v[100:103], 0\n\t" \
    "v_mfma_f32_16x16x32_bf16 v[252:255], v[180:183], v[164:167], v[252:255]\n\t" \
    "v_mfma_f32_16x16x32_bf16 v[252:255], v[84:87], v[28:31], v[252:255]\n\t" \
    "v_mfma_f32_16x16x32_bf16 v[140:143], v[76:79], v[104:107], 0\n\t" \
    "v_mfma_f32_16x16x32_bf16 v[140:143], v[180:183], v[168:171], v[140:143]\n\t" \
    "v_mfma_f32_16x16x32_bf16 v[140:143], v[84:87], v[228:231], v[140:143]\n\t" \
    "v_mfma_f32_16x16x32_bf16 v[48:51], v[76:79], v[96:99], v[48:51]\n\t" \
    "v_mfma_f32_16x16x32_bf16 v[48:51], v[180:183], v[152:155], v[48:51]\n\t" \
    "v_mfma_f32_16x16x32_bf16 v[48:51], v[84:87], v[220:223], v[48:51]\n\t" \
    "v_mfma_f32_16x16x32_bf16 v[200:203], v[76:79], v[124:127], v[200:203]\n\t" \
    "s_nop 0\n\t" \
    "v_sub_f32 v92, v92, v252\n\t" \
    "v_sub_f32 v93, v93, v253\n\t" \
    "v_mfma_f32_16x16x32_bf16 v[200:203], v[180:183], v[172:175], v[200:203]\n\t" \
    "v_sub_f32 v94, v94, v254\n\t" \
    "v_sub_f32 v95, v95, v255\n\t" \
    "v_mfma_f32_16x16x32_bf16 v[200:203], v[84:87], v[128:131], v[200:203]\n\t" \
    "v_sub_f32 v132, v132, v140\n\t" \
    "v_sub_f32 v133, v133, v141\n\t" \
    "v_sub_f32 v134, v134, v142\n\t" \
    "v_sub_f32 v135, v135, v143\n\t" \
    ".Lk37_%=:\n\t" \
    "s_waitcnt vmcnt(9)\n\t" \
    "v_mfma_f32_16x16x32_bf16 v[156:159], v[52:55], v[120:123], 0\n\t" \
    "buffer_load_dwordx4 v[16:19], v42, s[4:7], s46 offen offset:1024 sc1\n\t" \
    "v_mfma_f32_16x16x32_bf16 v[160:163], v[52:55], v[88:91], 0\n\t" \
    "v_cvt_pk_bf16_f32 v190, v48, v49\n\t" \
    "v_cvt_pk_bf16_f32 v191, v50, v51\n\t" \
    "v_mfma_f32_16x16x32_bf16 v[176:179], v[52:55], v[116:119], 0\n\t" \
    "v_cvt_pk_bf16_f32 v188, v92, v93\n\t" \
    "v_cvt_pk_bf16_f32 v189, v94, v95\n\t" \
    "v_mfma_f32_16x16x32_bf16 v[72:75], v[52:55], v[144:147], 0\n\t" \
    "v_cvt_pk_bf16_f32 v114, v200, v201\n\t" \
    "v_cvt_pk_bf16_f32 v115, v202, v203\n\t" \
    "buffer_load_dwordx4 v[80:83], v42, s[4:7], s47 offen offset:1024 sc1\n\t" \
    "s_waitcnt vmcnt(10)\n\t" \
    "v_mfma_f32_16x16x32_bf16 v[60:63], v[184:187], v[88:91], 0\n\t" \
    "ds_write_b128 v40, v[188:191] offset:60432\n\t" \
    "v_cvt_pk_bf16_f32 v112, v132, v133\n\t" \
    "v_mfma_f32_16x16x32_bf16 v[160:163], v[184:187], v[120:123], v[160:163]\n\t" \
    "v_cvt_pk_bf16_f32 v113, v134, v135\n\t" \
    "v_mfma_f32_16x16x32_bf16 v[56:59], v[184:187], v[144:147], 0\n\t" \
    "ds_write_b128 v40, v[112:115] offset:61456\n\t" \
    "s_mov_b32 s45, 688128\n\t" \
    "buffer_load_dwordx4 v[68:71], v1, s[4:7], s45 offen offset:0 sc1\n\t" \
    "v_mfma_f32_16x16x32_bf16 v[72:75], v[184:187], v[116:119], v[72:75]\n\t" \
    "s_waitcnt vmcnt(10)\n\t" \
    "v_mfma_f32_16x16x32_bf16 v[156:159], v[108:111], v[4:7], v[156:159]\n\t" \
    "v_mfma_f32_16x16x32_bf16 v[160:163], v[108:111], v[136:139], v[160:163]\n\t" \
    "s_waitcnt lgkmcnt(0)\n\t" \
    "s_xor_b32 s13, s13, s34\n\t" \
    "s_xor_b32 s14, s14, s34\n\t" \
    "s_or_b32 s13, s13, s14\n\t" \
    "s_cbranch_scc1 .Lq1024_%=\n\t" \
    ".Lb23_%=:\n\t" \
    "s_waitcnt lgkmcnt(0)\n\t" \
    "s_barrier\n\t" \
    "s_memrealtime s[16:17]\n\t" \
    "s_setprio 2\n\t" \
    "ds_read_b64_tr_b16 v[244:245], v43 offset:58368\n\t" \
    "ds_read_b64_tr_b16 v[246:247], v43 offset:62496\n\t" \
    "ds_read_b64_tr_b16 v[64:65], v43 offset:58624\n\t" \
    "ds_read_b64_tr_b16 v[66:67], v43 offset:62752\n\t" \
    "ds_read_b64_tr_b16 v[248:249], v43 offset:58384\n\t" \
    "ds_read_b64_tr_b16 v[250:251], v43 offset:62512\n\t" \
    "ds_read_b64_tr_b16 v[76:77], v43 offset:58640\n\t" \
    "ds_read_b64_tr_b16 v[78:79], v43 offset:62768\n\t" \
    "buffer_load_dwordx4 v[180:183], v1, s[4:7], s45 offen offset:1024 sc1\n\t" \
    "v_mfma_f32_16x16x32_bf16 v[176:179], v[108:111], v[20:23], v[176:179]\n\t" \
    "v_mfma_f32_16x16x32_bf16 v[72:75], v[108:111], v[148:151], v[72:75]\n\t" \
    "s_waitcnt vmcnt(10)\n\t" \
    "v_mfma_f32_16x16x32_bf16 v[60:63], v[24:27], v[136:139], v[60:63]\n\t" \
    "s_waitcnt lgkmcnt(6)\n\t" \
    "v_mfma_f32_16x16x32_bf16 v[252:255], v[244:247], v[44:47], 0\n\t" \
    "s_waitcnt lgkmcnt(4)\n\t" \
    "v_mfma_f32_16x16x32_bf16 v[84:87], v[64:67], v[44:47], 0\n\t" \
    "s_mov_b32 s46, 802816\n\t" \
    "buffer_load_dwordx4 v[140:143], v1, s[4:7], s46 offen offset:0 sc1\n\t" \
    "v_mfma_f32_16x16x32_bf16 v[160:163], v[24:27], v[4:7], v[160:163]\n\t" \
    "s_waitcnt lgkmcnt(2)\n\t" \
    "v_mfma_f32_16x16x32_bf16 v[48:51], v[248:251], v[44:47], 0\n\t" \
    "s_waitcnt lgkmcnt(0)\n\t" \
    "v_mfma_f32_16x16x32_bf16 v[92:95], v[76:79], v[44:47], 0\n\t" \
    "v_mfma_f32_16x16x32_bf16 v[56:59], v[24:27], v[148:151], v[56:59]\n\t" \
    "v_mfma_f32_16x16x32_bf16 v[72:75], v[24:27], v[20:23], v[72:75]\n\t" \
    "v_bfi_b32 v252, s41, v34, v252\n\t" \
    "v_bfi_b32 v84, s41, v34, v84\n\t" \
    "v_cvt_pk_bf16_f32 v53, v254, v255\n\t" \
    "buffer_load_dwordx4 v[200:203], v1, s[4:7], s46 offen offset:1024 sc1\n\t" \
    "s_waitcnt vmcnt(11)\n\t" \
    "v_mfma_f32_16x16x32_bf16 v[156:159], v[196:199], v[192:195], v[156:159]\n\t" \
    "v_cvt_pk_bf16_f32 v55, v86, v87\n\t" \
    "v_cvt_pk_bf16_f32 v52, v252, v253\n\t" \
    "v_cvt_pk_bf16_f32 v54, v84, v85\n\t" \
    "v_mfma_f32_16x16x32_bf16 v[160:163], v[196:199], v[204:207], v[160:163]\n\t" \
    "v_bfi_b32 v48, s41, v34, v48\n\t" \
    "v_bfi_b32 v92, s41, v34, v92\n\t" \
    "buffer_store_dwordx4 v[52:55], v14, s[4:7], s48 offen offset:0 sc1\n\t" \
    "v_mfma_f32_16x16x32_bf16 v[176:179], v[196:199], v[208:211], v[176:179]\n\t" \
    "v_cvt_pk_bf16_f32 v189, v50, v51\n\t" \
    "v_cvt_pk_bf16_f32 v191, v94, v95\n\t" \
    "v_cvt_pk_bf16_f32 v188, v48, v49\n\t" \
    "s_mov_b32 s47, 917504\n\t" \
    "buffer_load_dwordx4 v[132:135], v1, s[4:7], s47 offen offset:0 sc1\n\t" \
    "v_mfma_f32_16x16x32_bf16 v[72:75], v[196:199], v[212:215], v[72:75]\n\t" \
    "v_cvt_pk_bf16_f32 v190, v92, v93\n\t" \
    "buffer_store_dwordx4 v[188:191], v14, s[4:7], s48 offen offset:1024 sc1\n\t" \
    "s_setprio 0\n\t" \
    "s_waitcnt vmcnt(13)\n\t" \
    "v_mfma_f32_16x16x32_bf16 v[60:63], v[216:219], v[204:207], v[60:63]\n\t" \
    "s_cmp_lt_u32 s24, 4\n\t" \
    "s_cbranch_scc1 .Lp38_%=\n\t" \
    "s_setprio 1\n\t" \
    ".Lp38_%=:\n\t" \
    "s_sub_u32 s37, s24, s36\n\t" \
    "s_and_b32 s37, s37, 7\n\t" \
    "s_mul_i32 s39, s27, 28\n\t" \
    "s_add_u32 s39, s39, s26\n\t" \
    "s_lshr_b32 s39, s39, 5\n\t" \
    "s_lshl4_add_u32 s37, s39, s37\n\t" \
    "s_lshl_b32 s37, s37, 7\n\t" \
    "s_add_u32 s37, s37, 34603008\n\t" \
    "s_buffer_load_dword s58, s[4:7], s37 offset:0x4\n\t" \
    "s_buffer_load_dword s59, s[4:7], s37 offset:0x404\n\t" \
    "s_buffer_load_dword s60, s[4:7], s37 offset:0x4004\n\t" \
    "s_buffer_load_dword s61, s[4:7], s37 offset:0x4404\n\t" \
    "v_mfma_f32_16x16x32_bf16 v[160:163], v[216:219], v[192:195], v[160:163]\n\t" \
    "buffer_load_dwordx4 v[112:115], v1, s[4:7], s47 offen offset:1024 sc1\n\t" \
    "v_mfma_f32_16x16x32_bf16 v[56:59], v[216:219], v[212:215], v[56:59]\n\t" \
    "v_mfma_f32_16x16x32_bf16 v[72:75], v[216:219], v[208:211], v[72:75]\n\t" \
    "s_waitcnt vmcnt(13)\n\t" \
    "v_mfma_f32_16x16x32_bf16 v[184:187], v[224:227], v[96:99], 0\n\t" \
    "buffer_load_dwordx4 v[108:111], v3, s[4:7], s45 offen offset:0 sc1\n\t" \
    "v_mfma_f32_16x16x32_bf16 v[244:247], v[224:227], v[100:103], 0\n\t" \
    "v_mfma_f32_16x16x32_bf16 v[64:67], v[224:227], v[124:127], 0\n\t" \
    "s_cmp_lg_u32 s24, 0\n\t" \
    "s_cbranch_scc1 .Lph_%=\n\t" \
    "s_waitcnt lgkmcnt(0)\n\t" \
    "v_mov_b32 v248, s16\n\t" \
    "v_mov_b32 v249, s16\n\t" \
    "v_mov_b32 v250, s16\n\t" \
    "v_mov_b32 v251, s16\n\t" \
    "buffer_store_dwordx4 v[248:251], v2, s[4:7], s32 offen sc1\n\t" \
    "s_nop 0\n\t" \
    ".Lph_%=:\n\t" \
    "v_sub_f32 v156, v156, v60\n\t" \
    "v_sub_f32 v157, v157, v61\n\t" \
    "v_mfma_f32_16x16x32_bf16 v[76:79], v[224:227], v[104:107], 0\n\t" \
    "v_sub_f32 v158, v158, v62\n\t" \
    "v_sub_f32 v159, v159, v63\n\t" \
    "buffer_load_dwordx4 v[24:27], v3, s[4:7], s46 offen offset:0 sc1\n\t" \
    "s_waitcnt vmcnt(14)\n\t" \
    "v_mfma_f32_16x16x32_bf16 v[184:187], v[232:235], v[152:155], v[184:187]\n\t" \
    "v_sub_f32 v176, v176, v56\n\t" \
    "v_sub_f32 v177, v177, v57\n\t" \
    "v_mfma_f32_16x16x32_bf16 v[244:247], v[232:235], v[164:167], v[244:247]\n\t" \
    "v_sub_f32 v178, v178, v58\n\t" \
    "v_sub_f32 v179, v179, v59\n\t" \
    "v_mfma_f32_16x16x32_bf16 v[64:67], v[232:235], v[172:175], v[64:67]\n\t" \
    "v_cvt_pk_bf16_f32 v254, v160, v161\n\t" \
    "v_cvt_pk_bf16_f32 v255, v162, v163\n\t" \
    "buffer_load_dwordx4 v[84:87], v3, s[4:7], s47 offen offset:0 sc1\n\t" \
    "v_mfma_f32_16x16x32_bf16 v[76:79], v[232:235], v[168:171], v[76:79]\n\t" \
    "v_cvt_pk_bf16_f32 v252, v156, v157\n\t" \
    "v_cvt_pk_bf16_f32 v253, v158, v159\n\t" \
    "s_waitcnt vmcnt(14)\n\t" \
    "v_mfma_f32_16x16x32_bf16 v[184:187], v[236:239], v[220:223], v[184:187]\n\t" \
    "v_cvt_pk_bf16_f32 v54, v72, v73\n\t" \
    "v_cvt_pk_bf16_f32 v55, v74, v75\n\t" \
    "v_mfma_f32_16x16x32_bf16 v[244:247], v[236:239], v[28:31], v[244:247]\n\t" \
    "ds_write_b128 v40, v[252:255] offset:0\n\t" \
    "v_cvt_pk_bf16_f32 v52, v176, v177\n\t" \
    "buffer_load_dwordx4 v[48:51], v42, s[4:7], s45 offen offset:1024 sc1\n\t" \
    "v_mfma_f32_16x16x32_bf16 v[64:67], v[236:239], v[128:131], v[64:67]\n\t" \
    "v_cvt_pk_bf16_f32 v53, v178, v179\n\t" \
    "ds_write_b128 v40, v[52:55] offset:1024\n\t" \
    "v_mfma_f32_16x16x32_bf16 v[76:79], v[236:239], v[228:231], v[76:79]\n\t" \
    "s_waitcnt vmcnt(12)\n\t" \
    "s_cmp_ge_u32 s24, 4\n\t" \
    "s_cbranch_scc1 .Lk39_%=\n\t" \
    "v_mfma_f32_16x16x32_bf16 v[196:199], v[240:243], v[100:103], 0\n\t" \
    "v_mfma_f32_16x16x32_bf16 v[196:199], v[16:19], v[164:167], v[196:199]\n\t" \
    "v_mfma_f32_16x16x32_bf16 v[196:199], v[80:83], v[28:31], v[196:199]\n\t" \
    "v_mfma_f32_16x16x32_bf16 v[92:95], v[240:243], v[104:107], 0\n\t" \
    "v_mfma_f32_16x16x32_bf16 v[92:95], v[16:19], v[168:171], v[92:95]\n\t" \
    "v_mfma_f32_16x16x32_bf16 v[92:95], v[80:83], v[228:231], v[92:95]\n\t" \
    "v_mfma_f32_16x16x32_bf16 v[244:247], v[240:243], v[96:99], v[244:247]\n\t" \
    "v_mfma_f32_16x16x32_bf16 v[244:247], v[16:19], v[152:155], v[244:247]\n\t" \
    "v_mfma_f32_16x16x32_bf16 v[244:247], v[80:83], v[220:223], v[244:247]\n\t" \
    "v_mfma_f32_16x16x32_bf16 v[76:79], v[240:243], v[124:127], v[76:79]\n\t" \
    "s_nop 0\n\t" \
    "v_sub_f32 v184, v184, v196\n\t" \
    "v_sub_f32 v185, v185, v197\n\t" \
    "v_mfma_f32_16x16x32_bf16 v[76:79], v[16:19], v[172:175], v[76:79]\n\t" \
    "v_sub_f32 v186, v186, v198\n\t" \
    "v_sub_f32 v187, v187, v199\n\t" \
    "v_mfma_f32_16x16x32_bf16 v[76:79], v[80:83], v[128:131], v[76:79]\n\t" \
    "v_sub_f32 v64, v64, v92\n\t" \
    "v_sub_f32 v65, v65, v93\n\t" \
    "v_sub_f32 v66, v66, v94\n\t" \
    "v_sub_f32 v67, v67, v95\n\t" \
    ".Lk39_%=:\n\t" \
    "s_waitcnt vmcnt(11)\n\t" \
    "v_mfma_f32_16x16x32_bf16 v[188:191], v[68:71], v[120:123], 0\n\t" \
    "buffer_load_dwordx4 v[216:219], v42, s[4:7], s46 offen offset:1024 sc1\n\t" \
    "v_mfma_f32_16x16x32_bf16 v[248:251], v[68:71], v[88:91], 0\n\t" \
    "v_cvt_pk_bf16_f32 v226, v244, v245\n\t" \
    "v_cvt_pk_bf16_f32 v227, v246, v247\n\t" \
    "v_mfma_f32_16x16x32_bf16 v[60:63], v[68:71], v[116:119], 0\n\t" \
    "v_cvt_pk_bf16_f32 v224, v184, v185\n\t" \
    "v_cvt_pk_bf16_f32 v225, v186, v187\n\t" \
    "v_mfma_f32_16x16x32_bf16 v[56:59], v[68:71], v[144:147], 0\n\t" \
    "v_cvt_pk_bf16_f32 v162, v76, v77\n\t" \
    "v_cvt_pk_bf16_f32 v163, v78, v79\n\t" \
    "buffer_load_dwordx4 v[232:235], v42, s[4:7], s47 offen offset:1024 sc1\n\t" \
    "s_waitcnt vmcnt(12)\n\t" \
    "v_mfma_f32_16x16x32_bf16 v[156:159], v[180:183], v[88:91], 0\n\t" \
    "ds_write_b128 v40, v[224:227] offset:2064\n\t" \
    "v_cvt_pk_bf16_f32 v160, v64, v65\n\t" \
    "v_mfma_f32_16x16x32_bf16 v[248:251], v[180:183], v[120:123], v[248:251]\n\t" \
    "v_cvt_pk_bf16_f32 v161, v66, v67\n\t" \
    "v_mfma_f32_16x16x32_bf16 v[72:75], v[180:183], v[144:147], 0\n\t" \
    "ds_write_b128 v40, v[160:163] offset:3088\n\t" \
    "v_mfma_f32_16x16x32_bf16 v[56:59], v[180:183], v[116:119], v[56:59]\n\t" \
    "s_waitcnt vmcnt(11)\n\t" \
    "v_mfma_f32_16x16x32_bf16 v[188:191], v[140:143], v[4:7], v[188:191]\n\t" \
    "v_mfma_f32_16x16x32_bf16 v[248:251], v[140:143], v[136:139], v[248:251]\n\t" \
    "s_waitcnt lgkmcnt(0)\n\t" \
    "s_barrier\n\t" \
    "s_memrealtime s[18:19]\n\t" \
    "s_setprio 2\n\t" \
    "ds_read_b64_tr_b16 v[252:253], v43 offset:0\n\t" \
    "ds_read_b64_tr_b16 v[254:255], v43 offset:4128\n\t" \
    "ds_read_b64_tr_b16 v[176:177], v43 offset:256\n\t" \
    "ds_read_b64_tr_b16 v[178:179], v43 offset:4384\n\t" \
    "ds_read_b64_tr_b16 v[52:53], v43 offset:16\n\t" \
    "ds_read_b64_tr_b16 v[54:55], v43 offset:4144\n\t" \
    "ds_read_b64_tr_b16 v[236:237], v43 offset:272\n\t" \
    "ds_read_b64_tr_b16 v[238:239], v43 offset:4400\n\t" \
    "v_mfma_f32_16x16x32_bf16 v[60:63], v[140:143], v[20:23], v[60:63]\n\t" \
    "v_mfma_f32_16x16x32_bf16 v[56:59], v[140:143], v[148:151], v[56:59]\n\t" \
    "s_waitcnt vmcnt(10)\n\t" \
    "v_mfma_f32_16x16x32_bf16 v[156:159], v[200:203], v[136:139], v[156:159]\n\t" \
    "s_waitcnt lgkmcnt(6)\n\t" \
    "v_mfma_f32_16x16x32_bf16 v[240:243], v[252:255], v[44:47], 0\n\t" \
    "s_waitcnt lgkmcnt(4)\n\t" \
    "v_mfma_f32_16x16x32_bf16 v[16:19], v[176:179], v[44:47], 0\n\t" \
    "v_mfma_f32_16x16x32_bf16 v[248:251], v[200:203], v[4:7], v[248:251]\n\t" \
    "s_waitcnt lgkmcnt(2)\n\t" \
    "v_mfma_f32_16x16x32_bf16 v[196:199], v[52:55], v[44:47], 0\n\t" \
    "s_waitcnt lgkmcnt(0)\n\t" \
    "v_mfma_f32_16x16x32_bf16 v[80:83], v[236:239], v[44:47], 0\n\t" \
    "v_mfma_f32_16x16x32_bf16 v[72:75], v[200:203], v[148:151], v[72:75]\n\t" \
    "v_mfma_f32_16x16x32_bf16 v[56:59], v[200:203], v[20:23], v[56:59]\n\t" \
    "v_bfi_b32 v240, s41, v34, v240\n\t" \
    "v_bfi_b32 v16, s41, v34, v16\n\t" \
    "v_cvt_pk_bf16_f32 v93, v242, v243\n\t" \
    "s_waitcnt vmcnt(8)\n\t" \
    "v_mfma_f32_16x16x32_bf16 v[188:191], v[132:135], v[192:195], v[188:191]\n\t" \
    "v_cvt_pk_bf16_f32 v95, v18, v19\n\t" \
    "v_cvt_pk_bf16_f32 v92, v240, v241\n\t" \
    "v_cvt_pk_bf16_f32 v94, v16, v17\n\t" \
    "s_waitcnt lgkmcnt(0)\n\t" \
    "s_sub_u32 s37, s64, 300\n\t" \
    "s_mov_b32 s62, 300\n\t" \
    "s_sub_u32 s39, s58, s37\n\t" \
    "s_cmp_gt_u32 s39, 600\n\t" \
    "s_cselect_b32 s39, 400, s39\n\t" \
    "s_max_u32 s62, s62, s39\n\t" \
    "s_sub_u32 s39, s59, s37\n\t" \
    "s_cmp_gt_u32 s39, 600\n\t" \
    "s_cselect_b32 s39, 400, s39\n\t" \
    "s_max_u32 s62, s62, s39\n\t" \
    "s_sub_u32 s39, s60, s37\n\t" \
    "s_cmp_gt_u32 s39, 600\n\t" \
    "s_cselect_b32 s39, 400, s39\n\t" \
    "s_max_u32 s62, s62, s39\n\t" \
    "s_sub_u32 s39, s61, s37\n\t" \
    "s_cmp_gt_u32 s39, 600\n\t" \
    "s_cselect_b32 s39, 400, s39\n\t" \
    "s_max_u32 s62, s62, s39\n\t" \
    "s_sub_u32 s62, s62, 300\n\t" \
    "s_mul_i32 s62, s62, 5\n\t" \
    "s_lshr_b32 s62, s62, 2\n\t" \
    "s_add_u32 s37, s62, 115\n\t" \
    ".Lh41_%=:\n\t" \
    "s_memrealtime s[22:23]\n\t" \
    "s_waitcnt lgkmcnt(0)\n\t" \
    "s_sub_u32 s22, s22, s16\n\t" \
    "s_cmp_lt_u32 s22, s37\n\t" \
    "s_cbranch_scc1 .Lh41_%=\n\t" \
    "s_add_u32 s54, s53, 0\n\t" \
    "buffer_load_dwordx4 v[244:247], v0, s[4:7], s54 offen offset:0 sc1\n\t" \
    "s_add_u32 s54, s53, 4243456\n\t" \
    "buffer_load_dwordx4 v[184:187], v0, s[4:7], s54 offen offset:0 sc1\n\t" \
    "v_mfma_f32_16x16x32_bf16 v[248:251], v[132:135], v[204:207], v[248:251]\n\t" \
    "v_bfi_b32 v196, s41, v34, v196\n\t" \
    "v_bfi_b32 v80, s41, v34, v80\n\t" \
    "buffer_store_dwordx4 v[92:95], v14, s[4:7], s49 offen offset:0 sc1\n\t" \
    "v_mfma_f32_16x16x32_bf16 v[60:63], v[132:135], v[208:211], v[60:63]\n\t" \
    "v_cvt_pk_bf16_f32 v69, v198, v199\n\t" \
    "v_cvt_pk_bf16_f32 v71, v82, v83\n\t" \
    "v_cvt_pk_bf16_f32 v68, v196, v197\n\t" \
    "v_mfma_f32_16x16x32_bf16 v[56:59], v[132:135], v[212:215], v[56:59]\n\t" \
    "v_cvt_pk_bf16_f32 v70, v80, v81\n\t" \
    "buffer_store_dwordx4 v[68:71], v14, s[4:7], s49 offen offset:1024 sc1\n\t" \
    "s_setprio 0\n\t" \
    "s_waitcnt vmcnt(10)\n\t" \
    "v_mfma_f32_16x16x32_bf16 v[156:159], v[112:115], v[204:207], v[156:159]\n\t" \
    "s_cmp_lt_u32 s24, 4\n\t" \
    "s_cbranch_scc1 .Lp40_%=\n\t" \
    "s_setprio 1\n\t" \
    ".Lp40_%=:\n\t" \
    "v_add_u32 v40, 91392, v40\n\t" \
    "v_mfma_f32_16x16x32_bf16 v[248:251], v[112:115], v[192:195], v[248:251]\n\t" \
    "v_mfma_f32_16x16x32_bf16 v[72:75], v[112:115], v[212:215], v[72:75]\n\t" \
    "v_mfma_f32_16x16x32_bf16 v[56:59], v[112:115], v[208:211], v[56:59]\n\t" \
    "s_waitcnt vmcnt(9)\n\t" \
    "v_mfma_f32_16x16x32_bf16 v[76:79], v[108:111], v[96:99], 0\n\t" \
    "v_mfma_f32_16x16x32_bf16 v[88:91], v[108:111], v[100:103], 0\n\t" \
    "s_sub_u32 s37, s24, s36\n\t" \
    "s_and_b32 s37, s37, 7\n\t" \
    "s_mul_i32 s39, s27, 28\n\t" \
    "s_add_u32 s39, s39, s26\n\t" \
    "s_lshr_b32 s39, s39, 5\n\t" \
    "s_lshl4_add_u32 s37, s39, s37\n\t" \
    "s_lshl_b32 s37, s37, 7\n\t" \
    "s_add_u32 s37, s37, s33\n\t" \
    "s_buffer_load_dword s58, s[4:7], s37 offset:0x0\n\t" \
    "s_buffer_load_dword s59, s[4:7], s37 offset:0x400\n\t" \
    "s_buffer_load_dword s60, s[4:7], s37 offset:0x4000\n\t" \
    "s_buffer_load_dword s61, s[4:7], s37 offset:0x4400\n\t" \
    "v_mfma_f32_16x16x32_bf16 v[224:227], v[108:111], v[124:127], 0\n\t" \
    "v_sub_f32 v188, v188, v156\n\t" \
    "v_sub_f32 v189, v189, v157\n\t" \
    "v_mfma_f32_16x16x32_bf16 v[120:123], v[108:111], v[104:107], 0\n\t" \
    "v_sub_f32 v190, v190, v158\n\t" \
    "v_sub_f32 v191, v191, v159\n\t" \
    "s_waitcnt vmcnt(8)\n\t" \
    "v_mfma_f32_16x16x32_bf16 v[76:79], v[24:27], v[152:155], v[76:79]\n\t" \
    "v_sub_f32 v60, v60, v72\n\t" \
    "v_sub_f32 v61, v61, v73\n\t" \
    "v_mfma_f32_16x16x32_bf16 v[88:91], v[24:27], v[164:167], v[88:91]\n\t" \
    "v_sub_f32 v62, v62, v74\n\t" \
    "v_sub_f32 v63, v63, v75\n\t" \
    "v_mfma_f32_16x16x32_bf16 v[224:227], v[24:27], v[172:175], v[224:227]\n\t" \
    "v_cvt_pk_bf16_f32 v66, v248, v249\n\t" \
    "v_cvt_pk_bf16_f32 v67, v250, v251\n\t" \
    "v_mfma_f32_16x16x32_bf16 v[120:123], v[24:27], v[168:171], v[120:123]\n\t" \
    "v_cvt_pk_bf16_f32 v64, v188, v189\n\t" \
    "v_cvt_pk_bf16_f32 v65, v190, v191\n\t" \
    "s_waitcnt vmcnt(7)\n\t" \
    "v_mfma_f32_16x16x32_bf16 v[76:79], v[84:87], v[220:223], v[76:79]\n\t" \
    "v_cvt_pk_bf16_f32 v146, v56, v57\n\t" \
    "v_cvt_pk_bf16_f32 v147, v58, v59\n\t" \
    "v_mfma_f32_16x16x32_bf16 v[88:91], v[84:87], v[28:31], v[88:91]\n\t" \
    "ds_write_b128 v40, v[64:67] offset:0\n\t" \
    "v_cvt_pk_bf16_f32 v144, v60, v61\n\t" \
    "v_mfma_f32_16x16x32_bf16 v[224:227], v[84:87], v[128:131], v[224:227]\n\t" \
    "v_cvt_pk_bf16_f32 v145, v62, v63\n\t" \
    "ds_write_b128 v40, v[144:147] offset:1024\n\t" \
    "v_mfma_f32_16x16x32_bf16 v[120:123], v[84:87], v[228:231], v[120:123]\n\t" \
    "s_waitcnt vmcnt(4)\n\t" \
    "s_cmp_ge_u32 s24, 4\n\t" \
    "s_cbranch_scc1 .Lk42_%=\n\t" \
    "v_mfma_f32_16x16x32_bf16 v[160:163], v[48:51], v[100:103], 0\n\t" \
    "v_mfma_f32_16x16x32_bf16 v[160:163], v[216:219], v[164:167], v[160:163]\n\t" \
    "v_mfma_f32_16x16x32_bf16 v[160:163], v[232:235], v[28:31], v[160:163]\n\t" \
    "v_mfma_f32_16x16x32_bf16 v[116:119], v[48:51], v[104:107], 0\n\t" \
    "v_mfma_f32_16x16x32_bf16 v[116:119], v[216:219], v[168:171], v[116:119]\n\t" \
    "v_mfma_f32_16x16x32_bf16 v[116:119], v[232:235], v[228:231], v[116:119]\n\t" \
    "v_mfma_f32_16x16x32_bf16 v[88:91], v[48:51], v[96:99], v[88:91]\n\t" \
    "v_mfma_f32_16x16x32_bf16 v[88:91], v[216:219], v[152:155], v[88:91]\n\t" \
    "v_mfma_f32_16x16x32_bf16 v[88:91], v[232:235], v[220:223], v[88:91]\n\t" \
    "v_mfma_f32_16x16x32_bf16 v[120:123], v[48:51], v[124:127], v[120:123]\n\t" \
    "s_nop 0\n\t" \
    "v_sub_f32 v76, v76, v160\n\t" \
    "v_sub_f32 v77, v77, v161\n\t" \
    "v_mfma_f32_16x16x32_bf16 v[120:123], v[216:219], v[172:175], v[120:123]\n\t" \
    "v_sub_f32 v78, v78, v162\n\t" \
    "v_sub_f32 v79, v79, v163\n\t" \
    "v_mfma_f32_16x16x32_bf16 v[120:123], v[232:235], v[128:131], v[120:123]\n\t" \
    "v_sub_f32 v224, v224, v116\n\t" \
    "v_sub_f32 v225, v225, v117\n\t" \
    "v_sub_f32 v226, v226, v118\n\t" \
    "v_sub_f32 v227, v227, v119\n\t" \
    ".Lk42_%=:\n\t" \
    "ds_read_b128 v[180:183], v10 offset:3072\n\t" \
    "ds_read_b128 v[140:143], v10 offset:4096\n\t" \
    "v_cvt_pk_bf16_f32 v138, v88, v89\n\t" \
    "v_cvt_pk_bf16_f32 v139, v90, v91\n\t" \
    "v_cvt_pk_bf16_f32 v136, v76, v77\n\t" \
    "v_cvt_pk_bf16_f32 v137, v78, v79\n\t" \
    "v_cvt_pk_bf16_f32 v254, v120, v121\n\t" \
    "v_cvt_pk_bf16_f32 v255, v122, v123\n\t" \
    "ds_write_b128 v40, v[136:139] offset:2064\n\t" \
    "v_cvt_pk_bf16_f32 v252, v224, v225\n\t" \
    "v_cvt_pk_bf16_f32 v253, v226, v227\n\t" \
    "v_add_u32 v43, 91392, v43\n\t" \
    "ds_write_b128 v40, v[252:255] offset:3088\n\t" \
    "s_waitcnt lgkmcnt(0)\n\t" \
    "s_sub_u32 s37, s16, 300\n\t" \
    "s_mov_b32 s62, 300\n\t" \
    "s_sub_u32 s39, s58, s37\n\t" \
    "s_cmp_gt_u32 s39, 600\n\t" \
    "s_cselect_b32 s39, 400, s39\n\t" \
    "s_max_u32 s62, s62, s39\n\t" \
    "s_sub_u32 s39, s59, s37\n\t" \
    "s_cmp_gt_u32 s39, 600\n\t" \
    "s_cselect_b32 s39, 400, s39\n\t" \
    "s_max_u32 s62, s62, s39\n\t" \
    "s_sub_u32 s39, s60, s37\n\t" \
    "s_cmp_gt_u32 s39, 600\n\t" \
    "s_cselect_b32 s39, 400, s39\n\t" \
    "s_max_u32 s62, s62, s39\n\t" \
    "s_sub_u32 s39, s61, s37\n\t" \
    "s_cmp_gt_u32 s39, 600\n\t" \
    "s_cselect_b32 s39, 400, s39\n\t" \
    "s_max_u32 s62, s62, s39\n\t" \
    "s_sub_u32 s62, s62, 300\n\t" \
    "s_mul_i32 s62, s62, 5\n\t" \
    "s_lshr_b32 s62, s62, 2\n\t" \
    "s_waitcnt vmcnt(2)\n\t" \
    "v_and_b32 v12, v244, v246\n\t" \
    "v_bitop3_b32 v12, v12, v184, v186 bitop3:0x80\n\t" \
    "v_and_b32 v12, 1, v12\n\t" \
    "v_cmp_eq_u32 vcc, 0, v12\n\t" \
    "s_nop 1\n\t" \
    "s_cbranch_vccnz .Ls43_%=\n\t" \
    ".Lb43_%=:\n\t" \
    "ds_write_b128 v41, v[244:247] offset:33024\n\t" \
    "ds_write_b128 v41, v[184:187] offset:33152\n\t" \
    "s_waitcnt lgkmcnt(0)\n\t" \
    "s_barrier\n\t" \
    "s_memrealtime s[56:57]\n\t" \
    "ds_read_b64_tr_b16 v[176:177], v43 offset:0\n\t" \
    "ds_read_b64_tr_b16 v[178:179], v43 offset:4128\n\t" \
    "ds_read_b64_tr_b16 v[4:5], v43 offset:256\n\t" \
    "ds_read_b64_tr_b16 v[6:7], v43 offset:4384\n\t" \
    "ds_read_b64_tr_b16 v[52:53], v43 offset:16\n\t" \
    "ds_read_b64_tr_b16 v[54:55], v43 offset:4144\n\t" \
    "ds_read_b64_tr_b16 v[236:237], v43 offset:272\n\t" \
    "ds_read_b64_tr_b16 v[238:239], v43 offset:4400\n\t" \
    "ds_read_b64_tr_b16 v[148:149], v35 offset:33024\n\t" \
    "ds_read_b64_tr_b16 v[150:151], v35 offset:33280\n\t" \
    "ds_read_b64_tr_b16 v[20:21], v35 offset:33032\n\t" \
    "ds_read_b64_tr_b16 v[22:23], v35 offset:33288\n\t" \
    "s_waitcnt lgkmcnt(10)\n\t" \
    "v_mfma_f32_16x16x32_bf16 v[200:203], v[176:179], v[44:47], 0\n\t" \
    "s_waitcnt lgkmcnt(8)\n\t" \
    "v_mfma_f32_16x16x32_bf16 v[240:243], v[4:7], v[44:47], 0\n\t" \
    "s_waitcnt lgkmcnt(6)\n\t" \
    "v_mfma_f32_16x16x32_bf16 v[16:19], v[52:55], v[44:47], 0\n\t" \
    "s_waitcnt lgkmcnt(4)\n\t" \
    "v_mfma_f32_16x16x32_bf16 v[92:95], v[236:239], v[44:47], 0\n\t" \
    "s_nop 1\n\t" \
    "v_bfi_b32 v200, s41, v34, v200\n\t" \
    "s_nop 0\n\t" \
    "v_bfi_b32 v240, s41, v34, v240\n\t" \
    "v_cvt_pk_bf16_f32 v197, v202, v203\n\t" \
    "v_cvt_pk_bf16_f32 v199, v242, v243\n\t" \
    "v_cvt_pk_bf16_f32 v196, v200, v201\n\t" \
    "v_cvt_pk_bf16_f32 v198, v240, v241\n\t" \
    "v_bfi_b32 v16, s41, v34, v16\n\t" \
    "v_bfi_b32 v92, s41, v34, v92\n\t" \
    "buffer_store_dwordx4 v[196:199], v14, s[4:7], s50 offen offset:0 sc1\n\t" \
    "v_cvt_pk_bf16_f32 v133, v18, v19\n\t" \
    "v_cvt_pk_bf16_f32 v135, v94, v95\n\t" \
    "v_cvt_pk_bf16_f32 v132, v16, v17\n\t" \
    "v_cvt_pk_bf16_f32 v134, v92, v93\n\t" \
    "buffer_store_dwordx4 v[132:135], v14, s[4:7], s50 offen offset:1024 sc1\n\t" \
    "s_cmp_ge_u32 s25, 252\n\t" \
    "s_cbranch_scc1 .Lend_%=\n\t" \
    "s_add_u32 s37, s62, 100\n\t" \
    ".Lh46_%=:\n\t" \
    "s_memrealtime s[22:23]\n\t" \
    "s_waitcnt lgkmcnt(0)\n\t" \
    "s_sub_u32 s22, s22, s18\n\t" \
    "s_cmp_lt_u32 s22, s37\n\t" \
    "s_cbranch_scc1 .Lh46_%=\n\t" \
    "s_add_u32 s54, s53, 524288\n\t" \
    "buffer_load_dwordx4 v[80:83], v0, s[4:7], s54 offen offset:0 sc1\n\t" \
    "s_add_u32 s54, s53, 4767744\n\t" \
    "buffer_load_dwordx4 v[68:71], v0, s[4:7], s54 offen offset:0 sc1\n\t" \
    "s_waitcnt lgkmcnt(0)\n\t" \
    "s_barrier\n\t" \
    "v_mfma_f32_16x16x32_bf16 v[204:207], v[148:151], v[180:183], 0\n\t" \
    "v_mfma_f32_16x16x32_bf16 v[192:195], v[20:23], v[180:183], 0\n\t" \
    "v_mfma_f32_16x16x32_bf16 v[212:215], v[148:151], v[140:143], 0\n\t" \
    "v_mfma_f32_16x16x32_bf16 v[112:115], v[20:23], v[140:143], 0\n\t" \
    "s_nop 4\n\t" \
    "v_cvt_pk_bf16_f32 v208, v204, v205\n\t" \
    "v_cvt_pk_bf16_f32 v209, v206, v207\n\t" \
    "v_cvt_pk_bf16_f32 v210, v192, v193\n\t" \
    "v_cvt_pk_bf16_f32 v211, v194, v195\n\t" \
    "buffer_store_dwordx4 v[208:211], v39, s[8:11], 0 offen offset:0 sc1\n\t" \
    "v_cvt_pk_bf16_f32 v108, v212, v213\n\t" \
    "v_cvt_pk_bf16_f32 v109, v214, v215\n\t" \
    "v_cvt_pk_bf16_f32 v110, v112, v113\n\t" \
    "v_cvt_pk_bf16_f32 v111, v114, v115\n\t" \
    "buffer_store_dwordx4 v[108:111], v39, s[8:11], 0 offen offset:3072 sc1\n\t" \
    "s_add_u32 s37, s62, 90\n\t" \
    ".Lh47_%=:\n\t" \
    "s_memrealtime s[22:23]\n\t" \
    "s_waitcnt lgkmcnt(0)\n\t" \
    "s_sub_u32 s22, s22, s56\n\t" \
    "s_cmp_lt_u32 s22, s37\n\t" \
    "s_cbranch_scc1 .Lh47_%=\n\t" \
    "s_add_u32 s54, s53, 1048576\n\t" \
    "buffer_load_dwordx4 v[156:159], v0, s[4:7], s54 offen offset:0 sc1\n\t" \
    "s_add_u32 s54, s53, 5292032\n\t" \
    "buffer_load_dwordx4 v[72:75], v0, s[4:7], s54 offen offset:0 sc1\n\t" \
    "s_waitcnt vmcnt(4)\n\t" \
    "v_and_b32 v32, v80, v82\n\t" \
    "v_bitop3_b32 v32, v32, v68, v70 bitop3:0x80\n\t" \
    "v_and_b32 v32, 1, v32\n\t" \
    "v_cmp_eq_u32 vcc, 0, v32\n\t" \
    "s_nop 1\n\t" \
    "s_cbranch_vccnz .Ls44_%=\n\t" \
    ".Lb44_%=:\n\t" \
    "ds_write_b128 v41, v[80:83] offset:0\n\t" \
    "ds_write_b128 v41, v[68:71] offset:128\n\t" \
    "s_waitcnt lgkmcnt(0)\n\t" \
    "s_barrier\n\t" \
    "ds_read_b64_tr_b16 v[248:249], v35 offset:0\n\t" \
    "ds_read_b64_tr_b16 v[250:251], v35 offset:256\n\t" \
    "ds_read_b64_tr_b16 v[24:25], v35 offset:8\n\t" \
    "ds_read_b64_tr_b16 v[26:27], v35 offset:264\n\t" \
    "s_waitcnt lgkmcnt(2)\n\t" \
    "v_mfma_f32_16x16x32_bf16 v[188:191], v[248:251], v[180:183], 0\n\t" \
    "s_waitcnt lgkmcnt(0)\n\t" \
    "v_mfma_f32_16x16x32_bf16 v[56:59], v[24:27], v[180:183], 0\n\t" \
    "v_mfma_f32_16x16x32_bf16 v[64:67], v[248:251], v[140:143], 0\n\t" \
    "v_mfma_f32_16x16x32_bf16 v[60:63], v[24:27], v[140:143], 0\n\t" \
    "s_nop 3\n\t" \
    "v_cvt_pk_bf16_f32 v144, v188, v189\n\t" \
    "v_cvt_pk_bf16_f32 v145, v190, v191\n\t" \
    "v_cvt_pk_bf16_f32 v146, v56, v57\n\t" \
    "v_cvt_pk_bf16_f32 v147, v58, v59\n\t" \
    "buffer_store_dwordx4 v[144:147], v39, s[8:11], 0 offen offset:64 sc1\n\t" \
    "v_cvt_pk_bf16_f32 v84, v64, v65\n\t" \
    "v_cvt_pk_bf16_f32 v85, v66, v67\n\t" \
    "v_cvt_pk_bf16_f32 v86, v60, v61\n\t" \
    "v_cvt_pk_bf16_f32 v87, v62, v63\n\t" \
    "buffer_store_dwordx4 v[84:87], v39, s[8:11], 0 offen offset:3136 sc1\n\t" \
    "s_waitcnt vmcnt(2)\n\t" \
    "v_and_b32 v33, v156, v158\n\t" \
    "v_bitop3_b32 v33, v33, v72, v74 bitop3:0x80\n\t" \
    "v_and_b32 v33, 1, v33\n\t" \
    "v_cmp_eq_u32 vcc, 0, v33\n\t" \
    "s_nop 1\n\t" \
    "s_cbranch_vccnz .Ls45_%=\n\t" \
    ".Lb45_%=:\n\t" \
    "ds_write_b128 v41, v[156:159] offset:16512\n\t" \
    "ds_write_b128 v41, v[72:75] offset:16640\n\t" \
    "s_waitcnt lgkmcnt(0)\n\t" \
    "s_barrier\n\t" \
    "ds_read_b64_tr_b16 v[100:101], v35 offset:16512\n\t" \
    "ds_read_b64_tr_b16 v[102:103], v35 offset:16768\n\t" \
    "ds_read_b64_tr_b16 v[164:165], v35 offset:16520\n\t" \
    "ds_read_b64_tr_b16 v[166:167], v35 offset:16776\n\t" \
    "s_waitcnt lgkmcnt(2)\n\t" \
    "v_mfma_f32_16x16x32_bf16 v[28:31], v[100:103], v[180:183], 0\n\t" \
    "s_waitcnt lgkmcnt(0)\n\t" \
    "v_mfma_f32_16x16x32_bf16 v[104:107], v[164:167], v[180:183], 0\n\t" \
    "v_mfma_f32_16x16x32_bf16 v[168:171], v[100:103], v[140:143], 0\n\t" \
    "v_mfma_f32_16x16x32_bf16 v[228:231], v[164:167], v[140:143], 0\n\t" \
    "s_nop 3\n\t" \
    "v_cvt_pk_bf16_f32 v96, v28, v29\n\t" \
    "v_cvt_pk_bf16_f32 v97, v30, v31\n\t" \
    "v_cvt_pk_bf16_f32 v98, v104, v105\n\t" \
    "v_cvt_pk_bf16_f32 v99, v106, v107\n\t" \
    "buffer_store_dwordx4 v[96:99], v39, s[8:11], 0 offen offset:128 sc1\n\t" \
    "v_cvt_pk_bf16_f32 v152, v168, v169\n\t" \
    "v_cvt_pk_bf16_f32 v153, v170, v171\n\t" \
    "v_cvt_pk_bf16_f32 v154, v228, v229\n\t" \
    "v_cvt_pk_bf16_f32 v155, v230, v231\n\t" \
    "buffer_store_dwordx4 v[152:155], v39, s[8:11], 0 offen offset:3200 sc1\n\t" \
    "s_or_b32 s37, s2, s24\n\t" \
    "s_cbranch_scc1 .Lend_%=\n\t" \
    "s_mov_b32 s37, 34635776\n\t" \
    "v_mov_b32 v37, s34\n\t" \
    "s_mov_b64 exec, 1\n\t" \
    "buffer_store_dword v37, v0, s[4:7], s37 offen sc1\n\t" \
    "s_mov_b64 exec, -1\n\t" \
    ".Lend_%=:\n\t" \
    "s_endpgm\n\t" \
    ".Lq1024_%=:\n\t" \
    "s_mov_b32 s40, 0\n\t" \
    ".Lr1024_%=:\n\t" \
    "s_add_u32 s12, s55, 34603008\n\t" \
    "s_buffer_load_dword s13, s[4:7], s12 glc\n\t" \
    "s_buffer_load_dword s14, s[4:7], s12 offset:0x400 glc\n\t" \
    "s_waitcnt lgkmcnt(0)\n\t" \
    "s_xor_b32 s13, s13, s34\n\t" \
    "s_xor_b32 s14, s14, s34\n\t" \
    "s_or_b32 s13, s13, s14\n\t" \
    "s_cbranch_scc0 .Lb23_%=\n\t" \
    "s_add_u32 s40, s40, 1\n\t" \
    "s_cmp_lt_u32 s40, 1\n\t" \
    "s_cbranch_scc1 .Lr1024_%=\n\t" \
    "s_branch .Ls23_%=\n\t" \
    ".Lq1021_%=:\n\t" \
    "s_mov_b32 s40, 0\n\t" \
    ".Lr1021_%=:\n\t" \
    "s_nop 0\n\t" \
    "v_readlane_b32 s12, v15, 0\n\t" \
    "v_readlane_b32 s13, v15, 1\n\t" \
    "v_readlane_b32 s14, v15, 2\n\t" \
    "v_readlane_b32 s15, v15, 3\n\t" \
    "v_readlane_b32 s16, v15, 4\n\t" \
    "v_readlane_b32 s17, v15, 5\n\t" \
    "v_readlane_b32 s18, v15, 6\n\t" \
    "v_readlane_b32 s19, v15, 7\n\t" \
    "v_readlane_b32 s3, v15, 8\n\t" \
    "s_add_u32 s12, s12, s33\n\t" \
    "s_add_u32 s13, s13, s33\n\t" \
    "s_add_u32 s14, s14, s33\n\t" \
    "s_add_u32 s15, s15, s33\n\t" \
    "s_add_u32 s16, s16, s33\n\t" \
    "s_add_u32 s17, s17, s33\n\t" \
    "s_add_u32 s18, s18, s33\n\t" \
    "s_add_u32 s19, s19, s33\n\t" \
    "s_add_u32 s3, s3, s33\n\t" \
    "s_buffer_load_dword s12, s[4:7], s12 offset:0xc0000 glc\n\t" \
    "s_buffer_load_dword s13, s[4:7], s13 offset:0xc0000 glc\n\t" \
    "s_buffer_load_dword s14, s[4:7], s14 offset:0xc0000 glc\n\t" \
    "s_buffer_load_dword s15, s[4:7], s15 offset:0xc0000 glc\n\t" \
    "s_buffer_load_dword s16, s[4:7], s16 offset:0xc0000 glc\n\t" \
    "s_buffer_load_dword s17, s[4:7], s17 offset:0xc0000 glc\n\t" \
    "s_buffer_load_dword s18, s[4:7], s18 offset:0xc0000 glc\n\t" \
    "s_buffer_load_dword s19, s[4:7], s19 offset:0xc0000 glc\n\t" \
    "s_buffer_load_dword s3, s[4:7], s3 offset:0xc0000 glc\n\t" \
    "s_waitcnt lgkmcnt(0)\n\t" \
    "s_xor_b32 s12, s12, s34\n\t" \
    "s_xor_b32 s13, s13, s34\n\t" \
    "s_xor_b32 s14, s14, s34\n\t" \
    "s_xor_b32 s15, s15, s34\n\t" \
    "s_xor_b32 s16, s16, s34\n\t" \
    "s_xor_b32 s17, s17, s34\n\t" \
    "s_xor_b32 s18, s18, s34\n\t" \
    "s_xor_b32 s19, s19, s34\n\t" \
    "s_xor_b32 s3, s3, s34\n\t" \
    "s_or_b32 s12, s12, s13\n\t" \
    "s_or_b32 s12, s12, s14\n\t" \
    "s_or_b32 s12, s12, s15\n\t" \
    "s_or_b32 s12, s12, s16\n\t" \
    "s_or_b32 s12, s12, s17\n\t" \
    "s_or_b32 s12, s12, s18\n\t" \
    "s_or_b32 s12, s12, s19\n\t" \
    "s_or_b32 s12, s12, s3\n\t" \
    "s_cbranch_scc0 .Lb20_%=\n\t" \
    "s_add_u32 s40, s40, 1\n\t" \
    "s_cmp_lt_u32 s40, 1\n\t" \
    "s_cbranch_scc1 .Lr1021_%=\n\t" \
    "s_branch .Ls20_%=\n\t" \
    ".Lpb_%=:\n\t" \
    "s_sub_u32 s37, s24, 4\n\t" \
    "s_cbranch_scc1 .Lpe_%=\n\t" \
    "s_mul_i32 s38, s37, 0x2100\n\t" \
    "s_and_b32 s38, s38, 0x2300\n\t" \
    "s_add_u32 s38, s38, 34652160\n\t" \
    "s_mov_b32 s60, -1\n\t" \
    "s_mov_b32 s61, -1\n\t" \
    "s_memrealtime s[62:63]\n\t" \
    "s_mov_b64 exec, 0xff\n\t" \
    ".Lpc1_%=:\n\t" \
    "s_memtime s[56:57]\n\t" \
    "s_waitcnt lgkmcnt(0)\n\t" \
    "buffer_store_dwordx4 v[56:59], v0, s[4:7], s38 offen sc1\n\t" \
    "s_waitcnt vmcnt(0)\n\t" \
    "s_memtime s[58:59]\n\t" \
    "s_memrealtime s[22:23]\n\t" \
    "s_waitcnt lgkmcnt(0)\n\t" \
    "s_sub_u32 s56, s58, s56\n\t" \
    "s_min_u32 s56, s56, s60\n\t" \
    "s_mov_b32 s60, s61\n\t" \
    "s_mov_b32 s61, s56\n\t" \
    "s_xor_b32 s38, s38, 0x2000\n\t" \
    "s_add_u32 s38, s38, 0x400\n\t" \
    "s_andn2_b32 s38, s38, 0x1000\n\t" \
    "s_sub_u32 s22, s22, s62\n\t" \
    "s_cmp_lt_u32 s22, 60\n\t" \
    "s_cbranch_scc1 .Lpc1_%=\n\t" \
    "s_mov_b64 exec, -1\n\t" \
    "s_bitcmp1_b32 s38, 13\n\t" \
    "s_cselect_b32 s56, s61, s60\n\t" \
    "s_cselect_b32 s57, s60, s61\n\t" \
    "v_mov_b32 v56, s56\n\t" \
    "v_mov_b32 v57, s57\n\t" \
    "s_lshl3_add_u32 s37, s37, 156992\n\t" \
    "v_mov_b32 v58, s37\n\t" \
    "ds_write_b64 v58, v[56:57]\n\t" \
    "s_branch .Lpe_%=\n\t" \
    ".Lr2_%=:\n\t" \
    "s_add_u32 s40, s40, 1\n\t" \
    "s_cmp_gt_u32 s40, 32\n\t" \
    "s_cbranch_scc1 .Lr3_%=\n\t" \
    "s_sleep 2\n\t" \
    "s_and_b32 s37, s2, 7\n\t" \
    "s_lshl_b32 s37, s37, 10\n\t" \
    "s_add_u32 s37, s37, 34643968\n\t" \
    "v_and_b32 v34, 0x70, v0\n\t" \
    "v_lshlrev_b32 v34, 3, v34\n\t" \
    "buffer_load_dword v42, v34, s[4:7], s37 offen sc1\n\t" \
    "s_waitcnt vmcnt(0)\n\t" \
    "v_readlane_b32 s56, v42, 0\n\t" \
    "v_readlane_b32 s57, v42, 1\n\t" \
    "v_readlane_b32 s58, v42, 2\n\t" \
    "v_readlane_b32 s59, v42, 3\n\t" \
    "v_readlane_b32 s60, v42, 4\n\t" \
    "v_readlane_b32 s61, v42, 5\n\t" \
    "v_readlane_b32 s62, v42, 6\n\t" \
    "v_readlane_b32 s63, v42, 7\n\t" \
    "s_branch .Lr1_%=\n\t" \
    ".Lpp_%=:\n\t" \
    "ds_read_b128 v[112:115], v9 offset:64\n\t" \
    "ds_read_b128 v[116:119], v9 offset:80\n\t" \
    "s_getreg_b32 s37, hwreg(HW_REG_XCC_ID, 0, 2)\n\t" \
    "s_waitcnt lgkmcnt(0)\n\t" \
    "v_min3_u32 v112, v112, v114, v116\n\t" \
    "v_min3_u32 v113, v113, v115, v117\n\t" \
    "v_min_u32 v112, v112, v118\n\t" \
    "v_min_u32 v113, v113, v119\n\t" \
    "v_cmp_gt_u32 vcc, v112, v113\n\t" \
    "s_nop 4\n\t" \
    "s_and_b32 s38, vcc_lo, 4\n\t" \
    "s_add_u32 s37, s37, s38\n\t" \
    "s_add_u32 s37, s37, 1\n\t" \
    "s_and_b32 s37, s37, 7\n\t" \
    "s_lshl_b32 s37, s37, 1\n\t" \
    "s_xor_b32 s37, s37, s34\n\t" \
    "v_mov_b32 v112, s37\n\t" \
    "v_mov_b32 v113, s37\n\t" \
    "v_mov_b32 v114, s37\n\t" \
    "v_mov_b32 v115, s37\n\t" \
    "s_mov_b32 s39, 34636800\n\t" \
    "buffer_store_dwordx4 v[112:115], v0, s[4:7], s39 offen sc1\n\t" \
    "s_nop 1\n\t" \
    "s_branch .Lpq_%=\n\t" \
    ".Lr5_%=:\n\t" \
    "s_add_u32 s40, s40, 1\n\t" \
    "s_cmp_gt_u32 s40, 8192\n\t" \
    "s_cbranch_scc1 .Lr6_%=\n\t" \
    "s_sleep 4\n\t" \
    "v_mov_b32 v37, 0\n\t" \
    "buffer_load_dword v11, v37, s[4:7], s52 offen sc1\n\t" \
    "s_waitcnt vmcnt(0)\n\t" \
    "v_readfirstlane_b32 s36, v11\n\t" \
    "s_branch .Lr4_%=\n\t" \
    ".Ls20_%=:\n\t" \
    "s_mov_b32 s40, 0\n\t" \
    "s_add_u32 s38, s33, 786432\n\t" \
    ".Ll20_%=:\n\t" \
    "s_sleep 2\n\t" \
    "buffer_load_dword v18, v15, s[4:7], s38 offen sc1\n\t" \
    "s_waitcnt vmcnt(0)\n\t" \
    "v_cmp_ne_u32 vcc, s34, v18\n\t" \
    "s_add_u32 s40, s40, 1\n\t" \
    "s_cmp_ge_u32 s40, 8192\n\t" \
    "s_cbranch_scc1 .Lb20_%=\n\t" \
    "s_cbranch_vccnz .Ll20_%=\n\t" \
    "s_branch .Lb20_%=\n\t" \
    ".Ls23_%=:\n\t" \
    "s_mov_b32 s40, 0\n\t" \
    "s_add_u32 s38, s55, 34603008\n\t" \
    ".Ll23_%=:\n\t" \
    "s_sleep 2\n\t" \
    "buffer_load_dword v8, v38, s[4:7], s38 offen sc1\n\t" \
    "s_waitcnt vmcnt(0)\n\t" \
    "v_cmp_ne_u32 vcc, s34, v8\n\t" \
    "s_add_u32 s40, s40, 1\n\t" \
    "s_cmp_ge_u32 s40, 8192\n\t" \
    "s_cbranch_scc1 .Lb23_%=\n\t" \
    "s_cbranch_vccnz .Ll23_%=\n\t" \
    "s_branch .Lb23_%=\n\t" \
    ".Ls43_%=:\n\t" \
    "s_mov_b32 s40, 0\n\t" \
    ".Ll43_%=:\n\t" \
    "s_add_u32 s54, s53, 0\n\t" \
    "buffer_load_dwordx4 v[244:247], v0, s[4:7], s54 offen offset:0 sc1\n\t" \
    "s_add_u32 s54, s53, 4243456\n\t" \
    "buffer_load_dwordx4 v[184:187], v0, s[4:7], s54 offen offset:0 sc1\n\t" \
    "s_waitcnt vmcnt(0)\n\t" \
    "v_and_b32 v12, v244, v246\n\t" \
    "v_bitop3_b32 v12, v12, v184, v186 bitop3:0x80\n\t" \
    "v_and_b32 v12, 1, v12\n\t" \
    "v_cmp_eq_u32 vcc, 0, v12\n\t" \
    "s_add_u32 s40, s40, 1\n\t" \
    "s_cmp_ge_u32 s40, 8192\n\t" \
    "s_cbranch_scc1 .Lb43_%=\n\t" \
    "s_cbranch_vccnz .Ll43_%=\n\t" \
    "s_branch .Lb43_%=\n\t" \
    ".Ls44_%=:\n\t" \
    "s_mov_b32 s40, 0\n\t" \
    ".Ll44_%=:\n\t" \
    "s_add_u32 s54, s53, 524288\n\t" \
    "buffer_load_dwordx4 v[80:83], v0, s[4:7], s54 offen offset:0 sc1\n\t" \
    "s_add_u32 s54, s53, 4767744\n\t" \
    "buffer_load_dwordx4 v[68:71], v0, s[4:7], s54 offen offset:0 sc1\n\t" \
    "s_waitcnt vmcnt(0)\n\t" \
    "v_and_b32 v32, v80, v82\n\t" \
    "v_bitop3_b32 v32, v32, v68, v70 bitop3:0x80\n\t" \
    "v_and_b32 v32, 1, v32\n\t" \
    "v_cmp_eq_u32 vcc, 0, v32\n\t" \
    "s_add_u32 s40, s40, 1\n\t" \
    "s_cmp_ge_u32 s40, 8192\n\t" \
    "s_cbranch_scc1 .Lb44_%=\n\t" \
    "s_cbranch_vccnz .Ll44_%=\n\t" \
    "s_branch .Lb44_%=\n\t" \
    ".Ls45_%=:\n\t" \
    "s_mov_b32 s40, 0\n\t" \
    ".Ll45_%=:\n\t" \
    "s_add_u32 s54, s53, 1048576\n\t" \
    "buffer_load_dwordx4 v[156:159], v0, s[4:7], s54 offen offset:0 sc1\n\t" \
    "s_add_u32 s54, s53, 5292032\n\t" \
    "buffer_load_dwordx4 v[72:75], v0, s[4:7], s54 offen offset:0 sc1\n\t" \
    "s_waitcnt vmcnt(0)\n\t" \
    "v_and_b32 v33, v156, v158\n\t" \
    "v_bitop3_b32 v33, v33, v72, v74 bitop3:0x80\n\t" \
    "v_and_b32 v33, 1, v33\n\t" \
    "v_cmp_eq_u32 vcc, 0, v33\n\t" \
    "s_add_u32 s40, s40, 1\n\t" \
    "s_cmp_ge_u32 s40, 8192\n\t" \
    "s_cbranch_scc1 .Lb45_%=\n\t" \
    "s_cbranch_vccnz .Ll45_%=\n\t" \
    "s_branch .Lb45_%=\n\t" \
    ".Lfin_%=:\n\t"
#define K_CLOBBERS "v1", "v2", "v3", "v4", "v5", "v6", "v7", "v8", "v9", "v10", "v11", "v12", "v13", "v14", "v15", "v16", "v17", "v18", "v19", "v20", "v21", "v22", "v23", "v24", "v25", "v26", "v27", "v28", "v29", "v30", "v31", "v32", "v33", "v34", "v35", "v36", "v37", "v38", "v39", "v40", "v41", "v42", "v43", "v44", "v45", "v46", "v47", "v48", "v49", "v50", "v51", "v52", "v53", "v54", "v55", "v56", "v57", "v58", "v59", "v60", "v61", "v62", "v63", "v64", "v65", "v66", "v67", "v68", "v69", "v70", "v71", "v72", "v73", "v74", "v75", "v76", "v77", "v78", "v79", "v80", "v81", "v82", "v83", "v84", "v85", "v86", "v87", "v88", "v89", "v90", "v91", "v92", "v93", "v94", "v95", "v96", "v97", "v98", "v99", "v100", "v101", "v102", "v103", "v104", "v105", "v106", "v107", "v108", "v109", "v110", "v111", "v112", "v113", "v114", "v115", "v116", "v117", "v118", "v119", "v120", "v121", "v122", "v123", "v124", "v125", "v126", "v127", "v128", "v129", "v130", "v131", "v132", "v133", "v134", "v135", "v136", "v137", "v138", "v139", "v140", "v141", "v142", "v143", "v144", "v145", "v146", "v147", "v148", "v149", "v150", "v151", "v152", "v153", "v154", "v155", "v156", "v157", "v158", "v159", "v160", "v161", "v162", "v163", "v164", "v165", "v166", "v167", "v168", "v169", "v170", "v171", "v172", "v173", "v174", "v175", "v176", "v177", "v178", "v179", "v180", "v181", "v182", "v183", "v184", "v185", "v186", "v187", "v188", "v189", "v190", "v191", "v192", "v193", "v194", "v195", "v196", "v197", "v198", "v199", "v200", "v201", "v202", "v203", "v204", "v205", "v206", "v207", "v208", "v209", "v210", "v211", "v212", "v213", "v214", "v215", "v216", "v217", "v218", "v219", "v220", "v221", "v222", "v223", "v224", "v225", "v226", "v227", "v228", "v229", "v230", "v231", "v232", "v233", "v234", "v235", "v236", "v237", "v238", "v239", "v240", "v241", "v242", "v243", "v244", "v245", "v246", "v247", "v248", "v249", "v250", "v251", "v252", "v253", "v254", "v255", "s3", "s4", "s5", "s6", "s7", "s8", "s9", "s10", "s11", "s12", "s13", "s14", "s15", "s16", "s17", "s18", "s19", "s20", "s21", "s22", "s23", "s24", "s25", "s26", "s27", "s28", "s29", "s30", "s31", "s32", "s33", "s34", "s35", "s36", "s37", "s38", "s39", "s40", "s41", "s42", "s43", "s44", "s45", "s46", "s47", "s48", "s49", "s50", "s51", "s52", "s53", "s54", "s55", "s56", "s57", "s58", "s59", "s60", "s61", "s62", "s63", "s64", "s65"

extern "C" __global__ __launch_bounds__(512, 2)
void k_conv(const char* __restrict__ x, const char* __restrict__ wgt, char* __restrict__ ws, char* __restrict__ out)
{
  __shared__ __attribute__((aligned(256))) char smem[157568];
  unsigned tid = threadIdx.x;
  auto kp = __builtin_amdgcn_kernarg_segment_ptr();
  asm volatile(K_BODY : "+{v0}"(tid), "+{s[0:1]}"(kp)
               : "{s2}"(blockIdx.x), "i"((__attribute__((address_space(3))) char*)smem)
               : "memory", "scc", "vcc", K_CLOBBERS);
}

extern "C" void kernel_launch(void* const* d_in, const int* in_sizes, int n_in,
                              void* d_out, int out_size, void* d_ws, size_t ws_size,
                              hipStream_t stream) {
  k_conv<<<dim3(256), dim3(512), 0, stream>>>((const char*)d_in[0], (const char*)d_in[1], (char*)d_ws, (char*)d_out);
}
```

```cpp
#include <hip/hip_runtime.h>
#include <hip/hip_bf16.h>


#define K_BODY \
    "s_load_dwordx2 s[16:17], s[0:1], 0x0\n\t" \
    "s_load_dwordx2 s[4:5], s[0:1], 0x10\n\t" \
    "s_load_dwordx2 s[12:13], s[0:1], 0x8\n\t" \
    "s_load_dwordx2 s[8:9], s[0:1], 0x18\n\t" \
    "s_getpc_b64 s[22:23]\n\t" \
    ".Lpc_%=:\n\t" \
    "s_cmp_ge_u32 s2, 16\n\t" \
    "s_cbranch_scc1 .Le1_%=\n\t" \
    "s_mov_b32 m0, 157312\n\t" \
    "v_lshlrev_b32 v8, 6, v0\n\t" \
    "v_min_u32 v8, .Lfin_%=-.Lpc_%=, v8\n\t" \
    "global_load_lds_dword v8, s[22:23]\n\t" \
    ".Le1_%=:\n\t" \
    "v_readfirstlane_b32 s24, v0\n\t" \
    "s_and_b32 s25, s2, 7\n\t" \
    "s_lshl_b32 s25, s25, 5\n\t" \
    "s_lshr_b32 s37, s2, 3\n\t" \
    "s_add_u32 s25, s25, s37\n\t" \
    "s_min_u32 s28, s25, 251\n\t" \
    "s_mul_i32 s26, s28, 57\n\t" \
    "s_lshr_b32 s26, s26, 9\n\t" \
    "s_mul_i32 s37, s26, 9\n\t" \
    "s_sub_u32 s27, s28, s37\n\t" \
    "v_and_b32 v0, 63, v0\n\t" \
    "v_lshlrev_b32 v0, 4, v0\n\t" \
    "s_lshr_b32 s24, s24, 6\n\t" \
    "s_sub_u32 s37, s24, 4\n\t" \
    "s_cbranch_scc1 .Le2_%=\n\t" \
    "s_cmp_ge_u32 s25, 252\n\t" \
    "s_cbranch_scc1 .Le2_%=\n\t" \
    "s_mul_i32 m0, s37, 12416\n\t" \
    "s_lshl2_add_u32 s37, s26, s37\n\t" \
    "s_mul_i32 s37, s37, 86016\n\t" \
    "s_mul_i32 s38, s27, 4992\n\t" \
    "s_add_u32 s37, s37, s38\n\t" \
    "s_addk_i32 s37, 896\n\t" \
    "v_add_u32 v9, s37, v0\n\t" \
    "s_cmp_eq_u32 s27, 0\n\t" \
    "s_cselect_b32 s38, 43008, 0\n\t" \
    "s_sub_u32 s39, s38, 2048\n\t" \
    "v_add_u32 v7, s39, v9\n\t" \
    "v_subrev_u32 v8, 0x400, v9\n\t" \
    "s_mov_b64 exec, 0xff\n\t" \
    "v_add_u32 v8, s38, v8\n\t" \
    "s_mov_b64 exec, -1\n\t" \
    "s_mov_b32 s18, 9633792\n\t" \
    "s_mov_b32 s19, 0x20000\n\t" \
    "s_mov_b32 s39, 43008\n\t" \
    "s_waitcnt lgkmcnt(0)\n\t" \
    "buffer_load_dwordx4 v7, s[16:19], 0 offen offset:0 nt lds\n\t" \
    "s_add_u32 m0, m0, 0x400\n\t" \
    "s_nop 0\n\t" \
    "buffer_load_dwordx4 v8, s[16:19], 0 offen offset:0 nt lds\n\t" \
    "s_add_u32 m0, m0, 0x400\n\t" \
    "s_nop 0\n\t" \
    "buffer_load_dwordx4 v9, s[16:19], 0 offen offset:0 nt lds\n\t" \
    "s_add_u32 m0, m0, 32\n\t" \
    "s_nop 0\n\t" \
    "buffer_load_dwordx4 v9, s[16:19], 0 offen offset:1024 nt lds\n\t" \
    "buffer_load_dwordx4 v9, s[16:19], 0 offen offset:2048 nt lds\n\t" \
    "buffer_load_dwordx4 v9, s[16:19], 0 offen offset:3072 nt lds\n\t" \
    "s_add_u32 m0, m0, 0x1020\n\t" \
    "s_nop 0\n\t" \
    "buffer_load_dwordx4 v7, s[16:19], s39 offen offset:0 nt lds\n\t" \
    "s_add_u32 m0, m0, 0x400\n\t" \
    "s_nop 0\n\t" \
    "buffer_load_dwordx4 v8, s[16:19], s39 offen offset:0 nt lds\n\t" \
    "s_add_u32 m0, m0, 0x400\n\t" \
    "s_nop 0\n\t" \
    "buffer_load_dwordx4 v9, s[16:19], s39 offen offset:0 nt lds\n\t" \
    "s_add_u32 m0, m0, 32\n\t" \
    "s_nop 0\n\t" \
    "buffer_load_dwordx4 v9, s[16:19], s39 offen offset:1024 nt lds\n\t" \
    "buffer_load_dwordx4 v9, s[16:19], s39 offen offset:2048 nt lds\n\t" \
    "buffer_load_dwordx4 v9, s[16:19], s39 offen offset:3072 nt lds\n\t" \
    ".Le2_%=:\n\t" \
    "s_mov_b32 s6, 34668544\n\t" \
    "s_mov_b32 s7, 0x20000\n\t" \
    "v_lshrrev_b32 v8, 7, v0\n\t" \
    "v_and_b32 v9, 0x70, v0\n\t" \
    "v_lshl_or_b32 v2, v8, 15, v9\n\t" \
    "s_lshl_b32 s32, s2, 7\n\t" \
    "s_add_u32 s32, s32, 33554432\n\t" \
    "s_cmp_ge_u32 s24, 4\n\t" \
    "s_cbranch_scc1 .Le3_%=\n\t" \
    "v_mov_b32 v12, 0\n\t" \
    "v_mov_b32 v13, 0\n\t" \
    "v_mov_b32 v14, 0\n\t" \
    "v_mov_b32 v15, 0\n\t" \
    "s_lshl_b32 s37, s24, 18\n\t" \
    "s_add_u32 s37, s37, s32\n\t" \
    "s_xor_b32 s38, s24, 2\n\t" \
    "s_lshr_b32 s39, s2, 3\n\t" \
    "s_or_b32 s38, s38, s39\n\t" \
    "s_memrealtime s[22:23]\n\t" \
    "s_waitcnt lgkmcnt(0)\n\t" \
    "s_cbranch_scc1 .Le4_%=\n\t" \
    "v_lshl_or_b32 v8, v8, 10, v9\n\t" \
    "v_mov_b32 v16, s22\n\t" \
    "v_mov_b32 v17, s22\n\t" \
    "v_mov_b32 v18, s22\n\t" \
    "v_mov_b32 v19, s22\n\t" \
    "s_add_u32 s38, s32, 1089536\n\t" \
    "buffer_store_dwordx4 v[16:19], v8, s[4:7], s38 offen sc1\n\t" \
    ".Le4_%=:\n\t" \
    "\n\t" \
    ".Le3_%=:\n\t" \
    "s_mul_i32 s30, s28, 293\n\t" \
    "s_lshr_b32 s30, s30, 11\n\t" \
    "s_mul_i32 s37, s30, 7\n\t" \
    "s_sub_u32 s28, s28, s37\n\t" \
    "s_mul_i32 s29, s30, 43\n\t" \
    "s_lshr_b32 s29, s29, 7\n\t" \
    "s_mul_i32 s37, s29, 3\n\t" \
    "s_sub_u32 s30, s30, s37\n\t" \
    "s_and_b32 s37, s29, 12\n\t" \
    "s_add_u32 s37, s37, s29\n\t" \
    "s_mul_i32 s31, s37, 768\n\t" \
    "s_lshl_b32 s37, s30, 6\n\t" \
    "s_add_u32 s31, s31, s37\n\t" \
    "s_add_u32 s41, s29, s30\n\t" \
    "s_add_u32 s41, s41, s28\n\t" \
    "s_and_b32 s41, s41, 3\n\t" \
    "s_cmp_lt_u32 s25, 252\n\t" \
    "s_cselect_b32 s14, 903168, 0\n\t" \
    "s_mov_b32 s15, 0x20000\n\t" \
    "v_lshrrev_b32 v10, 6, v0\n\t" \
    "v_lshrrev_b32 v11, 9, v0\n\t" \
    "v_sub_u32 v8, v10, v11\n\t" \
    "v_mul_u32_u24 v8, 18432, v8\n\t" \
    "v_and_b32 v9, 0x30, v0\n\t" \
    "v_lshlrev_b32 v11, 5, v11\n\t" \
    "v_bitop3_b32 v1, v8, v9, v11 bitop3:0xf6\n\t" \
    "s_mov_b32 exec_lo, 0xf0000000\n\t" \
    "s_mov_b32 exec_hi, 0xf0000000\n\t" \
    "v_bfrev_b32 v1, 1\n\t" \
    "s_mov_b64 exec, -1\n\t" \
    "s_cmp_ge_u32 s24, 4\n\t" \
    "s_cbranch_scc1 .Le9_%=\n\t" \
    "v_bfe_u32 v20, v0, 4, 4\n\t" \
    "v_and_b32 v10, 12, v10\n\t" \
    "v_cvt_f32_ubyte0 v21, v10\n\t" \
    "v_and_b32 v22, 1, v20\n\t" \
    "v_cvt_f32_ubyte0 v22, v22\n\t" \
    "v_mul_f32 v22, 0x3e800000, v22\n\t" \
    "v_lshrrev_b32 v23, 1, v20\n\t" \
    "v_mov_b32 v32, 0\n\t" \
    "v_mov_b32 v33, -1\n\t" \
    "v_mov_b32 v31, 0x3e800000\n\t" \
    "s_mov_b32 s65, 0\n\t" \
    "s_mov_b32 s40, 0\n\t" \
    "s_cmp_eq_u32 s24, 1\n\t" \
    "s_cbranch_scc1 .Lm1_%=\n\t" \
    "s_cmp_eq_u32 s24, 2\n\t" \
    "s_cbranch_scc1 .Lm2_%=\n\t" \
    "s_cmp_eq_u32 s24, 3\n\t" \
    "s_cbranch_scc1 .Lm3_%=\n\t" \
    "s_add_u32 s37, s32, 1048576\n\t" \
    "s_mov_b64 exec, 0xff\n\t" \
    "\n\t" \
    "s_mov_b64 exec, -1\n\t" \
    "v_or_b32 v24, 1, v20\n\t" \
    "v_cvt_f32_ubyte0 v24, v24\n\t" \
    "v_mul_f32 v24, 0x3c800000, v24\n\t" \
    "v_lshrrev_b32 v25, 4, v0\n\t" \
    "v_and_b32 v25, 16, v25\n\t" \
    "v_lshrrev_b32 v26, 2, v11\n\t" \
    "v_or_b32 v25, v25, v26\n\t" \
    "v_cvt_f32_ubyte0 v25, v25\n\t" \
    "v_add_f32 v26, 2.0, v25\n\t" \
    "v_add_f32 v27, 4.0, v25\n\t" \
    "v_add_f32 v28, 4.0, v26\n\t" \
    "v_mov_b32 v29, v22\n\t" \
    "v_mov_b32 v30, v22\n\t" \
    "v_mov_b32 v31, v24\n\t" \
    "s_mov_b32 s64, 0x3e800000\n\t" \
    "s_mov_b32 s65, 0x3e800000\n\t" \
    "v_add_u32 v34, 154880, v0\n\t" \
    "s_branch .Ltl_%=\n\t" \
    ".Lm1_%=:\n\t" \
    "v_lshrrev_b32 v24, 5, v0\n\t" \
    "v_and_b32 v24, 12, v24\n\t" \
    "v_and_or_b32 v24, v20, 3, v24\n\t" \
    "v_cvt_f32_ubyte0 v24, v24\n\t" \
    "v_mul_f32 v24, 0x3d924925, v24\n\t" \
    "v_mul_f32 v32, 0x41000000, v24\n\t" \
    "v_lshrrev_b32 v27, 3, v11\n\t" \
    "v_cvt_f32_ubyte0 v27, v27\n\t" \
    "v_add_f32 v25, -2.0, v27\n\t" \
    "v_add_f32 v26, -1.0, v27\n\t" \
    "v_add_f32 v28, 1.0, v27\n\t" \
    "v_and_b32 v29, 4, v20\n\t" \
    "v_cvt_f32_ubyte0 v29, v29\n\t" \
    "v_mul_f32 v29, 0x3d800000, v29\n\t" \
    "v_mov_b32 v30, v29\n\t" \
    "v_mov_b32 v31, 0xbe800000\n\t" \
    "s_mov_b32 s64, 0\n\t" \
    "s_mov_b32 exec_hi, 0\n\t" \
    "v_mov_b32 v33, 0\n\t" \
    "s_mov_b64 exec, -1\n\t" \
    "v_add_u32 v34, 149760, v0\n\t" \
    "s_branch .Ltl_%=\n\t" \
    ".Lm2_%=:\n\t" \
    "v_lshl_add_u32 v24, v20, 1, 6\n\t" \
    "v_cvt_f32_ubyte0 v24, v24\n\t" \
    "v_mul_f32 v24, 0x3d000000, v24\n\t" \
    "v_add_f32 v25, 0.5, v21\n\t" \
    "v_add_f32 v26, 1.0, v25\n\t" \
    "v_add_f32 v27, 2.0, v25\n\t" \
    "v_add_f32 v28, 2.0, v26\n\t" \
    "v_mov_b32 v29, 0\n\t" \
    "v_mov_b32 v30, 0\n\t" \
    "s_mov_b32 s64, 0x3d000000\n\t" \
    "v_add_u32 v34, 152832, v0\n\t" \
    "s_branch .Ltl_%=\n\t" \
    ".Lm3_%=:\n\t" \
    "s_mov_b32 s37, 34635776\n\t" \
    "buffer_load_dword v3, v12, s[4:7], s37 offen sc1\n\t" \
    "v_lshrrev_b32 v8, 7, v0\n\t" \
    "v_mul_u32_u24 v8, 96, v8\n\t" \
    "v_and_b32 v9, 16, v0\n\t" \
    "v_bfe_u32 v10, v0, 5, 2\n\t" \
    "v_add3_u32 v8, v8, v9, v10\n\t" \
    "v_mul_u32_u24 v8, 192, v8\n\t" \
    "s_mul_i32 s37, s28, 129024\n\t" \
    "s_add_u32 s37, s37, s31\n\t" \
    "s_mov_b32 m0, 157312\n\t" \
    "s_mov_b32 exec_hi, 0xffffff\n\t" \
    "buffer_load_dword v8, s[12:15], s37 offen  lds\n\t" \
    "s_mov_b64 exec, -1\n\t" \
    "v_lshrrev_b32 v8, 2, v0\n\t" \
    "v_and_b32 v8, 28, v8\n\t" \
    "v_add_u32 v8, 156928, v8\n\t" \
    "ds_write_b32 v8, v12\n\t" \
    "v_add_u32 v24, 6, v23\n\t" \
    "v_cvt_f32_ubyte0 v24, v24\n\t" \
    "v_mul_f32 v24, 0x3d924925, v24\n\t" \
    "v_mov_b32 v25, v21\n\t" \
    "v_add_f32 v26, 1.0, v21\n\t" \
    "v_add_f32 v27, 2.0, v21\n\t" \
    "v_add_f32 v28, 2.0, v26\n\t" \
    "v_sub_f32 v29, 0, v22\n\t" \
    "v_sub_f32 v30, 0, v22\n\t" \
    "s_mov_b32 exec_lo, 0\n\t" \
    "v_fma_f32 v25, v21, 0.5, 4.0\n\t" \
    "v_add_f32 v27, 1.0, v25\n\t" \
    "v_fmaak_f32 v26, v21, v31, 0x41200000\n\t" \
    "v_mov_b32 v28, v26\n\t" \
    "v_add_f32 v30, v31, v30\n\t" \
    "s_mov_b64 exec, -1\n\t" \
    "s_mov_b32 s40, 0x11\n\t" \
    "v_add_u32 v34, 151808, v0\n\t" \
    ".Ltl_%=:\n\t" \
    "v_fma_f32 v36, v24, v25, v29\n\t" \
    "v_fma_f32 v38, v24, v26, v29\n\t" \
    "v_fma_f32 v40, v24, v27, v29\n\t" \
    "v_fma_f32 v42, v24, v28, v30\n\t" \
    "v_add_f32 v37, v36, v31\n\t" \
    "v_add_f32 v39, v38, v31\n\t" \
    "v_add_f32 v41, v40, v31\n\t" \
    "v_add_f32 v43, v42, v31\n\t" \
    "v_cos_f32 v36, v36\n\t" \
    "v_cos_f32 v37, v37\n\t" \
    "v_cos_f32 v38, v38\n\t" \
    "v_cos_f32 v39, v39\n\t" \
    "v_cos_f32 v40, v40\n\t" \
    "v_cos_f32 v41, v41\n\t" \
    "v_cos_f32 v42, v42\n\t" \
    "v_cos_f32 v43, v43\n\t" \
    "s_bitcmp0_b32 s40, 4\n\t" \
    "s_cbranch_scc1 .Lt2_%=\n\t" \
    "v_mul_f32 v36, 0x3b924925, v36\n\t" \
    "v_mul_f32 v37, 0x3b924925, v37\n\t" \
    "v_mul_f32 v38, 0x3b924925, v38\n\t" \
    "v_mul_f32 v39, 0x3b924925, v39\n\t" \
    "v_mul_f32 v40, 0x3b924925, v40\n\t" \
    "v_mul_f32 v41, 0x3b924925, v41\n\t" \
    "v_mul_f32 v42, 0x3b924925, v42\n\t" \
    "v_mul_f32 v43, 0x3b924925, v43\n\t" \
    ".Lt2_%=:\n\t" \
    "v_cvt_pk_bf16_f32 v44, v36, v37\n\t" \
    "v_cvt_pk_bf16_f32 v45, v38, v39\n\t" \
    "v_cvt_pk_bf16_f32 v46, v40, v41\n\t" \
    "v_cvt_pk_bf16_f32 v47, v42, v43\n\t" \
    "v_and_b32 v44, v33, v44\n\t" \
    "v_and_b32 v45, v33, v45\n\t" \
    "v_mov_b32 v33, -1\n\t" \
    "v_add_f32 v24, s64, v24\n\t" \
    "v_add_f32 v31, s65, v31\n\t" \
    "v_add_f32 v29, v32, v29\n\t" \
    "v_add_f32 v30, v32, v30\n\t" \
    "ds_write_b128 v34, v[44:47]\n\t" \
    "s_add_u32 s40, s40, 1\n\t" \
    "s_bitcmp1_b32 s40, 0\n\t" \
    "v_add_u32 v34, 0x400, v34\n\t" \
    "s_cbranch_scc1 .Ltl_%=\n\t" \
    "s_bitcmp1_b32 s40, 2\n\t" \
    "s_cbranch_scc1 .Lt3_%=\n\t" \
    "s_mov_b32 s40, 2\n\t" \
    "s_lshl2_add_u32 s37, s28, s24\n\t" \
    "s_lshl_b32 s37, s37, 2\n\t" \
    "v_lshrrev_b32 v25, 2, v20\n\t" \
    "v_add_u32 v25, s37, v25\n\t" \
    "v_mul_u32_u24 v26, 147, v25\n\t" \
    "v_lshrrev_b32 v26, 10, v26\n\t" \
    "v_lshl_or_b32 v24, v26, 1, 1\n\t" \
    "v_cvt_f32_ubyte0 v24, v24\n\t" \
    "v_mul_f32 v24, 0x3c800000, v24\n\t" \
    "v_mul_u32_u24 v26, 7, v26\n\t" \
    "v_sub_u32 v25, v25, v26\n\t" \
    "v_xor_b32 v22, s41, v20\n\t" \
    "v_bfe_u32 v32, v22, 1, 1\n\t" \
    "v_and_b32 v22, 1, v22\n\t" \
    "v_cvt_f32_ubyte0 v22, v22\n\t" \
    "v_mul_f32 v22, 0x3e800000, v22\n\t" \
    "v_lshl_or_b32 v32, v25, 1, v32\n\t" \
    "v_cvt_f32_ubyte0 v32, v32\n\t" \
    "v_mul_f32 v32, 0x3e924925, v32\n\t" \
    "v_mul_f32 v29, 0x3d800000, v21\n\t" \
    "v_fma_f32 v29, v29, v32, v22\n\t" \
    "v_mov_b32 v30, v29\n\t" \
    "v_mov_b32 v31, v24\n\t" \
    "v_mov_b32 v25, 0\n\t" \
    "v_mov_b32 v26, 2.0\n\t" \
    "v_mov_b32 v27, 4.0\n\t" \
    "v_mov_b32 v28, 0x40c00000\n\t" \
    "s_mov_b32 s64, 0\n\t" \
    "s_mov_b32 s65, 0\n\t" \
    "s_lshl_b32 s37, s24, 11\n\t" \
    "s_add_u32 s37, s37, 49664\n\t" \
    "v_add_u32 v34, s37, v0\n\t" \
    "s_branch .Ltl_%=\n\t" \
    ".Lt3_%=:\n\t" \
    ".Le9_%=:\n\t" \
    "s_lshr_b32 s37, s24, 2\n\t" \
    "s_and_b32 s38, s24, 2\n\t" \
    "s_mul_i32 s39, s37, 24832\n\t" \
    "s_lshl4_add_u32 s39, s38, s39\n\t" \
    "v_lshrrev_b32 v4, 6, v0\n\t" \
    "v_and_b32 v5, 12, v4\n\t" \
    "v_add_u32 v6, v4, v5\n\t" \
    "v_lshrrev_b32 v7, 1, v0\n\t" \
    "v_and_b32 v7, 24, v7\n\t" \
    "v_and_b32 v4, 11, v4\n\t" \
    "v_mul_u32_u24 v4, 192, v4\n\t" \
    "v_bfe_u32 v5, v0, 8, 1\n\t" \
    "v_mul_u32_u24 v5, 3104, v5\n\t" \
    "v_add3_u32 v4, v4, v5, v7\n\t" \
    "v_add_u32 v8, s39, v4\n\t" \
    "v_mov_b32 v9, 156928\n\t" \
    "v_add_u32 v10, 149760, v0\n\t" \
    "s_lshl2_add_u32 s37, s37, s38\n\t" \
    "s_lshl_b32 s37, s37, 7\n\t" \
    "s_mul_i32 s53, s27, 28\n\t" \
    "s_add_u32 s53, s53, s26\n\t" \
    "s_andn2_b32 s39, s53, 7\n\t" \
    "s_add_u32 s53, s53, s39\n\t" \
    "s_lshl_b32 s53, s53, 10\n\t" \
    "s_add_u32 s37, s37, s53\n\t" \
    "s_and_b32 s38, s24, 1\n\t" \
    "s_lshl_b32 s39, s38, 13\n\t" \
    "s_add_u32 s37, s37, s39\n\t" \
    "s_cmp_lt_u32 s25, 252\n\t" \
    "s_cselect_b32 s43, 0, 0x80000000\n\t" \
    "s_add_u32 s37, s37, s43\n\t" \
    "v_lshrrev_b32 v11, 8, v0\n\t" \
    "v_and_b32 v12, 0xf0, v0\n\t" \
    "v_mul_u32_u24 v4, 8486912, v11\n\t" \
    "v_add3_u32 v13, v4, v12, s37\n\t" \
    "s_lshl_b32 s39, s38, 10\n\t" \
    "v_add_u32 v14, s39, v10\n\t" \
    "s_lshl_b32 s39, s38, 5\n\t" \
    "s_add_u32 s39, s39, 116736\n\t" \
    "v_lshl_add_u32 v4, v6, 6, v7\n\t" \
    "v_lshrrev_b32 v5, 3, v0\n\t" \
    "v_and_b32 v5, 32, v5\n\t" \
    "v_xor_b32 v5, s39, v5\n\t" \
    "v_add_u32 v15, v4, v5\n\t" \
    "s_and_b32 s37, s24, 6\n\t" \
    "s_lshl_b32 s37, s37, 10\n\t" \
    "v_add_u32 v16, s37, v0\n\t" \
    "s_waitcnt lgkmcnt(0)\n\t" \
    "s_barrier\n\t" \
    "ds_read_b128 v[20:23], v14 offset:5120\n\t" \
    "ds_read_b128 v[24:27], v16 offset:49664\n\t" \
    "ds_read_b128 v[28:31], v16 offset:50688\n\t" \
    "s_and_b32 s33, s2, 7\n\t" \
    "s_lshl_b32 s33, s33, 15\n\t" \
    "s_add_u32 s33, s33, 33554432\n\t" \
    "s_lshr_b32 s37, s29, 2\n\t" \
    "s_mul_i32 s39, s30, 3\n\t" \
    "s_add_u32 s37, s37, s39\n\t" \
    "s_mul_i32 s37, s37, 114688\n\t" \
    "s_and_b32 s39, s29, 3\n\t" \
    "s_lshl_b32 s39, s39, 8\n\t" \
    "s_add_u32 s37, s37, s39\n\t" \
    "s_add_u32 s37, s37, s43\n\t" \
    "s_add_u32 s37, s37, 2097152\n\t" \
    "s_and_b32 s39, s24, 6\n\t" \
    "s_lshl3_add_u32 s39, s28, s39\n\t" \
    "s_lshl_b32 s39, s39, 1\n\t" \
    "v_add_u32 v14, s39, v11\n\t" \
    "v_mul_u32_u24 v17, 147, v14\n\t" \
    "v_lshrrev_b32 v17, 10, v17\n\t" \
    "s_and_b32 s39, s24, 1\n\t" \
    "v_subrev_u32 v18, s39, v17\n\t" \
    "v_mul_i32_i24 v18, 7, v18\n\t" \
    "v_sub_u32 v14, v14, v18\n\t" \
    "v_and_b32 v18, 14, v14\n\t" \
    "v_add_lshl_u32 v14, v14, v18, 12\n\t" \
    "v_and_b32 v18, 7, v17\n\t" \
    "v_mul_u32_u24 v18, 4243456, v18\n\t" \
    "v_add3_u32 v14, v14, v18, v12\n\t" \
    "v_and_b32 v17, 8, v17\n\t" \
    "v_xor_b32 v17, 8, v17\n\t" \
    "v_lshl_add_u32 v14, v17, 10, v14\n\t" \
    "v_add_u32 v19, s37, v14\n\t" \
    "s_lshl_b32 s43, s41, 10\n\t" \
    "s_cmp_lt_u32 s30, 3\n\t" \
    "s_cselect_b64 s[20:21], -1, 0\n\t" \
    "s_and_b32 s37, s2, 0x70\n\t" \
    "s_lshl_b32 s37, s37, 1\n\t" \
    "s_lshl2_add_u32 s57, s24, s37\n\t" \
    "s_min_u32 s58, s57, 248\n\t" \
    "s_mul_i32 s59, s58, 147\n\t" \
    "s_lshr_b32 s59, s59, 12\n\t" \
    "s_mul_i32 s37, s59, 28\n\t" \
    "s_sub_u32 s60, s58, s37\n\t" \
    "s_andn2_b32 s39, s58, 7\n\t" \
    "s_add_u32 s51, s58, s39\n\t" \
    "s_lshl_b32 s51, s51, 10\n\t" \
    "s_sub_u32 s37, s60, 1\n\t" \
    "s_cselect_b32 s52, 27, -1\n\t" \
    "s_add_u32 s52, s52, s58\n\t" \
    "s_andn2_b32 s39, s52, 7\n\t" \
    "s_add_u32 s52, s52, s39\n\t" \
    "s_lshl_b32 s52, s52, 10\n\t" \
    "v_lshrrev_b32 v16, 9, v0\n\t" \
    "v_and_b32 v18, 0x2f0, v0\n\t" \
    "v_and_b32 v17, 1, v11\n\t" \
    "v_lshl_add_u32 v14, v17, 11, v18\n\t" \
    "v_add_u32 v32, s51, v14\n\t" \
    "s_sub_u32 s37, s51, s52\n\t" \
    "s_add_u32 s37, s37, 1024\n\t" \
    "v_mul_i32_i24 v17, s37, v17\n\t" \
    "v_add3_u32 v33, v17, v18, s52\n\t" \
    "s_mul_i32 s44, s24, 7296\n\t" \
    "s_lshl_b32 s37, s24, 2\n\t" \
    "s_lshl1_add_u32 s39, s24, 8\n\t" \
    "s_min_u32 s61, s37, s39\n\t" \
    "s_add_u32 s37, s37, 2\n\t" \
    "s_add_u32 s39, s24, 20\n\t" \
    "s_cmp_lt_u32 s24, 4\n\t" \
    "s_cselect_b32 s62, s37, s39\n\t" \
    "s_bfe_u32 s63, s2, 0x10003\n\t" \
    "s_mul_i32 s47, s63, 28\n\t" \
    "s_add_u32 s45, s47, s61\n\t" \
    "s_add_u32 s46, s47, s62\n\t" \
    "s_andn2_b32 s39, s45, 7\n\t" \
    "s_add_u32 s45, s45, s39\n\t" \
    "s_lshl_b32 s45, s45, 10\n\t" \
    "s_andn2_b32 s39, s46, 7\n\t" \
    "s_add_u32 s46, s46, s39\n\t" \
    "s_lshl_b32 s46, s46, 10\n\t" \
    "v_lshlrev_b32 v34, 4, v11\n\t" \
    "v_lshrrev_b32 v35, 6, v12\n\t" \
    "v_mul_u32_u24 v35, 7296, v35\n\t" \
    "v_and_b32 v14, 16, v0\n\t" \
    "v_mul_u32_u24 v14, 226, v14\n\t" \
    "v_and_b32 v18, 32, v0\n\t" \
    "v_lshl_add_u32 v14, v18, 1, v14\n\t" \
    "v_add3_u32 v35, v35, v14, v34\n\t" \
    "s_lshl_b32 s37, s61, 7\n\t" \
    "v_add_u32 v17, s37, v35\n\t" \
    "s_and_b32 s37, s62, 30\n\t" \
    "s_lshl_b32 s37, s37, 7\n\t" \
    "v_add_u32 v36, s37, v35\n\t" \
    "s_mov_b32 s1, 0\n\t" \
    "s_mov_b32 s41, 0x18000\n\t" \
    "s_lshl_b32 s37, s63, 9\n\t" \
    "s_add_u32 s51, s51, s37\n\t" \
    "s_cmp_lt_u32 s57, 252\n\t" \
    "s_cselect_b32 s51, s51, 0x80000000\n\t" \
    "s_and_b32 s37, s24, 3\n\t" \
    "s_mul_i32 s37, s37, 8486912\n\t" \
    "s_add_u32 s53, s53, s37\n\t" \
    "s_and_b32 s37, s24, 4\n\t" \
    "s_lshl_b32 s37, s37, 11\n\t" \
    "s_add_u32 s53, s53, s37\n\t" \
    "s_and_b32 s37, s2, 0x87\n\t" \
    "s_lshl4_add_u32 s37, s24, s37\n\t" \
    "s_lshl_b32 s55, s37, 7\n\t" \
    "v_and_b32 v37, 16, v0\n\t" \
    "v_lshlrev_b32 v38, 6, v37\n\t" \
    "s_lshl3_add_u32 s37, s26, s24\n\t" \
    "s_mul_i32 s37, s37, 43008\n\t" \
    "s_mul_i32 s39, s27, 4992\n\t" \
    "s_add_u32 s37, s37, s39\n\t" \
    "s_add_u32 s8, s8, s37\n\t" \
    "s_addc_u32 s9, s9, 0\n\t" \
    "s_cmp_eq_u32 s27, 8\n\t" \
    "s_movk_i32 s10, 4992\n\t" \
    "s_cselect_b32 s10, 3072, s10\n\t" \
    "s_mov_b32 s11, 0x20000\n\t" \
    "v_and_b32 v39, 0x70, v0\n\t" \
    "v_mad_u32_u24 v39, v39, 24, v34\n\t" \
    "v_lshrrev_b32 v18, 1, v0\n\t" \
    "v_and_or_b32 v14, v18, 64, v39\n\t" \
    "v_and_b32 v35, 0x1f0, v0\n\t" \
    "v_lshl_add_u32 v35, v16, 11, v35\n\t" \
    "v_add_u32 v40, s51, v35\n\t" \
    "s_mul_i32 s37, s24, 2064\n\t" \
    "v_and_b32 v35, 0x300, v0\n\t" \
    "v_add3_u32 v41, v35, v0, s37\n\t" \
    "s_lshl_b32 s37, s37, 1\n\t" \
    "v_lshlrev_b32 v35, 7, v16\n\t" \
    "v_xor_b32 v35, v35, v0\n\t" \
    "v_add_u32 v42, s37, v35\n\t" \
    "v_lshrrev_b32 v35, 5, v12\n\t" \
    "v_mul_u32_u24 v35, 2064, v35\n\t" \
    "v_lshl_add_u32 v35, v37, 2, v35\n\t" \
    "s_lshl_b32 s37, s24, 8\n\t" \
    "v_add3_u32 v43, v35, v34, s37\n\t" \
    "v_bfe_u32 v35, v0, 7, 1\n\t" \
    "v_lshl_add_u32 v35, v11, 2, v35\n\t" \
    "v_mul_u32_u24 v35, 2064, v35\n\t" \
    "v_lshrrev_b32 v39, 3, v0\n\t" \
    "v_and_or_b32 v35, v39, 8, v35\n\t" \
    "s_and_b32 s37, s24, 3\n\t" \
    "s_lshl_b32 s37, s37, 6\n\t" \
    "s_and_b32 s39, s24, 4\n\t" \
    "s_lshl_b32 s39, s39, 8\n\t" \
    "s_or_b32 s37, s37, s39\n\t" \
    "v_and_or_b32 v39, v0, 32, s37\n\t" \
    "v_mul_u32_u24 v37, 40, v37\n\t" \
    "v_xor_b32 v39, v39, v37\n\t" \
    "v_add_u32 v18, v35, v39\n\t" \
    "s_and_b32 s37, s2, 0x70\n\t" \
    "s_lshl_b32 s37, s37, 1\n\t" \
    "v_lshrrev_b32 v16, 7, v0\n\t" \
    "v_lshl_add_u32 v16, v16, 2, s37\n\t" \
    "v_min_u32 v16, 248, v16\n\t" \
    "v_mul_u32_u24 v12, 147, v16\n\t" \
    "v_lshrrev_b32 v12, 12, v12\n\t" \
    "v_mul_u32_u24 v34, 28, v12\n\t" \
    "v_sub_u32 v16, v16, v34\n\t" \
    "v_bfe_u32 v34, v0, 4, 3\n\t" \
    "v_min_u32 v34, 4, v34\n\t" \
    "v_add3_u32 v16, v16, v34, -1\n\t" \
    "v_add_u32 v34, 28, v16\n\t" \
    "v_min_u32 v16, v16, v34\n\t" \
    "v_mad_u32_u24 v16, v16, 9, v12\n\t" \
    "v_and_b32 v11, 31, v16\n\t" \
    "v_lshrrev_b32 v37, 5, v16\n\t" \
    "v_lshlrev_b32 v37, 7, v37\n\t" \
    "v_lshl_or_b32 v35, v11, 10, v37\n\t" \
    "ds_read_b128 v[44:47], v10 offset:0\n\t" \
    "ds_read_b128 v[48:51], v10 offset:1024\n\t" \
    "s_and_b32 s37, s24, 1\n\t" \
    "s_lshl_b32 s37, s37, 4\n\t" \
    "s_lshr_b32 s39, s24, 1\n\t" \
    "s_add_u32 s37, s37, s39\n\t" \
    "s_mul_i32 s37, s37, 192\n\t" \
    "s_add_u32 s37, s37, s31\n\t" \
    "s_lshl_b32 s39, s24, 12\n\t" \
    "s_add_u32 m0, s39, 116736\n\t" \
    "s_add_u32 s38, s37, 0\n\t" \
    "buffer_load_dwordx4 v1, s[12:15], s38 offen  lds\n\t" \
    "s_add_u32 m0, s39, 117760\n\t" \
    "s_add_u32 s38, s37, 258048\n\t" \
    "buffer_load_dwordx4 v1, s[12:15], s38 offen  lds\n\t" \
    "s_add_u32 m0, s39, 118784\n\t" \
    "s_add_u32 s38, s37, 516096\n\t" \
    "buffer_load_dwordx4 v1, s[12:15], s38 offen  lds\n\t" \
    "s_add_u32 m0, s39, 119808\n\t" \
    "s_add_u32 s38, s37, 774144\n\t" \
    "buffer_load_dwordx4 v1, s[12:15], s38 offen  lds\n\t" \
    "s_cmp_lg_u32 s24, 3\n\t" \
    "s_cbranch_scc1 .Lr9_%=\n\t" \
    "s_and_b32 s37, s2, 7\n\t" \
    "s_lshl_b32 s37, s37, 10\n\t" \
    "s_add_u32 s37, s37, 34643968\n\t" \
    "s_buffer_load_dword s56, s[4:7], s37 offset:0x0\n\t" \
    "s_buffer_load_dword s57, s[4:7], s37 offset:0x80\n\t" \
    "s_buffer_load_dword s58, s[4:7], s37 offset:0x100\n\t" \
    "s_buffer_load_dword s59, s[4:7], s37 offset:0x180\n\t" \
    "s_buffer_load_dword s60, s[4:7], s37 offset:0x200\n\t" \
    "s_buffer_load_dword s61, s[4:7], s37 offset:0x280\n\t" \
    "s_buffer_load_dword s62, s[4:7], s37 offset:0x300\n\t" \
    "s_buffer_load_dword s63, s[4:7], s37 offset:0x380\n\t" \
    "s_mov_b32 s40, 0\n\t" \
    ".Lr1_%=:\n\t" \
    "s_waitcnt lgkmcnt(0)\n\t" \
    "s_memrealtime s[22:23]\n\t" \
    "s_waitcnt lgkmcnt(0)\n\t" \
    "s_mov_b32 s37, 1000\n\t" \
    "s_mov_b32 s38, 0\n\t" \
    "s_sub_u32 s39, s22, s56\n\t" \
    "s_min_u32 s37, s37, s39\n\t" \
    "s_max_u32 s38, s38, s39\n\t" \
    "s_sub_u32 s39, s22, s57\n\t" \
    "s_min_u32 s37, s37, s39\n\t" \
    "s_max_u32 s38, s38, s39\n\t" \
    "s_sub_u32 s39, s22, s58\n\t" \
    "s_min_u32 s37, s37, s39\n\t" \
    "s_max_u32 s38, s38, s39\n\t" \
    "s_sub_u32 s39, s22, s59\n\t" \
    "s_min_u32 s37, s37, s39\n\t" \
    "s_max_u32 s38, s38, s39\n\t" \
    "s_sub_u32 s39, s22, s60\n\t" \
    "s_min_u32 s37, s37, s39\n\t" \
    "s_max_u32 s38, s38, s39\n\t" \
    "s_sub_u32 s39, s22, s61\n\t" \
    "s_min_u32 s37, s37, s39\n\t" \
    "s_max_u32 s38, s38, s39\n\t" \
    "s_sub_u32 s39, s22, s62\n\t" \
    "s_min_u32 s37, s37, s39\n\t" \
    "s_max_u32 s38, s38, s39\n\t" \
    "s_sub_u32 s39, s22, s63\n\t" \
    "s_min_u32 s37, s37, s39\n\t" \
    "s_max_u32 s38, s38, s39\n\t" \
    "s_cmp_gt_u32 s38, 1000\n\t" \
    "s_cbranch_scc1 .Lr2_%=\n\t" \
    ".Lr3_%=:\n\t" \
    "s_sub_u32 s35, s22, s37\n\t" \
    "s_waitcnt vmcnt(5)\n\t" \
    "v_readfirstlane_b32 s34, v3\n\t" \
    "s_mul_i32 s34, s34, 1664525\n\t" \
    "s_add_u32 s34, s34, 1013904223\n\t" \
    "s_or_b32 s34, s34, 1\n\t" \
    "v_mov_b32 v52, s34\n\t" \
    "v_mov_b32 v53, s35\n\t" \
    "ds_write_b64 v9, v[52:53] offset:32\n\t" \
    ".Lr9_%=:\n\t" \
    "s_cmp_eq_u32 s2, 255\n\t" \
    "s_cbranch_scc1 .Lpb_%=\n\t" \
    ".Lpe_%=:\n\t" \
    "s_waitcnt vmcnt(4)\n\t" \
    "s_waitcnt lgkmcnt(0)\n\t" \
    "s_barrier\n\t" \
    "ds_read_b64_tr_b16 v[60:61], v8 offset:0\n\t" \
    "ds_read_b64_tr_b16 v[62:63], v8 offset:768\n\t" \
    "ds_read_b64_tr_b16 v[64:65], v8 offset:12416\n\t" \
    "ds_read_b64_tr_b16 v[66:67], v8 offset:13184\n\t" \
    "ds_read_b64_tr_b16 v[68:69], v8 offset:6208\n\t" \
    "ds_read_b64_tr_b16 v[70:71], v8 offset:6976\n\t" \
    "ds_read_b64_tr_b16 v[72:73], v8 offset:18624\n\t" \
    "ds_read_b64_tr_b16 v[74:75], v8 offset:19392\n\t" \
    "ds_read_b128 v[76:79], v9 offset:32\n\t" \
    "s_waitcnt lgkmcnt(7)\n\t" \
    "v_mfma_f32_16x16x32_bf16 v[80:83], v[20:23], v[60:63], 0\n\t" \
    "s_waitcnt lgkmcnt(5)\n\t" \
    "v_mfma_f32_16x16x32_bf16 v[84:87], v[20:23], v[64:67], 0\n\t" \
    "s_waitcnt lgkmcnt(3)\n\t" \
    "v_mfma_f32_16x16x32_bf16 v[88:91], v[20:23], v[68:71], 0\n\t" \
    "s_waitcnt lgkmcnt(1)\n\t" \
    "v_mfma_f32_16x16x32_bf16 v[92:95], v[20:23], v[72:75], 0\n\t" \
    "ds_read_b64_tr_b16 v[96:97], v8 offset:64\n\t" \
    "ds_read_b64_tr_b16 v[98:99], v8 offset:832\n\t" \
    "ds_read_b64_tr_b16 v[100:101], v8 offset:12480\n\t" \
    "ds_read_b64_tr_b16 v[102:103], v8 offset:13248\n\t" \
    "ds_read_b64_tr_b16 v[104:105], v8 offset:6272\n\t" \
    "ds_read_b64_tr_b16 v[106:107], v8 offset:7040\n\t" \
    "ds_read_b64_tr_b16 v[108:109], v8 offset:18688\n\t" \
    "ds_read_b64_tr_b16 v[110:111], v8 offset:19456\n\t" \
    "s_waitcnt lgkmcnt(8)\n\t" \
    "v_readfirstlane_b32 s34, v76\n\t" \
    "v_readfirstlane_b32 s35, v77\n\t" \
    "s_lshl_b32 s37, s24, 8\n\t" \
    "s_add_u32 s37, s37, s2\n\t" \
    "s_cmp_eq_u32 s37, 255\n\t" \
    "s_cbranch_scc1 .Lpp_%=\n\t" \
    ".Lpq_%=:\n\t" \
    "v_add_u32 v80, 0x8000, v80\n\t" \
    "v_add_u32 v82, 0x8000, v82\n\t" \
    "v_add_u32 v84, 0x8000, v84\n\t" \
    "v_add_u32 v86, 0x8000, v86\n\t" \
    "v_cvt_pk_bf16_f32 v120, v80, v81\n\t" \
    "v_cvt_pk_bf16_f32 v122, v84, v85\n\t" \
    "v_cvt_pk_bf16_f32 v121, v88, v89\n\t" \
    "v_cvt_pk_bf16_f32 v123, v92, v93\n\t" \
    "v_and_b32 v120, -2, v120\n\t" \
    "v_and_b32 v122, -2, v122\n\t" \
    "v_cvt_pk_bf16_f32 v124, v82, v83\n\t" \
    "v_cvt_pk_bf16_f32 v126, v86, v87\n\t" \
    "v_cvt_pk_bf16_f32 v125, v90, v91\n\t" \
    "v_cvt_pk_bf16_f32 v127, v94, v95\n\t" \
    "v_and_b32 v124, -2, v124\n\t" \
    "v_and_b32 v126, -2, v126\n\t" \
    "ds_read_b64_tr_b16 v[128:129], v8 offset:128\n\t" \
    "ds_read_b64_tr_b16 v[130:131], v8 offset:896\n\t" \
    "ds_read_b64_tr_b16 v[132:133], v8 offset:12544\n\t" \
    "ds_read_b64_tr_b16 v[134:135], v8 offset:13312\n\t" \
    "ds_read_b64_tr_b16 v[136:137], v8 offset:6336\n\t" \
    "ds_read_b64_tr_b16 v[138:139], v8 offset:7104\n\t" \
    "ds_read_b64_tr_b16 v[140:141], v8 offset:18752\n\t" \
    "ds_read_b64_tr_b16 v[142:143], v8 offset:19520\n\t" \
    ".Lh25_%=:\n\t" \
    "s_memrealtime s[22:23]\n\t" \
    "s_waitcnt lgkmcnt(0)\n\t" \
    "s_sub_u32 s22, s22, s35\n\t" \
    "s_cmp_lt_u32 s22, 205\n\t" \
    "s_cbranch_scc1 .Lh25_%=\n\t" \
    "buffer_store_dwordx4 v[120:123], v13, s[4:7], 0 offen offset:0 sc1\n\t" \
    "s_mov_b32 s42, 4243456\n\t" \
    "buffer_store_dwordx4 v[124:127], v13, s[4:7], s42 offen offset:0 sc1\n\t" \
    "s_lshl_b32 s37, s24, 18\n\t" \
    "s_add_u32 s37, s37, s32\n\t" \
    "s_bitcmp1_b32 29, s24\n\t" \
    "s_cselect_b32 s39, 0, 0x80000000\n\t" \
    "v_add_u32 v12, s39, v2\n\t" \
    "v_mov_b32 v144, 0\n\t" \
    "v_mov_b32 v145, 0\n\t" \
    "v_mov_b32 v146, 0\n\t" \
    "v_mov_b32 v147, 0\n\t" \
    "s_cmp_eq_u32 s24, 4\n\t" \
    "s_cselect_b64 exec, 0xff, -1\n\t" \
    "buffer_store_dwordx4 v[144:147], v12, s[4:7], s37 offen sc1\n\t" \
    "s_mov_b64 exec, -1\n\t" \
    "v_mfma_f32_16x16x32_bf16 v[148:151], v[20:23], v[96:99], 0\n\t" \
    "v_mfma_f32_16x16x32_bf16 v[152:155], v[20:23], v[100:103], 0\n\t" \
    "v_mfma_f32_16x16x32_bf16 v[156:159], v[20:23], v[104:107], 0\n\t" \
    "v_mfma_f32_16x16x32_bf16 v[160:163], v[20:23], v[108:111], 0\n\t" \
    "v_mfma_f32_16x16x32_bf16 v[164:167], v[20:23], v[128:131], 0\n\t" \
    "v_mfma_f32_16x16x32_bf16 v[168:171], v[20:23], v[132:135], 0\n\t" \
    "v_mfma_f32_16x16x32_bf16 v[172:175], v[20:23], v[136:139], 0\n\t" \
    "v_mfma_f32_16x16x32_bf16 v[176:179], v[20:23], v[140:143], 0\n\t" \
    "s_nop 0\n\t" \
    "v_add_u32 v148, 0x8000, v148\n\t" \
    "v_add_u32 v150, 0x8000, v150\n\t" \
    "v_add_u32 v152, 0x8000, v152\n\t" \
    "v_add_u32 v154, 0x8000, v154\n\t" \
    "v_cvt_pk_bf16_f32 v180, v148, v149\n\t" \
    "v_cvt_pk_bf16_f32 v182, v152, v153\n\t" \
    "v_cvt_pk_bf16_f32 v181, v156, v157\n\t" \
    "v_cvt_pk_bf16_f32 v183, v160, v161\n\t" \
    "v_and_b32 v180, -2, v180\n\t" \
    "v_and_b32 v182, -2, v182\n\t" \
    "v_cvt_pk_bf16_f32 v184, v150, v151\n\t" \
    "v_cvt_pk_bf16_f32 v186, v154, v155\n\t" \
    "v_cvt_pk_bf16_f32 v185, v158, v159\n\t" \
    "v_cvt_pk_bf16_f32 v187, v162, v163\n\t" \
    "v_and_b32 v184, -2, v184\n\t" \
    "v_and_b32 v186, -2, v186\n\t" \
    "v_add_u32 v164, 0x8000, v164\n\t" \
    "v_add_u32 v166, 0x8000, v166\n\t" \
    "v_add_u32 v168, 0x8000, v168\n\t" \
    "v_add_u32 v170, 0x8000, v170\n\t" \
    "v_cvt_pk_bf16_f32 v188, v164, v165\n\t" \
    "v_cvt_pk_bf16_f32 v190, v168, v169\n\t" \
    "v_cvt_pk_bf16_f32 v189, v172, v173\n\t" \
    "v_cvt_pk_bf16_f32 v191, v176, v177\n\t" \
    "v_and_b32 v188, -2, v188\n\t" \
    "v_and_b32 v190, -2, v190\n\t" \
    "v_cvt_pk_bf16_f32 v4, v166, v167\n\t" \
    "v_cvt_pk_bf16_f32 v6, v170, v171\n\t" \
    "v_cvt_pk_bf16_f32 v5, v174, v175\n\t" \
    "v_cvt_pk_bf16_f32 v7, v178, v179\n\t" \
    "v_and_b32 v4, -2, v4\n\t" \
    "v_and_b32 v6, -2, v6\n\t" \
    "v_mov_b32 v52, s34\n\t" \
    "v_mov_b32 v53, s34\n\t" \
    "v_mov_b32 v54, s34\n\t" \
    "v_mov_b32 v55, s34\n\t" \
    "s_waitcnt vmcnt(3)\n\t" \
    "; wait\n\t" \
    "s_waitcnt lgkmcnt(0)\n\t" \
    "s_barrier\n\t" \
    "ds_read_b64_tr_b16 v[56:57], v15 offset:0\n\t" \
    "ds_read_b64_tr_b16 v[58:59], v15 offset:256\n\t" \
    "ds_read_b64_tr_b16 v[60:61], v15 offset:2048\n\t" \
    "ds_read_b64_tr_b16 v[62:63], v15 offset:2304\n\t" \
    "ds_read_b64_tr_b16 v[64:65], v15 offset:4096\n\t" \
    "ds_read_b64_tr_b16 v[66:67], v15 offset:4352\n\t" \
    "ds_read_b64_tr_b16 v[68:69], v15 offset:6144\n\t" \
    "ds_read_b64_tr_b16 v[70:71], v15 offset:6400\n\t" \
    "ds_read_b64_tr_b16 v[72:73], v15 offset:8192\n\t" \
    "ds_read_b64_tr_b16 v[74:75], v15 offset:8448\n\t" \
    "ds_read_b64_tr_b16 v[76:77], v15 offset:10240\n\t" \
    "ds_read_b64_tr_b16 v[78:79], v15 offset:10496\n\t" \
    "ds_read_b64_tr_b16 v[112:113], v15 offset:12288\n\t" \
    "ds_read_b64_tr_b16 v[114:115], v15 offset:12544\n\t" \
    "s_waitcnt lgkmcnt(12)\n\t" \
    "v_mfma_f32_16x16x32_bf16 v[116:119], v[24:27], v[56:59], 0\n\t" \
    "ds_read_b64_tr_b16 v[80:81], v15 offset:14336\n\t" \
    "ds_read_b64_tr_b16 v[82:83], v15 offset:14592\n\t" \
    "s_waitcnt lgkmcnt(12)\n\t" \
    "v_mfma_f32_16x16x32_bf16 v[116:119], v[28:31], v[60:63], v[116:119]\n\t" \
    "ds_read_b64_tr_b16 v[84:85], v15 offset:16384\n\t" \
    "ds_read_b64_tr_b16 v[86:87], v15 offset:16640\n\t" \
    "s_waitcnt lgkmcnt(12)\n\t" \
    "v_mfma_f32_16x16x32_bf16 v[88:91], v[24:27], v[64:67], 0\n\t" \
    "ds_read_b64_tr_b16 v[92:93], v15 offset:18432\n\t" \
    "ds_read_b64_tr_b16 v[94:95], v15 offset:18688\n\t" \
    "s_waitcnt lgkmcnt(12)\n\t" \
    "v_mfma_f32_16x16x32_bf16 v[88:91], v[28:31], v[68:71], v[88:91]\n\t" \
    "ds_read_b64_tr_b16 v[120:121], v15 offset:20480\n\t" \
    "ds_read_b64_tr_b16 v[122:123], v15 offset:20736\n\t" \
    "s_waitcnt lgkmcnt(12)\n\t" \
    "v_mfma_f32_16x16x32_bf16 v[124:127], v[24:27], v[72:75], 0\n\t" \
    "ds_read_b64_tr_b16 v[144:145], v15 offset:22528\n\t" \
    "ds_read_b64_tr_b16 v[146:147], v15 offset:22784\n\t" \
    "s_waitcnt lgkmcnt(12)\n\t" \
    "v_mfma_f32_16x16x32_bf16 v[124:127], v[28:31], v[76:79], v[124:127]\n\t" \
    "ds_read_b64_tr_b16 v[96:97], v15 offset:24576\n\t" \
    "ds_read_b64_tr_b16 v[98:99], v15 offset:24832\n\t" \
    "s_waitcnt lgkmcnt(12)\n\t" \
    "v_mfma_f32_16x16x32_bf16 v[100:103], v[24:27], v[112:115], 0\n\t" \
    "ds_read_b64_tr_b16 v[104:105], v15 offset:26624\n\t" \
    "ds_read_b64_tr_b16 v[106:107], v15 offset:26880\n\t" \
    "s_waitcnt lgkmcnt(12)\n\t" \
    "v_mfma_f32_16x16x32_bf16 v[100:103], v[28:31], v[80:83], v[100:103]\n\t" \
    "ds_read_b64_tr_b16 v[108:109], v15 offset:28672\n\t" \
    "ds_read_b64_tr_b16 v[110:111], v15 offset:28928\n\t" \
    ".Lh26_%=:\n\t" \
    "s_memrealtime s[22:23]\n\t" \
    "s_waitcnt lgkmcnt(0)\n\t" \
    "s_sub_u32 s22, s22, s35\n\t" \
    "s_cmp_lt_u32 s22, 255\n\t" \
    "s_cbranch_scc1 .Lh26_%=\n\t" \
    "s_mov_b32 s42, 524288\n\t" \
    "buffer_store_dwordx4 v[180:183], v13, s[4:7], s42 offen offset:0 sc1\n\t" \
    "s_mov_b32 s42, 4767744\n\t" \
    "buffer_store_dwordx4 v[184:187], v13, s[4:7], s42 offen offset:0 sc1\n\t" \
    "v_mfma_f32_16x16x32_bf16 v[128:131], v[24:27], v[84:87], 0\n\t" \
    "ds_read_b64_tr_b16 v[132:133], v15 offset:30720\n\t" \
    "ds_read_b64_tr_b16 v[134:135], v15 offset:30976\n\t" \
    "v_mfma_f32_16x16x32_bf16 v[128:131], v[28:31], v[92:95], v[128:131]\n\t" \
    "v_cvt_pk_bf16_f32 v136, v116, v88\n\t" \
    "v_cvt_pk_bf16_f32 v20, v117, v89\n\t" \
    "v_mfma_f32_16x16x32_bf16 v[140:143], v[24:27], v[120:123], 0\n\t" \
    "v_cvt_pk_bf16_f32 v148, v118, v90\n\t" \
    "v_cvt_pk_bf16_f32 v152, v119, v91\n\t" \
    "v_mfma_f32_16x16x32_bf16 v[140:143], v[28:31], v[144:147], v[140:143]\n\t" \
    "v_cvt_pk_bf16_f32 v137, v124, v100\n\t" \
    "v_cvt_pk_bf16_f32 v21, v125, v101\n\t" \
    "v_mfma_f32_16x16x32_bf16 v[156:159], v[24:27], v[96:99], 0\n\t" \
    "v_cvt_pk_bf16_f32 v149, v126, v102\n\t" \
    "v_cvt_pk_bf16_f32 v153, v127, v103\n\t" \
    "v_mfma_f32_16x16x32_bf16 v[156:159], v[28:31], v[104:107], v[156:159]\n\t" \
    "v_mfma_f32_16x16x32_bf16 v[160:163], v[24:27], v[108:111], 0\n\t" \
    "s_waitcnt lgkmcnt(0)\n\t" \
    "v_mfma_f32_16x16x32_bf16 v[160:163], v[28:31], v[132:135], v[160:163]\n\t" \
    "v_cvt_pk_bf16_f32 v138, v128, v140\n\t" \
    "v_cvt_pk_bf16_f32 v22, v129, v141\n\t" \
    "v_cvt_pk_bf16_f32 v150, v130, v142\n\t" \
    "v_cvt_pk_bf16_f32 v154, v131, v143\n\t" \
    "s_nop 3\n\t" \
    "v_cvt_pk_bf16_f32 v139, v156, v160\n\t" \
    "v_cvt_pk_bf16_f32 v23, v157, v161\n\t" \
    "v_cvt_pk_bf16_f32 v151, v158, v162\n\t" \
    "v_cvt_pk_bf16_f32 v155, v159, v163\n\t" \
    ".Lh27_%=:\n\t" \
    "s_memrealtime s[22:23]\n\t" \
    "s_waitcnt lgkmcnt(0)\n\t" \
    "s_sub_u32 s22, s22, s35\n\t" \
    "s_cmp_lt_u32 s22, 305\n\t" \
    "s_cbranch_scc1 .Lh27_%=\n\t" \
    "s_mov_b32 s42, 1048576\n\t" \
    "buffer_store_dwordx4 v[188:191], v13, s[4:7], s42 offen offset:0 sc1\n\t" \
    "s_mov_b32 s42, 5292032\n\t" \
    "buffer_store_dwordx4 v[4:7], v13, s[4:7], s42 offen offset:0 sc1\n\t" \
    ".Lh28_%=:\n\t" \
    "s_memrealtime s[22:23]\n\t" \
    "s_waitcnt lgkmcnt(0)\n\t" \
    "s_sub_u32 s22, s22, s35\n\t" \
    "s_cmp_lt_u32 s22, 370\n\t" \
    "s_cbranch_scc1 .Lh28_%=\n\t" \
    "s_cmp_eq_u32 s24, 0\n\t" \
    "s_cbranch_scc1 .Lj30_%=\n\t" \
    ".Lh29_%=:\n\t" \
    "s_memrealtime s[22:23]\n\t" \
    "s_waitcnt lgkmcnt(0)\n\t" \
    "s_sub_u32 s22, s22, s35\n\t" \
    "s_cmp_lt_u32 s22, 385\n\t" \
    "s_cbranch_scc1 .Lh29_%=\n\t" \
    ".Lj30_%=:\n\t" \
    "s_waitcnt lgkmcnt(0)\n\t" \
    "s_xor_b32 s42, s43, 0\n\t" \
    "buffer_store_dwordx4 v[136:139], v19, s[4:7], s42 offen offset:0 sc1\n\t" \
    "s_waitcnt vmcnt(1)\n\t" \
    "s_mov_b64 exec, 1\n\t" \
    "ds_add_rtn_u32 v16, v9, v9 offset:8\n\t" \
    "s_add_u32 s37, s32, 524288\n\t" \
    "s_waitcnt lgkmcnt(0)\n\t" \
    "v_cmp_eq_u32 vcc, 1098496, v16\n\t" \
    "s_mov_b64 exec, -1\n\t" \
    "s_nop 0\n\t" \
    "s_cbranch_vccz .La13_%=\n\t" \
    "buffer_store_dwordx4 v[52:55], v2, s[4:7], s37 offen sc1\n\t" \
    ".La13_%=:\n\t" \
    "s_mov_b64 exec, -1\n\t" \
    "s_xor_b32 s42, s43, 1024\n\t" \
    "buffer_store_dwordx4 v[20:23], v19, s[4:7], s42 offen offset:0 sc1\n\t" \
    "s_cmp_eq_u32 s24, 0\n\t" \
    "s_cbranch_scc1 .Lj32_%=\n\t" \
    ".Lh31_%=:\n\t" \
    "s_memrealtime s[22:23]\n\t" \
    "s_waitcnt lgkmcnt(0)\n\t" \
    "s_sub_u32 s22, s22, s35\n\t" \
    "s_cmp_lt_u32 s22, 435\n\t" \
    "s_cbranch_scc1 .Lh31_%=\n\t" \
    ".Lj32_%=:\n\t" \
    "s_waitcnt lgkmcnt(0)\n\t" \
    "s_xor_b32 s42, s43, 2048\n\t" \
    "buffer_store_dwordx4 v[148:151], v19, s[4:7], s42 offen offset:0 sc1\n\t" \
    "s_xor_b32 s42, s43, 3072\n\t" \
    "buffer_store_dwordx4 v[152:155], v19, s[4:7], s42 offen offset:0 sc1\n\t" \
    ".Lh33_%=:\n\t" \
    "s_memrealtime s[22:23]\n\t" \
    "s_waitcnt lgkmcnt(0)\n\t" \
    "s_sub_u32 s22, s22, s35\n\t" \
    "s_cmp_lt_u32 s22, 390\n\t" \
    "s_cbranch_scc1 .Lh33_%=\n\t" \
    "s_and_b32 s52, s2, 7\n\t" \
    "s_lshl_b32 s52, s52, 7\n\t" \
    "s_add_u32 s52, s52, 34636800\n\t" \
    "s_buffer_load_dword s36, s[4:7], s52\n\t" \
    "s_mov_b32 s40, 0\n\t" \
    "s_waitcnt lgkmcnt(0)\n\t" \
    ".Lr4_%=:\n\t" \
    "s_xor_b32 s36, s36, s34\n\t" \
    "s_andn2_b32 s37, s36, 14\n\t" \
    "s_cbranch_scc1 .Lr5_%=\n\t" \
    ".Lr6_%=:\n\t" \
    "s_bfe_u32 s36, s36, 0x30001\n\t" \
    "s_add_u32 s56, s2, s36\n\t" \
    "s_and_b32 s56, s56, 7\n\t" \
    "s_lshr_b32 s37, s2, 7\n\t" \
    "s_lshl1_add_u32 s56, s56, s37\n\t" \
    "s_and_b32 s37, s56, 7\n\t" \
    "s_mul_i32 s48, s37, 4243456\n\t" \
    "s_and_b32 s37, s56, 8\n\t" \
    "s_lshl_b32 s37, s37, 10\n\t" \
    "s_add_u32 s48, s48, s37\n\t" \
    "s_add_u32 s49, s48, 524288\n\t" \
    "s_add_u32 s50, s48, 1048576\n\t" \
    "s_xor_b32 s37, s48, 0x2000\n\t" \
    "s_add_u32 s37, s37, 2097152\n\t" \
    "s_add_u32 s45, s45, s37\n\t" \
    "s_add_u32 s46, s46, s37\n\t" \
    "s_cmp_lt_u32 s24, 4\n\t" \
    "s_cselect_b32 s47, s46, 0x80000000\n\t" \
    "v_add_u32 v1, s45, v0\n\t" \
    "v_add_u32 v3, s46, v0\n\t" \
    "v_add_u32 v39, s47, v0\n\t" \
    "s_lshr_b32 s37, s24, 2\n\t" \
    "s_mul_i32 s37, s37, 6\n\t" \
    "s_mul_i32 s39, s56, 7\n\t" \
    "s_add_u32 s37, s37, s39\n\t" \
    "s_lshr_b32 s37, s37, 4\n\t" \
    "s_and_b32 s39, s24, 3\n\t" \
    "s_mul_i32 s39, s39, 63\n\t" \
    "s_add_u32 s37, s37, s39\n\t" \
    "v_lshrrev_b32 v34, 4, v0\n\t" \
    "v_min_u32 v34, 8, v34\n\t" \
    "v_mad_u32_u24 v34, v34, 7, s37\n\t" \
    "v_and_b32 v8, 31, v34\n\t" \
    "v_lshrrev_b32 v12, 5, v34\n\t" \
    "v_lshlrev_b32 v12, 7, v12\n\t" \
    "v_lshl_or_b32 v15, v8, 10, v12\n\t" \
    "s_sub_u32 s37, s24, s36\n\t" \
    "s_and_b32 s37, s37, 7\n\t" \
    "s_mul_i32 s39, s27, 28\n\t" \
    "s_add_u32 s39, s39, s26\n\t" \
    "s_lshr_b32 s39, s39, 5\n\t" \
    "s_lshl4_add_u32 s37, s39, s37\n\t" \
    "s_lshl_b32 s52, s37, 7\n\t" \
    "s_nop 0\n\t" \
    "v_readlane_b32 s12, v15, 0\n\t" \
    "v_readlane_b32 s13, v15, 1\n\t" \
    "v_readlane_b32 s14, v15, 2\n\t" \
    "v_readlane_b32 s15, v15, 3\n\t" \
    "v_readlane_b32 s16, v15, 4\n\t" \
    "v_readlane_b32 s17, v15, 5\n\t" \
    "v_readlane_b32 s18, v15, 6\n\t" \
    "v_readlane_b32 s19, v15, 7\n\t" \
    "v_readlane_b32 s3, v15, 8\n\t" \
    "s_add_u32 s12, s12, s33\n\t" \
    "s_add_u32 s13, s13, s33\n\t" \
    "s_add_u32 s14, s14, s33\n\t" \
    "s_add_u32 s15, s15, s33\n\t" \
    "s_add_u32 s16, s16, s33\n\t" \
    "s_add_u32 s17, s17, s33\n\t" \
    "s_add_u32 s18, s18, s33\n\t" \
    "s_add_u32 s19, s19, s33\n\t" \
    "s_add_u32 s3, s3, s33\n\t" \
    "s_waitcnt vmcnt(0)\n\t" \
    "s_mov_b64 exec, 1\n\t" \
    "ds_add_rtn_u32 v13, v9, v9 offset:12\n\t" \
    "s_add_u32 s37, s32, 786432\n\t" \
    "s_waitcnt lgkmcnt(0)\n\t" \
    "v_cmp_eq_u32 vcc, 1098496, v13\n\t" \
    "s_mov_b64 exec, -1\n\t" \
    "s_nop 0\n\t" \
    "s_cbranch_vccz .La18_%=\n\t" \
    "buffer_store_dwordx4 v[52:55], v2, s[4:7], s37 offen sc1\n\t" \
    ".La18_%=:\n\t" \
    "s_mov_b64 exec, -1\n\t" \
    ".Lh34_%=:\n\t" \
    "s_memrealtime s[22:23]\n\t" \
    "s_waitcnt lgkmcnt(0)\n\t" \
    "s_sub_u32 s22, s22, s35\n\t" \
    "s_cmp_lt_u32 s22, 472\n\t" \
    "s_cbranch_scc1 .Lh34_%=\n\t" \
    "s_cmp_lg_u32 s24, 0\n\t" \
    "s_cbranch_scc1 .Lb15_%=\n\t" \
    "s_mov_b32 s40, 0\n\t" \
    "s_add_u32 s38, s33, 524288\n\t" \
    ".Ll15_%=:\n\t" \
    "buffer_load_dword v16, v35, s[4:7], s38 offen sc1\n\t" \
    "s_waitcnt vmcnt(0)\n\t" \
    "v_cmp_ne_u32 vcc, s34, v16\n\t" \
    "s_add_u32 s40, s40, 1\n\t" \
    "s_cmp_ge_u32 s40, 8192\n\t" \
    "s_cbranch_scc1 .Lb15_%=\n\t" \
    "s_cbranch_vccz .Lb15_%=\n\t" \
    "s_sleep 2\n\t" \
    "s_branch .Ll15_%=\n\t" \
    ".Lb15_%=:\n\t" \
    "s_waitcnt lgkmcnt(0)\n\t" \
    "s_barrier\n\t" \
    "buffer_load_dwordx4 v[164:167], v33, s[4:7], s48 offen offset:0 sc1\n\t" \
    "buffer_load_dwordx4 v[168:171], v32, s[4:7], s48 offen offset:0 sc1\n\t" \
    "buffer_load_dwordx4 v[172:175], v33, s[4:7], s48 offen offset:256 sc1\n\t" \
    "buffer_load_dwordx4 v[176:179], v32, s[4:7], s48 offen offset:256 sc1\n\t" \
    "buffer_load_dwordx4 v[56:59], v32, s[4:7], s48 offen offset:1024 sc1\n\t" \
    "buffer_load_dwordx4 v[60:63], v32, s[4:7], s48 offen offset:1280 sc1\n\t" \
    "s_waitcnt vmcnt(5)\n\t" \
    "v_mfma_f32_32x32x16_bf16 v[192:207], v[44:47], v[164:167], 0\n\t" \
    "buffer_load_dwordx4 v[64:67], v33, s[4:7], s49 offen offset:0 sc1\n\t" \
    "s_waitcnt vmcnt(5)\n\t" \
    "v_mfma_f32_32x32x16_bf16 v[192:207], v[48:51], v[168:171], v[192:207]\n\t" \
    "buffer_load_dwordx4 v[68:71], v32, s[4:7], s49 offen offset:0 sc1\n\t" \
    "s_waitcnt vmcnt(5)\n\t" \
    "v_mfma_f32_32x32x16_bf16 v[208:223], v[44:47], v[172:175], 0\n\t" \
    "buffer_load_dwordx4 v[72:75], v33, s[4:7], s49 offen offset:256 sc1\n\t" \
    "s_waitcnt vmcnt(5)\n\t" \
    "v_mfma_f32_32x32x16_bf16 v[208:223], v[48:51], v[176:179], v[208:223]\n\t" \
    "buffer_load_dwordx4 v[76:79], v32, s[4:7], s49 offen offset:256 sc1\n\t" \
    "v_mfma_f32_32x32x16_bf16 v[224:239], v[44:47], v[168:171], 0\n\t" \
    "buffer_load_dwordx4 v[112:115], v32, s[4:7], s49 offen offset:1024 sc1\n\t" \
    "s_waitcnt vmcnt(6)\n\t" \
    "v_mfma_f32_32x32x16_bf16 v[224:239], v[48:51], v[56:59], v[224:239]\n\t" \
    "v_mfma_f32_32x32x16_bf16 v[240:255], v[44:47], v[176:179], 0\n\t" \
    "buffer_load_dwordx4 v[80:83], v32, s[4:7], s49 offen offset:1280 sc1\n\t" \
    "s_waitcnt vmcnt(6)\n\t" \
    "v_mfma_f32_32x32x16_bf16 v[240:255], v[48:51], v[60:63], v[240:255]\n\t" \
    "s_add_u32 m0, s44, 0\n\t" \
    "s_nop 1\n\t" \
    "v_cvt_pk_bf16_f32 v19, v192, v208\n\t" \
    "v_cvt_pk_bf16_f32 v11, v193, v209\n\t" \
    "v_cvt_pk_bf16_f32 v37, v194, v210\n\t" \
    "ds_write_addtid_b32 v19 offset:0\n\t" \
    "v_cvt_pk_bf16_f32 v34, v195, v211\n\t" \
    "ds_write_addtid_b32 v11 offset:256\n\t" \
    "v_cvt_pk_bf16_f32 v8, v196, v212\n\t" \
    "ds_write_addtid_b32 v37 offset:512\n\t" \
    "v_cvt_pk_bf16_f32 v12, v197, v213\n\t" \
    "ds_write_addtid_b32 v34 offset:768\n\t" \
    "v_cvt_pk_bf16_f32 v9, v198, v214\n\t" \
    "ds_write_addtid_b32 v8 offset:1024\n\t" \
    "v_cvt_pk_bf16_f32 v13, v199, v215\n\t" \
    "ds_write_addtid_b32 v12 offset:1280\n\t" \
    "v_cvt_pk_bf16_f32 v35, v200, v216\n\t" \
    "ds_write_addtid_b32 v9 offset:1536\n\t" \
    "v_cvt_pk_bf16_f32 v16, v201, v217\n\t" \
    "ds_write_addtid_b32 v13 offset:1792\n\t" \
    "v_cvt_pk_bf16_f32 v19, v202, v218\n\t" \
    "ds_write_addtid_b32 v35 offset:2048\n\t" \
    "v_cvt_pk_bf16_f32 v11, v203, v219\n\t" \
    "ds_write_addtid_b32 v16 offset:2304\n\t" \
    "v_cvt_pk_bf16_f32 v37, v204, v220\n\t" \
    "ds_write_addtid_b32 v19 offset:2560\n\t" \
    "v_cvt_pk_bf16_f32 v34, v205, v221\n\t" \
    "ds_write_addtid_b32 v11 offset:2816\n\t" \
    "ds_write_addtid_b32 v37 offset:3072\n\t" \
    "ds_write_addtid_b32 v34 offset:3328\n\t" \
    ".Lh35_%=:\n\t" \
    "s_memrealtime s[22:23]\n\t" \
    "s_waitcnt lgkmcnt(0)\n\t" \
    "s_sub_u32 s22, s22, s35\n\t" \
    "s_cmp_lt_u32 s22, 605\n\t" \
    "s_cbranch_scc1 .Lh35_%=\n\t" \
    "s_buffer_load_dword s12, s[4:7], s12 offset:0xc0000\n\t" \
    "s_buffer_load_dword s13, s[4:7], s13 offset:0xc0000\n\t" \
    "s_buffer_load_dword s14, s[4:7], s14 offset:0xc0000\n\t" \
    "s_buffer_load_dword s15, s[4:7], s15 offset:0xc0000\n\t" \
    "s_buffer_load_dword s16, s[4:7], s16 offset:0xc0000\n\t" \
    "s_buffer_load_dword s17, s[4:7], s17 offset:0xc0000\n\t" \
    "s_buffer_load_dword s18, s[4:7], s18 offset:0xc0000\n\t" \
    "s_buffer_load_dword s19, s[4:7], s19 offset:0xc0000\n\t" \
    "s_buffer_load_dword s3, s[4:7], s3 offset:0xc0000\n\t" \
    "s_waitcnt vmcnt(5)\n\t" \
    "v_mfma_f32_32x32x16_bf16 v[192:207], v[44:47], v[64:67], 0\n\t" \
    "v_cvt_pk_bf16_f32 v8, v224, v240\n\t" \
    "v_cvt_pk_bf16_f32 v12, v225, v241\n\t" \
    "v_cvt_pk_bf16_f32 v9, v226, v242\n\t" \
    "ds_write_addtid_b32 v8 offset:3616\n\t" \
    "buffer_load_dwordx4 v[180:183], v33, s[4:7], s50 offen offset:0 sc1\n\t" \
    "v_cvt_pk_bf16_f32 v13, v227, v243\n\t" \
    "ds_write_addtid_b32 v12 offset:3872\n\t" \
    "v_cvt_pk_bf16_f32 v35, v228, v244\n\t" \
    "s_waitcnt vmcnt(5)\n\t" \
    "v_mfma_f32_32x32x16_bf16 v[192:207], v[48:51], v[68:71], v[192:207]\n\t" \
    "ds_write_addtid_b32 v9 offset:4128\n\t" \
    "v_cvt_pk_bf16_f32 v16, v229, v245\n\t" \
    "ds_write_addtid_b32 v13 offset:4384\n\t" \
    "v_cvt_pk_bf16_f32 v19, v230, v246\n\t" \
    "buffer_load_dwordx4 v[184:187], v32, s[4:7], s50 offen offset:0 sc1\n\t" \
    "ds_write_addtid_b32 v35 offset:4640\n\t" \
    "v_cvt_pk_bf16_f32 v11, v231, v247\n\t" \
    "ds_write_addtid_b32 v16 offset:4896\n\t" \
    "s_waitcnt vmcnt(5)\n\t" \
    "v_mfma_f32_32x32x16_bf16 v[208:223], v[44:47], v[72:75], 0\n\t" \
    "v_cvt_pk_bf16_f32 v37, v232, v248\n\t" \
    "ds_write_addtid_b32 v19 offset:5152\n\t" \
    "v_cvt_pk_bf16_f32 v34, v233, v249\n\t" \
    "ds_write_addtid_b32 v11 offset:5408\n\t" \
    "buffer_load_dwordx4 v[84:87], v33, s[4:7], s50 offen offset:256 sc1\n\t" \
    "v_cvt_pk_bf16_f32 v8, v234, v250\n\t" \
    "ds_write_addtid_b32 v37 offset:5664\n\t" \
    "v_cvt_pk_bf16_f32 v12, v235, v251\n\t" \
    "s_waitcnt vmcnt(5)\n\t" \
    "v_mfma_f32_32x32x16_bf16 v[208:223], v[48:51], v[76:79], v[208:223]\n\t" \
    "ds_write_addtid_b32 v34 offset:5920\n\t" \
    "v_cvt_pk_bf16_f32 v9, v236, v252\n\t" \
    "ds_write_addtid_b32 v8 offset:6176\n\t" \
    "v_cvt_pk_bf16_f32 v13, v237, v253\n\t" \
    "buffer_load_dwordx4 v[92:95], v32, s[4:7], s50 offen offset:256 sc1\n\t" \
    "ds_write_addtid_b32 v12 offset:6432\n\t" \
    "ds_write_addtid_b32 v9 offset:6688\n\t" \
    "ds_write_addtid_b32 v13 offset:6944\n\t" \
    "s_waitcnt lgkmcnt(0)\n\t" \
    "s_xor_b32 s12, s12, s34\n\t" \
    "s_xor_b32 s13, s13, s34\n\t" \
    "s_xor_b32 s14, s14, s34\n\t" \
    "s_xor_b32 s15, s15, s34\n\t" \
    "s_xor_b32 s16, s16, s34\n\t" \
    "s_xor_b32 s17, s17, s34\n\t" \
    "s_xor_b32 s18, s18, s34\n\t" \
    "s_xor_b32 s19, s19, s34\n\t" \
    "s_xor_b32 s3, s3, s34\n\t" \
    "s_or_b32 s12, s12, s13\n\t" \
    "s_or_b32 s12, s12, s14\n\t" \
    "s_or_b32 s12, s12, s15\n\t" \
    "s_or_b32 s12, s12, s16\n\t" \
    "s_or_b32 s12, s12, s17\n\t" \
    "s_or_b32 s12, s12, s18\n\t" \
    "s_or_b32 s12, s12, s19\n\t" \
    "s_or_b32 s12, s12, s3\n\t" \
    "s_cbranch_scc1 .Lq1021_%=\n\t" \
    ".Lb20_%=:\n\t" \
    "s_waitcnt lgkmcnt(0)\n\t" \
    "s_barrier\n\t" \
    "ds_read_b128 v[120:123], v17 offset:0\n\t" \
    "ds_read_b128 v[88:91], v17 offset:128\n\t" \
    "ds_read_b128 v[116:119], v17 offset:29184\n\t" \
    "ds_read_b128 v[144:147], v17 offset:29312\n\t" \
    "ds_read_b128 v[96:99], v36 offset:0\n\t" \
    "ds_read_b128 v[100:103], v36 offset:128\n\t" \
    "ds_read_b128 v[124:127], v36 offset:29184\n\t" \
    "ds_read_b128 v[104:107], v36 offset:29312\n\t" \
    "v_mfma_f32_32x32x16_bf16 v[224:239], v[44:47], v[68:71], 0\n\t" \
    "v_cvt_pk_bf16_f32 v16, v192, v208\n\t" \
    "v_cvt_pk_bf16_f32 v19, v193, v209\n\t" \
    "v_cvt_pk_bf16_f32 v11, v194, v210\n\t" \
    "ds_write_addtid_b32 v16 offset:58368\n\t" \
    "v_cvt_pk_bf16_f32 v33, v195, v211\n\t" \
    "s_mov_b32 s45, 0\n\t" \
    "buffer_load_dwordx4 v[24:27], v1, s[4:7], s45 offen offset:0 sc1\n\t" \
    "ds_write_addtid_b32 v19 offset:58624\n\t" \
    "v_cvt_pk_bf16_f32 v37, v196, v212\n\t" \
    "s_waitcnt vmcnt(6)\n\t" \
    "v_mfma_f32_32x32x16_bf16 v[224:239], v[48:51], v[112:115], v[224:239]\n\t" \
    "buffer_load_dwordx4 v[108:111], v32, s[4:7], s50 offen offset:1024 sc1\n\t" \
    "ds_write_addtid_b32 v11 offset:58880\n\t" \
    "v_cvt_pk_bf16_f32 v34, v197, v213\n\t" \
    "ds_write_addtid_b32 v33 offset:59136\n\t" \
    "v_cvt_pk_bf16_f32 v8, v198, v214\n\t" \
    "ds_write_addtid_b32 v37 offset:59392\n\t" \
    "v_cvt_pk_bf16_f32 v12, v199, v215\n\t" \
    "ds_write_addtid_b32 v34 offset:59648\n\t" \
    "v_mfma_f32_32x32x16_bf16 v[240:255], v[44:47], v[76:79], 0\n\t" \
    "buffer_load_dwordx4 v[28:31], v1, s[4:7], s45 offen offset:1024 sc1\n\t" \
    "v_cvt_pk_bf16_f32 v9, v200, v216\n\t" \
    "ds_write_addtid_b32 v8 offset:59904\n\t" \
    "v_cvt_pk_bf16_f32 v13, v201, v217\n\t" \
    "ds_write_addtid_b32 v12 offset:60160\n\t" \
    "v_cvt_pk_bf16_f32 v15, v202, v218\n\t" \
    "ds_write_addtid_b32 v9 offset:60416\n\t" \
    "v_cvt_pk_bf16_f32 v35, v203, v219\n\t" \
    "s_waitcnt vmcnt(7)\n\t" \
    "v_mfma_f32_32x32x16_bf16 v[240:255], v[48:51], v[80:83], v[240:255]\n\t" \
    "buffer_load_dwordx4 v[132:135], v32, s[4:7], s50 offen offset:1280 sc1\n\t" \
    "ds_write_addtid_b32 v13 offset:60672\n\t" \
    "v_cvt_pk_bf16_f32 v16, v204, v220\n\t" \
    "ds_write_addtid_b32 v15 offset:60928\n\t" \
    "s_mov_b32 s46, 114688\n\t" \
    "buffer_load_dwordx4 v[128:131], v1, s[4:7], s46 offen offset:0 sc1\n\t" \
    "v_cvt_pk_bf16_f32 v19, v205, v221\n\t" \
    "ds_write_addtid_b32 v35 offset:61184\n\t" \
    "ds_write_addtid_b32 v16 offset:61440\n\t" \
    "ds_write_addtid_b32 v19 offset:61696\n\t" \
    "s_waitcnt vmcnt(8)\n\t" \
    "v_mfma_f32_32x32x16_bf16 v[192:207], v[44:47], v[180:183], 0\n\t" \
    "v_cvt_pk_bf16_f32 v11, v224, v240\n\t" \
    "v_cvt_pk_bf16_f32 v37, v225, v241\n\t" \
    "v_cvt_pk_bf16_f32 v8, v226, v242\n\t" \
    "ds_write_addtid_b32 v11 offset:61984\n\t" \
    "buffer_load_dwordx4 v[140:143], v1, s[4:7], s46 offen offset:1024 sc1\n\t" \
    "v_cvt_pk_bf16_f32 v12, v227, v243\n\t" \
    "ds_write_addtid_b32 v37 offset:62240\n\t" \
    "v_cvt_pk_bf16_f32 v9, v228, v244\n\t" \
    "s_waitcnt vmcnt(8)\n\t" \
    "v_mfma_f32_32x32x16_bf16 v[192:207], v[48:51], v[184:187], v[192:207]\n\t" \
    "ds_write_addtid_b32 v8 offset:62496\n\t" \
    "v_cvt_pk_bf16_f32 v13, v229, v245\n\t" \
    "ds_write_addtid_b32 v12 offset:62752\n\t" \
    "v_cvt_pk_bf16_f32 v15, v230, v246\n\t" \
    "buffer_load_dwordx4 v[156:159], v3, s[4:7], s45 offen offset:0 sc1\n\t" \
    "ds_write_addtid_b32 v9 offset:63008\n\t" \
    "v_cvt_pk_bf16_f32 v16, v231, v247\n\t" \
    "ds_write_addtid_b32 v13 offset:63264\n\t" \
    "s_waitcnt vmcnt(8)\n\t" \
    "v_mfma_f32_32x32x16_bf16 v[208:223], v[44:47], v[84:87], 0\n\t" \
    "v_cvt_pk_bf16_f32 v19, v232, v248\n\t" \
    "ds_write_addtid_b32 v15 offset:63520\n\t" \
    "v_cvt_pk_bf16_f32 v11, v233, v249\n\t" \
    "ds_write_addtid_b32 v16 offset:63776\n\t" \
    "buffer_load_dwordx4 v[160:163], v3, s[4:7], s46 offen offset:0 sc1\n\t" \
    "v_cvt_pk_bf16_f32 v37, v234, v250\n\t" \
    "ds_write_addtid_b32 v19 offset:64032\n\t" \
    "v_cvt_pk_bf16_f32 v8, v235, v251\n\t" \
    "s_waitcnt vmcnt(8)\n\t" \
    "v_mfma_f32_32x32x16_bf16 v[208:223], v[48:51], v[92:95], v[208:223]\n\t" \
    "ds_write_addtid_b32 v11 offset:64288\n\t" \
    "v_cvt_pk_bf16_f32 v12, v236, v252\n\t" \
    "ds_write_addtid_b32 v37 offset:64544\n\t" \
    "v_cvt_pk_bf16_f32 v9, v237, v253\n\t" \
    "s_mov_b32 s47, 229376\n\t" \
    "buffer_load_dwordx4 v[188:191], v1, s[4:7], s47 offen offset:0 sc1\n\t" \
    "ds_write_addtid_b32 v8 offset:64800\n\t" \
    "ds_write_addtid_b32 v12 offset:65056\n\t" \
    "ds_write_addtid_b32 v9 offset:65312\n\t" \
    "s_waitcnt vmcnt(5)\n\t" \
    "; wait\n\t" \
    "s_waitcnt lgkmcnt(0)\n\t" \
    "s_barrier\n\t" \
    "s_memrealtime s[64:65]\n\t" \
    "s_cmp_lg_u32 s24, 7\n\t" \
    "s_cbranch_scc1 .Lar_%=\n\t" \
    "s_waitcnt lgkmcnt(0)\n\t" \
    "v_mov_b32 v53, s64\n\t" \
    "s_add_u32 s37, s32, 1048576\n\t" \
    "s_mov_b64 exec, 0xff\n\t" \
    "buffer_store_dwordx4 v[52:55], v0, s[4:7], s37 offen sc1\n\t" \
    "s_mov_b64 exec, -1\n\t" \
    ".Lar_%=:\n\t" \
    "v_add_u32 v17, 58368, v17\n\t" \
    "v_add_u32 v36, 58368, v36\n\t" \
    "ds_read_b128 v[4:7], v17 offset:0\n\t" \
    "ds_read_b128 v[136:139], v17 offset:128\n\t" \
    "ds_read_b128 v[20:23], v17 offset:29184\n\t" \
    "ds_read_b128 v[148:151], v17 offset:29312\n\t" \
    "ds_read_b128 v[152:155], v36 offset:0\n\t" \
    "ds_read_b128 v[164:167], v36 offset:128\n\t" \
    "ds_read_b128 v[172:175], v36 offset:29184\n\t" \
    "ds_read_b128 v[168:171], v36 offset:29312\n\t" \
    "v_mfma_f32_32x32x16_bf16 v[224:239], v[44:47], v[184:187], 0\n\t" \
    "v_cvt_pk_bf16_f32 v13, v192, v208\n\t" \
    "v_cvt_pk_bf16_f32 v15, v193, v209\n\t" \
    "v_mfma_f32_16x16x32_bf16 v[56:59], v[24:27], v[120:123], 0\n\t" \
    "v_cvt_pk_bf16_f32 v16, v194, v210\n\t" \
    "ds_write_addtid_b32 v13 offset:0\n\t" \
    "buffer_load_dwordx4 v[176:179], v1, s[4:7], s47 offen offset:1024 sc1\n\t" \
    "v_cvt_pk_bf16_f32 v19, v195, v211\n\t" \
    "ds_write_addtid_b32 v15 offset:256\n\t" \
    "v_mfma_f32_16x16x32_bf16 v[60:63], v[24:27], v[88:91], 0\n\t" \
    "v_cvt_pk_bf16_f32 v11, v196, v212\n\t" \
    "v_mfma_f32_32x32x16_bf16 v[224:239], v[48:51], v[108:111], v[224:239]\n\t" \
    "ds_write_addtid_b32 v16 offset:512\n\t" \
    "v_cvt_pk_bf16_f32 v37, v197, v213\n\t" \
    "v_mfma_f32_16x16x32_bf16 v[64:67], v[24:27], v[116:119], 0\n\t" \
    "ds_write_addtid_b32 v19 offset:768\n\t" \
    "v_cvt_pk_bf16_f32 v8, v198, v214\n\t" \
    "buffer_load_dwordx4 v[72:75], v3, s[4:7], s47 offen offset:0 sc1\n\t" \
    "ds_write_addtid_b32 v11 offset:1024\n\t" \
    "v_cvt_pk_bf16_f32 v12, v199, v215\n\t" \
    "v_mfma_f32_16x16x32_bf16 v[68:71], v[24:27], v[144:147], 0\n\t" \
    "ds_write_addtid_b32 v37 offset:1280\n\t" \
    "v_mfma_f32_32x32x16_bf16 v[240:255], v[44:47], v[92:95], 0\n\t" \
    "v_cvt_pk_bf16_f32 v9, v200, v216\n\t" \
    "ds_write_addtid_b32 v8 offset:1536\n\t" \
    "v_mfma_f32_16x16x32_bf16 v[112:115], v[28:31], v[88:91], 0\n\t" \
    "v_cvt_pk_bf16_f32 v13, v201, v217\n\t" \
    "ds_write_addtid_b32 v12 offset:1792\n\t" \
    "buffer_load_dwordx4 v[76:79], v39, s[4:7], s45 offen offset:1024 sc1\n\t" \
    "v_cvt_pk_bf16_f32 v15, v202, v218\n\t" \
    "ds_write_addtid_b32 v9 offset:2048\n\t" \
    "v_mfma_f32_16x16x32_bf16 v[60:63], v[28:31], v[120:123], v[60:63]\n\t" \
    "v_cvt_pk_bf16_f32 v16, v203, v219\n\t" \
    "v_mfma_f32_32x32x16_bf16 v[240:255], v[48:51], v[132:135], v[240:255]\n\t" \
    "ds_write_addtid_b32 v13 offset:2304\n\t" \
    "v_cvt_pk_bf16_f32 v19, v204, v220\n\t" \
    "v_mfma_f32_16x16x32_bf16 v[80:83], v[28:31], v[144:147], 0\n\t" \
    "ds_write_addtid_b32 v15 offset:2560\n\t" \
    "v_cvt_pk_bf16_f32 v11, v205, v221\n\t" \
    "buffer_load_dwordx4 v[32:35], v39, s[4:7], s46 offen offset:1024 sc1\n\t" \
    "ds_write_addtid_b32 v16 offset:2816\n\t" \
    "ds_write_addtid_b32 v19 offset:3072\n\t" \
    "v_mfma_f32_16x16x32_bf16 v[68:71], v[28:31], v[116:119], v[68:71]\n\t" \
    "ds_write_addtid_b32 v11 offset:3328\n\t" \
    "s_nop 1\n\t" \
    "v_cvt_pk_bf16_f32 v37, v224, v240\n\t" \
    "s_waitcnt vmcnt(8) lgkmcnt(15)\n\t" \
    "v_mfma_f32_16x16x32_bf16 v[56:59], v[128:131], v[4:7], v[56:59]\n\t" \
    "v_cvt_pk_bf16_f32 v8, v225, v241\n\t" \
    "v_cvt_pk_bf16_f32 v12, v226, v242\n\t" \
    "buffer_load_dwordx4 v[180:183], v39, s[4:7], s47 offen offset:1024 sc1\n\t" \
    "ds_write_addtid_b32 v37 offset:3616\n\t" \
    "v_mfma_f32_16x16x32_bf16 v[60:63], v[128:131], v[136:139], v[60:63]\n\t" \
    "v_cvt_pk_bf16_f32 v9, v227, v243\n\t" \
    "ds_write_addtid_b32 v8 offset:3872\n\t" \
    "v_cvt_pk_bf16_f32 v13, v228, v244\n\t" \
    "ds_write_addtid_b32 v12 offset:4128\n\t" \
    "v_mfma_f32_16x16x32_bf16 v[64:67], v[128:131], v[20:23], v[64:67]\n\t" \
    "v_cvt_pk_bf16_f32 v15, v229, v245\n\t" \
    "s_mov_b32 s45, 344064\n\t" \
    "buffer_load_dwordx4 v[84:87], v1, s[4:7], s45 offen offset:0 sc1\n\t" \
    "ds_write_addtid_b32 v9 offset:4384\n\t" \
    "v_cvt_pk_bf16_f32 v16, v230, v246\n\t" \
    "v_mfma_f32_16x16x32_bf16 v[68:71], v[128:131], v[148:151], v[68:71]\n\t" \
    "ds_write_addtid_b32 v13 offset:4640\n\t" \
    "v_cvt_pk_bf16_f32 v19, v231, v247\n\t" \
    "ds_write_addtid_b32 v15 offset:4896\n\t" \
    "v_cvt_pk_bf16_f32 v11, v232, v248\n\t" \
    "buffer_load_dwordx4 v[52:55], v1, s[4:7], s45 offen offset:1024 sc1\n\t" \
    "s_waitcnt vmcnt(10)\n\t" \
    "v_mfma_f32_16x16x32_bf16 v[112:115], v[140:143], v[136:139], v[112:115]\n\t" \
    "ds_write_addtid_b32 v16 offset:5152\n\t" \
    "v_cvt_pk_bf16_f32 v37, v233, v249\n\t" \
    "ds_write_addtid_b32 v19 offset:5408\n\t" \
    "v_mfma_f32_16x16x32_bf16 v[60:63], v[140:143], v[4:7], v[60:63]\n\t" \
    "v_cvt_pk_bf16_f32 v8, v234, v250\n\t" \
    "ds_write_addtid_b32 v11 offset:5664\n\t" \
    "s_mov_b32 s46, 458752\n\t" \
    "buffer_load_dwordx4 v[184:187], v1, s[4:7], s46 offen offset:0 sc1\n\t" \
    "v_cvt_pk_bf16_f32 v12, v235, v251\n\t" \
    "ds_write_addtid_b32 v37 offset:5920\n\t" \
    "v_mfma_f32_16x16x32_bf16 v[80:83], v[140:143], v[148:151], v[80:83]\n\t" \
    "v_cvt_pk_bf16_f32 v9, v236, v252\n\t" \
    "ds_write_addtid_b32 v8 offset:6176\n\t" \
    "v_cvt_pk_bf16_f32 v13, v237, v253\n\t" \
    "v_mfma_f32_16x16x32_bf16 v[68:71], v[140:143], v[20:23], v[68:71]\n\t" \
    "ds_write_addtid_b32 v12 offset:6432\n\t" \
    "buffer_load_dwordx4 v[108:111], v1, s[4:7], s46 offen offset:1024 sc1\n\t" \
    "ds_write_addtid_b32 v9 offset:6688\n\t" \
    "ds_write_addtid_b32 v13 offset:6944\n\t" \
    "s_waitcnt lgkmcnt(0)\n\t" \
    "s_barrier\n\t" \
    "s_cmp_lt_u32 s24, 4\n\t" \
    "s_cbranch_scc1 .Lp36_%=\n\t" \
    "s_setprio 1\n\t" \
    ".Lp36_%=:\n\t" \
    "ds_read_b128 v[24:27], v10 offset:2048\n\t" \
    "v_mov_b32 v15, 0x10000\n\t" \
    "s_waitcnt vmcnt(11)\n\t" \
    "v_mfma_f32_16x16x32_bf16 v[44:47], v[156:159], v[96:99], 0\n\t" \
    "v_subrev_u32 v17, 58368, v17\n\t" \
    "v_mfma_f32_16x16x32_bf16 v[92:95], v[156:159], v[100:103], 0\n\t" \
    "v_subrev_u32 v36, 58368, v36\n\t" \
    "v_mfma_f32_16x16x32_bf16 v[48:51], v[156:159], v[124:127], 0\n\t" \
    "ds_read_b128 v[132:135], v17 offset:0\n\t" \
    "s_mov_b32 s47, 573440\n\t" \
    "buffer_load_dwordx4 v[192:195], v1, s[4:7], s47 offen offset:0 sc1\n\t" \
    "v_mfma_f32_16x16x32_bf16 v[196:199], v[156:159], v[104:107], 0\n\t" \
    "ds_read_b128 v[200:203], v17 offset:128\n\t" \
    "s_waitcnt vmcnt(11)\n\t" \
    "v_mfma_f32_16x16x32_bf16 v[44:47], v[160:163], v[152:155], v[44:47]\n\t" \
    "ds_read_b128 v[204:207], v17 offset:29184\n\t" \
    "v_mfma_f32_16x16x32_bf16 v[92:95], v[160:163], v[164:167], v[92:95]\n\t" \
    "ds_read_b128 v[208:211], v17 offset:29312\n\t" \
    "buffer_load_dwordx4 v[212:215], v1, s[4:7], s47 offen offset:1024 sc1\n\t" \
    "v_mfma_f32_16x16x32_bf16 v[48:51], v[160:163], v[172:175], v[48:51]\n\t" \
    "ds_read_b128 v[216:219], v36 offset:0\n\t" \
    "v_mfma_f32_16x16x32_bf16 v[196:199], v[160:163], v[168:171], v[196:199]\n\t" \
    "ds_read_b128 v[220:223], v36 offset:128\n\t" \
    "s_waitcnt vmcnt(11) lgkmcnt(5)\n\t" \
    "v_mfma_f32_16x16x32_bf16 v[56:59], v[188:191], v[132:135], v[56:59]\n\t" \
    "ds_read_b128 v[28:31], v36 offset:29184\n\t" \
    "buffer_load_dwordx4 v[128:131], v3, s[4:7], s45 offen offset:0 sc1\n\t" \
    "s_waitcnt lgkmcnt(5)\n\t" \
    "v_mfma_f32_16x16x32_bf16 v[60:63], v[188:191], v[200:203], v[60:63]\n\t" \
    "ds_read_b128 v[224:227], v36 offset:29312\n\t" \
    "s_waitcnt lgkmcnt(5)\n\t" \
    "v_mfma_f32_16x16x32_bf16 v[64:67], v[188:191], v[204:207], v[64:67]\n\t" \
    "s_waitcnt lgkmcnt(4)\n\t" \
    "v_mfma_f32_16x16x32_bf16 v[68:71], v[188:191], v[208:211], v[68:71]\n\t" \
    "buffer_load_dwordx4 v[228:231], v3, s[4:7], s46 offen offset:0 sc1\n\t" \
    "s_waitcnt vmcnt(12)\n\t" \
    "v_mfma_f32_16x16x32_bf16 v[112:115], v[176:179], v[200:203], v[112:115]\n\t" \
    "s_waitcnt lgkmcnt(0)\n\t" \
    "s_add_u32 s12, s55, 34603008\n\t" \
    "s_buffer_load_dword s13, s[4:7], s12\n\t" \
    "s_buffer_load_dword s14, s[4:7], s12 offset:0x400\n\t" \
    "v_mfma_f32_16x16x32_bf16 v[60:63], v[176:179], v[132:135], v[60:63]\n\t" \
    "v_mfma_f32_16x16x32_bf16 v[80:83], v[176:179], v[208:211], v[80:83]\n\t" \
    "buffer_load_dwordx4 v[232:235], v3, s[4:7], s47 offen offset:0 sc1\n\t" \
    "v_mfma_f32_16x16x32_bf16 v[68:71], v[176:179], v[204:207], v[68:71]\n\t" \
    "s_waitcnt vmcnt(12)\n\t" \
    "v_mfma_f32_16x16x32_bf16 v[44:47], v[72:75], v[216:219], v[44:47]\n\t" \
    "v_mfma_f32_16x16x32_bf16 v[92:95], v[72:75], v[220:223], v[92:95]\n\t" \
    "buffer_load_dwordx4 v[236:239], v39, s[4:7], s45 offen offset:1024 sc1\n\t" \
    "v_mfma_f32_16x16x32_bf16 v[48:51], v[72:75], v[28:31], v[48:51]\n\t" \
    "v_sub_f32 v56, v56, v112\n\t" \
    "v_sub_f32 v57, v57, v113\n\t" \
    "v_sub_f32 v58, v58, v114\n\t" \
    "v_mfma_f32_16x16x32_bf16 v[196:199], v[72:75], v[224:227], v[196:199]\n\t" \
    "v_sub_f32 v59, v59, v115\n\t" \
    "v_sub_f32 v64, v64, v80\n\t" \
    "v_sub_f32 v65, v65, v81\n\t" \
    "v_sub_f32 v66, v66, v82\n\t" \
    "v_sub_f32 v67, v67, v83\n\t" \
    "v_cvt_pk_bf16_f32 v242, v60, v61\n\t" \
    "v_cvt_pk_bf16_f32 v243, v62, v63\n\t" \
    "v_cvt_pk_bf16_f32 v240, v56, v57\n\t" \
    "v_cvt_pk_bf16_f32 v241, v58, v59\n\t" \
    "v_cvt_pk_bf16_f32 v246, v68, v69\n\t" \
    "v_cvt_pk_bf16_f32 v247, v70, v71\n\t" \
    "ds_write_b128 v42, v[240:243] offset:58368\n\t" \
    "v_cvt_pk_bf16_f32 v244, v64, v65\n\t" \
    "v_cvt_pk_bf16_f32 v245, v66, v67\n\t" \
    "ds_write_b128 v42, v[244:247] offset:59392\n\t" \
    "s_waitcnt vmcnt(10)\n\t" \
    "s_cmp_ge_u32 s24, 4\n\t" \
    "s_cbranch_scc1 .Lk37_%=\n\t" \
    "v_mfma_f32_16x16x32_bf16 v[248:251], v[76:79], v[100:103], 0\n\t" \
    "v_mfma_f32_16x16x32_bf16 v[248:251], v[32:35], v[164:167], v[248:251]\n\t" \
    "v_mfma_f32_16x16x32_bf16 v[248:251], v[180:183], v[220:223], v[248:251]\n\t" \
    "v_mfma_f32_16x16x32_bf16 v[252:255], v[76:79], v[104:107], 0\n\t" \
    "v_mfma_f32_16x16x32_bf16 v[252:255], v[32:35], v[168:171], v[252:255]\n\t" \
    "v_mfma_f32_16x16x32_bf16 v[252:255], v[180:183], v[224:227], v[252:255]\n\t" \
    "v_mfma_f32_16x16x32_bf16 v[92:95], v[76:79], v[96:99], v[92:95]\n\t" \
    "v_mfma_f32_16x16x32_bf16 v[92:95], v[32:35], v[152:155], v[92:95]\n\t" \
    "v_mfma_f32_16x16x32_bf16 v[92:95], v[180:183], v[216:219], v[92:95]\n\t" \
    "v_mfma_f32_16x16x32_bf16 v[196:199], v[76:79], v[124:127], v[196:199]\n\t" \
    "s_nop 0\n\t" \
    "v_sub_f32 v44, v44, v248\n\t" \
    "v_sub_f32 v45, v45, v249\n\t" \
    "v_mfma_f32_16x16x32_bf16 v[196:199], v[32:35], v[172:175], v[196:199]\n\t" \
    "v_sub_f32 v46, v46, v250\n\t" \
    "v_sub_f32 v47, v47, v251\n\t" \
    "v_mfma_f32_16x16x32_bf16 v[196:199], v[180:183], v[28:31], v[196:199]\n\t" \
    "v_sub_f32 v48, v48, v252\n\t" \
    "v_sub_f32 v49, v49, v253\n\t" \
    "v_sub_f32 v50, v50, v254\n\t" \
    "v_sub_f32 v51, v51, v255\n\t" \
    ".Lk37_%=:\n\t" \
    "s_waitcnt vmcnt(9)\n\t" \
    "v_mfma_f32_16x16x32_bf16 v[140:143], v[84:87], v[120:123], 0\n\t" \
    "buffer_load_dwordx4 v[156:159], v39, s[4:7], s46 offen offset:1024 sc1\n\t" \
    "v_mfma_f32_16x16x32_bf16 v[160:163], v[84:87], v[88:91], 0\n\t" \
    "v_cvt_pk_bf16_f32 v190, v92, v93\n\t" \
    "v_cvt_pk_bf16_f32 v191, v94, v95\n\t" \
    "v_mfma_f32_16x16x32_bf16 v[176:179], v[84:87], v[116:119], 0\n\t" \
    "v_cvt_pk_bf16_f32 v188, v44, v45\n\t" \
    "v_cvt_pk_bf16_f32 v189, v46, v47\n\t" \
    "v_mfma_f32_16x16x32_bf16 v[72:75], v[84:87], v[144:147], 0\n\t" \
    "v_cvt_pk_bf16_f32 v114, v196, v197\n\t" \
    "v_cvt_pk_bf16_f32 v115, v198, v199\n\t" \
    "buffer_load_dwordx4 v[80:83], v39, s[4:7], s47 offen offset:1024 sc1\n\t" \
    "s_waitcnt vmcnt(10)\n\t" \
    "v_mfma_f32_16x16x32_bf16 v[60:63], v[52:55], v[88:91], 0\n\t" \
    "ds_write_b128 v42, v[188:191] offset:60432\n\t" \
    "v_cvt_pk_bf16_f32 v112, v48, v49\n\t" \
    "v_mfma_f32_16x16x32_bf16 v[160:163], v[52:55], v[120:123], v[160:163]\n\t" \
    "v_cvt_pk_bf16_f32 v113, v50, v51\n\t" \
    "v_mfma_f32_16x16x32_bf16 v[56:59], v[52:55], v[144:147], 0\n\t" \
    "ds_write_b128 v42, v[112:115] offset:61456\n\t" \
    "s_mov_b32 s45, 688128\n\t" \
    "buffer_load_dwordx4 v[68:71], v1, s[4:7], s45 offen offset:0 sc1\n\t" \
    "v_mfma_f32_16x16x32_bf16 v[72:75], v[52:55], v[116:119], v[72:75]\n\t" \
    "s_waitcnt vmcnt(10)\n\t" \
    "v_mfma_f32_16x16x32_bf16 v[140:143], v[184:187], v[4:7], v[140:143]\n\t" \
    "v_mfma_f32_16x16x32_bf16 v[160:163], v[184:187], v[136:139], v[160:163]\n\t" \
    "s_waitcnt lgkmcnt(0)\n\t" \
    "s_xor_b32 s13, s13, s34\n\t" \
    "s_xor_b32 s14, s14, s34\n\t" \
    "s_or_b32 s13, s13, s14\n\t" \
    "s_cbranch_scc1 .Lq1024_%=\n\t" \
    ".Lb23_%=:\n\t" \
    "s_waitcnt lgkmcnt(0)\n\t" \
    "s_barrier\n\t" \
    "s_memrealtime s[16:17]\n\t" \
    "ds_read_b64_tr_b16 v[240:241], v18 offset:58368\n\t" \
    "ds_read_b64_tr_b16 v[242:243], v18 offset:62496\n\t" \
    "buffer_load_dwordx4 v[64:67], v1, s[4:7], s45 offen offset:1024 sc1\n\t" \
    "v_mfma_f32_16x16x32_bf16 v[176:179], v[184:187], v[20:23], v[176:179]\n\t" \
    "ds_read_b64_tr_b16 v[244:245], v18 offset:58624\n\t" \
    "ds_read_b64_tr_b16 v[246:247], v18 offset:62752\n\t" \
    "v_mfma_f32_16x16x32_bf16 v[72:75], v[184:187], v[148:151], v[72:75]\n\t" \
    "ds_read_b64_tr_b16 v[76:77], v18 offset:58384\n\t" \
    "ds_read_b64_tr_b16 v[78:79], v18 offset:62512\n\t" \
    "s_waitcnt vmcnt(10)\n\t" \
    "v_mfma_f32_16x16x32_bf16 v[60:63], v[108:111], v[136:139], v[60:63]\n\t" \
    "ds_read_b64_tr_b16 v[32:33], v18 offset:58640\n\t" \
    "ds_read_b64_tr_b16 v[34:35], v18 offset:62768\n\t" \
    "s_mov_b32 s46, 802816\n\t" \
    "buffer_load_dwordx4 v[248:251], v1, s[4:7], s46 offen offset:0 sc1\n\t" \
    "v_mfma_f32_16x16x32_bf16 v[160:163], v[108:111], v[4:7], v[160:163]\n\t" \
    "v_mfma_f32_16x16x32_bf16 v[56:59], v[108:111], v[148:151], v[56:59]\n\t" \
    "v_mfma_f32_16x16x32_bf16 v[72:75], v[108:111], v[20:23], v[72:75]\n\t" \
    "s_waitcnt lgkmcnt(6)\n\t" \
    "v_mfma_f32_16x16x32_bf16 v[180:183], v[240:243], v[24:27], 0\n\t" \
    "s_waitcnt lgkmcnt(4)\n\t" \
    "v_mfma_f32_16x16x32_bf16 v[252:255], v[244:247], v[24:27], 0\n\t" \
    "buffer_load_dwordx4 v[92:95], v1, s[4:7], s46 offen offset:1024 sc1\n\t" \
    "s_waitcnt vmcnt(11)\n\t" \
    "v_mfma_f32_16x16x32_bf16 v[140:143], v[192:195], v[132:135], v[140:143]\n\t" \
    "s_waitcnt lgkmcnt(2)\n\t" \
    "v_mfma_f32_16x16x32_bf16 v[44:47], v[76:79], v[24:27], 0\n\t" \
    "s_waitcnt lgkmcnt(0)\n\t" \
    "v_mfma_f32_16x16x32_bf16 v[84:87], v[32:35], v[24:27], 0\n\t" \
    "v_mfma_f32_16x16x32_bf16 v[160:163], v[192:195], v[200:203], v[160:163]\n\t" \
    "s_cmp_lg_u32 s24, 4\n\t" \
    "s_cbranch_scc1 .Lph_%=\n\t" \
    "s_waitcnt lgkmcnt(0)\n\t" \
    "v_mov_b32 v196, s16\n\t" \
    "v_mov_b32 v197, s16\n\t" \
    "v_mov_b32 v198, s16\n\t" \
    "v_mov_b32 v199, s16\n\t" \
    "buffer_store_dwordx4 v[196:199], v2, s[4:7], s32 offen sc1\n\t" \
    "s_nop 0\n\t" \
    ".Lph_%=:\n\t" \
    "v_mfma_f32_16x16x32_bf16 v[176:179], v[192:195], v[204:207], v[176:179]\n\t" \
    "s_mov_b32 s47, 917504\n\t" \
    "buffer_load_dwordx4 v[188:191], v1, s[4:7], s47 offen offset:0 sc1\n\t" \
    "v_mfma_f32_16x16x32_bf16 v[72:75], v[192:195], v[208:211], v[72:75]\n\t" \
    "v_bfi_b32 v180, s41, v15, v180\n\t" \
    "v_bfi_b32 v252, s41, v15, v252\n\t" \
    "s_waitcnt vmcnt(11)\n\t" \
    "v_mfma_f32_16x16x32_bf16 v[60:63], v[212:215], v[200:203], v[60:63]\n\t" \
    "v_cvt_pk_bf16_f32 v49, v182, v183\n\t" \
    "v_cvt_pk_bf16_f32 v51, v254, v255\n\t" \
    "v_mfma_f32_16x16x32_bf16 v[160:163], v[212:215], v[132:135], v[160:163]\n\t" \
    "v_cvt_pk_bf16_f32 v48, v180, v181\n\t" \
    "v_cvt_pk_bf16_f32 v50, v252, v253\n\t" \
    "v_mfma_f32_16x16x32_bf16 v[56:59], v[212:215], v[208:211], v[56:59]\n\t" \
    "v_bfi_b32 v44, s41, v15, v44\n\t" \
    "v_bfi_b32 v84, s41, v15, v84\n\t" \
    "v_mfma_f32_16x16x32_bf16 v[72:75], v[212:215], v[204:207], v[72:75]\n\t" \
    "buffer_store_dwordx4 v[48:51], v40, s[4:7], s48 offen offset:0 sc1\n\t" \
    "v_cvt_pk_bf16_f32 v113, v46, v47\n\t" \
    "s_waitcnt vmcnt(11)\n\t" \
    "v_mfma_f32_16x16x32_bf16 v[52:55], v[128:131], v[96:99], 0\n\t" \
    "v_cvt_pk_bf16_f32 v115, v86, v87\n\t" \
    "v_cvt_pk_bf16_f32 v112, v44, v45\n\t" \
    "v_mfma_f32_16x16x32_bf16 v[184:187], v[128:131], v[100:103], 0\n\t" \
    "v_cvt_pk_bf16_f32 v114, v84, v85\n\t" \
    "buffer_store_dwordx4 v[112:115], v40, s[4:7], s48 offen offset:1024 sc1\n\t" \
    "v_mfma_f32_16x16x32_bf16 v[108:111], v[128:131], v[124:127], 0\n\t" \
    "v_sub_f32 v140, v140, v60\n\t" \
    "v_sub_f32 v141, v141, v61\n\t" \
    "v_mfma_f32_16x16x32_bf16 v[240:243], v[128:131], v[104:107], 0\n\t" \
    "v_sub_f32 v142, v142, v62\n\t" \
    "v_sub_f32 v143, v143, v63\n\t" \
    "s_waitcnt vmcnt(11)\n\t" \
    "v_mfma_f32_16x16x32_bf16 v[52:55], v[228:231], v[152:155], v[52:55]\n\t" \
    "v_sub_f32 v176, v176, v56\n\t" \
    "v_sub_f32 v177, v177, v57\n\t" \
    "v_mfma_f32_16x16x32_bf16 v[184:187], v[228:231], v[164:167], v[184:187]\n\t" \
    "v_sub_f32 v178, v178, v58\n\t" \
    "v_sub_f32 v179, v179, v59\n\t" \
    "v_mfma_f32_16x16x32_bf16 v[108:111], v[228:231], v[172:175], v[108:111]\n\t" \
    "v_cvt_pk_bf16_f32 v246, v160, v161\n\t" \
    "v_cvt_pk_bf16_f32 v247, v162, v163\n\t" \
    "buffer_load_dwordx4 v[76:79], v1, s[4:7], s47 offen offset:1024 sc1\n\t" \
    "v_mfma_f32_16x16x32_bf16 v[240:243], v[228:231], v[168:171], v[240:243]\n\t" \
    "v_cvt_pk_bf16_f32 v244, v140, v141\n\t" \
    "v_cvt_pk_bf16_f32 v245, v142, v143\n\t" \
    "s_waitcnt vmcnt(11)\n\t" \
    "v_mfma_f32_16x16x32_bf16 v[52:55], v[232:235], v[216:219], v[52:55]\n\t" \
    "v_cvt_pk_bf16_f32 v34, v72, v73\n\t" \
    "v_cvt_pk_bf16_f32 v35, v74, v75\n\t" \
    "v_mfma_f32_16x16x32_bf16 v[184:187], v[232:235], v[220:223], v[184:187]\n\t" \
    "ds_write_b128 v42, v[244:247] offset:0\n\t" \
    "v_cvt_pk_bf16_f32 v32, v176, v177\n\t" \
    "buffer_load_dwordx4 v[196:199], v3, s[4:7], s45 offen offset:0 sc1\n\t" \
    "v_mfma_f32_16x16x32_bf16 v[108:111], v[232:235], v[28:31], v[108:111]\n\t" \
    "v_cvt_pk_bf16_f32 v33, v178, v179\n\t" \
    "ds_write_b128 v42, v[32:35] offset:1024\n\t" \
    "v_mfma_f32_16x16x32_bf16 v[240:243], v[232:235], v[224:227], v[240:243]\n\t" \
    "s_waitcnt vmcnt(9)\n\t" \
    "s_cmp_ge_u32 s24, 4\n\t" \
    "s_cbranch_scc1 .Lk38_%=\n\t" \
    "v_mfma_f32_16x16x32_bf16 v[192:195], v[236:239], v[100:103], 0\n\t" \
    "v_mfma_f32_16x16x32_bf16 v[192:195], v[156:159], v[164:167], v[192:195]\n\t" \
    "v_mfma_f32_16x16x32_bf16 v[192:195], v[80:83], v[220:223], v[192:195]\n\t" \
    "v_mfma_f32_16x16x32_bf16 v[180:183], v[236:239], v[104:107], 0\n\t" \
    "v_mfma_f32_16x16x32_bf16 v[180:183], v[156:159], v[168:171], v[180:183]\n\t" \
    "v_mfma_f32_16x16x32_bf16 v[180:183], v[80:83], v[224:227], v[180:183]\n\t" \
    "v_mfma_f32_16x16x32_bf16 v[184:187], v[236:239], v[96:99], v[184:187]\n\t" \
    "v_mfma_f32_16x16x32_bf16 v[184:187], v[156:159], v[152:155], v[184:187]\n\t" \
    "v_mfma_f32_16x16x32_bf16 v[184:187], v[80:83], v[216:219], v[184:187]\n\t" \
    "v_mfma_f32_16x16x32_bf16 v[240:243], v[236:239], v[124:127], v[240:243]\n\t" \
    "s_nop 0\n\t" \
    "v_sub_f32 v52, v52, v192\n\t" \
    "v_sub_f32 v53, v53, v193\n\t" \
    "v_mfma_f32_16x16x32_bf16 v[240:243], v[156:159], v[172:175], v[240:243]\n\t" \
    "v_sub_f32 v54, v54, v194\n\t" \
    "v_sub_f32 v55, v55, v195\n\t" \
    "v_mfma_f32_16x16x32_bf16 v[240:243], v[80:83], v[28:31], v[240:243]\n\t" \
    "v_sub_f32 v108, v108, v180\n\t" \
    "v_sub_f32 v109, v109, v181\n\t" \
    "v_sub_f32 v110, v110, v182\n\t" \
    "v_sub_f32 v111, v111, v183\n\t" \
    ".Lk38_%=:\n\t" \
    "s_waitcnt vmcnt(8)\n\t" \
    "v_mfma_f32_16x16x32_bf16 v[252:255], v[68:71], v[120:123], 0\n\t" \
    "buffer_load_dwordx4 v[212:215], v3, s[4:7], s46 offen offset:0 sc1\n\t" \
    "v_mfma_f32_16x16x32_bf16 v[48:51], v[68:71], v[88:91], 0\n\t" \
    "v_cvt_pk_bf16_f32 v46, v184, v185\n\t" \
    "v_cvt_pk_bf16_f32 v47, v186, v187\n\t" \
    "v_mfma_f32_16x16x32_bf16 v[84:87], v[68:71], v[116:119], 0\n\t" \
    "v_cvt_pk_bf16_f32 v44, v52, v53\n\t" \
    "v_cvt_pk_bf16_f32 v45, v54, v55\n\t" \
    "v_mfma_f32_16x16x32_bf16 v[112:115], v[68:71], v[144:147], 0\n\t" \
    "v_cvt_pk_bf16_f32 v130, v240, v241\n\t" \
    "v_cvt_pk_bf16_f32 v131, v242, v243\n\t" \
    "buffer_load_dwordx4 v[60:63], v3, s[4:7], s47 offen offset:0 sc1\n\t" \
    "s_waitcnt vmcnt(9)\n\t" \
    "v_mfma_f32_16x16x32_bf16 v[56:59], v[64:67], v[88:91], 0\n\t" \
    "ds_write_b128 v42, v[44:47] offset:2064\n\t" \
    "v_cvt_pk_bf16_f32 v128, v108, v109\n\t" \
    "v_mfma_f32_16x16x32_bf16 v[48:51], v[64:67], v[120:123], v[48:51]\n\t" \
    "v_cvt_pk_bf16_f32 v129, v110, v111\n\t" \
    "v_mfma_f32_16x16x32_bf16 v[160:163], v[64:67], v[144:147], 0\n\t" \
    "ds_write_b128 v42, v[128:131] offset:3088\n\t" \
    "buffer_load_dwordx4 v[228:231], v39, s[4:7], s45 offen offset:1024 sc1\n\t" \
    "v_mfma_f32_16x16x32_bf16 v[112:115], v[64:67], v[116:119], v[112:115]\n\t" \
    "s_waitcnt vmcnt(9)\n\t" \
    "v_mfma_f32_16x16x32_bf16 v[252:255], v[248:251], v[4:7], v[252:255]\n\t" \
    "v_mfma_f32_16x16x32_bf16 v[48:51], v[248:251], v[136:139], v[48:51]\n\t" \
    "s_waitcnt lgkmcnt(0)\n\t" \
    "s_barrier\n\t" \
    "s_memrealtime s[18:19]\n\t" \
    "s_setprio 2\n\t" \
    "ds_read_b64_tr_b16 v[140:141], v18 offset:0\n\t" \
    "ds_read_b64_tr_b16 v[142:143], v18 offset:4128\n\t" \
    "buffer_load_dwordx4 v[72:75], v39, s[4:7], s46 offen offset:1024 sc1\n\t" \
    "v_mfma_f32_16x16x32_bf16 v[84:87], v[248:251], v[20:23], v[84:87]\n\t" \
    "ds_read_b64_tr_b16 v[244:245], v18 offset:256\n\t" \
    "ds_read_b64_tr_b16 v[246:247], v18 offset:4384\n\t" \
    "v_mfma_f32_16x16x32_bf16 v[112:115], v[248:251], v[148:151], v[112:115]\n\t" \
    "ds_read_b64_tr_b16 v[176:177], v18 offset:16\n\t" \
    "ds_read_b64_tr_b16 v[178:179], v18 offset:4144\n\t" \
    "s_waitcnt vmcnt(9)\n\t" \
    "v_mfma_f32_16x16x32_bf16 v[56:59], v[92:95], v[136:139], v[56:59]\n\t" \
    "ds_read_b64_tr_b16 v[32:33], v18 offset:272\n\t" \
    "ds_read_b64_tr_b16 v[34:35], v18 offset:4400\n\t" \
    "buffer_load_dwordx4 v[232:235], v39, s[4:7], s47 offen offset:1024 sc1\n\t" \
    "v_mfma_f32_16x16x32_bf16 v[48:51], v[92:95], v[4:7], v[48:51]\n\t" \
    "v_mfma_f32_16x16x32_bf16 v[160:163], v[92:95], v[148:151], v[160:163]\n\t" \
    "v_mfma_f32_16x16x32_bf16 v[112:115], v[92:95], v[20:23], v[112:115]\n\t" \
    "s_waitcnt lgkmcnt(6)\n\t" \
    "v_mfma_f32_16x16x32_bf16 v[236:239], v[140:143], v[24:27], 0\n\t" \
    "s_waitcnt lgkmcnt(4)\n\t" \
    "v_mfma_f32_16x16x32_bf16 v[156:159], v[244:247], v[24:27], 0\n\t" \
    "s_waitcnt vmcnt(9)\n\t" \
    "v_mfma_f32_16x16x32_bf16 v[252:255], v[188:191], v[132:135], v[252:255]\n\t" \
    "s_waitcnt lgkmcnt(2)\n\t" \
    "v_mfma_f32_16x16x32_bf16 v[192:195], v[176:179], v[24:27], 0\n\t" \
    "s_waitcnt lgkmcnt(0)\n\t" \
    "v_mfma_f32_16x16x32_bf16 v[80:83], v[32:35], v[24:27], 0\n\t" \
    "v_mfma_f32_16x16x32_bf16 v[48:51], v[188:191], v[200:203], v[48:51]\n\t" \
    "v_mfma_f32_16x16x32_bf16 v[84:87], v[188:191], v[204:207], v[84:87]\n\t" \
    "s_add_u32 s37, s52, s33\n\t" \
    "s_buffer_load_dword s58, s[4:7], s37 offset:0x0\n\t" \
    "s_buffer_load_dword s59, s[4:7], s37 offset:0x400\n\t" \
    "s_buffer_load_dword s60, s[4:7], s37 offset:0x4000\n\t" \
    "s_buffer_load_dword s61, s[4:7], s37 offset:0x4400\n\t" \
    "v_mfma_f32_16x16x32_bf16 v[112:115], v[188:191], v[208:211], v[112:115]\n\t" \
    "v_bfi_b32 v236, s41, v15, v236\n\t" \
    "v_bfi_b32 v156, s41, v15, v156\n\t" \
    "s_waitcnt vmcnt(6)\n\t" \
    "v_mfma_f32_16x16x32_bf16 v[56:59], v[76:79], v[200:203], v[56:59]\n\t" \
    "v_cvt_pk_bf16_f32 v181, v238, v239\n\t" \
    "v_cvt_pk_bf16_f32 v183, v158, v159\n\t" \
    "v_add_u32 v42, 91392, v42\n\t" \
    "v_mfma_f32_16x16x32_bf16 v[48:51], v[76:79], v[132:135], v[48:51]\n\t" \
    "v_cvt_pk_bf16_f32 v180, v236, v237\n\t" \
    "v_cvt_pk_bf16_f32 v182, v156, v157\n\t" \
    "v_mfma_f32_16x16x32_bf16 v[160:163], v[76:79], v[208:211], v[160:163]\n\t" \
    "v_bfi_b32 v192, s41, v15, v192\n\t" \
    "v_bfi_b32 v80, s41, v15, v80\n\t" \
    "v_mfma_f32_16x16x32_bf16 v[112:115], v[76:79], v[204:207], v[112:115]\n\t" \
    "buffer_store_dwordx4 v[180:183], v40, s[4:7], s49 offen offset:0 sc1\n\t" \
    "v_cvt_pk_bf16_f32 v185, v194, v195\n\t" \
    "s_waitcnt vmcnt(6)\n\t" \
    "v_mfma_f32_16x16x32_bf16 v[52:55], v[196:199], v[96:99], 0\n\t" \
    "v_cvt_pk_bf16_f32 v187, v82, v83\n\t" \
    "v_cvt_pk_bf16_f32 v184, v192, v193\n\t" \
    "v_mfma_f32_16x16x32_bf16 v[68:71], v[196:199], v[100:103], 0\n\t" \
    "v_cvt_pk_bf16_f32 v186, v80, v81\n\t" \
    "buffer_store_dwordx4 v[184:187], v40, s[4:7], s49 offen offset:1024 sc1\n\t" \
    "v_mfma_f32_16x16x32_bf16 v[240:243], v[196:199], v[124:127], 0\n\t" \
    "s_setprio 0\n\t" \
    "s_cmp_lt_u32 s24, 4\n\t" \
    "s_cbranch_scc1 .Lp39_%=\n\t" \
    "s_setprio 1\n\t" \
    ".Lp39_%=:\n\t" \
    "v_sub_f32 v252, v252, v56\n\t" \
    "v_sub_f32 v253, v253, v57\n\t" \
    "v_mfma_f32_16x16x32_bf16 v[88:91], v[196:199], v[104:107], 0\n\t" \
    "v_sub_f32 v254, v254, v58\n\t" \
    "v_sub_f32 v255, v255, v59\n\t" \
    "s_waitcnt vmcnt(6)\n\t" \
    "v_mfma_f32_16x16x32_bf16 v[52:55], v[212:215], v[152:155], v[52:55]\n\t" \
    "v_sub_f32 v84, v84, v160\n\t" \
    "v_sub_f32 v85, v85, v161\n\t" \
    "v_mfma_f32_16x16x32_bf16 v[68:71], v[212:215], v[164:167], v[68:71]\n\t" \
    "v_sub_f32 v86, v86, v162\n\t" \
    "v_sub_f32 v87, v87, v163\n\t" \
    "v_mfma_f32_16x16x32_bf16 v[240:243], v[212:215], v[172:175], v[240:243]\n\t" \
    "v_cvt_pk_bf16_f32 v46, v48, v49\n\t" \
    "v_cvt_pk_bf16_f32 v47, v50, v51\n\t" \
    "v_mfma_f32_16x16x32_bf16 v[88:91], v[212:215], v[168:171], v[88:91]\n\t" \
    "v_cvt_pk_bf16_f32 v44, v252, v253\n\t" \
    "v_cvt_pk_bf16_f32 v45, v254, v255\n\t" \
    "s_waitcnt vmcnt(5)\n\t" \
    "v_mfma_f32_16x16x32_bf16 v[52:55], v[60:63], v[216:219], v[52:55]\n\t" \
    "v_cvt_pk_bf16_f32 v122, v112, v113\n\t" \
    "v_cvt_pk_bf16_f32 v123, v114, v115\n\t" \
    "v_mfma_f32_16x16x32_bf16 v[68:71], v[60:63], v[220:223], v[68:71]\n\t" \
    "ds_write_b128 v42, v[44:47] offset:0\n\t" \
    "v_cvt_pk_bf16_f32 v120, v84, v85\n\t" \
    "v_mfma_f32_16x16x32_bf16 v[240:243], v[60:63], v[28:31], v[240:243]\n\t" \
    "v_cvt_pk_bf16_f32 v121, v86, v87\n\t" \
    "ds_write_b128 v42, v[120:123] offset:1024\n\t" \
    "v_mfma_f32_16x16x32_bf16 v[88:91], v[60:63], v[224:227], v[88:91]\n\t" \
    "s_waitcnt vmcnt(2)\n\t" \
    "s_cmp_ge_u32 s24, 4\n\t" \
    "s_cbranch_scc1 .Lk40_%=\n\t" \
    "v_mfma_f32_16x16x32_bf16 v[108:111], v[228:231], v[100:103], 0\n\t" \
    "v_mfma_f32_16x16x32_bf16 v[108:111], v[72:75], v[164:167], v[108:111]\n\t" \
    "v_mfma_f32_16x16x32_bf16 v[108:111], v[232:235], v[220:223], v[108:111]\n\t" \
    "v_mfma_f32_16x16x32_bf16 v[144:147], v[228:231], v[104:107], 0\n\t" \
    "v_mfma_f32_16x16x32_bf16 v[144:147], v[72:75], v[168:171], v[144:147]\n\t" \
    "v_mfma_f32_16x16x32_bf16 v[144:147], v[232:235], v[224:227], v[144:147]\n\t" \
    "v_mfma_f32_16x16x32_bf16 v[68:71], v[228:231], v[96:99], v[68:71]\n\t" \
    "v_mfma_f32_16x16x32_bf16 v[68:71], v[72:75], v[152:155], v[68:71]\n\t" \
    "v_mfma_f32_16x16x32_bf16 v[68:71], v[232:235], v[216:219], v[68:71]\n\t" \
    "v_mfma_f32_16x16x32_bf16 v[88:91], v[228:231], v[124:127], v[88:91]\n\t" \
    "s_nop 0\n\t" \
    "v_sub_f32 v52, v52, v108\n\t" \
    "v_sub_f32 v53, v53, v109\n\t" \
    "v_mfma_f32_16x16x32_bf16 v[88:91], v[72:75], v[172:175], v[88:91]\n\t" \
    "v_sub_f32 v54, v54, v110\n\t" \
    "v_sub_f32 v55, v55, v111\n\t" \
    "v_mfma_f32_16x16x32_bf16 v[88:91], v[232:235], v[28:31], v[88:91]\n\t" \
    "v_sub_f32 v240, v240, v144\n\t" \
    "v_sub_f32 v241, v241, v145\n\t" \
    "v_sub_f32 v242, v242, v146\n\t" \
    "v_sub_f32 v243, v243, v147\n\t" \
    ".Lk40_%=:\n\t" \
    "ds_read_b128 v[128:131], v10 offset:3072\n\t" \
    "ds_read_b128 v[64:67], v10 offset:4096\n\t" \
    "v_cvt_pk_bf16_f32 v118, v68, v69\n\t" \
    "v_cvt_pk_bf16_f32 v119, v70, v71\n\t" \
    "v_cvt_pk_bf16_f32 v116, v52, v53\n\t" \
    "v_cvt_pk_bf16_f32 v117, v54, v55\n\t" \
    "v_cvt_pk_bf16_f32 v250, v88, v89\n\t" \
    "v_cvt_pk_bf16_f32 v251, v90, v91\n\t" \
    "ds_write_b128 v42, v[116:119] offset:2064\n\t" \
    "v_cvt_pk_bf16_f32 v248, v240, v241\n\t" \
    "v_cvt_pk_bf16_f32 v249, v242, v243\n\t" \
    "v_add_u32 v18, 91392, v18\n\t" \
    "ds_write_b128 v42, v[248:251] offset:3088\n\t" \
    "s_add_u32 s54, s53, 0\n\t" \
    "buffer_load_dwordx4 v[136:139], v0, s[4:7], s54 offen offset:0 sc1\n\t" \
    "s_add_u32 s54, s53, 4243456\n\t" \
    "buffer_load_dwordx4 v[36:39], v0, s[4:7], s54 offen offset:0 sc1\n\t" \
    "s_waitcnt lgkmcnt(0)\n\t" \
    "s_barrier\n\t" \
    "s_memrealtime s[56:57]\n\t" \
    "ds_read_b64_tr_b16 v[4:5], v18 offset:0\n\t" \
    "ds_read_b64_tr_b16 v[6:7], v18 offset:4128\n\t" \
    "ds_read_b64_tr_b16 v[148:149], v18 offset:256\n\t" \
    "ds_read_b64_tr_b16 v[150:151], v18 offset:4384\n\t" \
    "ds_read_b64_tr_b16 v[20:21], v18 offset:16\n\t" \
    "ds_read_b64_tr_b16 v[22:23], v18 offset:4144\n\t" \
    "ds_read_b64_tr_b16 v[92:93], v18 offset:272\n\t" \
    "ds_read_b64_tr_b16 v[94:95], v18 offset:4400\n\t" \
    "s_waitcnt lgkmcnt(6)\n\t" \
    "v_mfma_f32_16x16x32_bf16 v[140:143], v[4:7], v[24:27], 0\n\t" \
    "s_waitcnt lgkmcnt(4)\n\t" \
    "v_mfma_f32_16x16x32_bf16 v[244:247], v[148:151], v[24:27], 0\n\t" \
    "s_waitcnt lgkmcnt(2)\n\t" \
    "v_mfma_f32_16x16x32_bf16 v[176:179], v[20:23], v[24:27], 0\n\t" \
    "s_waitcnt lgkmcnt(0)\n\t" \
    "v_mfma_f32_16x16x32_bf16 v[32:35], v[92:95], v[24:27], 0\n\t" \
    "s_nop 1\n\t" \
    "v_bfi_b32 v140, s41, v15, v140\n\t" \
    "s_nop 0\n\t" \
    "v_bfi_b32 v244, s41, v15, v244\n\t" \
    "v_cvt_pk_bf16_f32 v189, v142, v143\n\t" \
    "v_cvt_pk_bf16_f32 v191, v246, v247\n\t" \
    "v_cvt_pk_bf16_f32 v188, v140, v141\n\t" \
    "v_cvt_pk_bf16_f32 v190, v244, v245\n\t" \
    "v_bfi_b32 v176, s41, v15, v176\n\t" \
    "v_bfi_b32 v32, s41, v15, v32\n\t" \
    "buffer_store_dwordx4 v[188:191], v40, s[4:7], s50 offen offset:0 sc1\n\t" \
    "v_cvt_pk_bf16_f32 v201, v178, v179\n\t" \
    "v_cvt_pk_bf16_f32 v203, v34, v35\n\t" \
    "v_cvt_pk_bf16_f32 v200, v176, v177\n\t" \
    "v_cvt_pk_bf16_f32 v202, v32, v33\n\t" \
    "buffer_store_dwordx4 v[200:203], v40, s[4:7], s50 offen offset:1024 sc1\n\t" \
    "s_cmp_ge_u32 s25, 252\n\t" \
    "s_cbranch_scc1 .Lend_%=\n\t" \
    "v_xor_b32 v11, 0x80, v10\n\t" \
    "v_xor_b32 v8, 64, v14\n\t" \
    "s_mov_b32 s20, 0xff00ff00\n\t" \
    "s_mov_b32 s21, 0xff00ff00\n\t" \
    "ds_read_b128 v[132:135], v11 offset:3072\n\t" \
    "ds_read_b128 v[236:239], v11 offset:4096\n\t" \
    "s_waitcnt lgkmcnt(0)\n\t" \
    "s_sub_u32 s37, s16, 300\n\t" \
    "s_mov_b32 s62, 300\n\t" \
    "s_sub_u32 s39, s58, s37\n\t" \
    "s_cmp_gt_u32 s39, 600\n\t" \
    "s_cselect_b32 s39, 400, s39\n\t" \
    "s_max_u32 s62, s62, s39\n\t" \
    "s_sub_u32 s39, s59, s37\n\t" \
    "s_cmp_gt_u32 s39, 600\n\t" \
    "s_cselect_b32 s39, 400, s39\n\t" \
    "s_max_u32 s62, s62, s39\n\t" \
    "s_sub_u32 s39, s60, s37\n\t" \
    "s_cmp_gt_u32 s39, 600\n\t" \
    "s_cselect_b32 s39, 400, s39\n\t" \
    "s_max_u32 s62, s62, s39\n\t" \
    "s_sub_u32 s39, s61, s37\n\t" \
    "s_cmp_gt_u32 s39, 600\n\t" \
    "s_cselect_b32 s39, 400, s39\n\t" \
    "s_max_u32 s62, s62, s39\n\t" \
    "s_sub_u32 s62, s62, 300\n\t" \
    "s_mul_i32 s62, s62, 5\n\t" \
    "s_lshr_b32 s62, s62, 2\n\t" \
    "s_waitcnt vmcnt(2)\n\t" \
    "v_and_b32 v12, v136, v138\n\t" \
    "v_bitop3_b32 v12, v12, v36, v38 bitop3:0x80\n\t" \
    "v_and_b32 v12, 1, v12\n\t" \
    "v_cmp_eq_u32 vcc, 0, v12\n\t" \
    "s_nop 1\n\t" \
    "s_cbranch_vccnz .Ls41_%=\n\t" \
    ".Lb41_%=:\n\t" \
    "ds_write_b128 v43, v[136:139] offset:33024\n\t" \
    "ds_write_b128 v43, v[36:39] offset:33152\n\t" \
    "s_add_u32 s37, s62, 110\n\t" \
    ".Lh44_%=:\n\t" \
    "s_memrealtime s[22:23]\n\t" \
    "s_waitcnt lgkmcnt(0)\n\t" \
    "s_sub_u32 s22, s22, s18\n\t" \
    "s_cmp_lt_u32 s22, s37\n\t" \
    "s_cbranch_scc1 .Lh44_%=\n\t" \
    "s_add_u32 s54, s53, 524288\n\t" \
    "buffer_load_dwordx4 v[156:159], v0, s[4:7], s54 offen offset:0 sc1\n\t" \
    "s_add_u32 s54, s53, 4767744\n\t" \
    "buffer_load_dwordx4 v[208:211], v0, s[4:7], s54 offen offset:0 sc1\n\t" \
    "s_waitcnt lgkmcnt(0)\n\t" \
    "s_barrier\n\t" \
    "ds_read_b64_tr_b16 v[76:77], v41 offset:33024\n\t" \
    "ds_read_b64_tr_b16 v[78:79], v41 offset:33280\n\t" \
    "ds_read_b64_tr_b16 v[204:205], v41 offset:33032\n\t" \
    "ds_read_b64_tr_b16 v[206:207], v41 offset:33288\n\t" \
    "s_waitcnt lgkmcnt(2)\n\t" \
    "v_mfma_f32_16x16x32_bf16 v[180:183], v[76:79], v[128:131], 0\n\t" \
    "s_waitcnt lgkmcnt(0)\n\t" \
    "v_mfma_f32_16x16x32_bf16 v[192:195], v[204:207], v[128:131], 0\n\t" \
    "v_mfma_f32_16x16x32_bf16 v[80:83], v[76:79], v[236:239], 0\n\t" \
    "v_mfma_f32_16x16x32_bf16 v[184:187], v[204:207], v[236:239], 0\n\t" \
    "s_nop 3\n\t" \
    "v_cvt_pk_bf16_f32 v196, v180, v181\n\t" \
    "v_cvt_pk_bf16_f32 v197, v182, v183\n\t" \
    "v_cvt_pk_bf16_f32 v198, v192, v193\n\t" \
    "v_cvt_pk_bf16_f32 v199, v194, v195\n\t" \
    "v_cvt_pk_bf16_f32 v56, v80, v81\n\t" \
    "v_cvt_pk_bf16_f32 v57, v82, v83\n\t" \
    "v_cvt_pk_bf16_f32 v58, v184, v185\n\t" \
    "v_cvt_pk_bf16_f32 v59, v186, v187\n\t" \
    "s_waitcnt vmcnt(0)\n\t" \
    "v_and_b32 v9, v156, v158\n\t" \
    "v_bitop3_b32 v9, v9, v208, v210 bitop3:0x80\n\t" \
    "v_and_b32 v9, 1, v9\n\t" \
    "v_cmp_eq_u32 vcc, 0, v9\n\t" \
    "s_nop 1\n\t" \
    "s_cbranch_vccnz .Ls42_%=\n\t" \
    ".Lb42_%=:\n\t" \
    "ds_write_b128 v43, v[156:159] offset:0\n\t" \
    "ds_write_b128 v43, v[208:211] offset:128\n\t" \
    "s_add_u32 s37, s62, 95\n\t" \
    ".Lh45_%=:\n\t" \
    "s_memrealtime s[22:23]\n\t" \
    "s_waitcnt lgkmcnt(0)\n\t" \
    "s_sub_u32 s22, s22, s56\n\t" \
    "s_cmp_lt_u32 s22, s37\n\t" \
    "s_cbranch_scc1 .Lh45_%=\n\t" \
    "s_add_u32 s54, s53, 1048576\n\t" \
    "buffer_load_dwordx4 v[160:163], v0, s[4:7], s54 offen offset:0 sc1\n\t" \
    "s_add_u32 s54, s53, 5292032\n\t" \
    "buffer_load_dwordx4 v[48:51], v0, s[4:7], s54 offen offset:0 sc1\n\t" \
    "s_waitcnt lgkmcnt(0)\n\t" \
    "s_barrier\n\t" \
    "ds_read_b64_tr_b16 v[212:213], v41 offset:0\n\t" \
    "ds_read_b64_tr_b16 v[214:215], v41 offset:256\n\t" \
    "ds_read_b64_tr_b16 v[252:253], v41 offset:8\n\t" \
    "ds_read_b64_tr_b16 v[254:255], v41 offset:264\n\t" \
    "s_waitcnt lgkmcnt(2)\n\t" \
    "v_mfma_f32_16x16x32_bf16 v[112:115], v[212:215], v[132:135], 0\n\t" \
    "s_waitcnt lgkmcnt(0)\n\t" \
    "v_mfma_f32_16x16x32_bf16 v[44:47], v[252:255], v[132:135], 0\n\t" \
    "v_mfma_f32_16x16x32_bf16 v[84:87], v[212:215], v[64:67], 0\n\t" \
    "v_mfma_f32_16x16x32_bf16 v[120:123], v[252:255], v[64:67], 0\n\t" \
    "s_nop 3\n\t" \
    "v_cvt_pk_bf16_f32 v60, v112, v113\n\t" \
    "v_cvt_pk_bf16_f32 v61, v114, v115\n\t" \
    "v_cvt_pk_bf16_f32 v62, v44, v45\n\t" \
    "v_cvt_pk_bf16_f32 v63, v46, v47\n\t" \
    "v_cvt_pk_bf16_f32 v100, v84, v85\n\t" \
    "v_cvt_pk_bf16_f32 v101, v86, v87\n\t" \
    "v_cvt_pk_bf16_f32 v102, v120, v121\n\t" \
    "v_cvt_pk_bf16_f32 v103, v122, v123\n\t" \
    "v_cndmask_b32 v164, v196, v60, s[20:21]\n\t" \
    "v_cndmask_b32 v165, v197, v61, s[20:21]\n\t" \
    "v_cndmask_b32 v166, v198, v62, s[20:21]\n\t" \
    "v_cndmask_b32 v167, v199, v63, s[20:21]\n\t" \
    "v_cndmask_b32 v220, v60, v196, s[20:21]\n\t" \
    "v_cndmask_b32 v221, v61, v197, s[20:21]\n\t" \
    "buffer_store_dwordx4 v[164:167], v14, s[8:11], 0 offen offset:0 sc1\n\t" \
    "v_cndmask_b32 v222, v62, v198, s[20:21]\n\t" \
    "v_cndmask_b32 v223, v63, v199, s[20:21]\n\t" \
    "buffer_store_dwordx4 v[220:223], v8, s[8:11], 0 offen offset:3072 sc1\n\t" \
    "s_waitcnt vmcnt(2)\n\t" \
    "v_and_b32 v13, v160, v162\n\t" \
    "v_bitop3_b32 v13, v13, v48, v50 bitop3:0x80\n\t" \
    "v_and_b32 v13, 1, v13\n\t" \
    "v_cmp_eq_u32 vcc, 0, v13\n\t" \
    "s_nop 1\n\t" \
    "s_cbranch_vccnz .Ls43_%=\n\t" \
    ".Lb43_%=:\n\t" \
    "ds_write_b128 v43, v[160:163] offset:16512\n\t" \
    "ds_write_b128 v43, v[48:51] offset:16640\n\t" \
    "s_waitcnt lgkmcnt(0)\n\t" \
    "s_barrier\n\t" \
    "ds_read_b64_tr_b16 v[104:105], v41 offset:16512\n\t" \
    "ds_read_b64_tr_b16 v[106:107], v41 offset:16768\n\t" \
    "ds_read_b64_tr_b16 v[168:169], v41 offset:16520\n\t" \
    "ds_read_b64_tr_b16 v[170:171], v41 offset:16776\n\t" \
    "s_waitcnt lgkmcnt(2)\n\t" \
    "v_mfma_f32_16x16x32_bf16 v[224:227], v[104:107], v[128:131], 0\n\t" \
    "s_waitcnt lgkmcnt(0)\n\t" \
    "v_mfma_f32_16x16x32_bf16 v[96:99], v[168:171], v[128:131], 0\n\t" \
    "v_mfma_f32_16x16x32_bf16 v[152:155], v[104:107], v[236:239], 0\n\t" \
    "v_mfma_f32_16x16x32_bf16 v[216:219], v[168:171], v[236:239], 0\n\t" \
    "s_nop 3\n\t" \
    "v_cvt_pk_bf16_f32 v124, v224, v225\n\t" \
    "v_cvt_pk_bf16_f32 v125, v226, v227\n\t" \
    "v_cvt_pk_bf16_f32 v126, v96, v97\n\t" \
    "v_cvt_pk_bf16_f32 v127, v98, v99\n\t" \
    "v_cvt_pk_bf16_f32 v228, v152, v153\n\t" \
    "v_cvt_pk_bf16_f32 v229, v154, v155\n\t" \
    "v_cvt_pk_bf16_f32 v230, v216, v217\n\t" \
    "v_cvt_pk_bf16_f32 v231, v218, v219\n\t" \
    "v_cndmask_b32 v72, v124, v56, s[20:21]\n\t" \
    "v_cndmask_b32 v73, v125, v57, s[20:21]\n\t" \
    "v_cndmask_b32 v74, v126, v58, s[20:21]\n\t" \
    "v_cndmask_b32 v75, v127, v59, s[20:21]\n\t" \
    "v_cndmask_b32 v172, v56, v124, s[20:21]\n\t" \
    "v_cndmask_b32 v173, v57, v125, s[20:21]\n\t" \
    "buffer_store_dwordx4 v[72:75], v14, s[8:11], 0 offen offset:128 sc1\n\t" \
    "v_cndmask_b32 v174, v58, v126, s[20:21]\n\t" \
    "v_cndmask_b32 v175, v59, v127, s[20:21]\n\t" \
    "buffer_store_dwordx4 v[172:175], v8, s[8:11], 0 offen offset:3200 sc1\n\t" \
    "v_cndmask_b32 v108, v100, v228, s[20:21]\n\t" \
    "v_cndmask_b32 v109, v101, v229, s[20:21]\n\t" \
    "v_cndmask_b32 v110, v102, v230, s[20:21]\n\t" \
    "v_cndmask_b32 v111, v103, v231, s[20:21]\n\t" \
    "v_cndmask_b32 v28, v228, v100, s[20:21]\n\t" \
    "v_cndmask_b32 v29, v229, v101, s[20:21]\n\t" \
    "buffer_store_dwordx4 v[108:111], v14, s[8:11], 0 offen offset:256 sc1\n\t" \
    "v_cndmask_b32 v30, v230, v102, s[20:21]\n\t" \
    "v_cndmask_b32 v31, v231, v103, s[20:21]\n\t" \
    "buffer_store_dwordx4 v[28:31], v8, s[8:11], 0 offen offset:3328 sc1\n\t" \
    "s_or_b32 s37, s2, s24\n\t" \
    "s_cbranch_scc1 .Lend_%=\n\t" \
    "s_mov_b32 s37, 34635776\n\t" \
    "v_mov_b32 v2, s34\n\t" \
    "s_mov_b64 exec, 1\n\t" \
    "buffer_store_dword v2, v0, s[4:7], s37 offen sc1\n\t" \
    "s_mov_b64 exec, -1\n\t" \
    ".Lend_%=:\n\t" \
    "s_endpgm\n\t" \
    ".Lq1024_%=:\n\t" \
    "s_mov_b32 s40, 0\n\t" \
    ".Lr1024_%=:\n\t" \
    "s_add_u32 s12, s55, 34603008\n\t" \
    "s_buffer_load_dword s13, s[4:7], s12 glc\n\t" \
    "s_buffer_load_dword s14, s[4:7], s12 offset:0x400 glc\n\t" \
    "s_waitcnt lgkmcnt(0)\n\t" \
    "s_xor_b32 s13, s13, s34\n\t" \
    "s_xor_b32 s14, s14, s34\n\t" \
    "s_or_b32 s13, s13, s14\n\t" \
    "s_cbranch_scc0 .Lb23_%=\n\t" \
    "s_add_u32 s40, s40, 1\n\t" \
    "s_cmp_lt_u32 s40, 1\n\t" \
    "s_cbranch_scc1 .Lr1024_%=\n\t" \
    "s_branch .Ls23_%=\n\t" \
    ".Lq1021_%=:\n\t" \
    "s_mov_b32 s40, 0\n\t" \
    ".Lr1021_%=:\n\t" \
    "s_nop 0\n\t" \
    "v_readlane_b32 s12, v15, 0\n\t" \
    "v_readlane_b32 s13, v15, 1\n\t" \
    "v_readlane_b32 s14, v15, 2\n\t" \
    "v_readlane_b32 s15, v15, 3\n\t" \
    "v_readlane_b32 s16, v15, 4\n\t" \
    "v_readlane_b32 s17, v15, 5\n\t" \
    "v_readlane_b32 s18, v15, 6\n\t" \
    "v_readlane_b32 s19, v15, 7\n\t" \
    "v_readlane_b32 s3, v15, 8\n\t" \
    "s_add_u32 s12, s12, s33\n\t" \
    "s_add_u32 s13, s13, s33\n\t" \
    "s_add_u32 s14, s14, s33\n\t" \
    "s_add_u32 s15, s15, s33\n\t" \
    "s_add_u32 s16, s16, s33\n\t" \
    "s_add_u32 s17, s17, s33\n\t" \
    "s_add_u32 s18, s18, s33\n\t" \
    "s_add_u32 s19, s19, s33\n\t" \
    "s_add_u32 s3, s3, s33\n\t" \
    "s_buffer_load_dword s12, s[4:7], s12 offset:0xc0000 glc\n\t" \
    "s_buffer_load_dword s13, s[4:7], s13 offset:0xc0000 glc\n\t" \
    "s_buffer_load_dword s14, s[4:7], s14 offset:0xc0000 glc\n\t" \
    "s_buffer_load_dword s15, s[4:7], s15 offset:0xc0000 glc\n\t" \
    "s_buffer_load_dword s16, s[4:7], s16 offset:0xc0000 glc\n\t" \
    "s_buffer_load_dword s17, s[4:7], s17 offset:0xc0000 glc\n\t" \
    "s_buffer_load_dword s18, s[4:7], s18 offset:0xc0000 glc\n\t" \
    "s_buffer_load_dword s19, s[4:7], s19 offset:0xc0000 glc\n\t" \
    "s_buffer_load_dword s3, s[4:7], s3 offset:0xc0000 glc\n\t" \
    "s_waitcnt lgkmcnt(0)\n\t" \
    "s_xor_b32 s12, s12, s34\n\t" \
    "s_xor_b32 s13, s13, s34\n\t" \
    "s_xor_b32 s14, s14, s34\n\t" \
    "s_xor_b32 s15, s15, s34\n\t" \
    "s_xor_b32 s16, s16, s34\n\t" \
    "s_xor_b32 s17, s17, s34\n\t" \
    "s_xor_b32 s18, s18, s34\n\t" \
    "s_xor_b32 s19, s19, s34\n\t" \
    "s_xor_b32 s3, s3, s34\n\t" \
    "s_or_b32 s12, s12, s13\n\t" \
    "s_or_b32 s12, s12, s14\n\t" \
    "s_or_b32 s12, s12, s15\n\t" \
    "s_or_b32 s12, s12, s16\n\t" \
    "s_or_b32 s12, s12, s17\n\t" \
    "s_or_b32 s12, s12, s18\n\t" \
    "s_or_b32 s12, s12, s19\n\t" \
    "s_or_b32 s12, s12, s3\n\t" \
    "s_cbranch_scc0 .Lb20_%=\n\t" \
    "s_add_u32 s40, s40, 1\n\t" \
    "s_cmp_lt_u32 s40, 1\n\t" \
    "s_cbranch_scc1 .Lr1021_%=\n\t" \
    "s_branch .Ls20_%=\n\t" \
    ".Lpb_%=:\n\t" \
    "s_sub_u32 s37, s24, 4\n\t" \
    "s_cbranch_scc1 .Lpe_%=\n\t" \
    "s_mul_i32 s38, s37, 0x2100\n\t" \
    "s_and_b32 s38, s38, 0x2300\n\t" \
    "s_add_u32 s38, s38, 34652160\n\t" \
    "s_mov_b32 s60, -1\n\t" \
    "s_mov_b32 s61, -1\n\t" \
    "s_memrealtime s[62:63]\n\t" \
    "s_mov_b64 exec, 0xff\n\t" \
    ".Lpc1_%=:\n\t" \
    "s_memtime s[56:57]\n\t" \
    "s_waitcnt lgkmcnt(0)\n\t" \
    "buffer_store_dwordx4 v[56:59], v0, s[4:7], s38 offen sc1\n\t" \
    "s_waitcnt vmcnt(0)\n\t" \
    "s_memtime s[58:59]\n\t" \
    "s_memrealtime s[22:23]\n\t" \
    "s_waitcnt lgkmcnt(0)\n\t" \
    "s_sub_u32 s56, s58, s56\n\t" \
    "s_min_u32 s56, s56, s60\n\t" \
    "s_mov_b32 s60, s61\n\t" \
    "s_mov_b32 s61, s56\n\t" \
    "s_xor_b32 s38, s38, 0x2000\n\t" \
    "s_add_u32 s38, s38, 0x400\n\t" \
    "s_andn2_b32 s38, s38, 0x1000\n\t" \
    "s_sub_u32 s22, s22, s62\n\t" \
    "s_cmp_lt_u32 s22, 60\n\t" \
    "s_cbranch_scc1 .Lpc1_%=\n\t" \
    "s_mov_b64 exec, -1\n\t" \
    "s_bitcmp1_b32 s38, 13\n\t" \
    "s_cselect_b32 s56, s61, s60\n\t" \
    "s_cselect_b32 s57, s60, s61\n\t" \
    "v_mov_b32 v56, s56\n\t" \
    "v_mov_b32 v57, s57\n\t" \
    "s_lshl3_add_u32 s37, s37, 156992\n\t" \
    "v_mov_b32 v58, s37\n\t" \
    "ds_write_b64 v58, v[56:57]\n\t" \
    "s_branch .Lpe_%=\n\t" \
    ".Lr2_%=:\n\t" \
    "s_add_u32 s40, s40, 1\n\t" \
    "s_cmp_gt_u32 s40, 32\n\t" \
    "s_cbranch_scc1 .Lr3_%=\n\t" \
    "s_sleep 2\n\t" \
    "s_and_b32 s37, s2, 7\n\t" \
    "s_lshl_b32 s37, s37, 10\n\t" \
    "s_add_u32 s37, s37, 34643968\n\t" \
    "v_and_b32 v34, 0x70, v0\n\t" \
    "v_lshlrev_b32 v34, 3, v34\n\t" \
    "buffer_load_dword v39, v34, s[4:7], s37 offen sc1\n\t" \
    "s_waitcnt vmcnt(0)\n\t" \
    "v_readlane_b32 s56, v39, 0\n\t" \
    "v_readlane_b32 s57, v39, 1\n\t" \
    "v_readlane_b32 s58, v39, 2\n\t" \
    "v_readlane_b32 s59, v39, 3\n\t" \
    "v_readlane_b32 s60, v39, 4\n\t" \
    "v_readlane_b32 s61, v39, 5\n\t" \
    "v_readlane_b32 s62, v39, 6\n\t" \
    "v_readlane_b32 s63, v39, 7\n\t" \
    "s_branch .Lr1_%=\n\t" \
    ".Lpp_%=:\n\t" \
    "ds_read_b128 v[112:115], v9 offset:64\n\t" \
    "ds_read_b128 v[116:119], v9 offset:80\n\t" \
    "s_getreg_b32 s37, hwreg(HW_REG_XCC_ID, 0, 2)\n\t" \
    "s_waitcnt lgkmcnt(0)\n\t" \
    "v_min3_u32 v112, v112, v114, v116\n\t" \
    "v_min3_u32 v113, v113, v115, v117\n\t" \
    "v_min_u32 v112, v112, v118\n\t" \
    "v_min_u32 v113, v113, v119\n\t" \
    "v_cmp_gt_u32 vcc, v112, v113\n\t" \
    "s_nop 4\n\t" \
    "s_and_b32 s38, vcc_lo, 4\n\t" \
    "s_add_u32 s37, s37, s38\n\t" \
    "s_add_u32 s37, s37, 1\n\t" \
    "s_and_b32 s37, s37, 7\n\t" \
    "s_lshl_b32 s37, s37, 1\n\t" \
    "s_xor_b32 s37, s37, s34\n\t" \
    "v_mov_b32 v112, s37\n\t" \
    "v_mov_b32 v113, s37\n\t" \
    "v_mov_b32 v114, s37\n\t" \
    "v_mov_b32 v115, s37\n\t" \
    "s_mov_b32 s39, 34636800\n\t" \
    "buffer_store_dwordx4 v[112:115], v0, s[4:7], s39 offen sc1\n\t" \
    "s_nop 1\n\t" \
    "s_branch .Lpq_%=\n\t" \
    ".Lr5_%=:\n\t" \
    "s_add_u32 s40, s40, 1\n\t" \
    "s_cmp_gt_u32 s40, 8192\n\t" \
    "s_cbranch_scc1 .Lr6_%=\n\t" \
    "s_sleep 4\n\t" \
    "v_mov_b32 v37, 0\n\t" \
    "buffer_load_dword v11, v37, s[4:7], s52 offen sc1\n\t" \
    "s_waitcnt vmcnt(0)\n\t" \
    "v_readfirstlane_b32 s36, v11\n\t" \
    "s_branch .Lr4_%=\n\t" \
    ".Ls20_%=:\n\t" \
    "s_mov_b32 s40, 0\n\t" \
    "s_add_u32 s38, s33, 786432\n\t" \
    ".Ll20_%=:\n\t" \
    "s_sleep 2\n\t" \
    "buffer_load_dword v35, v15, s[4:7], s38 offen sc1\n\t" \
    "s_waitcnt vmcnt(0)\n\t" \
    "v_cmp_ne_u32 vcc, s34, v35\n\t" \
    "s_add_u32 s40, s40, 1\n\t" \
    "s_cmp_ge_u32 s40, 8192\n\t" \
    "s_cbranch_scc1 .Lb20_%=\n\t" \
    "s_cbranch_vccnz .Ll20_%=\n\t" \
    "s_branch .Lb20_%=\n\t" \
    ".Ls23_%=:\n\t" \
    "s_mov_b32 s40, 0\n\t" \
    "s_add_u32 s38, s55, 34603008\n\t" \
    ".Ll23_%=:\n\t" \
    "s_sleep 2\n\t" \
    "buffer_load_dword v16, v38, s[4:7], s38 offen sc1\n\t" \
    "s_waitcnt vmcnt(0)\n\t" \
    "v_cmp_ne_u32 vcc, s34, v16\n\t" \
    "s_add_u32 s40, s40, 1\n\t" \
    "s_cmp_ge_u32 s40, 8192\n\t" \
    "s_cbranch_scc1 .Lb23_%=\n\t" \
    "s_cbranch_vccnz .Ll23_%=\n\t" \
    "s_branch .Lb23_%=\n\t" \
    ".Ls41_%=:\n\t" \
    "s_mov_b32 s40, 0\n\t" \
    ".Ll41_%=:\n\t" \
    "s_add_u32 s54, s53, 0\n\t" \
    "buffer_load_dwordx4 v[136:139], v0, s[4:7], s54 offen offset:0 sc1\n\t" \
    "s_add_u32 s54, s53, 4243456\n\t" \
    "buffer_load_dwordx4 v[36:39], v0, s[4:7], s54 offen offset:0 sc1\n\t" \
    "s_waitcnt vmcnt(0)\n\t" \
    "v_and_b32 v12, v136, v138\n\t" \
    "v_bitop3_b32 v12, v12, v36, v38 bitop3:0x80\n\t" \
    "v_and_b32 v12, 1, v12\n\t" \
    "v_cmp_eq_u32 vcc, 0, v12\n\t" \
    "s_add_u32 s40, s40, 1\n\t" \
    "s_cmp_ge_u32 s40, 8192\n\t" \
    "s_cbranch_scc1 .Lb41_%=\n\t" \
    "s_cbranch_vccnz .Ll41_%=\n\t" \
    "s_branch .Lb41_%=\n\t" \
    ".Ls42_%=:\n\t" \
    "s_mov_b32 s40, 0\n\t" \
    ".Ll42_%=:\n\t" \
    "s_add_u32 s54, s53, 524288\n\t" \
    "buffer_load_dwordx4 v[156:159], v0, s[4:7], s54 offen offset:0 sc1\n\t" \
    "s_add_u32 s54, s53, 4767744\n\t" \
    "buffer_load_dwordx4 v[208:211], v0, s[4:7], s54 offen offset:0 sc1\n\t" \
    "s_waitcnt vmcnt(0)\n\t" \
    "v_and_b32 v9, v156, v158\n\t" \
    "v_bitop3_b32 v9, v9, v208, v210 bitop3:0x80\n\t" \
    "v_and_b32 v9, 1, v9\n\t" \
    "v_cmp_eq_u32 vcc, 0, v9\n\t" \
    "s_add_u32 s40, s40, 1\n\t" \
    "s_cmp_ge_u32 s40, 8192\n\t" \
    "s_cbranch_scc1 .Lb42_%=\n\t" \
    "s_cbranch_vccnz .Ll42_%=\n\t" \
    "s_branch .Lb42_%=\n\t" \
    ".Ls43_%=:\n\t" \
    "s_mov_b32 s40, 0\n\t" \
    ".Ll43_%=:\n\t" \
    "s_add_u32 s54, s53, 1048576\n\t" \
    "buffer_load_dwordx4 v[160:163], v0, s[4:7], s54 offen offset:0 sc1\n\t" \
    "s_add_u32 s54, s53, 5292032\n\t" \
    "buffer_load_dwordx4 v[48:51], v0, s[4:7], s54 offen offset:0 sc1\n\t" \
    "s_waitcnt vmcnt(0)\n\t" \
    "v_and_b32 v13, v160, v162\n\t" \
    "v_bitop3_b32 v13, v13, v48, v50 bitop3:0x80\n\t" \
    "v_and_b32 v13, 1, v13\n\t" \
    "v_cmp_eq_u32 vcc, 0, v13\n\t" \
    "s_add_u32 s40, s40, 1\n\t" \
    "s_cmp_ge_u32 s40, 8192\n\t" \
    "s_cbranch_scc1 .Lb43_%=\n\t" \
    "s_cbranch_vccnz .Ll43_%=\n\t" \
    "s_branch .Lb43_%=\n\t" \
    ".Lfin_%=:\n\t"
#define K_CLOBBERS "v1", "v2", "v3", "v4", "v5", "v6", "v7", "v8", "v9", "v10", "v11", "v12", "v13", "v14", "v15", "v16", "v17", "v18", "v19", "v20", "v21", "v22", "v23", "v24", "v25", "v26", "v27", "v28", "v29", "v30", "v31", "v32", "v33", "v34", "v35", "v36", "v37", "v38", "v39", "v40", "v41", "v42", "v43", "v44", "v45", "v46", "v47", "v48", "v49", "v50", "v51", "v52", "v53", "v54", "v55", "v56", "v57", "v58", "v59", "v60", "v61", "v62", "v63", "v64", "v65", "v66", "v67", "v68", "v69", "v70", "v71", "v72", "v73", "v74", "v75", "v76", "v77", "v78", "v79", "v80", "v81", "v82", "v83", "v84", "v85", "v86", "v87", "v88", "v89", "v90", "v91", "v92", "v93", "v94", "v95", "v96", "v97", "v98", "v99", "v100", "v101", "v102", "v103", "v104", "v105", "v106", "v107", "v108", "v109", "v110", "v111", "v112", "v113", "v114", "v115", "v116", "v117", "v118", "v119", "v120", "v121", "v122", "v123", "v124", "v125", "v126", "v127", "v128", "v129", "v130", "v131", "v132", "v133", "v134", "v135", "v136", "v137", "v138", "v139", "v140", "v141", "v142", "v143", "v144", "v145", "v146", "v147", "v148", "v149", "v150", "v151", "v152", "v153", "v154", "v155", "v156", "v157", "v158", "v159", "v160", "v161", "v162", "v163", "v164", "v165", "v166", "v167", "v168", "v169", "v170", "v171", "v172", "v173", "v174", "v175", "v176", "v177", "v178", "v179", "v180", "v181", "v182", "v183", "v184", "v185", "v186", "v187", "v188", "v189", "v190", "v191", "v192", "v193", "v194", "v195", "v196", "v197", "v198", "v199", "v200", "v201", "v202", "v203", "v204", "v205", "v206", "v207", "v208", "v209", "v210", "v211", "v212", "v213", "v214", "v215", "v216", "v217", "v218", "v219", "v220", "v221", "v222", "v223", "v224", "v225", "v226", "v227", "v228", "v229", "v230", "v231", "v232", "v233", "v234", "v235", "v236", "v237", "v238", "v239", "v240", "v241", "v242", "v243", "v244", "v245", "v246", "v247", "v248", "v249", "v250", "v251", "v252", "v253", "v254", "v255", "s3", "s4", "s5", "s6", "s7", "s8", "s9", "s10", "s11", "s12", "s13", "s14", "s15", "s16", "s17", "s18", "s19", "s20", "s21", "s22", "s23", "s24", "s25", "s26", "s27", "s28", "s29", "s30", "s31", "s32", "s33", "s34", "s35", "s36", "s37", "s38", "s39", "s40", "s41", "s42", "s43", "s44", "s45", "s46", "s47", "s48", "s49", "s50", "s51", "s52", "s53", "s54", "s55", "s56", "s57", "s58", "s59", "s60", "s61", "s62", "s63", "s64", "s65"

extern "C" __global__ __launch_bounds__(512, 2)
void k_conv(const char* __restrict__ x, const char* __restrict__ wgt, char* __restrict__ ws, char* __restrict__ out)
{
  __shared__ __attribute__((aligned(256))) char smem[157568];
  unsigned tid = threadIdx.x;
  auto kp = __builtin_amdgcn_kernarg_segment_ptr();
  asm volatile(K_BODY : "+{v0}"(tid), "+{s[0:1]}"(kp)
               : "{s2}"(blockIdx.x), "i"((__attribute__((address_space(3))) char*)smem)
               : "memory", "scc", "vcc", K_CLOBBERS);
}

extern "C" void kernel_launch(void* const* d_in, const int* in_sizes, int n_in,
                              void* d_out, int out_size, void* d_ws, size_t ws_size,
                              hipStream_t stream) {
  k_conv<<<dim3(256), dim3(512), 0, stream>>>((const char*)d_in[0], (const char*)d_in[1], (char*)d_ws, (char*)d_out);
}
```

```cpp
#include <hip/hip_runtime.h>
#include <hip/hip_bf16.h>


#define K_BODY \
    "s_load_dwordx2 s[16:17], s[0:1], 0x0\n\t" \
    "s_load_dwordx2 s[4:5], s[0:1], 0x10\n\t" \
    "s_load_dwordx2 s[12:13], s[0:1], 0x8\n\t" \
    "s_load_dwordx2 s[8:9], s[0:1], 0x18\n\t" \
    "s_getpc_b64 s[22:23]\n\t" \
    ".Lpc_%=:\n\t" \
    "s_cmp_ge_u32 s2, 16\n\t" \
    "s_cbranch_scc1 .Le1_%=\n\t" \
    "s_mov_b32 m0, 157312\n\t" \
    "v_lshlrev_b32 v8, 6, v0\n\t" \
    "v_min_u32 v8, .Lfin_%=-.Lpc_%=, v8\n\t" \
    "global_load_lds_dword v8, s[22:23]\n\t" \
    ".Le1_%=:\n\t" \
    "v_readfirstlane_b32 s24, v0\n\t" \
    "s_and_b32 s25, s2, 7\n\t" \
    "s_lshl_b32 s25, s25, 5\n\t" \
    "s_lshr_b32 s37, s2, 3\n\t" \
    "s_add_u32 s25, s25, s37\n\t" \
    "s_min_u32 s28, s25, 251\n\t" \
    "s_mul_i32 s26, s28, 57\n\t" \
    "s_lshr_b32 s26, s26, 9\n\t" \
    "s_mul_i32 s37, s26, 9\n\t" \
    "s_sub_u32 s27, s28, s37\n\t" \
    "v_and_b32 v0, 63, v0\n\t" \
    "v_lshlrev_b32 v0, 4, v0\n\t" \
    "s_lshr_b32 s24, s24, 6\n\t" \
    "s_sub_u32 s37, s24, 4\n\t" \
    "s_cbranch_scc1 .Le2_%=\n\t" \
    "s_cmp_ge_u32 s25, 252\n\t" \
    "s_cbranch_scc1 .Le2_%=\n\t" \
    "s_mul_i32 m0, s37, 12416\n\t" \
    "s_lshl2_add_u32 s37, s26, s37\n\t" \
    "s_mul_i32 s37, s37, 86016\n\t" \
    "s_mul_i32 s38, s27, 4992\n\t" \
    "s_add_u32 s37, s37, s38\n\t" \
    "s_addk_i32 s37, 896\n\t" \
    "v_add_u32 v9, s37, v0\n\t" \
    "s_cmp_eq_u32 s27, 0\n\t" \
    "s_cselect_b32 s38, 43008, 0\n\t" \
    "s_sub_u32 s39, s38, 2048\n\t" \
    "v_add_u32 v7, s39, v9\n\t" \
    "v_subrev_u32 v8, 0x400, v9\n\t" \
    "s_mov_b64 exec, 0xff\n\t" \
    "v_add_u32 v8, s38, v8\n\t" \
    "s_mov_b64 exec, -1\n\t" \
    "s_mov_b32 s18, 9633792\n\t" \
    "s_mov_b32 s19, 0x20000\n\t" \
    "s_mov_b32 s39, 43008\n\t" \
    "s_waitcnt lgkmcnt(0)\n\t" \
    "buffer_load_dwordx4 v7, s[16:19], 0 offen offset:0 nt lds\n\t" \
    "s_add_u32 m0, m0, 0x400\n\t" \
    "s_nop 0\n\t" \
    "buffer_load_dwordx4 v8, s[16:19], 0 offen offset:0 nt lds\n\t" \
    "s_add_u32 m0, m0, 0x400\n\t" \
    "s_nop 0\n\t" \
    "buffer_load_dwordx4 v9, s[16:19], 0 offen offset:0 nt lds\n\t" \
    "s_add_u32 m0, m0, 32\n\t" \
    "s_nop 0\n\t" \
    "buffer_load_dwordx4 v9, s[16:19], 0 offen offset:1024 nt lds\n\t" \
    "buffer_load_dwordx4 v9, s[16:19], 0 offen offset:2048 nt lds\n\t" \
    "buffer_load_dwordx4 v9, s[16:19], 0 offen offset:3072 nt lds\n\t" \
    "s_add_u32 m0, m0, 0x1020\n\t" \
    "s_nop 0\n\t" \
    "buffer_load_dwordx4 v7, s[16:19], s39 offen offset:0 nt lds\n\t" \
    "s_add_u32 m0, m0, 0x400\n\t" \
    "s_nop 0\n\t" \
    "buffer_load_dwordx4 v8, s[16:19], s39 offen offset:0 nt lds\n\t" \
    "s_add_u32 m0, m0, 0x400\n\t" \
    "s_nop 0\n\t" \
    "buffer_load_dwordx4 v9, s[16:19], s39 offen offset:0 nt lds\n\t" \
    "s_add_u32 m0, m0, 32\n\t" \
    "s_nop 0\n\t" \
    "buffer_load_dwordx4 v9, s[16:19], s39 offen offset:1024 nt lds\n\t" \
    "buffer_load_dwordx4 v9, s[16:19], s39 offen offset:2048 nt lds\n\t" \
    "buffer_load_dwordx4 v9, s[16:19], s39 offen offset:3072 nt lds\n\t" \
    ".Le2_%=:\n\t" \
    "s_mov_b32 s6, 34668544\n\t" \
    "s_mov_b32 s7, 0x20000\n\t" \
    "v_lshrrev_b32 v8, 7, v0\n\t" \
    "v_and_b32 v9, 0x70, v0\n\t" \
    "v_lshl_or_b32 v2, v8, 15, v9\n\t" \
    "s_lshl_b32 s32, s2, 7\n\t" \
    "s_add_u32 s32, s32, 33554432\n\t" \
    "s_cmp_ge_u32 s24, 4\n\t" \
    "s_cbranch_scc1 .Le3_%=\n\t" \
    "v_mov_b32 v12, 0\n\t" \
    "v_mov_b32 v13, 0\n\t" \
    "v_mov_b32 v14, 0\n\t" \
    "v_mov_b32 v15, 0\n\t" \
    "s_lshl_b32 s37, s24, 18\n\t" \
    "s_add_u32 s37, s37, s32\n\t" \
    "s_xor_b32 s38, s24, 2\n\t" \
    "s_lshr_b32 s39, s2, 3\n\t" \
    "s_or_b32 s38, s38, s39\n\t" \
    "s_memrealtime s[22:23]\n\t" \
    "s_waitcnt lgkmcnt(0)\n\t" \
    "s_cbranch_scc1 .Le4_%=\n\t" \
    "v_lshl_or_b32 v8, v8, 10, v9\n\t" \
    "v_mov_b32 v16, s22\n\t" \
    "v_mov_b32 v17, s22\n\t" \
    "v_mov_b32 v18, s22\n\t" \
    "v_mov_b32 v19, s22\n\t" \
    "s_add_u32 s38, s32, 1089536\n\t" \
    "buffer_store_dwordx4 v[16:19], v8, s[4:7], s38 offen sc1\n\t" \
    ".Le4_%=:\n\t" \
    "\n\t" \
    ".Le3_%=:\n\t" \
    "s_mul_i32 s30, s28, 293\n\t" \
    "s_lshr_b32 s30, s30, 11\n\t" \
    "s_mul_i32 s37, s30, 7\n\t" \
    "s_sub_u32 s28, s28, s37\n\t" \
    "s_mul_i32 s29, s30, 43\n\t" \
    "s_lshr_b32 s29, s29, 7\n\t" \
    "s_mul_i32 s37, s29, 3\n\t" \
    "s_sub_u32 s30, s30, s37\n\t" \
    "s_and_b32 s37, s29, 12\n\t" \
    "s_add_u32 s37, s37, s29\n\t" \
    "s_mul_i32 s31, s37, 768\n\t" \
    "s_lshl_b32 s37, s30, 6\n\t" \
    "s_add_u32 s31, s31, s37\n\t" \
    "s_add_u32 s41, s29, s30\n\t" \
    "s_add_u32 s41, s41, s28\n\t" \
    "s_and_b32 s41, s41, 3\n\t" \
    "s_cmp_lt_u32 s25, 252\n\t" \
    "s_cselect_b32 s14, 903168, 0\n\t" \
    "s_mov_b32 s15, 0x20000\n\t" \
    "v_lshrrev_b32 v10, 6, v0\n\t" \
    "v_lshrrev_b32 v11, 9, v0\n\t" \
    "v_sub_u32 v8, v10, v11\n\t" \
    "v_mul_u32_u24 v8, 18432, v8\n\t" \
    "v_and_b32 v9, 0x30, v0\n\t" \
    "v_lshlrev_b32 v11, 5, v11\n\t" \
    "v_bitop3_b32 v1, v8, v9, v11 bitop3:0xf6\n\t" \
    "s_mov_b32 exec_lo, 0xf0000000\n\t" \
    "s_mov_b32 exec_hi, 0xf0000000\n\t" \
    "v_bfrev_b32 v1, 1\n\t" \
    "s_mov_b64 exec, -1\n\t" \
    "s_cmp_ge_u32 s24, 4\n\t" \
    "s_cbranch_scc1 .Le9_%=\n\t" \
    "v_bfe_u32 v20, v0, 4, 4\n\t" \
    "v_and_b32 v10, 12, v10\n\t" \
    "v_cvt_f32_ubyte0 v21, v10\n\t" \
    "v_and_b32 v22, 1, v20\n\t" \
    "v_cvt_f32_ubyte0 v22, v22\n\t" \
    "v_mul_f32 v22, 0x3e800000, v22\n\t" \
    "v_lshrrev_b32 v23, 1, v20\n\t" \
    "v_mov_b32 v32, 0\n\t" \
    "v_mov_b32 v33, -1\n\t" \
    "v_mov_b32 v31, 0x3e800000\n\t" \
    "s_mov_b32 s65, 0\n\t" \
    "s_mov_b32 s40, 0\n\t" \
    "s_cmp_eq_u32 s24, 1\n\t" \
    "s_cbranch_scc1 .Lm1_%=\n\t" \
    "s_cmp_eq_u32 s24, 2\n\t" \
    "s_cbranch_scc1 .Lm2_%=\n\t" \
    "s_cmp_eq_u32 s24, 3\n\t" \
    "s_cbranch_scc1 .Lm3_%=\n\t" \
    "s_add_u32 s37, s32, 1048576\n\t" \
    "s_mov_b64 exec, 0xff\n\t" \
    "\n\t" \
    "s_mov_b64 exec, -1\n\t" \
    "v_or_b32 v24, 1, v20\n\t" \
    "v_cvt_f32_ubyte0 v24, v24\n\t" \
    "v_mul_f32 v24, 0x3c800000, v24\n\t" \
    "v_lshrrev_b32 v25, 4, v0\n\t" \
    "v_and_b32 v25, 16, v25\n\t" \
    "v_lshrrev_b32 v26, 2, v11\n\t" \
    "v_or_b32 v25, v25, v26\n\t" \
    "v_cvt_f32_ubyte0 v25, v25\n\t" \
    "v_add_f32 v26, 2.0, v25\n\t" \
    "v_add_f32 v27, 4.0, v25\n\t" \
    "v_add_f32 v28, 4.0, v26\n\t" \
    "v_mov_b32 v29, v22\n\t" \
    "v_mov_b32 v30, v22\n\t" \
    "v_mov_b32 v31, v24\n\t" \
    "s_mov_b32 s64, 0x3e800000\n\t" \
    "s_mov_b32 s65, 0x3e800000\n\t" \
    "v_add_u32 v34, 154880, v0\n\t" \
    "s_branch .Ltl_%=\n\t" \
    ".Lm1_%=:\n\t" \
    "v_lshrrev_b32 v24, 5, v0\n\t" \
    "v_and_b32 v24, 12, v24\n\t" \
    "v_and_or_b32 v24, v20, 3, v24\n\t" \
    "v_cvt_f32_ubyte0 v24, v24\n\t" \
    "v_mul_f32 v24, 0x3d924925, v24\n\t" \
    "v_mul_f32 v32, 0x41000000, v24\n\t" \
    "v_lshrrev_b32 v27, 3, v11\n\t" \
    "v_cvt_f32_ubyte0 v27, v27\n\t" \
    "v_add_f32 v25, -2.0, v27\n\t" \
    "v_add_f32 v26, -1.0, v27\n\t" \
    "v_add_f32 v28, 1.0, v27\n\t" \
    "v_and_b32 v29, 4, v20\n\t" \
    "v_cvt_f32_ubyte0 v29, v29\n\t" \
    "v_mul_f32 v29, 0x3d800000, v29\n\t" \
    "v_mov_b32 v30, v29\n\t" \
    "v_mov_b32 v31, 0xbe800000\n\t" \
    "s_mov_b32 s64, 0\n\t" \
    "s_mov_b32 exec_hi, 0\n\t" \
    "v_mov_b32 v33, 0\n\t" \
    "s_mov_b64 exec, -1\n\t" \
    "v_add_u32 v34, 149760, v0\n\t" \
    "s_branch .Ltl_%=\n\t" \
    ".Lm2_%=:\n\t" \
    "v_lshl_add_u32 v24, v20, 1, 6\n\t" \
    "v_cvt_f32_ubyte0 v24, v24\n\t" \
    "v_mul_f32 v24, 0x3d000000, v24\n\t" \
    "v_add_f32 v25, 0.5, v21\n\t" \
    "v_add_f32 v26, 1.0, v25\n\t" \
    "v_add_f32 v27, 2.0, v25\n\t" \
    "v_add_f32 v28, 2.0, v26\n\t" \
    "v_mov_b32 v29, 0\n\t" \
    "v_mov_b32 v30, 0\n\t" \
    "s_mov_b32 s64, 0x3d000000\n\t" \
    "v_add_u32 v34, 152832, v0\n\t" \
    "s_branch .Ltl_%=\n\t" \
    ".Lm3_%=:\n\t" \
    "s_mov_b32 s37, 34635776\n\t" \
    "buffer_load_dword v3, v12, s[4:7], s37 offen sc1\n\t" \
    "v_lshrrev_b32 v8, 7, v0\n\t" \
    "v_mul_u32_u24 v8, 96, v8\n\t" \
    "v_and_b32 v9, 16, v0\n\t" \
    "v_bfe_u32 v10, v0, 5, 2\n\t" \
    "v_add3_u32 v8, v8, v9, v10\n\t" \
    "v_mul_u32_u24 v8, 192, v8\n\t" \
    "s_mul_i32 s37, s28, 129024\n\t" \
    "s_add_u32 s37, s37, s31\n\t" \
    "s_mov_b32 m0, 157312\n\t" \
    "s_mov_b32 exec_hi, 0xffffff\n\t" \
    "buffer_load_dword v8, s[12:15], s37 offen  lds\n\t" \
    "s_mov_b64 exec, -1\n\t" \
    "v_lshrrev_b32 v8, 2, v0\n\t" \
    "v_and_b32 v8, 28, v8\n\t" \
    "v_add_u32 v8, 156928, v8\n\t" \
    "ds_write_b32 v8, v12\n\t" \
    "v_add_u32 v24, 6, v23\n\t" \
    "v_cvt_f32_ubyte0 v24, v24\n\t" \
    "v_mul_f32 v24, 0x3d924925, v24\n\t" \
    "v_mov_b32 v25, v21\n\t" \
    "v_add_f32 v26, 1.0, v21\n\t" \
    "v_add_f32 v27, 2.0, v21\n\t" \
    "v_add_f32 v28, 2.0, v26\n\t" \
    "v_sub_f32 v29, 0, v22\n\t" \
    "v_sub_f32 v30, 0, v22\n\t" \
    "s_mov_b32 exec_lo, 0\n\t" \
    "v_fma_f32 v25, v21, 0.5, 4.0\n\t" \
    "v_add_f32 v27, 1.0, v25\n\t" \
    "v_fmaak_f32 v26, v21, v31, 0x41200000\n\t" \
    "v_mov_b32 v28, v26\n\t" \
    "v_add_f32 v30, v31, v30\n\t" \
    "s_mov_b64 exec, -1\n\t" \
    "s_mov_b32 s40, 0x11\n\t" \
    "v_add_u32 v34, 151808, v0\n\t" \
    ".Ltl_%=:\n\t" \
    "v_fma_f32 v36, v24, v25, v29\n\t" \
    "v_fma_f32 v38, v24, v26, v29\n\t" \
    "v_fma_f32 v40, v24, v27, v29\n\t" \
    "v_fma_f32 v42, v24, v28, v30\n\t" \
    "v_add_f32 v37, v36, v31\n\t" \
    "v_add_f32 v39, v38, v31\n\t" \
    "v_add_f32 v41, v40, v31\n\t" \
    "v_add_f32 v43, v42, v31\n\t" \
    "v_cos_f32 v36, v36\n\t" \
    "v_cos_f32 v37, v37\n\t" \
    "v_cos_f32 v38, v38\n\t" \
    "v_cos_f32 v39, v39\n\t" \
    "v_cos_f32 v40, v40\n\t" \
    "v_cos_f32 v41, v41\n\t" \
    "v_cos_f32 v42, v42\n\t" \
    "v_cos_f32 v43, v43\n\t" \
    "s_bitcmp0_b32 s40, 4\n\t" \
    "s_cbranch_scc1 .Lt2_%=\n\t" \
    "v_mul_f32 v36, 0x3b924925, v36\n\t" \
    "v_mul_f32 v37, 0x3b924925, v37\n\t" \
    "v_mul_f32 v38, 0x3b924925, v38\n\t" \
    "v_mul_f32 v39, 0x3b924925, v39\n\t" \
    "v_mul_f32 v40, 0x3b924925, v40\n\t" \
    "v_mul_f32 v41, 0x3b924925, v41\n\t" \
    "v_mul_f32 v42, 0x3b924925, v42\n\t" \
    "v_mul_f32 v43, 0x3b924925, v43\n\t" \
    ".Lt2_%=:\n\t" \
    "v_cvt_pk_bf16_f32 v44, v36, v37\n\t" \
    "v_cvt_pk_bf16_f32 v45, v38, v39\n\t" \
    "v_cvt_pk_bf16_f32 v46, v40, v41\n\t" \
    "v_cvt_pk_bf16_f32 v47, v42, v43\n\t" \
    "v_and_b32 v44, v33, v44\n\t" \
    "v_and_b32 v45, v33, v45\n\t" \
    "v_mov_b32 v33, -1\n\t" \
    "v_add_f32 v24, s64, v24\n\t" \
    "v_add_f32 v31, s65, v31\n\t" \
    "v_add_f32 v29, v32, v29\n\t" \
    "v_add_f32 v30, v32, v30\n\t" \
    "ds_write_b128 v34, v[44:47]\n\t" \
    "s_add_u32 s40, s40, 1\n\t" \
    "s_bitcmp1_b32 s40, 0\n\t" \
    "v_add_u32 v34, 0x400, v34\n\t" \
    "s_cbranch_scc1 .Ltl_%=\n\t" \
    "s_bitcmp1_b32 s40, 2\n\t" \
    "s_cbranch_scc1 .Lt3_%=\n\t" \
    "s_mov_b32 s40, 2\n\t" \
    "s_lshl2_add_u32 s37, s28, s24\n\t" \
    "s_lshl_b32 s37, s37, 2\n\t" \
    "v_lshrrev_b32 v25, 2, v20\n\t" \
    "v_add_u32 v25, s37, v25\n\t" \
    "v_mul_u32_u24 v26, 147, v25\n\t" \
    "v_lshrrev_b32 v26, 10, v26\n\t" \
    "v_lshl_or_b32 v24, v26, 1, 1\n\t" \
    "v_cvt_f32_ubyte0 v24, v24\n\t" \
    "v_mul_f32 v24, 0x3c800000, v24\n\t" \
    "v_mul_u32_u24 v26, 7, v26\n\t" \
    "v_sub_u32 v25, v25, v26\n\t" \
    "v_xor_b32 v22, s41, v20\n\t" \
    "v_bfe_u32 v32, v22, 1, 1\n\t" \
    "v_and_b32 v22, 1, v22\n\t" \
    "v_cvt_f32_ubyte0 v22, v22\n\t" \
    "v_mul_f32 v22, 0x3e800000, v22\n\t" \
    "v_lshl_or_b32 v32, v25, 1, v32\n\t" \
    "v_cvt_f32_ubyte0 v32, v32\n\t" \
    "v_mul_f32 v32, 0x3e924925, v32\n\t" \
    "v_mul_f32 v29, 0x3d800000, v21\n\t" \
    "v_fma_f32 v29, v29, v32, v22\n\t" \
    "v_mov_b32 v30, v29\n\t" \
    "v_mov_b32 v31, v24\n\t" \
    "v_mov_b32 v25, 0\n\t" \
    "v_mov_b32 v26, 2.0\n\t" \
    "v_mov_b32 v27, 4.0\n\t" \
    "v_mov_b32 v28, 0x40c00000\n\t" \
    "s_mov_b32 s64, 0\n\t" \
    "s_mov_b32 s65, 0\n\t" \
    "s_lshl_b32 s37, s24, 11\n\t" \
    "s_add_u32 s37, s37, 49664\n\t" \
    "v_add_u32 v34, s37, v0\n\t" \
    "s_branch .Ltl_%=\n\t" \
    ".Lt3_%=:\n\t" \
    ".Le9_%=:\n\t" \
    "s_lshr_b32 s37, s24, 2\n\t" \
    "s_and_b32 s38, s24, 2\n\t" \
    "s_mul_i32 s39, s37, 24832\n\t" \
    "s_lshl4_add_u32 s39, s38, s39\n\t" \
    "v_lshrrev_b32 v4, 6, v0\n\t" \
    "v_and_b32 v5, 12, v4\n\t" \
    "v_add_u32 v6, v4, v5\n\t" \
    "v_lshrrev_b32 v7, 1, v0\n\t" \
    "v_and_b32 v7, 24, v7\n\t" \
    "v_and_b32 v4, 11, v4\n\t" \
    "v_mul_u32_u24 v4, 192, v4\n\t" \
    "v_bfe_u32 v5, v0, 8, 1\n\t" \
    "v_mul_u32_u24 v5, 3104, v5\n\t" \
    "v_add3_u32 v4, v4, v5, v7\n\t" \
    "v_add_u32 v8, s39, v4\n\t" \
    "v_mov_b32 v9, 156928\n\t" \
    "v_add_u32 v10, 149760, v0\n\t" \
    "s_lshl2_add_u32 s37, s37, s38\n\t" \
    "s_lshl_b32 s37, s37, 7\n\t" \
    "s_mul_i32 s53, s27, 28\n\t" \
    "s_add_u32 s53, s53, s26\n\t" \
    "s_andn2_b32 s39, s53, 7\n\t" \
    "s_add_u32 s53, s53, s39\n\t" \
    "s_lshl_b32 s53, s53, 10\n\t" \
    "s_add_u32 s37, s37, s53\n\t" \
    "s_and_b32 s38, s24, 1\n\t" \
    "s_lshl_b32 s39, s38, 13\n\t" \
    "s_add_u32 s37, s37, s39\n\t" \
    "s_cmp_lt_u32 s25, 252\n\t" \
    "s_cselect_b32 s43, 0, 0x80000000\n\t" \
    "s_add_u32 s37, s37, s43\n\t" \
    "v_lshrrev_b32 v11, 8, v0\n\t" \
    "v_and_b32 v12, 0xf0, v0\n\t" \
    "v_mul_u32_u24 v4, 8486912, v11\n\t" \
    "v_add3_u32 v13, v4, v12, s37\n\t" \
    "s_lshl_b32 s39, s38, 10\n\t" \
    "v_add_u32 v14, s39, v10\n\t" \
    "s_lshl_b32 s39, s38, 5\n\t" \
    "s_add_u32 s39, s39, 116736\n\t" \
    "v_lshl_add_u32 v4, v6, 6, v7\n\t" \
    "v_lshrrev_b32 v5, 3, v0\n\t" \
    "v_and_b32 v5, 32, v5\n\t" \
    "v_xor_b32 v5, s39, v5\n\t" \
    "v_add_u32 v15, v4, v5\n\t" \
    "s_and_b32 s37, s24, 6\n\t" \
    "s_lshl_b32 s37, s37, 10\n\t" \
    "v_add_u32 v16, s37, v0\n\t" \
    "s_waitcnt lgkmcnt(0)\n\t" \
    "s_barrier\n\t" \
    "ds_read_b128 v[20:23], v14 offset:5120\n\t" \
    "ds_read_b128 v[24:27], v16 offset:49664\n\t" \
    "ds_read_b128 v[28:31], v16 offset:50688\n\t" \
    "s_and_b32 s33, s2, 7\n\t" \
    "s_lshl_b32 s33, s33, 15\n\t" \
    "s_add_u32 s33, s33, 33554432\n\t" \
    "s_lshr_b32 s37, s29, 2\n\t" \
    "s_mul_i32 s39, s30, 3\n\t" \
    "s_add_u32 s37, s37, s39\n\t" \
    "s_mul_i32 s37, s37, 114688\n\t" \
    "s_and_b32 s39, s29, 3\n\t" \
    "s_lshl_b32 s39, s39, 8\n\t" \
    "s_add_u32 s37, s37, s39\n\t" \
    "s_add_u32 s37, s37, s43\n\t" \
    "s_add_u32 s37, s37, 2097152\n\t" \
    "s_and_b32 s39, s24, 6\n\t" \
    "s_lshl3_add_u32 s39, s28, s39\n\t" \
    "s_lshl_b32 s39, s39, 1\n\t" \
    "v_add_u32 v14, s39, v11\n\t" \
    "v_mul_u32_u24 v17, 147, v14\n\t" \
    "v_lshrrev_b32 v17, 10, v17\n\t" \
    "s_and_b32 s39, s24, 1\n\t" \
    "v_subrev_u32 v18, s39, v17\n\t" \
    "v_mul_i32_i24 v18, 7, v18\n\t" \
    "v_sub_u32 v14, v14, v18\n\t" \
    "v_and_b32 v18, 14, v14\n\t" \
    "v_add_lshl_u32 v14, v14, v18, 12\n\t" \
    "v_and_b32 v18, 7, v17\n\t" \
    "v_mul_u32_u24 v18, 4243456, v18\n\t" \
    "v_add3_u32 v14, v14, v18, v12\n\t" \
    "v_and_b32 v17, 8, v17\n\t" \
    "v_xor_b32 v17, 8, v17\n\t" \
    "v_lshl_add_u32 v14, v17, 10, v14\n\t" \
    "v_add_u32 v19, s37, v14\n\t" \
    "s_lshl_b32 s43, s41, 10\n\t" \
    "s_cmp_lt_u32 s30, 3\n\t" \
    "s_cselect_b64 s[20:21], -1, 0\n\t" \
    "s_and_b32 s37, s2, 0x70\n\t" \
    "s_lshl_b32 s37, s37, 1\n\t" \
    "s_lshl2_add_u32 s57, s24, s37\n\t" \
    "s_min_u32 s58, s57, 248\n\t" \
    "s_mul_i32 s59, s58, 147\n\t" \
    "s_lshr_b32 s59, s59, 12\n\t" \
    "s_mul_i32 s37, s59, 28\n\t" \
    "s_sub_u32 s60, s58, s37\n\t" \
    "s_andn2_b32 s39, s58, 7\n\t" \
    "s_add_u32 s51, s58, s39\n\t" \
    "s_lshl_b32 s51, s51, 10\n\t" \
    "s_sub_u32 s37, s60, 1\n\t" \
    "s_cselect_b32 s52, 27, -1\n\t" \
    "s_add_u32 s52, s52, s58\n\t" \
    "s_andn2_b32 s39, s52, 7\n\t" \
    "s_add_u32 s52, s52, s39\n\t" \
    "s_lshl_b32 s52, s52, 10\n\t" \
    "v_lshrrev_b32 v16, 9, v0\n\t" \
    "v_and_b32 v18, 0x2f0, v0\n\t" \
    "v_and_b32 v17, 1, v11\n\t" \
    "v_lshl_add_u32 v14, v17, 11, v18\n\t" \
    "v_add_u32 v32, s51, v14\n\t" \
    "v_add_u32 v33, s52, v0\n\t" \
    "s_mul_i32 s44, s24, 7296\n\t" \
    "s_lshl_b32 s37, s24, 2\n\t" \
    "s_lshl1_add_u32 s39, s24, 8\n\t" \
    "s_min_u32 s61, s37, s39\n\t" \
    "s_add_u32 s37, s37, 2\n\t" \
    "s_add_u32 s39, s24, 20\n\t" \
    "s_cmp_lt_u32 s24, 4\n\t" \
    "s_cselect_b32 s62, s37, s39\n\t" \
    "s_bfe_u32 s63, s2, 0x10003\n\t" \
    "s_mul_i32 s47, s63, 28\n\t" \
    "s_add_u32 s45, s47, s61\n\t" \
    "s_add_u32 s46, s47, s62\n\t" \
    "s_andn2_b32 s39, s45, 7\n\t" \
    "s_add_u32 s45, s45, s39\n\t" \
    "s_lshl_b32 s45, s45, 10\n\t" \
    "s_andn2_b32 s39, s46, 7\n\t" \
    "s_add_u32 s46, s46, s39\n\t" \
    "s_lshl_b32 s46, s46, 10\n\t" \
    "v_lshlrev_b32 v34, 4, v11\n\t" \
    "v_lshrrev_b32 v35, 6, v12\n\t" \
    "v_mul_u32_u24 v35, 7296, v35\n\t" \
    "v_and_b32 v18, 16, v0\n\t" \
    "v_mul_u32_u24 v18, 226, v18\n\t" \
    "v_and_b32 v17, 32, v0\n\t" \
    "v_lshl_add_u32 v18, v17, 1, v18\n\t" \
    "v_add3_u32 v35, v35, v18, v34\n\t" \
    "s_lshl_b32 s37, s61, 7\n\t" \
    "v_add_u32 v14, s37, v35\n\t" \
    "s_and_b32 s37, s62, 30\n\t" \
    "s_lshl_b32 s37, s37, 7\n\t" \
    "v_add_u32 v36, s37, v35\n\t" \
    "s_mov_b32 s1, 0\n\t" \
    "s_mov_b32 s41, 0x18000\n\t" \
    "s_lshl_b32 s37, s63, 9\n\t" \
    "s_add_u32 s51, s51, s37\n\t" \
    "s_cmp_lt_u32 s57, 252\n\t" \
    "s_cselect_b32 s51, s51, 0x80000000\n\t" \
    "s_and_b32 s37, s24, 3\n\t" \
    "s_mul_i32 s37, s37, 8486912\n\t" \
    "s_add_u32 s53, s53, s37\n\t" \
    "s_and_b32 s37, s24, 4\n\t" \
    "s_lshl_b32 s37, s37, 11\n\t" \
    "s_add_u32 s53, s53, s37\n\t" \
    "s_and_b32 s37, s2, 0x87\n\t" \
    "s_lshl4_add_u32 s37, s24, s37\n\t" \
    "s_lshl_b32 s55, s37, 7\n\t" \
    "v_and_b32 v37, 16, v0\n\t" \
    "v_lshlrev_b32 v38, 6, v37\n\t" \
    "s_lshl3_add_u32 s37, s26, s24\n\t" \
    "s_mul_i32 s37, s37, 43008\n\t" \
    "s_mul_i32 s39, s27, 4992\n\t" \
    "s_add_u32 s37, s37, s39\n\t" \
    "s_add_u32 s8, s8, s37\n\t" \
    "s_addc_u32 s9, s9, 0\n\t" \
    "s_cmp_eq_u32 s27, 8\n\t" \
    "s_movk_i32 s10, 4992\n\t" \
    "s_cselect_b32 s10, 3072, s10\n\t" \
    "s_mov_b32 s11, 0x20000\n\t" \
    "v_and_b32 v39, 0x70, v0\n\t" \
    "v_mad_u32_u24 v39, v39, 24, v34\n\t" \
    "v_lshrrev_b32 v17, 1, v0\n\t" \
    "v_and_or_b32 v18, v17, 64, v39\n\t" \
    "v_and_b32 v35, 0x1f0, v0\n\t" \
    "v_lshl_add_u32 v35, v16, 11, v35\n\t" \
    "v_add_u32 v40, s51, v35\n\t" \
    "s_mul_i32 s37, s24, 2064\n\t" \
    "v_and_b32 v35, 0x300, v0\n\t" \
    "v_add3_u32 v41, v35, v0, s37\n\t" \
    "s_lshl_b32 s37, s37, 1\n\t" \
    "v_lshlrev_b32 v35, 7, v16\n\t" \
    "v_xor_b32 v35, v35, v0\n\t" \
    "v_add_u32 v42, s37, v35\n\t" \
    "v_lshrrev_b32 v35, 5, v12\n\t" \
    "v_mul_u32_u24 v35, 2064, v35\n\t" \
    "v_lshl_add_u32 v35, v37, 2, v35\n\t" \
    "s_lshl_b32 s37, s24, 8\n\t" \
    "v_add3_u32 v43, v35, v34, s37\n\t" \
    "v_bfe_u32 v35, v0, 7, 1\n\t" \
    "v_lshl_add_u32 v35, v11, 2, v35\n\t" \
    "v_mul_u32_u24 v35, 2064, v35\n\t" \
    "v_lshrrev_b32 v39, 3, v0\n\t" \
    "v_and_or_b32 v35, v39, 8, v35\n\t" \
    "s_and_b32 s37, s24, 3\n\t" \
    "s_lshl_b32 s37, s37, 6\n\t" \
    "s_and_b32 s39, s24, 4\n\t" \
    "s_lshl_b32 s39, s39, 8\n\t" \
    "s_or_b32 s37, s37, s39\n\t" \
    "v_and_or_b32 v39, v0, 32, s37\n\t" \
    "v_mul_u32_u24 v37, 40, v37\n\t" \
    "v_xor_b32 v39, v39, v37\n\t" \
    "v_add_u32 v17, v35, v39\n\t" \
    "s_and_b32 s37, s2, 0x70\n\t" \
    "s_lshl_b32 s37, s37, 1\n\t" \
    "v_lshrrev_b32 v16, 7, v0\n\t" \
    "v_lshl_add_u32 v16, v16, 2, s37\n\t" \
    "v_min_u32 v16, 248, v16\n\t" \
    "v_mul_u32_u24 v12, 147, v16\n\t" \
    "v_lshrrev_b32 v12, 12, v12\n\t" \
    "v_mul_u32_u24 v34, 28, v12\n\t" \
    "v_sub_u32 v16, v16, v34\n\t" \
    "v_bfe_u32 v34, v0, 4, 3\n\t" \
    "v_min_u32 v34, 4, v34\n\t" \
    "v_add3_u32 v16, v16, v34, -1\n\t" \
    "v_add_u32 v34, 28, v16\n\t" \
    "v_min_u32 v16, v16, v34\n\t" \
    "v_mad_u32_u24 v16, v16, 9, v12\n\t" \
    "v_and_b32 v11, 31, v16\n\t" \
    "v_lshrrev_b32 v37, 5, v16\n\t" \
    "v_lshlrev_b32 v37, 7, v37\n\t" \
    "v_lshl_or_b32 v35, v11, 10, v37\n\t" \
    "ds_read_b128 v[44:47], v10 offset:0\n\t" \
    "ds_read_b128 v[48:51], v10 offset:1024\n\t" \
    "s_and_b32 s37, s24, 1\n\t" \
    "s_lshl_b32 s37, s37, 4\n\t" \
    "s_lshr_b32 s39, s24, 1\n\t" \
    "s_add_u32 s37, s37, s39\n\t" \
    "s_mul_i32 s37, s37, 192\n\t" \
    "s_add_u32 s37, s37, s31\n\t" \
    "s_lshl_b32 s39, s24, 12\n\t" \
    "s_add_u32 m0, s39, 116736\n\t" \
    "s_add_u32 s38, s37, 0\n\t" \
    "buffer_load_dwordx4 v1, s[12:15], s38 offen  lds\n\t" \
    "s_add_u32 m0, s39, 117760\n\t" \
    "s_add_u32 s38, s37, 258048\n\t" \
    "buffer_load_dwordx4 v1, s[12:15], s38 offen  lds\n\t" \
    "s_add_u32 m0, s39, 118784\n\t" \
    "s_add_u32 s38, s37, 516096\n\t" \
    "buffer_load_dwordx4 v1, s[12:15], s38 offen  lds\n\t" \
    "s_add_u32 m0, s39, 119808\n\t" \
    "s_add_u32 s38, s37, 774144\n\t" \
    "buffer_load_dwordx4 v1, s[12:15], s38 offen  lds\n\t" \
    "s_cmp_lg_u32 s24, 3\n\t" \
    "s_cbranch_scc1 .Lr9_%=\n\t" \
    "s_and_b32 s37, s2, 7\n\t" \
    "s_lshl_b32 s37, s37, 10\n\t" \
    "s_add_u32 s37, s37, 34643968\n\t" \
    "s_buffer_load_dword s56, s[4:7], s37 offset:0x0\n\t" \
    "s_buffer_load_dword s57, s[4:7], s37 offset:0x80\n\t" \
    "s_buffer_load_dword s58, s[4:7], s37 offset:0x100\n\t" \
    "s_buffer_load_dword s59, s[4:7], s37 offset:0x180\n\t" \
    "s_buffer_load_dword s60, s[4:7], s37 offset:0x200\n\t" \
    "s_buffer_load_dword s61, s[4:7], s37 offset:0x280\n\t" \
    "s_buffer_load_dword s62, s[4:7], s37 offset:0x300\n\t" \
    "s_buffer_load_dword s63, s[4:7], s37 offset:0x380\n\t" \
    "s_mov_b32 s40, 0\n\t" \
    ".Lr1_%=:\n\t" \
    "s_waitcnt lgkmcnt(0)\n\t" \
    "s_memrealtime s[22:23]\n\t" \
    "s_waitcnt lgkmcnt(0)\n\t" \
    "s_mov_b32 s37, 1000\n\t" \
    "s_mov_b32 s38, 0\n\t" \
    "s_sub_u32 s39, s22, s56\n\t" \
    "s_min_u32 s37, s37, s39\n\t" \
    "s_max_u32 s38, s38, s39\n\t" \
    "s_sub_u32 s39, s22, s57\n\t" \
    "s_min_u32 s37, s37, s39\n\t" \
    "s_max_u32 s38, s38, s39\n\t" \
    "s_sub_u32 s39, s22, s58\n\t" \
    "s_min_u32 s37, s37, s39\n\t" \
    "s_max_u32 s38, s38, s39\n\t" \
    "s_sub_u32 s39, s22, s59\n\t" \
    "s_min_u32 s37, s37, s39\n\t" \
    "s_max_u32 s38, s38, s39\n\t" \
    "s_sub_u32 s39, s22, s60\n\t" \
    "s_min_u32 s37, s37, s39\n\t" \
    "s_max_u32 s38, s38, s39\n\t" \
    "s_sub_u32 s39, s22, s61\n\t" \
    "s_min_u32 s37, s37, s39\n\t" \
    "s_max_u32 s38, s38, s39\n\t" \
    "s_sub_u32 s39, s22, s62\n\t" \
    "s_min_u32 s37, s37, s39\n\t" \
    "s_max_u32 s38, s38, s39\n\t" \
    "s_sub_u32 s39, s22, s63\n\t" \
    "s_min_u32 s37, s37, s39\n\t" \
    "s_max_u32 s38, s38, s39\n\t" \
    "s_cmp_gt_u32 s38, 1000\n\t" \
    "s_cbranch_scc1 .Lr2_%=\n\t" \
    ".Lr3_%=:\n\t" \
    "s_sub_u32 s35, s22, s37\n\t" \
    "s_waitcnt vmcnt(5)\n\t" \
    "v_readfirstlane_b32 s34, v3\n\t" \
    "s_mul_i32 s34, s34, 1664525\n\t" \
    "s_add_u32 s34, s34, 1013904223\n\t" \
    "s_or_b32 s34, s34, 1\n\t" \
    "v_mov_b32 v52, s34\n\t" \
    "v_mov_b32 v53, s35\n\t" \
    "ds_write_b64 v9, v[52:53] offset:32\n\t" \
    ".Lr9_%=:\n\t" \
    "s_cmp_eq_u32 s2, 255\n\t" \
    "s_cbranch_scc1 .Lpb_%=\n\t" \
    ".Lpe_%=:\n\t" \
    "s_waitcnt vmcnt(4)\n\t" \
    "s_waitcnt lgkmcnt(0)\n\t" \
    "s_barrier\n\t" \
    "ds_read_b64_tr_b16 v[60:61], v8 offset:0\n\t" \
    "ds_read_b64_tr_b16 v[62:63], v8 offset:768\n\t" \
    "ds_read_b64_tr_b16 v[64:65], v8 offset:12416\n\t" \
    "ds_read_b64_tr_b16 v[66:67], v8 offset:13184\n\t" \
    "ds_read_b64_tr_b16 v[68:69], v8 offset:6208\n\t" \
    "ds_read_b64_tr_b16 v[70:71], v8 offset:6976\n\t" \
    "ds_read_b64_tr_b16 v[72:73], v8 offset:18624\n\t" \
    "ds_read_b64_tr_b16 v[74:75], v8 offset:19392\n\t" \
    "ds_read_b128 v[76:79], v9 offset:32\n\t" \
    "s_waitcnt lgkmcnt(7)\n\t" \
    "v_mfma_f32_16x16x32_bf16 v[80:83], v[20:23], v[60:63], 0\n\t" \
    "s_waitcnt lgkmcnt(5)\n\t" \
    "v_mfma_f32_16x16x32_bf16 v[84:87], v[20:23], v[64:67], 0\n\t" \
    "s_waitcnt lgkmcnt(3)\n\t" \
    "v_mfma_f32_16x16x32_bf16 v[88:91], v[20:23], v[68:71], 0\n\t" \
    "s_waitcnt lgkmcnt(1)\n\t" \
    "v_mfma_f32_16x16x32_bf16 v[92:95], v[20:23], v[72:75], 0\n\t" \
    "ds_read_b64_tr_b16 v[96:97], v8 offset:64\n\t" \
    "ds_read_b64_tr_b16 v[98:99], v8 offset:832\n\t" \
    "ds_read_b64_tr_b16 v[100:101], v8 offset:12480\n\t" \
    "ds_read_b64_tr_b16 v[102:103], v8 offset:13248\n\t" \
    "ds_read_b64_tr_b16 v[104:105], v8 offset:6272\n\t" \
    "ds_read_b64_tr_b16 v[106:107], v8 offset:7040\n\t" \
    "ds_read_b64_tr_b16 v[108:109], v8 offset:18688\n\t" \
    "ds_read_b64_tr_b16 v[110:111], v8 offset:19456\n\t" \
    "s_waitcnt lgkmcnt(8)\n\t" \
    "v_readfirstlane_b32 s34, v76\n\t" \
    "v_readfirstlane_b32 s35, v77\n\t" \
    "s_lshl_b32 s37, s24, 8\n\t" \
    "s_add_u32 s37, s37, s2\n\t" \
    "s_cmp_eq_u32 s37, 255\n\t" \
    "s_cbranch_scc1 .Lpp_%=\n\t" \
    ".Lpq_%=:\n\t" \
    "v_add_u32 v80, 0x8000, v80\n\t" \
    "v_add_u32 v82, 0x8000, v82\n\t" \
    "v_add_u32 v84, 0x8000, v84\n\t" \
    "v_add_u32 v86, 0x8000, v86\n\t" \
    "v_cvt_pk_bf16_f32 v120, v80, v81\n\t" \
    "v_cvt_pk_bf16_f32 v122, v84, v85\n\t" \
    "v_cvt_pk_bf16_f32 v121, v88, v89\n\t" \
    "v_cvt_pk_bf16_f32 v123, v92, v93\n\t" \
    "v_and_b32 v120, -2, v120\n\t" \
    "v_and_b32 v122, -2, v122\n\t" \
    "v_cvt_pk_bf16_f32 v124, v82, v83\n\t" \
    "v_cvt_pk_bf16_f32 v126, v86, v87\n\t" \
    "v_cvt_pk_bf16_f32 v125, v90, v91\n\t" \
    "v_cvt_pk_bf16_f32 v127, v94, v95\n\t" \
    "v_and_b32 v124, -2, v124\n\t" \
    "v_and_b32 v126, -2, v126\n\t" \
    "ds_read_b64_tr_b16 v[128:129], v8 offset:128\n\t" \
    "ds_read_b64_tr_b16 v[130:131], v8 offset:896\n\t" \
    "ds_read_b64_tr_b16 v[132:133], v8 offset:12544\n\t" \
    "ds_read_b64_tr_b16 v[134:135], v8 offset:13312\n\t" \
    "ds_read_b64_tr_b16 v[136:137], v8 offset:6336\n\t" \
    "ds_read_b64_tr_b16 v[138:139], v8 offset:7104\n\t" \
    "ds_read_b64_tr_b16 v[140:141], v8 offset:18752\n\t" \
    "ds_read_b64_tr_b16 v[142:143], v8 offset:19520\n\t" \
    ".Lh25_%=:\n\t" \
    "s_memrealtime s[22:23]\n\t" \
    "s_waitcnt lgkmcnt(0)\n\t" \
    "s_sub_u32 s22, s22, s35\n\t" \
    "s_cmp_lt_u32 s22, 205\n\t" \
    "s_cbranch_scc1 .Lh25_%=\n\t" \
    "buffer_store_dwordx4 v[120:123], v13, s[4:7], 0 offen offset:0 sc1\n\t" \
    "s_mov_b32 s42, 4243456\n\t" \
    "buffer_store_dwordx4 v[124:127], v13, s[4:7], s42 offen offset:0 sc1\n\t" \
    "s_lshl_b32 s37, s24, 18\n\t" \
    "s_add_u32 s37, s37, s32\n\t" \
    "s_bitcmp1_b32 29, s24\n\t" \
    "s_cselect_b32 s39, 0, 0x80000000\n\t" \
    "v_add_u32 v12, s39, v2\n\t" \
    "v_mov_b32 v144, 0\n\t" \
    "v_mov_b32 v145, 0\n\t" \
    "v_mov_b32 v146, 0\n\t" \
    "v_mov_b32 v147, 0\n\t" \
    "s_cmp_eq_u32 s24, 4\n\t" \
    "s_cselect_b64 exec, 0xff, -1\n\t" \
    "buffer_store_dwordx4 v[144:147], v12, s[4:7], s37 offen sc1\n\t" \
    "s_mov_b64 exec, -1\n\t" \
    "v_mfma_f32_16x16x32_bf16 v[148:151], v[20:23], v[96:99], 0\n\t" \
    "v_mfma_f32_16x16x32_bf16 v[152:155], v[20:23], v[100:103], 0\n\t" \
    "v_mfma_f32_16x16x32_bf16 v[156:159], v[20:23], v[104:107], 0\n\t" \
    "v_mfma_f32_16x16x32_bf16 v[160:163], v[20:23], v[108:111], 0\n\t" \
    "v_mfma_f32_16x16x32_bf16 v[164:167], v[20:23], v[128:131], 0\n\t" \
    "v_mfma_f32_16x16x32_bf16 v[168:171], v[20:23], v[132:135], 0\n\t" \
    "v_mfma_f32_16x16x32_bf16 v[172:175], v[20:23], v[136:139], 0\n\t" \
    "v_mfma_f32_16x16x32_bf16 v[176:179], v[20:23], v[140:143], 0\n\t" \
    "s_nop 0\n\t" \
    "v_add_u32 v148, 0x8000, v148\n\t" \
    "v_add_u32 v150, 0x8000, v150\n\t" \
    "v_add_u32 v152, 0x8000, v152\n\t" \
    "v_add_u32 v154, 0x8000, v154\n\t" \
    "v_cvt_pk_bf16_f32 v180, v148, v149\n\t" \
    "v_cvt_pk_bf16_f32 v182, v152, v153\n\t" \
    "v_cvt_pk_bf16_f32 v181, v156, v157\n\t" \
    "v_cvt_pk_bf16_f32 v183, v160, v161\n\t" \
    "v_and_b32 v180, -2, v180\n\t" \
    "v_and_b32 v182, -2, v182\n\t" \
    "v_cvt_pk_bf16_f32 v184, v150, v151\n\t" \
    "v_cvt_pk_bf16_f32 v186, v154, v155\n\t" \
    "v_cvt_pk_bf16_f32 v185, v158, v159\n\t" \
    "v_cvt_pk_bf16_f32 v187, v162, v163\n\t" \
    "v_and_b32 v184, -2, v184\n\t" \
    "v_and_b32 v186, -2, v186\n\t" \
    "v_add_u32 v164, 0x8000, v164\n\t" \
    "v_add_u32 v166, 0x8000, v166\n\t" \
    "v_add_u32 v168, 0x8000, v168\n\t" \
    "v_add_u32 v170, 0x8000, v170\n\t" \
    "v_cvt_pk_bf16_f32 v188, v164, v165\n\t" \
    "v_cvt_pk_bf16_f32 v190, v168, v169\n\t" \
    "v_cvt_pk_bf16_f32 v189, v172, v173\n\t" \
    "v_cvt_pk_bf16_f32 v191, v176, v177\n\t" \
    "v_and_b32 v188, -2, v188\n\t" \
    "v_and_b32 v190, -2, v190\n\t" \
    "v_cvt_pk_bf16_f32 v4, v166, v167\n\t" \
    "v_cvt_pk_bf16_f32 v6, v170, v171\n\t" \
    "v_cvt_pk_bf16_f32 v5, v174, v175\n\t" \
    "v_cvt_pk_bf16_f32 v7, v178, v179\n\t" \
    "v_and_b32 v4, -2, v4\n\t" \
    "v_and_b32 v6, -2, v6\n\t" \
    "v_mov_b32 v52, s34\n\t" \
    "v_mov_b32 v53, s34\n\t" \
    "v_mov_b32 v54, s34\n\t" \
    "v_mov_b32 v55, s34\n\t" \
    "s_waitcnt vmcnt(3)\n\t" \
    "; wait\n\t" \
    "s_waitcnt lgkmcnt(0)\n\t" \
    "s_barrier\n\t" \
    "ds_read_b64_tr_b16 v[56:57], v15 offset:0\n\t" \
    "ds_read_b64_tr_b16 v[58:59], v15 offset:256\n\t" \
    "ds_read_b64_tr_b16 v[60:61], v15 offset:2048\n\t" \
    "ds_read_b64_tr_b16 v[62:63], v15 offset:2304\n\t" \
    "ds_read_b64_tr_b16 v[64:65], v15 offset:4096\n\t" \
    "ds_read_b64_tr_b16 v[66:67], v15 offset:4352\n\t" \
    "ds_read_b64_tr_b16 v[68:69], v15 offset:6144\n\t" \
    "ds_read_b64_tr_b16 v[70:71], v15 offset:6400\n\t" \
    "ds_read_b64_tr_b16 v[72:73], v15 offset:8192\n\t" \
    "ds_read_b64_tr_b16 v[74:75], v15 offset:8448\n\t" \
    "ds_read_b64_tr_b16 v[76:77], v15 offset:10240\n\t" \
    "ds_read_b64_tr_b16 v[78:79], v15 offset:10496\n\t" \
    "ds_read_b64_tr_b16 v[112:113], v15 offset:12288\n\t" \
    "ds_read_b64_tr_b16 v[114:115], v15 offset:12544\n\t" \
    "s_waitcnt lgkmcnt(12)\n\t" \
    "v_mfma_f32_16x16x32_bf16 v[116:119], v[24:27], v[56:59], 0\n\t" \
    "ds_read_b64_tr_b16 v[80:81], v15 offset:14336\n\t" \
    "ds_read_b64_tr_b16 v[82:83], v15 offset:14592\n\t" \
    "s_waitcnt lgkmcnt(12)\n\t" \
    "v_mfma_f32_16x16x32_bf16 v[116:119], v[28:31], v[60:63], v[116:119]\n\t" \
    "ds_read_b64_tr_b16 v[84:85], v15 offset:16384\n\t" \
    "ds_read_b64_tr_b16 v[86:87], v15 offset:16640\n\t" \
    "s_waitcnt lgkmcnt(12)\n\t" \
    "v_mfma_f32_16x16x32_bf16 v[88:91], v[24:27], v[64:67], 0\n\t" \
    "ds_read_b64_tr_b16 v[92:93], v15 offset:18432\n\t" \
    "ds_read_b64_tr_b16 v[94:95], v15 offset:18688\n\t" \
    "s_waitcnt lgkmcnt(12)\n\t" \
    "v_mfma_f32_16x16x32_bf16 v[88:91], v[28:31], v[68:71], v[88:91]\n\t" \
    "ds_read_b64_tr_b16 v[120:121], v15 offset:20480\n\t" \
    "ds_read_b64_tr_b16 v[122:123], v15 offset:20736\n\t" \
    "s_waitcnt lgkmcnt(12)\n\t" \
    "v_mfma_f32_16x16x32_bf16 v[124:127], v[24:27], v[72:75], 0\n\t" \
    "ds_read_b64_tr_b16 v[144:145], v15 offset:22528\n\t" \
    "ds_read_b64_tr_b16 v[146:147], v15 offset:22784\n\t" \
    "s_waitcnt lgkmcnt(12)\n\t" \
    "v_mfma_f32_16x16x32_bf16 v[124:127], v[28:31], v[76:79], v[124:127]\n\t" \
    "ds_read_b64_tr_b16 v[96:97], v15 offset:24576\n\t" \
    "ds_read_b64_tr_b16 v[98:99], v15 offset:24832\n\t" \
    "s_waitcnt lgkmcnt(12)\n\t" \
    "v_mfma_f32_16x16x32_bf16 v[100:103], v[24:27], v[112:115], 0\n\t" \
    "ds_read_b64_tr_b16 v[104:105], v15 offset:26624\n\t" \
    "ds_read_b64_tr_b16 v[106:107], v15 offset:26880\n\t" \
    "s_waitcnt lgkmcnt(12)\n\t" \
    "v_mfma_f32_16x16x32_bf16 v[100:103], v[28:31], v[80:83], v[100:103]\n\t" \
    "ds_read_b64_tr_b16 v[108:109], v15 offset:28672\n\t" \
    "ds_read_b64_tr_b16 v[110:111], v15 offset:28928\n\t" \
    ".Lh26_%=:\n\t" \
    "s_memrealtime s[22:23]\n\t" \
    "s_waitcnt lgkmcnt(0)\n\t" \
    "s_sub_u32 s22, s22, s35\n\t" \
    "s_cmp_lt_u32 s22, 255\n\t" \
    "s_cbranch_scc1 .Lh26_%=\n\t" \
    "s_mov_b32 s42, 524288\n\t" \
    "buffer_store_dwordx4 v[180:183], v13, s[4:7], s42 offen offset:0 sc1\n\t" \
    "s_mov_b32 s42, 4767744\n\t" \
    "buffer_store_dwordx4 v[184:187], v13, s[4:7], s42 offen offset:0 sc1\n\t" \
    "v_mfma_f32_16x16x32_bf16 v[128:131], v[24:27], v[84:87], 0\n\t" \
    "ds_read_b64_tr_b16 v[132:133], v15 offset:30720\n\t" \
    "ds_read_b64_tr_b16 v[134:135], v15 offset:30976\n\t" \
    "v_mfma_f32_16x16x32_bf16 v[128:131], v[28:31], v[92:95], v[128:131]\n\t" \
    "v_cvt_pk_bf16_f32 v136, v116, v88\n\t" \
    "v_cvt_pk_bf16_f32 v20, v117, v89\n\t" \
    "v_mfma_f32_16x16x32_bf16 v[140:143], v[24:27], v[120:123], 0\n\t" \
    "v_cvt_pk_bf16_f32 v148, v118, v90\n\t" \
    "v_cvt_pk_bf16_f32 v152, v119, v91\n\t" \
    "v_mfma_f32_16x16x32_bf16 v[140:143], v[28:31], v[144:147], v[140:143]\n\t" \
    "v_cvt_pk_bf16_f32 v137, v124, v100\n\t" \
    "v_cvt_pk_bf16_f32 v21, v125, v101\n\t" \
    "v_mfma_f32_16x16x32_bf16 v[156:159], v[24:27], v[96:99], 0\n\t" \
    "v_cvt_pk_bf16_f32 v149, v126, v102\n\t" \
    "v_cvt_pk_bf16_f32 v153, v127, v103\n\t" \
    "v_mfma_f32_16x16x32_bf16 v[156:159], v[28:31], v[104:107], v[156:159]\n\t" \
    "v_mfma_f32_16x16x32_bf16 v[160:163], v[24:27], v[108:111], 0\n\t" \
    "s_waitcnt lgkmcnt(0)\n\t" \
    "v_mfma_f32_16x16x32_bf16 v[160:163], v[28:31], v[132:135], v[160:163]\n\t" \
    "v_cvt_pk_bf16_f32 v138, v128, v140\n\t" \
    "v_cvt_pk_bf16_f32 v22, v129, v141\n\t" \
    "v_cvt_pk_bf16_f32 v150, v130, v142\n\t" \
    "v_cvt_pk_bf16_f32 v154, v131, v143\n\t" \
    "s_nop 3\n\t" \
    "v_cvt_pk_bf16_f32 v139, v156, v160\n\t" \
    "v_cvt_pk_bf16_f32 v23, v157, v161\n\t" \
    "v_cvt_pk_bf16_f32 v151, v158, v162\n\t" \
    "v_cvt_pk_bf16_f32 v155, v159, v163\n\t" \
    ".Lh27_%=:\n\t" \
    "s_memrealtime s[22:23]\n\t" \
    "s_waitcnt lgkmcnt(0)\n\t" \
    "s_sub_u32 s22, s22, s35\n\t" \
    "s_cmp_lt_u32 s22, 305\n\t" \
    "s_cbranch_scc1 .Lh27_%=\n\t" \
    "s_mov_b32 s42, 1048576\n\t" \
    "buffer_store_dwordx4 v[188:191], v13, s[4:7], s42 offen offset:0 sc1\n\t" \
    "s_mov_b32 s42, 5292032\n\t" \
    "buffer_store_dwordx4 v[4:7], v13, s[4:7], s42 offen offset:0 sc1\n\t" \
    ".Lh28_%=:\n\t" \
    "s_memrealtime s[22:23]\n\t" \
    "s_waitcnt lgkmcnt(0)\n\t" \
    "s_sub_u32 s22, s22, s35\n\t" \
    "s_cmp_lt_u32 s22, 370\n\t" \
    "s_cbranch_scc1 .Lh28_%=\n\t" \
    "s_cmp_eq_u32 s24, 0\n\t" \
    "s_cbranch_scc1 .Lj30_%=\n\t" \
    ".Lh29_%=:\n\t" \
    "s_memrealtime s[22:23]\n\t" \
    "s_waitcnt lgkmcnt(0)\n\t" \
    "s_sub_u32 s22, s22, s35\n\t" \
    "s_cmp_lt_u32 s22, 385\n\t" \
    "s_cbranch_scc1 .Lh29_%=\n\t" \
    ".Lj30_%=:\n\t" \
    "s_waitcnt lgkmcnt(0)\n\t" \
    "s_xor_b32 s42, s43, 0\n\t" \
    "buffer_store_dwordx4 v[136:139], v19, s[4:7], s42 offen offset:0 sc1\n\t" \
    "s_waitcnt vmcnt(1)\n\t" \
    "s_mov_b64 exec, 1\n\t" \
    "ds_add_rtn_u32 v16, v9, v9 offset:8\n\t" \
    "s_add_u32 s37, s32, 524288\n\t" \
    "s_waitcnt lgkmcnt(0)\n\t" \
    "v_cmp_eq_u32 vcc, 1098496, v16\n\t" \
    "s_mov_b64 exec, -1\n\t" \
    "s_nop 0\n\t" \
    "s_cbranch_vccz .La13_%=\n\t" \
    "buffer_store_dwordx4 v[52:55], v2, s[4:7], s37 offen sc1\n\t" \
    ".La13_%=:\n\t" \
    "s_mov_b64 exec, -1\n\t" \
    "s_xor_b32 s42, s43, 1024\n\t" \
    "buffer_store_dwordx4 v[20:23], v19, s[4:7], s42 offen offset:0 sc1\n\t" \
    "s_cmp_eq_u32 s24, 0\n\t" \
    "s_cbranch_scc1 .Lj32_%=\n\t" \
    ".Lh31_%=:\n\t" \
    "s_memrealtime s[22:23]\n\t" \
    "s_waitcnt lgkmcnt(0)\n\t" \
    "s_sub_u32 s22, s22, s35\n\t" \
    "s_cmp_lt_u32 s22, 435\n\t" \
    "s_cbranch_scc1 .Lh31_%=\n\t" \
    ".Lj32_%=:\n\t" \
    "s_waitcnt lgkmcnt(0)\n\t" \
    "s_xor_b32 s42, s43, 2048\n\t" \
    "buffer_store_dwordx4 v[148:151], v19, s[4:7], s42 offen offset:0 sc1\n\t" \
    "s_xor_b32 s42, s43, 3072\n\t" \
    "buffer_store_dwordx4 v[152:155], v19, s[4:7], s42 offen offset:0 sc1\n\t" \
    ".Lh33_%=:\n\t" \
    "s_memrealtime s[22:23]\n\t" \
    "s_waitcnt lgkmcnt(0)\n\t" \
    "s_sub_u32 s22, s22, s35\n\t" \
    "s_cmp_lt_u32 s22, 390\n\t" \
    "s_cbranch_scc1 .Lh33_%=\n\t" \
    "s_and_b32 s52, s2, 7\n\t" \
    "s_lshl_b32 s52, s52, 7\n\t" \
    "s_add_u32 s52, s52, 34636800\n\t" \
    "s_buffer_load_dword s36, s[4:7], s52\n\t" \
    "s_mov_b32 s40, 0\n\t" \
    "s_waitcnt lgkmcnt(0)\n\t" \
    ".Lr4_%=:\n\t" \
    "s_xor_b32 s36, s36, s34\n\t" \
    "s_andn2_b32 s37, s36, 14\n\t" \
    "s_cbranch_scc1 .Lr5_%=\n\t" \
    ".Lr6_%=:\n\t" \
    "s_bfe_u32 s36, s36, 0x30001\n\t" \
    "s_add_u32 s56, s2, s36\n\t" \
    "s_and_b32 s56, s56, 7\n\t" \
    "s_lshr_b32 s37, s2, 7\n\t" \
    "s_lshl1_add_u32 s56, s56, s37\n\t" \
    "s_and_b32 s37, s56, 7\n\t" \
    "s_mul_i32 s48, s37, 4243456\n\t" \
    "s_and_b32 s37, s56, 8\n\t" \
    "s_lshl_b32 s37, s37, 10\n\t" \
    "s_add_u32 s48, s48, s37\n\t" \
    "s_add_u32 s49, s48, 524288\n\t" \
    "s_add_u32 s50, s48, 1048576\n\t" \
    "s_xor_b32 s37, s48, 0x2000\n\t" \
    "s_add_u32 s37, s37, 2097152\n\t" \
    "s_add_u32 s45, s45, s37\n\t" \
    "s_add_u32 s46, s46, s37\n\t" \
    "s_cmp_lt_u32 s24, 4\n\t" \
    "s_cselect_b32 s47, s46, 0x80000000\n\t" \
    "v_add_u32 v1, s45, v0\n\t" \
    "v_add_u32 v3, s46, v0\n\t" \
    "v_add_u32 v39, s47, v0\n\t" \
    "s_lshr_b32 s37, s24, 2\n\t" \
    "s_mul_i32 s37, s37, 6\n\t" \
    "s_mul_i32 s39, s56, 7\n\t" \
    "s_add_u32 s37, s37, s39\n\t" \
    "s_lshr_b32 s37, s37, 4\n\t" \
    "s_and_b32 s39, s24, 3\n\t" \
    "s_mul_i32 s39, s39, 63\n\t" \
    "s_add_u32 s37, s37, s39\n\t" \
    "v_lshrrev_b32 v34, 4, v0\n\t" \
    "v_min_u32 v34, 8, v34\n\t" \
    "v_mad_u32_u24 v34, v34, 7, s37\n\t" \
    "v_and_b32 v8, 31, v34\n\t" \
    "v_lshrrev_b32 v12, 5, v34\n\t" \
    "v_lshlrev_b32 v12, 7, v12\n\t" \
    "v_lshl_or_b32 v15, v8, 10, v12\n\t" \
    "s_sub_u32 s37, s24, s36\n\t" \
    "s_and_b32 s37, s37, 7\n\t" \
    "s_mul_i32 s39, s27, 28\n\t" \
    "s_add_u32 s39, s39, s26\n\t" \
    "s_lshr_b32 s39, s39, 5\n\t" \
    "s_lshl4_add_u32 s37, s39, s37\n\t" \
    "s_lshl_b32 s52, s37, 7\n\t" \
    "s_nop 0\n\t" \
    "v_readlane_b32 s12, v15, 0\n\t" \
    "v_readlane_b32 s13, v15, 1\n\t" \
    "v_readlane_b32 s14, v15, 2\n\t" \
    "v_readlane_b32 s15, v15, 3\n\t" \
    "v_readlane_b32 s16, v15, 4\n\t" \
    "v_readlane_b32 s17, v15, 5\n\t" \
    "v_readlane_b32 s18, v15, 6\n\t" \
    "v_readlane_b32 s19, v15, 7\n\t" \
    "v_readlane_b32 s3, v15, 8\n\t" \
    "s_add_u32 s12, s12, s33\n\t" \
    "s_add_u32 s13, s13, s33\n\t" \
    "s_add_u32 s14, s14, s33\n\t" \
    "s_add_u32 s15, s15, s33\n\t" \
    "s_add_u32 s16, s16, s33\n\t" \
    "s_add_u32 s17, s17, s33\n\t" \
    "s_add_u32 s18, s18, s33\n\t" \
    "s_add_u32 s19, s19, s33\n\t" \
    "s_add_u32 s3, s3, s33\n\t" \
    "s_waitcnt vmcnt(0)\n\t" \
    "s_mov_b64 exec, 1\n\t" \
    "ds_add_rtn_u32 v13, v9, v9 offset:12\n\t" \
    "s_add_u32 s37, s32, 786432\n\t" \
    "s_waitcnt lgkmcnt(0)\n\t" \
    "v_cmp_eq_u32 vcc, 1098496, v13\n\t" \
    "s_mov_b64 exec, -1\n\t" \
    "s_nop 0\n\t" \
    "s_cbranch_vccz .La18_%=\n\t" \
    "buffer_store_dwordx4 v[52:55], v2, s[4:7], s37 offen sc1\n\t" \
    ".La18_%=:\n\t" \
    "s_mov_b64 exec, -1\n\t" \
    ".Lh34_%=:\n\t" \
    "s_memrealtime s[22:23]\n\t" \
    "s_waitcnt lgkmcnt(0)\n\t" \
    "s_sub_u32 s22, s22, s35\n\t" \
    "s_cmp_lt_u32 s22, 482\n\t" \
    "s_cbranch_scc1 .Lh34_%=\n\t" \
    "s_cmp_lg_u32 s24, 0\n\t" \
    "s_cbranch_scc1 .Lb15_%=\n\t" \
    "s_mov_b32 s40, 0\n\t" \
    "s_add_u32 s38, s33, 524288\n\t" \
    ".Ll15_%=:\n\t" \
    "buffer_load_dword v16, v35, s[4:7], s38 offen sc1\n\t" \
    "s_waitcnt vmcnt(0)\n\t" \
    "v_cmp_ne_u32 vcc, s34, v16\n\t" \
    "s_add_u32 s40, s40, 1\n\t" \
    "s_cmp_ge_u32 s40, 8192\n\t" \
    "s_cbranch_scc1 .Lb15_%=\n\t" \
    "s_cbranch_vccz .Lb15_%=\n\t" \
    "s_sleep 2\n\t" \
    "s_branch .Ll15_%=\n\t" \
    ".Lb15_%=:\n\t" \
    "s_waitcnt lgkmcnt(0)\n\t" \
    "s_barrier\n\t" \
    "buffer_load_dwordx4 v[164:167], v32, s[4:7], s48 offen offset:0 sc1\n\t" \
    "buffer_load_dwordx4 v[168:171], v32, s[4:7], s48 offen offset:1024 sc1\n\t" \
    "buffer_load_dwordx4 v[172:175], v32, s[4:7], s48 offen offset:256 sc1\n\t" \
    "buffer_load_dwordx4 v[176:179], v32, s[4:7], s48 offen offset:1280 sc1\n\t" \
    "buffer_load_dwordx4 v[56:59], v33, s[4:7], s48 offen offset:0 sc1\n\t" \
    "s_cmp_lt_u32 s24, 4\n\t" \
    "s_cbranch_scc1 .Lp35_%=\n\t" \
    "s_setprio 1\n\t" \
    ".Lp35_%=:\n\t" \
    "s_waitcnt vmcnt(4)\n\t" \
    "v_mfma_f32_32x32x16_bf16 v[192:207], v[44:47], v[164:167], 0\n\t" \
    "buffer_load_dwordx4 v[60:63], v32, s[4:7], s49 offen offset:0 sc1\n\t" \
    "s_waitcnt vmcnt(4)\n\t" \
    "v_mfma_f32_32x32x16_bf16 v[192:207], v[48:51], v[168:171], v[192:207]\n\t" \
    "buffer_load_dwordx4 v[64:67], v32, s[4:7], s49 offen offset:1024 sc1\n\t" \
    "s_waitcnt vmcnt(4)\n\t" \
    "v_mfma_f32_32x32x16_bf16 v[208:223], v[44:47], v[172:175], 0\n\t" \
    "buffer_load_dwordx4 v[68:71], v32, s[4:7], s49 offen offset:256 sc1\n\t" \
    "s_waitcnt vmcnt(4)\n\t" \
    "v_mfma_f32_32x32x16_bf16 v[208:223], v[48:51], v[176:179], v[208:223]\n\t" \
    "buffer_load_dwordx4 v[72:75], v32, s[4:7], s49 offen offset:1280 sc1\n\t" \
    "v_mfma_f32_32x32x16_bf16 v[224:239], v[48:51], v[164:167], 0\n\t" \
    "s_add_u32 m0, s44, 0\n\t" \
    "s_nop 8\n\t" \
    "v_cvt_pk_bf16_f32 v19, v192, v208\n\t" \
    "v_cvt_pk_bf16_f32 v11, v193, v209\n\t" \
    "s_waitcnt vmcnt(4)\n\t" \
    "v_permlane16_swap_b32 v56, v168\n\t" \
    "v_cvt_pk_bf16_f32 v37, v194, v210\n\t" \
    "ds_write_addtid_b32 v19 offset:3616\n\t" \
    "v_permlane16_swap_b32 v57, v169\n\t" \
    "v_cvt_pk_bf16_f32 v34, v195, v211\n\t" \
    "ds_write_addtid_b32 v11 offset:3872\n\t" \
    "v_cvt_pk_bf16_f32 v8, v196, v212\n\t" \
    "v_permlane16_swap_b32 v58, v170\n\t" \
    "ds_write_addtid_b32 v37 offset:4128\n\t" \
    "v_cvt_pk_bf16_f32 v12, v197, v213\n\t" \
    "v_permlane16_swap_b32 v59, v171\n\t" \
    "ds_write_addtid_b32 v34 offset:4384\n\t" \
    "v_cvt_pk_bf16_f32 v9, v198, v214\n\t" \
    "ds_write_addtid_b32 v8 offset:4640\n\t" \
    "v_mfma_f32_32x32x16_bf16 v[240:255], v[48:51], v[172:175], 0\n\t" \
    "v_cvt_pk_bf16_f32 v13, v199, v215\n\t" \
    "ds_write_addtid_b32 v12 offset:4896\n\t" \
    "v_permlane16_swap_b32 v168, v176\n\t" \
    "buffer_load_dwordx4 v[76:79], v33, s[4:7], s49 offen offset:0 sc1\n\t" \
    "v_cvt_pk_bf16_f32 v35, v200, v216\n\t" \
    "ds_write_addtid_b32 v9 offset:5152\n\t" \
    "v_permlane16_swap_b32 v169, v177\n\t" \
    "v_cvt_pk_bf16_f32 v16, v201, v217\n\t" \
    "ds_write_addtid_b32 v13 offset:5408\n\t" \
    "v_cvt_pk_bf16_f32 v19, v202, v218\n\t" \
    "v_permlane16_swap_b32 v170, v178\n\t" \
    "ds_write_addtid_b32 v35 offset:5664\n\t" \
    "v_cvt_pk_bf16_f32 v11, v203, v219\n\t" \
    "v_permlane16_swap_b32 v171, v179\n\t" \
    "ds_write_addtid_b32 v16 offset:5920\n\t" \
    "v_cvt_pk_bf16_f32 v37, v204, v220\n\t" \
    "ds_write_addtid_b32 v19 offset:6176\n\t" \
    "v_mfma_f32_32x32x16_bf16 v[224:239], v[44:47], v[56:59], v[224:239]\n\t" \
    "v_cvt_pk_bf16_f32 v34, v205, v221\n\t" \
    "ds_write_addtid_b32 v11 offset:6432\n\t" \
    "v_mfma_f32_32x32x16_bf16 v[240:255], v[44:47], v[168:171], v[240:255]\n\t" \
    "ds_write_addtid_b32 v37 offset:6688\n\t" \
    "ds_write_addtid_b32 v34 offset:6944\n\t" \
    ".Lh36_%=:\n\t" \
    "s_memrealtime s[22:23]\n\t" \
    "s_waitcnt lgkmcnt(0)\n\t" \
    "s_sub_u32 s22, s22, s35\n\t" \
    "s_cmp_lt_u32 s22, 605\n\t" \
    "s_cbranch_scc1 .Lh36_%=\n\t" \
    "s_buffer_load_dword s12, s[4:7], s12 offset:0xc0000\n\t" \
    "s_buffer_load_dword s13, s[4:7], s13 offset:0xc0000\n\t" \
    "s_buffer_load_dword s14, s[4:7], s14 offset:0xc0000\n\t" \
    "s_buffer_load_dword s15, s[4:7], s15 offset:0xc0000\n\t" \
    "s_buffer_load_dword s16, s[4:7], s16 offset:0xc0000\n\t" \
    "s_buffer_load_dword s17, s[4:7], s17 offset:0xc0000\n\t" \
    "s_buffer_load_dword s18, s[4:7], s18 offset:0xc0000\n\t" \
    "s_buffer_load_dword s19, s[4:7], s19 offset:0xc0000\n\t" \
    "s_buffer_load_dword s3, s[4:7], s3 offset:0xc0000\n\t" \
    "s_waitcnt vmcnt(4)\n\t" \
    "v_mfma_f32_32x32x16_bf16 v[192:207], v[44:47], v[60:63], 0\n\t" \
    "v_cvt_pk_bf16_f32 v8, v224, v240\n\t" \
    "v_cvt_pk_bf16_f32 v12, v225, v241\n\t" \
    "v_cvt_pk_bf16_f32 v9, v226, v242\n\t" \
    "buffer_load_dwordx4 v[112:115], v32, s[4:7], s50 offen offset:0 sc1\n\t" \
    "ds_write_addtid_b32 v8 offset:0\n\t" \
    "v_cvt_pk_bf16_f32 v13, v227, v243\n\t" \
    "ds_write_addtid_b32 v12 offset:256\n\t" \
    "v_cvt_pk_bf16_f32 v35, v228, v244\n\t" \
    "s_waitcnt vmcnt(4)\n\t" \
    "v_mfma_f32_32x32x16_bf16 v[192:207], v[48:51], v[64:67], v[192:207]\n\t" \
    "ds_write_addtid_b32 v9 offset:512\n\t" \
    "v_cvt_pk_bf16_f32 v16, v229, v245\n\t" \
    "buffer_load_dwordx4 v[80:83], v32, s[4:7], s50 offen offset:1024 sc1\n\t" \
    "ds_write_addtid_b32 v13 offset:768\n\t" \
    "v_cvt_pk_bf16_f32 v19, v230, v246\n\t" \
    "ds_write_addtid_b32 v35 offset:1024\n\t" \
    "v_cvt_pk_bf16_f32 v11, v231, v247\n\t" \
    "ds_write_addtid_b32 v16 offset:1280\n\t" \
    "s_waitcnt vmcnt(4)\n\t" \
    "v_mfma_f32_32x32x16_bf16 v[208:223], v[44:47], v[68:71], 0\n\t" \
    "buffer_load_dwordx4 v[180:183], v32, s[4:7], s50 offen offset:256 sc1\n\t" \
    "v_cvt_pk_bf16_f32 v37, v232, v248\n\t" \
    "ds_write_addtid_b32 v19 offset:1536\n\t" \
    "v_cvt_pk_bf16_f32 v34, v233, v249\n\t" \
    "ds_write_addtid_b32 v11 offset:1792\n\t" \
    "v_cvt_pk_bf16_f32 v8, v234, v250\n\t" \
    "ds_write_addtid_b32 v37 offset:2048\n\t" \
    "buffer_load_dwordx4 v[184:187], v32, s[4:7], s50 offen offset:1280 sc1\n\t" \
    "v_cvt_pk_bf16_f32 v12, v235, v251\n\t" \
    "s_waitcnt vmcnt(5)\n\t" \
    "v_mfma_f32_32x32x16_bf16 v[208:223], v[48:51], v[72:75], v[208:223]\n\t" \
    "ds_write_addtid_b32 v34 offset:2304\n\t" \
    "v_cvt_pk_bf16_f32 v9, v236, v252\n\t" \
    "ds_write_addtid_b32 v8 offset:2560\n\t" \
    "v_cvt_pk_bf16_f32 v13, v237, v253\n\t" \
    "ds_write_addtid_b32 v12 offset:2816\n\t" \
    "buffer_load_dwordx4 v[84:87], v33, s[4:7], s50 offen offset:0 sc1\n\t" \
    "ds_write_addtid_b32 v9 offset:3072\n\t" \
    "ds_write_addtid_b32 v13 offset:3328\n\t" \
    "s_waitcnt lgkmcnt(0)\n\t" \
    "s_xor_b32 s12, s12, s34\n\t" \
    "s_xor_b32 s13, s13, s34\n\t" \
    "s_xor_b32 s14, s14, s34\n\t" \
    "s_xor_b32 s15, s15, s34\n\t" \
    "s_xor_b32 s16, s16, s34\n\t" \
    "s_xor_b32 s17, s17, s34\n\t" \
    "s_xor_b32 s18, s18, s34\n\t" \
    "s_xor_b32 s19, s19, s34\n\t" \
    "s_xor_b32 s3, s3, s34\n\t" \
    "s_or_b32 s12, s12, s13\n\t" \
    "s_or_b32 s12, s12, s14\n\t" \
    "s_or_b32 s12, s12, s15\n\t" \
    "s_or_b32 s12, s12, s16\n\t" \
    "s_or_b32 s12, s12, s17\n\t" \
    "s_or_b32 s12, s12, s18\n\t" \
    "s_or_b32 s12, s12, s19\n\t" \
    "s_or_b32 s12, s12, s3\n\t" \
    "s_cbranch_scc1 .Lq1021_%=\n\t" \
    ".Lb20_%=:\n\t" \
    "s_waitcnt lgkmcnt(0)\n\t" \
    "s_barrier\n\t" \
    "ds_read_b128 v[92:95], v14 offset:0\n\t" \
    "s_mov_b32 s45, 0\n\t" \
    "buffer_load_dwordx4 v[120:123], v1, s[4:7], s45 offen offset:0 sc1\n\t" \
    "ds_read_b128 v[88:91], v14 offset:128\n\t" \
    "ds_read_b128 v[116:119], v14 offset:29184\n\t" \
    "ds_read_b128 v[144:147], v14 offset:29312\n\t" \
    "ds_read_b128 v[96:99], v36 offset:0\n\t" \
    "buffer_load_dwordx4 v[100:103], v1, s[4:7], s45 offen offset:1024 sc1\n\t" \
    "ds_read_b128 v[124:127], v36 offset:128\n\t" \
    "ds_read_b128 v[104:107], v36 offset:29184\n\t" \
    "ds_read_b128 v[24:27], v36 offset:29312\n\t" \
    "v_mfma_f32_32x32x16_bf16 v[224:239], v[48:51], v[60:63], 0\n\t" \
    "v_cvt_pk_bf16_f32 v19, v192, v208\n\t" \
    "v_cvt_pk_bf16_f32 v11, v193, v209\n\t" \
    "v_cvt_pk_bf16_f32 v37, v194, v210\n\t" \
    "s_waitcnt vmcnt(7)\n\t" \
    "v_permlane16_swap_b32 v76, v64\n\t" \
    "ds_write_addtid_b32 v19 offset:61984\n\t" \
    "v_cvt_pk_bf16_f32 v8, v195, v211\n\t" \
    "v_permlane16_swap_b32 v77, v65\n\t" \
    "s_mov_b32 s46, 114688\n\t" \
    "buffer_load_dwordx4 v[108:111], v1, s[4:7], s46 offen offset:0 sc1\n\t" \
    "ds_write_addtid_b32 v11 offset:62240\n\t" \
    "v_cvt_pk_bf16_f32 v12, v196, v212\n\t" \
    "v_permlane16_swap_b32 v78, v66\n\t" \
    "ds_write_addtid_b32 v37 offset:62496\n\t" \
    "v_cvt_pk_bf16_f32 v9, v197, v213\n\t" \
    "ds_write_addtid_b32 v8 offset:62752\n\t" \
    "v_permlane16_swap_b32 v79, v67\n\t" \
    "v_cvt_pk_bf16_f32 v13, v198, v214\n\t" \
    "ds_write_addtid_b32 v12 offset:63008\n\t" \
    "v_mfma_f32_32x32x16_bf16 v[240:255], v[48:51], v[68:71], 0\n\t" \
    "v_cvt_pk_bf16_f32 v15, v199, v215\n\t" \
    "ds_write_addtid_b32 v9 offset:63264\n\t" \
    "v_permlane16_swap_b32 v64, v72\n\t" \
    "buffer_load_dwordx4 v[28:31], v1, s[4:7], s46 offen offset:1024 sc1\n\t" \
    "v_cvt_pk_bf16_f32 v16, v200, v216\n\t" \
    "ds_write_addtid_b32 v13 offset:63520\n\t" \
    "v_cvt_pk_bf16_f32 v19, v201, v217\n\t" \
    "v_permlane16_swap_b32 v65, v73\n\t" \
    "ds_write_addtid_b32 v15 offset:63776\n\t" \
    "v_cvt_pk_bf16_f32 v11, v202, v218\n\t" \
    "v_permlane16_swap_b32 v66, v74\n\t" \
    "ds_write_addtid_b32 v16 offset:64032\n\t" \
    "v_cvt_pk_bf16_f32 v37, v203, v219\n\t" \
    "v_permlane16_swap_b32 v67, v75\n\t" \
    "ds_write_addtid_b32 v19 offset:64288\n\t" \
    "v_cvt_pk_bf16_f32 v8, v204, v220\n\t" \
    "ds_write_addtid_b32 v11 offset:64544\n\t" \
    "v_mfma_f32_32x32x16_bf16 v[224:239], v[44:47], v[76:79], v[224:239]\n\t" \
    "buffer_load_dwordx4 v[132:135], v3, s[4:7], s45 offen offset:0 sc1\n\t" \
    "v_cvt_pk_bf16_f32 v12, v205, v221\n\t" \
    "ds_write_addtid_b32 v37 offset:64800\n\t" \
    "v_mfma_f32_32x32x16_bf16 v[240:255], v[44:47], v[64:67], v[240:255]\n\t" \
    "ds_write_addtid_b32 v8 offset:65056\n\t" \
    "ds_write_addtid_b32 v12 offset:65312\n\t" \
    "s_waitcnt vmcnt(9)\n\t" \
    "v_mfma_f32_32x32x16_bf16 v[192:207], v[44:47], v[112:115], 0\n\t" \
    "s_nop 7\n\t" \
    "v_cvt_pk_bf16_f32 v9, v224, v240\n\t" \
    "v_cvt_pk_bf16_f32 v13, v225, v241\n\t" \
    "v_cvt_pk_bf16_f32 v15, v226, v242\n\t" \
    "ds_write_addtid_b32 v9 offset:58368\n\t" \
    "v_cvt_pk_bf16_f32 v16, v227, v243\n\t" \
    "buffer_load_dwordx4 v[128:131], v3, s[4:7], s46 offen offset:0 sc1\n\t" \
    "ds_write_addtid_b32 v13 offset:58624\n\t" \
    "v_cvt_pk_bf16_f32 v19, v228, v244\n\t" \
    "s_waitcnt vmcnt(9)\n\t" \
    "v_mfma_f32_32x32x16_bf16 v[192:207], v[48:51], v[80:83], v[192:207]\n\t" \
    "ds_write_addtid_b32 v15 offset:58880\n\t" \
    "v_cvt_pk_bf16_f32 v11, v229, v245\n\t" \
    "ds_write_addtid_b32 v16 offset:59136\n\t" \
    "v_cvt_pk_bf16_f32 v37, v230, v246\n\t" \
    "ds_write_addtid_b32 v19 offset:59392\n\t" \
    "v_cvt_pk_bf16_f32 v8, v231, v247\n\t" \
    "ds_write_addtid_b32 v11 offset:59648\n\t" \
    "s_waitcnt vmcnt(8)\n\t" \
    "v_mfma_f32_32x32x16_bf16 v[208:223], v[44:47], v[180:183], 0\n\t" \
    "s_mov_b32 s47, 229376\n\t" \
    "buffer_load_dwordx4 v[140:143], v1, s[4:7], s47 offen offset:0 sc1\n\t" \
    "v_cvt_pk_bf16_f32 v12, v232, v248\n\t" \
    "ds_write_addtid_b32 v37 offset:59904\n\t" \
    "v_cvt_pk_bf16_f32 v9, v233, v249\n\t" \
    "ds_write_addtid_b32 v8 offset:60160\n\t" \
    "v_cvt_pk_bf16_f32 v13, v234, v250\n\t" \
    "ds_write_addtid_b32 v12 offset:60416\n\t" \
    "v_cvt_pk_bf16_f32 v15, v235, v251\n\t" \
    "s_waitcnt vmcnt(8)\n\t" \
    "v_mfma_f32_32x32x16_bf16 v[208:223], v[48:51], v[184:187], v[208:223]\n\t" \
    "ds_write_addtid_b32 v9 offset:60672\n\t" \
    "v_cvt_pk_bf16_f32 v16, v236, v252\n\t" \
    "ds_write_addtid_b32 v13 offset:60928\n\t" \
    "buffer_load_dwordx4 v[156:159], v1, s[4:7], s47 offen offset:1024 sc1\n\t" \
    "v_cvt_pk_bf16_f32 v19, v237, v253\n\t" \
    "ds_write_addtid_b32 v15 offset:61184\n\t" \
    "ds_write_addtid_b32 v16 offset:61440\n\t" \
    "ds_write_addtid_b32 v19 offset:61696\n\t" \
    "s_waitcnt vmcnt(8)\n\t" \
    "; wait\n\t" \
    "s_waitcnt lgkmcnt(0)\n\t" \
    "s_barrier\n\t" \
    "s_memrealtime s[64:65]\n\t" \
    "s_cmp_lg_u32 s24, 7\n\t" \
    "s_cbranch_scc1 .Lar_%=\n\t" \
    "s_waitcnt lgkmcnt(0)\n\t" \
    "v_mov_b32 v53, s64\n\t" \
    "s_add_u32 s37, s32, 1048576\n\t" \
    "s_mov_b64 exec, 0xff\n\t" \
    "buffer_store_dwordx4 v[52:55], v0, s[4:7], s37 offen sc1\n\t" \
    "s_mov_b64 exec, -1\n\t" \
    ".Lar_%=:\n\t" \
    "v_add_u32 v14, 58368, v14\n\t" \
    "buffer_load_dwordx4 v[160:163], v3, s[4:7], s47 offen offset:0 sc1\n\t" \
    "v_add_u32 v36, 58368, v36\n\t" \
    "ds_read_b128 v[188:191], v14 offset:0\n\t" \
    "ds_read_b128 v[4:7], v14 offset:128\n\t" \
    "ds_read_b128 v[136:139], v14 offset:29184\n\t" \
    "ds_read_b128 v[20:23], v14 offset:29312\n\t" \
    "buffer_load_dwordx4 v[148:151], v39, s[4:7], s45 offen offset:1024 sc1\n\t" \
    "ds_read_b128 v[152:155], v36 offset:0\n\t" \
    "ds_read_b128 v[164:167], v36 offset:128\n\t" \
    "ds_read_b128 v[172:175], v36 offset:29184\n\t" \
    "ds_read_b128 v[176:179], v36 offset:29312\n\t" \
    "v_mfma_f32_32x32x16_bf16 v[224:239], v[48:51], v[112:115], 0\n\t" \
    "v_cvt_pk_bf16_f32 v11, v192, v208\n\t" \
    "v_cvt_pk_bf16_f32 v37, v193, v209\n\t" \
    "s_waitcnt vmcnt(9)\n\t" \
    "v_mfma_f32_16x16x32_bf16 v[56:59], v[120:123], v[92:95], 0\n\t" \
    "v_cvt_pk_bf16_f32 v8, v194, v210\n\t" \
    "v_permlane16_swap_b32 v84, v80\n\t" \
    "ds_write_addtid_b32 v11 offset:3616\n\t" \
    "v_cvt_pk_bf16_f32 v12, v195, v211\n\t" \
    "v_permlane16_swap_b32 v85, v81\n\t" \
    "buffer_load_dwordx4 v[168:171], v39, s[4:7], s46 offen offset:1024 sc1\n\t" \
    "ds_write_addtid_b32 v37 offset:3872\n\t" \
    "v_mfma_f32_16x16x32_bf16 v[32:35], v[120:123], v[88:91], 0\n\t" \
    "v_cvt_pk_bf16_f32 v9, v196, v212\n\t" \
    "v_permlane16_swap_b32 v86, v82\n\t" \
    "ds_write_addtid_b32 v8 offset:4128\n\t" \
    "v_cvt_pk_bf16_f32 v13, v197, v213\n\t" \
    "v_mfma_f32_16x16x32_bf16 v[60:63], v[120:123], v[116:119], 0\n\t" \
    "ds_write_addtid_b32 v12 offset:4384\n\t" \
    "v_permlane16_swap_b32 v87, v83\n\t" \
    "v_cvt_pk_bf16_f32 v15, v198, v214\n\t" \
    "ds_write_addtid_b32 v9 offset:4640\n\t" \
    "v_mfma_f32_32x32x16_bf16 v[240:255], v[48:51], v[180:183], 0\n\t" \
    "v_cvt_pk_bf16_f32 v16, v199, v215\n\t" \
    "v_mfma_f32_16x16x32_bf16 v[68:71], v[120:123], v[144:147], 0\n\t" \
    "ds_write_addtid_b32 v13 offset:4896\n\t" \
    "v_permlane16_swap_b32 v80, v184\n\t" \
    "buffer_load_dwordx4 v[72:75], v39, s[4:7], s47 offen offset:1024 sc1\n\t" \
    "v_cvt_pk_bf16_f32 v19, v200, v216\n\t" \
    "ds_write_addtid_b32 v15 offset:5152\n\t" \
    "s_waitcnt vmcnt(10)\n\t" \
    "v_mfma_f32_16x16x32_bf16 v[76:79], v[100:103], v[88:91], 0\n\t" \
    "v_cvt_pk_bf16_f32 v11, v201, v217\n\t" \
    "v_permlane16_swap_b32 v81, v185\n\t" \
    "ds_write_addtid_b32 v16 offset:5408\n\t" \
    "v_cvt_pk_bf16_f32 v37, v202, v218\n\t" \
    "v_permlane16_swap_b32 v82, v186\n\t" \
    "ds_write_addtid_b32 v19 offset:5664\n\t" \
    "v_mfma_f32_16x16x32_bf16 v[32:35], v[100:103], v[92:95], v[32:35]\n\t" \
    "v_cvt_pk_bf16_f32 v8, v203, v219\n\t" \
    "v_permlane16_swap_b32 v83, v187\n\t" \
    "ds_write_addtid_b32 v11 offset:5920\n\t" \
    "v_cvt_pk_bf16_f32 v12, v204, v220\n\t" \
    "v_mfma_f32_16x16x32_bf16 v[64:67], v[100:103], v[144:147], 0\n\t" \
    "ds_write_addtid_b32 v37 offset:6176\n\t" \
    "v_mfma_f32_32x32x16_bf16 v[224:239], v[44:47], v[84:87], v[224:239]\n\t" \
    "s_mov_b32 s45, 344064\n\t" \
    "buffer_load_dwordx4 v[52:55], v1, s[4:7], s45 offen offset:0 sc1\n\t" \
    "v_cvt_pk_bf16_f32 v9, v205, v221\n\t" \
    "ds_write_addtid_b32 v8 offset:6432\n\t" \
    "v_mfma_f32_32x32x16_bf16 v[240:255], v[44:47], v[80:83], v[240:255]\n\t" \
    "ds_write_addtid_b32 v12 offset:6688\n\t" \
    "v_mfma_f32_16x16x32_bf16 v[68:71], v[100:103], v[116:119], v[68:71]\n\t" \
    "ds_write_addtid_b32 v9 offset:6944\n\t" \
    "s_nop 8\n\t" \
    "v_cvt_pk_bf16_f32 v13, v224, v240\n\t" \
    "s_waitcnt vmcnt(10) lgkmcnt(15)\n\t" \
    "v_mfma_f32_16x16x32_bf16 v[56:59], v[108:111], v[188:191], v[56:59]\n\t" \
    "v_cvt_pk_bf16_f32 v15, v225, v241\n\t" \
    "v_cvt_pk_bf16_f32 v16, v226, v242\n\t" \
    "ds_write_addtid_b32 v13 offset:0\n\t" \
    "buffer_load_dwordx4 v[112:115], v1, s[4:7], s45 offen offset:1024 sc1\n\t" \
    "v_mfma_f32_16x16x32_bf16 v[32:35], v[108:111], v[4:7], v[32:35]\n\t" \
    "v_cvt_pk_bf16_f32 v19, v227, v243\n\t" \
    "ds_write_addtid_b32 v15 offset:256\n\t" \
    "v_cvt_pk_bf16_f32 v11, v228, v244\n\t" \
    "ds_write_addtid_b32 v16 offset:512\n\t" \
    "v_mfma_f32_16x16x32_bf16 v[60:63], v[108:111], v[136:139], v[60:63]\n\t" \
    "v_cvt_pk_bf16_f32 v37, v229, v245\n\t" \
    "ds_write_addtid_b32 v19 offset:768\n\t" \
    "v_cvt_pk_bf16_f32 v8, v230, v246\n\t" \
    "s_mov_b32 s46, 458752\n\t" \
    "buffer_load_dwordx4 v[48:51], v1, s[4:7], s46 offen offset:0 sc1\n\t" \
    "v_mfma_f32_16x16x32_bf16 v[68:71], v[108:111], v[20:23], v[68:71]\n\t" \
    "ds_write_addtid_b32 v11 offset:1024\n\t" \
    "v_cvt_pk_bf16_f32 v12, v231, v247\n\t" \
    "ds_write_addtid_b32 v37 offset:1280\n\t" \
    "v_cvt_pk_bf16_f32 v9, v232, v248\n\t" \
    "s_waitcnt vmcnt(11)\n\t" \
    "v_mfma_f32_16x16x32_bf16 v[76:79], v[28:31], v[4:7], v[76:79]\n\t" \
    "ds_write_addtid_b32 v8 offset:1536\n\t" \
    "v_cvt_pk_bf16_f32 v13, v233, v249\n\t" \
    "ds_write_addtid_b32 v12 offset:1792\n\t" \
    "buffer_load_dwordx4 v[180:183], v1, s[4:7], s46 offen offset:1024 sc1\n\t" \
    "v_mfma_f32_16x16x32_bf16 v[32:35], v[28:31], v[188:191], v[32:35]\n\t" \
    "v_cvt_pk_bf16_f32 v15, v234, v250\n\t" \
    "ds_write_addtid_b32 v9 offset:2048\n\t" \
    "v_cvt_pk_bf16_f32 v16, v235, v251\n\t" \
    "ds_write_addtid_b32 v13 offset:2304\n\t" \
    "v_mfma_f32_16x16x32_bf16 v[64:67], v[28:31], v[20:23], v[64:67]\n\t" \
    "v_cvt_pk_bf16_f32 v19, v236, v252\n\t" \
    "ds_write_addtid_b32 v15 offset:2560\n\t" \
    "v_cvt_pk_bf16_f32 v11, v237, v253\n\t" \
    "s_mov_b32 s47, 573440\n\t" \
    "buffer_load_dwordx4 v[120:123], v1, s[4:7], s47 offen offset:0 sc1\n\t" \
    "v_mfma_f32_16x16x32_bf16 v[68:71], v[28:31], v[136:139], v[68:71]\n\t" \
    "ds_write_addtid_b32 v16 offset:2816\n\t" \
    "ds_write_addtid_b32 v19 offset:3072\n\t" \
    "ds_write_addtid_b32 v11 offset:3328\n\t" \
    "s_waitcnt lgkmcnt(0)\n\t" \
    "s_barrier\n\t" \
    "ds_read_b128 v[184:187], v10 offset:2048\n\t" \
    "v_mov_b32 v37, 0x10000\n\t" \
    "s_waitcnt vmcnt(12)\n\t" \
    "v_mfma_f32_16x16x32_bf16 v[84:87], v[132:135], v[96:99], 0\n\t" \
    "v_subrev_u32 v14, 58368, v14\n\t" \
    "v_mfma_f32_16x16x32_bf16 v[192:195], v[132:135], v[124:127], 0\n\t" \
    "v_subrev_u32 v36, 58368, v36\n\t" \
    "v_mfma_f32_16x16x32_bf16 v[196:199], v[132:135], v[104:107], 0\n\t" \
    "ds_read_b128 v[200:203], v14 offset:0\n\t" \
    "buffer_load_dwordx4 v[204:207], v1, s[4:7], s47 offen offset:1024 sc1\n\t" \
    "v_mfma_f32_16x16x32_bf16 v[208:211], v[132:135], v[24:27], 0\n\t" \
    "ds_read_b128 v[212:215], v14 offset:128\n\t" \
    "s_waitcnt vmcnt(12)\n\t" \
    "v_mfma_f32_16x16x32_bf16 v[84:87], v[128:131], v[152:155], v[84:87]\n\t" \
    "ds_read_b128 v[216:219], v14 offset:29184\n\t" \
    "v_mfma_f32_16x16x32_bf16 v[192:195], v[128:131], v[164:167], v[192:195]\n\t" \
    "ds_read_b128 v[220:223], v14 offset:29312\n\t" \
    "buffer_load_dwordx4 v[44:47], v3, s[4:7], s45 offen offset:0 sc1\n\t" \
    "v_mfma_f32_16x16x32_bf16 v[196:199], v[128:131], v[172:175], v[196:199]\n\t" \
    "ds_read_b128 v[80:83], v36 offset:0\n\t" \
    "v_mfma_f32_16x16x32_bf16 v[208:211], v[128:131], v[176:179], v[208:211]\n\t" \
    "ds_read_b128 v[100:103], v36 offset:128\n\t" \
    "s_waitcnt vmcnt(12) lgkmcnt(5)\n\t" \
    "v_mfma_f32_16x16x32_bf16 v[56:59], v[140:143], v[200:203], v[56:59]\n\t" \
    "ds_read_b128 v[108:111], v36 offset:29184\n\t" \
    "buffer_load_dwordx4 v[224:227], v3, s[4:7], s46 offen offset:0 sc1\n\t" \
    "s_waitcnt lgkmcnt(5)\n\t" \
    "v_mfma_f32_16x16x32_bf16 v[32:35], v[140:143], v[212:215], v[32:35]\n\t" \
    "ds_read_b128 v[228:231], v36 offset:29312\n\t" \
    "s_waitcnt lgkmcnt(5)\n\t" \
    "v_mfma_f32_16x16x32_bf16 v[60:63], v[140:143], v[216:219], v[60:63]\n\t" \
    "s_waitcnt lgkmcnt(4)\n\t" \
    "v_mfma_f32_16x16x32_bf16 v[68:71], v[140:143], v[220:223], v[68:71]\n\t" \
    "buffer_load_dwordx4 v[232:235], v3, s[4:7], s47 offen offset:0 sc1\n\t" \
    "s_waitcnt vmcnt(13)\n\t" \
    "v_mfma_f32_16x16x32_bf16 v[76:79], v[156:159], v[212:215], v[76:79]\n\t" \
    "s_waitcnt lgkmcnt(0)\n\t" \
    "s_add_u32 s12, s55, 34603008\n\t" \
    "s_buffer_load_dword s13, s[4:7], s12\n\t" \
    "s_buffer_load_dword s14, s[4:7], s12 offset:0x400\n\t" \
    "v_mfma_f32_16x16x32_bf16 v[32:35], v[156:159], v[200:203], v[32:35]\n\t" \
    "v_mfma_f32_16x16x32_bf16 v[64:67], v[156:159], v[220:223], v[64:67]\n\t" \
    "buffer_load_dwordx4 v[236:239], v39, s[4:7], s45 offen offset:1024 sc1\n\t" \
    "v_mfma_f32_16x16x32_bf16 v[68:71], v[156:159], v[216:219], v[68:71]\n\t" \
    "s_waitcnt vmcnt(13)\n\t" \
    "v_mfma_f32_16x16x32_bf16 v[84:87], v[160:163], v[80:83], v[84:87]\n\t" \
    "v_mfma_f32_16x16x32_bf16 v[192:195], v[160:163], v[100:103], v[192:195]\n\t" \
    "buffer_load_dwordx4 v[240:243], v39, s[4:7], s46 offen offset:1024 sc1\n\t" \
    "v_mfma_f32_16x16x32_bf16 v[196:199], v[160:163], v[108:111], v[196:199]\n\t" \
    "v_sub_f32 v56, v56, v76\n\t" \
    "v_sub_f32 v57, v57, v77\n\t" \
    "v_sub_f32 v58, v58, v78\n\t" \
    "v_mfma_f32_16x16x32_bf16 v[208:211], v[160:163], v[228:231], v[208:211]\n\t" \
    "v_sub_f32 v59, v59, v79\n\t" \
    "v_sub_f32 v60, v60, v64\n\t" \
    "v_sub_f32 v61, v61, v65\n\t" \
    "v_sub_f32 v62, v62, v66\n\t" \
    "v_sub_f32 v63, v63, v67\n\t" \
    "v_cvt_pk_bf16_f32 v246, v32, v33\n\t" \
    "v_cvt_pk_bf16_f32 v247, v34, v35\n\t" \
    "v_cvt_pk_bf16_f32 v244, v56, v57\n\t" \
    "v_cvt_pk_bf16_f32 v245, v58, v59\n\t" \
    "v_cvt_pk_bf16_f32 v250, v68, v69\n\t" \
    "v_cvt_pk_bf16_f32 v251, v70, v71\n\t" \
    "ds_write_b128 v42, v[244:247] offset:58368\n\t" \
    "v_cvt_pk_bf16_f32 v248, v60, v61\n\t" \
    "v_cvt_pk_bf16_f32 v249, v62, v63\n\t" \
    "ds_write_b128 v42, v[248:251] offset:59392\n\t" \
    "s_waitcnt vmcnt(11)\n\t" \
    "s_cmp_ge_u32 s24, 4\n\t" \
    "s_cbranch_scc1 .Lk40_%=\n\t" \
    "v_mfma_f32_16x16x32_bf16 v[252:255], v[148:151], v[124:127], 0\n\t" \
    "v_mfma_f32_16x16x32_bf16 v[252:255], v[168:171], v[164:167], v[252:255]\n\t" \
    "v_mfma_f32_16x16x32_bf16 v[252:255], v[72:75], v[100:103], v[252:255]\n\t" \
    "v_mfma_f32_16x16x32_bf16 v[28:31], v[148:151], v[24:27], 0\n\t" \
    "v_mfma_f32_16x16x32_bf16 v[28:31], v[168:171], v[176:179], v[28:31]\n\t" \
    "v_mfma_f32_16x16x32_bf16 v[28:31], v[72:75], v[228:231], v[28:31]\n\t" \
    "v_mfma_f32_16x16x32_bf16 v[192:195], v[148:151], v[96:99], v[192:195]\n\t" \
    "v_mfma_f32_16x16x32_bf16 v[192:195], v[168:171], v[152:155], v[192:195]\n\t" \
    "v_mfma_f32_16x16x32_bf16 v[192:195], v[72:75], v[80:83], v[192:195]\n\t" \
    "v_mfma_f32_16x16x32_bf16 v[208:211], v[148:151], v[104:107], v[208:211]\n\t" \
    "s_nop 0\n\t" \
    "v_sub_f32 v84, v84, v252\n\t" \
    "v_sub_f32 v85, v85, v253\n\t" \
    "v_mfma_f32_16x16x32_bf16 v[208:211], v[168:171], v[172:175], v[208:211]\n\t" \
    "v_sub_f32 v86, v86, v254\n\t" \
    "v_sub_f32 v87, v87, v255\n\t" \
    "v_mfma_f32_16x16x32_bf16 v[208:211], v[72:75], v[108:111], v[208:211]\n\t" \
    "v_sub_f32 v196, v196, v28\n\t" \
    "v_sub_f32 v197, v197, v29\n\t" \
    "v_sub_f32 v198, v198, v30\n\t" \
    "v_sub_f32 v199, v199, v31\n\t" \
    ".Lk40_%=:\n\t" \
    "s_waitcnt vmcnt(10)\n\t" \
    "v_mfma_f32_16x16x32_bf16 v[132:135], v[52:55], v[92:95], 0\n\t" \
    "buffer_load_dwordx4 v[12:15], v39, s[4:7], s47 offen offset:1024 sc1\n\t" \
    "v_mfma_f32_16x16x32_bf16 v[128:131], v[52:55], v[88:91], 0\n\t" \
    "v_cvt_pk_bf16_f32 v142, v192, v193\n\t" \
    "v_cvt_pk_bf16_f32 v143, v194, v195\n\t" \
    "v_mfma_f32_16x16x32_bf16 v[156:159], v[52:55], v[116:119], 0\n\t" \
    "v_cvt_pk_bf16_f32 v140, v84, v85\n\t" \
    "v_cvt_pk_bf16_f32 v141, v86, v87\n\t" \
    "v_mfma_f32_16x16x32_bf16 v[160:163], v[52:55], v[144:147], 0\n\t" \
    "v_cvt_pk_bf16_f32 v78, v208, v209\n\t" \
    "v_cvt_pk_bf16_f32 v79, v210, v211\n\t" \
    "s_mov_b32 s45, 688128\n\t" \
    "buffer_load_dwordx4 v[64:67], v1, s[4:7], s45 offen offset:0 sc1\n\t" \
    "s_waitcnt vmcnt(11)\n\t" \
    "v_mfma_f32_16x16x32_bf16 v[32:35], v[112:115], v[88:91], 0\n\t" \
    "ds_write_b128 v42, v[140:143] offset:60432\n\t" \
    "v_cvt_pk_bf16_f32 v76, v196, v197\n\t" \
    "v_mfma_f32_16x16x32_bf16 v[128:131], v[112:115], v[92:95], v[128:131]\n\t" \
    "v_cvt_pk_bf16_f32 v77, v198, v199\n\t" \
    "v_mfma_f32_16x16x32_bf16 v[56:59], v[112:115], v[144:147], 0\n\t" \
    "ds_write_b128 v42, v[76:79] offset:61456\n\t" \
    "buffer_load_dwordx4 v[68:71], v1, s[4:7], s45 offen offset:1024 sc1\n\t" \
    "v_mfma_f32_16x16x32_bf16 v[160:163], v[112:115], v[116:119], v[160:163]\n\t" \
    "s_waitcnt vmcnt(11)\n\t" \
    "v_mfma_f32_16x16x32_bf16 v[132:135], v[48:51], v[188:191], v[132:135]\n\t" \
    "v_mfma_f32_16x16x32_bf16 v[128:131], v[48:51], v[4:7], v[128:131]\n\t" \
    "s_waitcnt lgkmcnt(0)\n\t" \
    "s_xor_b32 s13, s13, s34\n\t" \
    "s_xor_b32 s14, s14, s34\n\t" \
    "s_or_b32 s13, s13, s14\n\t" \
    "s_cbranch_scc1 .Lq1024_%=\n\t" \
    ".Lb23_%=:\n\t" \
    "s_waitcnt lgkmcnt(0)\n\t" \
    "s_barrier\n\t" \
    "s_memrealtime s[16:17]\n\t" \
    "ds_read_b64_tr_b16 v[244:245], v17 offset:58368\n\t" \
    "ds_read_b64_tr_b16 v[246:247], v17 offset:62496\n\t" \
    "s_mov_b32 s46, 802816\n\t" \
    "buffer_load_dwordx4 v[60:63], v1, s[4:7], s46 offen offset:0 sc1\n\t" \
    "v_mfma_f32_16x16x32_bf16 v[156:159], v[48:51], v[136:139], v[156:159]\n\t" \
    "ds_read_b64_tr_b16 v[248:249], v17 offset:58624\n\t" \
    "ds_read_b64_tr_b16 v[250:251], v17 offset:62752\n\t" \
    "v_mfma_f32_16x16x32_bf16 v[160:163], v[48:51], v[20:23], v[160:163]\n\t" \
    "ds_read_b64_tr_b16 v[148:149], v17 offset:58384\n\t" \
    "ds_read_b64_tr_b16 v[150:151], v17 offset:62512\n\t" \
    "s_waitcnt vmcnt(11)\n\t" \
    "v_mfma_f32_16x16x32_bf16 v[32:35], v[180:183], v[4:7], v[32:35]\n\t" \
    "ds_read_b64_tr_b16 v[168:169], v17 offset:58640\n\t" \
    "ds_read_b64_tr_b16 v[170:171], v17 offset:62768\n\t" \
    "buffer_load_dwordx4 v[252:255], v1, s[4:7], s46 offen offset:1024 sc1\n\t" \
    "v_mfma_f32_16x16x32_bf16 v[128:131], v[180:183], v[188:191], v[128:131]\n\t" \
    "v_mfma_f32_16x16x32_bf16 v[56:59], v[180:183], v[20:23], v[56:59]\n\t" \
    "v_mfma_f32_16x16x32_bf16 v[160:163], v[180:183], v[136:139], v[160:163]\n\t" \
    "s_waitcnt lgkmcnt(6)\n\t" \
    "v_mfma_f32_16x16x32_bf16 v[72:75], v[244:247], v[184:187], 0\n\t" \
    "s_waitcnt lgkmcnt(4)\n\t" \
    "v_mfma_f32_16x16x32_bf16 v[28:31], v[248:251], v[184:187], 0\n\t" \
    "s_mov_b32 s47, 917504\n\t" \
    "buffer_load_dwordx4 v[192:195], v1, s[4:7], s47 offen offset:0 sc1\n\t" \
    "s_waitcnt vmcnt(12)\n\t" \
    "v_mfma_f32_16x16x32_bf16 v[132:135], v[120:123], v[200:203], v[132:135]\n\t" \
    "s_waitcnt lgkmcnt(2)\n\t" \
    "v_mfma_f32_16x16x32_bf16 v[84:87], v[148:151], v[184:187], 0\n\t" \
    "s_waitcnt lgkmcnt(0)\n\t" \
    "v_mfma_f32_16x16x32_bf16 v[52:55], v[168:171], v[184:187], 0\n\t" \
    "v_mfma_f32_16x16x32_bf16 v[128:131], v[120:123], v[212:215], v[128:131]\n\t" \
    "s_cmp_lg_u32 s24, 4\n\t" \
    "s_cbranch_scc1 .Lph_%=\n\t" \
    "s_waitcnt lgkmcnt(0)\n\t" \
    "v_mov_b32 v208, s16\n\t" \
    "v_mov_b32 v209, s16\n\t" \
    "v_mov_b32 v210, s16\n\t" \
    "v_mov_b32 v211, s16\n\t" \
    "buffer_store_dwordx4 v[208:211], v2, s[4:7], s32 offen sc1\n\t" \
    "s_nop 0\n\t" \
    ".Lph_%=:\n\t" \
    "v_mfma_f32_16x16x32_bf16 v[156:159], v[120:123], v[216:219], v[156:159]\n\t" \
    "buffer_load_dwordx4 v[140:143], v1, s[4:7], s47 offen offset:1024 sc1\n\t" \
    "v_mfma_f32_16x16x32_bf16 v[160:163], v[120:123], v[220:223], v[160:163]\n\t" \
    "v_bfi_b32 v72, s41, v37, v72\n\t" \
    "v_bfi_b32 v28, s41, v37, v28\n\t" \
    "s_waitcnt vmcnt(12)\n\t" \
    "v_mfma_f32_16x16x32_bf16 v[32:35], v[204:207], v[212:215], v[32:35]\n\t" \
    "v_cvt_pk_bf16_f32 v197, v74, v75\n\t" \
    "v_cvt_pk_bf16_f32 v199, v30, v31\n\t" \
    "v_mfma_f32_16x16x32_bf16 v[128:131], v[204:207], v[200:203], v[128:131]\n\t" \
    "v_cvt_pk_bf16_f32 v196, v72, v73\n\t" \
    "v_cvt_pk_bf16_f32 v198, v28, v29\n\t" \
    "v_mfma_f32_16x16x32_bf16 v[56:59], v[204:207], v[220:223], v[56:59]\n\t" \
    "v_bfi_b32 v84, s41, v37, v84\n\t" \
    "v_bfi_b32 v52, s41, v37, v52\n\t" \
    "v_mfma_f32_16x16x32_bf16 v[160:163], v[204:207], v[216:219], v[160:163]\n\t" \
    "buffer_store_dwordx4 v[196:199], v40, s[4:7], s48 offen offset:0 sc1\n\t" \
    "v_cvt_pk_bf16_f32 v77, v86, v87\n\t" \
    "s_waitcnt vmcnt(12)\n\t" \
    "v_mfma_f32_16x16x32_bf16 v[112:115], v[44:47], v[96:99], 0\n\t" \
    "v_cvt_pk_bf16_f32 v79, v54, v55\n\t" \
    "v_cvt_pk_bf16_f32 v76, v84, v85\n\t" \
    "v_mfma_f32_16x16x32_bf16 v[48:51], v[44:47], v[124:127], 0\n\t" \
    "v_cvt_pk_bf16_f32 v78, v52, v53\n\t" \
    "buffer_store_dwordx4 v[76:79], v40, s[4:7], s48 offen offset:1024 sc1\n\t" \
    "v_mfma_f32_16x16x32_bf16 v[180:183], v[44:47], v[104:107], 0\n\t" \
    "v_sub_f32 v132, v132, v32\n\t" \
    "v_sub_f32 v133, v133, v33\n\t" \
    "v_mfma_f32_16x16x32_bf16 v[244:247], v[44:47], v[24:27], 0\n\t" \
    "v_sub_f32 v134, v134, v34\n\t" \
    "v_sub_f32 v135, v135, v35\n\t" \
    "s_waitcnt vmcnt(12)\n\t" \
    "v_mfma_f32_16x16x32_bf16 v[112:115], v[224:227], v[152:155], v[112:115]\n\t" \
    "v_sub_f32 v156, v156, v56\n\t" \
    "v_sub_f32 v157, v157, v57\n\t" \
    "v_mfma_f32_16x16x32_bf16 v[48:51], v[224:227], v[164:167], v[48:51]\n\t" \
    "v_sub_f32 v158, v158, v58\n\t" \
    "v_sub_f32 v159, v159, v59\n\t" \
    "v_mfma_f32_16x16x32_bf16 v[180:183], v[224:227], v[172:175], v[180:183]\n\t" \
    "v_cvt_pk_bf16_f32 v250, v128, v129\n\t" \
    "v_cvt_pk_bf16_f32 v251, v130, v131\n\t" \
    "buffer_load_dwordx4 v[148:151], v3, s[4:7], s45 offen offset:0 sc1\n\t" \
    "v_mfma_f32_16x16x32_bf16 v[244:247], v[224:227], v[176:179], v[244:247]\n\t" \
    "v_cvt_pk_bf16_f32 v248, v132, v133\n\t" \
    "v_cvt_pk_bf16_f32 v249, v134, v135\n\t" \
    "s_waitcnt vmcnt(12)\n\t" \
    "v_mfma_f32_16x16x32_bf16 v[112:115], v[232:235], v[80:83], v[112:115]\n\t" \
    "v_cvt_pk_bf16_f32 v170, v160, v161\n\t" \
    "v_cvt_pk_bf16_f32 v171, v162, v163\n\t" \
    "v_mfma_f32_16x16x32_bf16 v[48:51], v[232:235], v[100:103], v[48:51]\n\t" \
    "ds_write_b128 v42, v[248:251] offset:0\n\t" \
    "v_cvt_pk_bf16_f32 v168, v156, v157\n\t" \
    "buffer_load_dwordx4 v[208:211], v3, s[4:7], s46 offen offset:0 sc1\n\t" \
    "v_mfma_f32_16x16x32_bf16 v[180:183], v[232:235], v[108:111], v[180:183]\n\t" \
    "v_cvt_pk_bf16_f32 v169, v158, v159\n\t" \
    "ds_write_b128 v42, v[168:171] offset:1024\n\t" \
    "v_mfma_f32_16x16x32_bf16 v[244:247], v[232:235], v[228:231], v[244:247]\n\t" \
    "s_waitcnt vmcnt(10)\n\t" \
    "s_cmp_ge_u32 s24, 4\n\t" \
    "s_cbranch_scc1 .Lk41_%=\n\t" \
    "v_mfma_f32_16x16x32_bf16 v[120:123], v[236:239], v[124:127], 0\n\t" \
    "v_mfma_f32_16x16x32_bf16 v[120:123], v[240:243], v[164:167], v[120:123]\n\t" \
    "v_mfma_f32_16x16x32_bf16 v[120:123], v[12:15], v[100:103], v[120:123]\n\t" \
    "v_mfma_f32_16x16x32_bf16 v[72:75], v[236:239], v[24:27], 0\n\t" \
    "v_mfma_f32_16x16x32_bf16 v[72:75], v[240:243], v[176:179], v[72:75]\n\t" \
    "v_mfma_f32_16x16x32_bf16 v[72:75], v[12:15], v[228:231], v[72:75]\n\t" \
    "v_mfma_f32_16x16x32_bf16 v[48:51], v[236:239], v[96:99], v[48:51]\n\t" \
    "v_mfma_f32_16x16x32_bf16 v[48:51], v[240:243], v[152:155], v[48:51]\n\t" \
    "v_mfma_f32_16x16x32_bf16 v[48:51], v[12:15], v[80:83], v[48:51]\n\t" \
    "v_mfma_f32_16x16x32_bf16 v[244:247], v[236:239], v[104:107], v[244:247]\n\t" \
    "s_nop 0\n\t" \
    "v_sub_f32 v112, v112, v120\n\t" \
    "v_sub_f32 v113, v113, v121\n\t" \
    "v_mfma_f32_16x16x32_bf16 v[244:247], v[240:243], v[172:175], v[244:247]\n\t" \
    "v_sub_f32 v114, v114, v122\n\t" \
    "v_sub_f32 v115, v115, v123\n\t" \
    "v_mfma_f32_16x16x32_bf16 v[244:247], v[12:15], v[108:111], v[244:247]\n\t" \
    "v_sub_f32 v180, v180, v72\n\t" \
    "v_sub_f32 v181, v181, v73\n\t" \
    "v_sub_f32 v182, v182, v74\n\t" \
    "v_sub_f32 v183, v183, v75\n\t" \
    ".Lk41_%=:\n\t" \
    "s_waitcnt vmcnt(9)\n\t" \
    "v_mfma_f32_16x16x32_bf16 v[28:31], v[64:67], v[92:95], 0\n\t" \
    "buffer_load_dwordx4 v[204:207], v3, s[4:7], s47 offen offset:0 sc1\n\t" \
    "v_mfma_f32_16x16x32_bf16 v[196:199], v[64:67], v[88:91], 0\n\t" \
    "v_cvt_pk_bf16_f32 v86, v48, v49\n\t" \
    "v_cvt_pk_bf16_f32 v87, v50, v51\n\t" \
    "v_mfma_f32_16x16x32_bf16 v[52:55], v[64:67], v[116:119], 0\n\t" \
    "v_cvt_pk_bf16_f32 v84, v112, v113\n\t" \
    "v_cvt_pk_bf16_f32 v85, v114, v115\n\t" \
    "v_mfma_f32_16x16x32_bf16 v[76:79], v[64:67], v[144:147], 0\n\t" \
    "v_cvt_pk_bf16_f32 v46, v244, v245\n\t" \
    "v_cvt_pk_bf16_f32 v47, v246, v247\n\t" \
    "buffer_load_dwordx4 v[32:35], v39, s[4:7], s45 offen offset:1024 sc1\n\t" \
    "s_waitcnt vmcnt(10)\n\t" \
    "v_mfma_f32_16x16x32_bf16 v[56:59], v[68:71], v[88:91], 0\n\t" \
    "ds_write_b128 v42, v[84:87] offset:2064\n\t" \
    "v_cvt_pk_bf16_f32 v44, v180, v181\n\t" \
    "v_mfma_f32_16x16x32_bf16 v[196:199], v[68:71], v[92:95], v[196:199]\n\t" \
    "v_cvt_pk_bf16_f32 v45, v182, v183\n\t" \
    "v_mfma_f32_16x16x32_bf16 v[128:131], v[68:71], v[144:147], 0\n\t" \
    "ds_write_b128 v42, v[44:47] offset:3088\n\t" \
    "buffer_load_dwordx4 v[224:227], v39, s[4:7], s46 offen offset:1024 sc1\n\t" \
    "v_mfma_f32_16x16x32_bf16 v[76:79], v[68:71], v[116:119], v[76:79]\n\t" \
    "s_waitcnt vmcnt(10)\n\t" \
    "v_mfma_f32_16x16x32_bf16 v[28:31], v[60:63], v[188:191], v[28:31]\n\t" \
    "v_mfma_f32_16x16x32_bf16 v[196:199], v[60:63], v[4:7], v[196:199]\n\t" \
    "s_waitcnt lgkmcnt(0)\n\t" \
    "s_barrier\n\t" \
    "s_memrealtime s[18:19]\n\t" \
    "s_setprio 2\n\t" \
    "ds_read_b64_tr_b16 v[132:133], v17 offset:0\n\t" \
    "ds_read_b64_tr_b16 v[134:135], v17 offset:4128\n\t" \
    "buffer_load_dwordx4 v[160:163], v39, s[4:7], s47 offen offset:1024 sc1\n\t" \
    "v_mfma_f32_16x16x32_bf16 v[52:55], v[60:63], v[136:139], v[52:55]\n\t" \
    "ds_read_b64_tr_b16 v[248:249], v17 offset:256\n\t" \
    "ds_read_b64_tr_b16 v[250:251], v17 offset:4384\n\t" \
    "v_mfma_f32_16x16x32_bf16 v[76:79], v[60:63], v[20:23], v[76:79]\n\t" \
    "ds_read_b64_tr_b16 v[156:157], v17 offset:16\n\t" \
    "ds_read_b64_tr_b16 v[158:159], v17 offset:4144\n\t" \
    "s_waitcnt vmcnt(10)\n\t" \
    "v_mfma_f32_16x16x32_bf16 v[56:59], v[252:255], v[4:7], v[56:59]\n\t" \
    "ds_read_b64_tr_b16 v[168:169], v17 offset:272\n\t" \
    "ds_read_b64_tr_b16 v[170:171], v17 offset:4400\n\t" \
    "v_mfma_f32_16x16x32_bf16 v[196:199], v[252:255], v[188:191], v[196:199]\n\t" \
    "v_mfma_f32_16x16x32_bf16 v[128:131], v[252:255], v[20:23], v[128:131]\n\t" \
    "v_mfma_f32_16x16x32_bf16 v[76:79], v[252:255], v[136:139], v[76:79]\n\t" \
    "s_waitcnt lgkmcnt(6)\n\t" \
    "v_mfma_f32_16x16x32_bf16 v[232:235], v[132:135], v[184:187], 0\n\t" \
    "s_waitcnt lgkmcnt(4)\n\t" \
    "v_mfma_f32_16x16x32_bf16 v[236:239], v[248:251], v[184:187], 0\n\t" \
    "s_waitcnt vmcnt(9)\n\t" \
    "v_mfma_f32_16x16x32_bf16 v[28:31], v[192:195], v[200:203], v[28:31]\n\t" \
    "s_waitcnt lgkmcnt(2)\n\t" \
    "v_mfma_f32_16x16x32_bf16 v[240:243], v[156:159], v[184:187], 0\n\t" \
    "s_waitcnt lgkmcnt(0)\n\t" \
    "v_mfma_f32_16x16x32_bf16 v[120:123], v[168:171], v[184:187], 0\n\t" \
    "v_mfma_f32_16x16x32_bf16 v[196:199], v[192:195], v[212:215], v[196:199]\n\t" \
    "v_mfma_f32_16x16x32_bf16 v[52:55], v[192:195], v[216:219], v[52:55]\n\t" \
    "s_add_u32 s37, s52, s33\n\t" \
    "s_buffer_load_dword s58, s[4:7], s37 offset:0x0\n\t" \
    "s_buffer_load_dword s59, s[4:7], s37 offset:0x400\n\t" \
    "s_buffer_load_dword s60, s[4:7], s37 offset:0x4000\n\t" \
    "s_buffer_load_dword s61, s[4:7], s37 offset:0x4400\n\t" \
    "v_mfma_f32_16x16x32_bf16 v[76:79], v[192:195], v[220:223], v[76:79]\n\t" \
    "v_bfi_b32 v232, s41, v37, v232\n\t" \
    "v_bfi_b32 v236, s41, v37, v236\n\t" \
    "s_waitcnt vmcnt(8)\n\t" \
    "v_mfma_f32_16x16x32_bf16 v[56:59], v[140:143], v[212:215], v[56:59]\n\t" \
    "v_cvt_pk_bf16_f32 v13, v234, v235\n\t" \
    "v_cvt_pk_bf16_f32 v15, v238, v239\n\t" \
    "v_add_u32 v42, 91392, v42\n\t" \
    "v_mfma_f32_16x16x32_bf16 v[196:199], v[140:143], v[200:203], v[196:199]\n\t" \
    "v_cvt_pk_bf16_f32 v12, v232, v233\n\t" \
    "v_cvt_pk_bf16_f32 v14, v236, v237\n\t" \
    "v_mfma_f32_16x16x32_bf16 v[128:131], v[140:143], v[220:223], v[128:131]\n\t" \
    "v_bfi_b32 v240, s41, v37, v240\n\t" \
    "v_bfi_b32 v120, s41, v37, v120\n\t" \
    "v_mfma_f32_16x16x32_bf16 v[76:79], v[140:143], v[216:219], v[76:79]\n\t" \
    "buffer_store_dwordx4 v[12:15], v40, s[4:7], s49 offen offset:0 sc1\n\t" \
    "v_cvt_pk_bf16_f32 v73, v242, v243\n\t" \
    "s_waitcnt vmcnt(6)\n\t" \
    "v_mfma_f32_16x16x32_bf16 v[48:51], v[148:151], v[96:99], 0\n\t" \
    "v_cvt_pk_bf16_f32 v75, v122, v123\n\t" \
    "v_cvt_pk_bf16_f32 v72, v240, v241\n\t" \
    "v_mfma_f32_16x16x32_bf16 v[112:115], v[148:151], v[124:127], 0\n\t" \
    "v_cvt_pk_bf16_f32 v74, v120, v121\n\t" \
    "buffer_store_dwordx4 v[72:75], v40, s[4:7], s49 offen offset:1024 sc1\n\t" \
    "v_mfma_f32_16x16x32_bf16 v[64:67], v[148:151], v[104:107], 0\n\t" \
    "s_setprio 0\n\t" \
    "s_cmp_lt_u32 s24, 4\n\t" \
    "s_cbranch_scc1 .Lp42_%=\n\t" \
    "s_setprio 1\n\t" \
    ".Lp42_%=:\n\t" \
    "v_sub_f32 v28, v28, v56\n\t" \
    "v_sub_f32 v29, v29, v57\n\t" \
    "v_mfma_f32_16x16x32_bf16 v[244:247], v[148:151], v[24:27], 0\n\t" \
    "v_sub_f32 v30, v30, v58\n\t" \
    "v_sub_f32 v31, v31, v59\n\t" \
    "s_waitcnt vmcnt(6)\n\t" \
    "v_mfma_f32_16x16x32_bf16 v[48:51], v[208:211], v[152:155], v[48:51]\n\t" \
    "v_sub_f32 v52, v52, v128\n\t" \
    "v_sub_f32 v53, v53, v129\n\t" \
    "v_mfma_f32_16x16x32_bf16 v[112:115], v[208:211], v[164:167], v[112:115]\n\t" \
    "v_sub_f32 v54, v54, v130\n\t" \
    "v_sub_f32 v55, v55, v131\n\t" \
    "v_mfma_f32_16x16x32_bf16 v[64:67], v[208:211], v[172:175], v[64:67]\n\t" \
    "v_cvt_pk_bf16_f32 v90, v196, v197\n\t" \
    "v_cvt_pk_bf16_f32 v91, v198, v199\n\t" \
    "v_mfma_f32_16x16x32_bf16 v[244:247], v[208:211], v[176:179], v[244:247]\n\t" \
    "v_cvt_pk_bf16_f32 v88, v28, v29\n\t" \
    "v_cvt_pk_bf16_f32 v89, v30, v31\n\t" \
    "s_waitcnt vmcnt(5)\n\t" \
    "v_mfma_f32_16x16x32_bf16 v[48:51], v[204:207], v[80:83], v[48:51]\n\t" \
    "v_cvt_pk_bf16_f32 v86, v76, v77\n\t" \
    "v_cvt_pk_bf16_f32 v87, v78, v79\n\t" \
    "v_mfma_f32_16x16x32_bf16 v[112:115], v[204:207], v[100:103], v[112:115]\n\t" \
    "ds_write_b128 v42, v[88:91] offset:0\n\t" \
    "v_cvt_pk_bf16_f32 v84, v52, v53\n\t" \
    "v_mfma_f32_16x16x32_bf16 v[64:67], v[204:207], v[108:111], v[64:67]\n\t" \
    "v_cvt_pk_bf16_f32 v85, v54, v55\n\t" \
    "ds_write_b128 v42, v[84:87] offset:1024\n\t" \
    "v_mfma_f32_16x16x32_bf16 v[244:247], v[204:207], v[228:231], v[244:247]\n\t" \
    "s_waitcnt vmcnt(2)\n\t" \
    "s_cmp_ge_u32 s24, 4\n\t" \
    "s_cbranch_scc1 .Lk43_%=\n\t" \
    "v_mfma_f32_16x16x32_bf16 v[92:95], v[32:35], v[124:127], 0\n\t" \
    "v_mfma_f32_16x16x32_bf16 v[92:95], v[224:227], v[164:167], v[92:95]\n\t" \
    "v_mfma_f32_16x16x32_bf16 v[92:95], v[160:163], v[100:103], v[92:95]\n\t" \
    "v_mfma_f32_16x16x32_bf16 v[180:183], v[32:35], v[24:27], 0\n\t" \
    "v_mfma_f32_16x16x32_bf16 v[180:183], v[224:227], v[176:179], v[180:183]\n\t" \
    "v_mfma_f32_16x16x32_bf16 v[180:183], v[160:163], v[228:231], v[180:183]\n\t" \
    "v_mfma_f32_16x16x32_bf16 v[112:115], v[32:35], v[96:99], v[112:115]\n\t" \
    "v_mfma_f32_16x16x32_bf16 v[112:115], v[224:227], v[152:155], v[112:115]\n\t" \
    "v_mfma_f32_16x16x32_bf16 v[112:115], v[160:163], v[80:83], v[112:115]\n\t" \
    "v_mfma_f32_16x16x32_bf16 v[244:247], v[32:35], v[104:107], v[244:247]\n\t" \
    "s_nop 0\n\t" \
    "v_sub_f32 v48, v48, v92\n\t" \
    "v_sub_f32 v49, v49, v93\n\t" \
    "v_mfma_f32_16x16x32_bf16 v[244:247], v[224:227], v[172:175], v[244:247]\n\t" \
    "v_sub_f32 v50, v50, v94\n\t" \
    "v_sub_f32 v51, v51, v95\n\t" \
    "v_mfma_f32_16x16x32_bf16 v[244:247], v[160:163], v[108:111], v[244:247]\n\t" \
    "v_sub_f32 v64, v64, v180\n\t" \
    "v_sub_f32 v65, v65, v181\n\t" \
    "v_sub_f32 v66, v66, v182\n\t" \
    "v_sub_f32 v67, v67, v183\n\t" \
    ".Lk43_%=:\n\t" \
    "v_cvt_pk_bf16_f32 v146, v112, v113\n\t" \
    "v_cvt_pk_bf16_f32 v147, v114, v115\n\t" \
    "v_cvt_pk_bf16_f32 v144, v48, v49\n\t" \
    "v_cvt_pk_bf16_f32 v145, v50, v51\n\t" \
    "s_nop 1\n\t" \
    "v_cvt_pk_bf16_f32 v46, v244, v245\n\t" \
    "v_cvt_pk_bf16_f32 v47, v246, v247\n\t" \
    "ds_write_b128 v42, v[144:147] offset:2064\n\t" \
    "v_cvt_pk_bf16_f32 v44, v64, v65\n\t" \
    "v_cvt_pk_bf16_f32 v45, v66, v67\n\t" \
    "v_add_u32 v17, 91392, v17\n\t" \
    "ds_write_b128 v42, v[44:47] offset:3088\n\t" \
    "s_add_u32 s54, s53, 0\n\t" \
    "buffer_load_dwordx4 v[68:71], v0, s[4:7], s54 offen offset:0 sc1\n\t" \
    "s_add_u32 s54, s53, 4243456\n\t" \
    "buffer_load_dwordx4 v[116:119], v0, s[4:7], s54 offen offset:0 sc1\n\t" \
    "s_waitcnt lgkmcnt(0)\n\t" \
    "s_barrier\n\t" \
    "s_memrealtime s[56:57]\n\t" \
    "ds_read_b64_tr_b16 v[60:61], v17 offset:0\n\t" \
    "ds_read_b64_tr_b16 v[62:63], v17 offset:4128\n\t" \
    "ds_read_b64_tr_b16 v[4:5], v17 offset:256\n\t" \
    "ds_read_b64_tr_b16 v[6:7], v17 offset:4384\n\t" \
    "ds_read_b64_tr_b16 v[188:189], v17 offset:16\n\t" \
    "ds_read_b64_tr_b16 v[190:191], v17 offset:4144\n\t" \
    "ds_read_b64_tr_b16 v[20:21], v17 offset:272\n\t" \
    "ds_read_b64_tr_b16 v[22:23], v17 offset:4400\n\t" \
    "s_waitcnt lgkmcnt(6)\n\t" \
    "v_mfma_f32_16x16x32_bf16 v[136:139], v[60:63], v[184:187], 0\n\t" \
    "s_waitcnt lgkmcnt(4)\n\t" \
    "v_mfma_f32_16x16x32_bf16 v[252:255], v[4:7], v[184:187], 0\n\t" \
    "s_waitcnt lgkmcnt(2)\n\t" \
    "v_mfma_f32_16x16x32_bf16 v[132:135], v[188:191], v[184:187], 0\n\t" \
    "s_waitcnt lgkmcnt(0)\n\t" \
    "v_mfma_f32_16x16x32_bf16 v[248:251], v[20:23], v[184:187], 0\n\t" \
    "s_nop 1\n\t" \
    "v_bfi_b32 v136, s41, v37, v136\n\t" \
    "s_nop 0\n\t" \
    "v_bfi_b32 v252, s41, v37, v252\n\t" \
    "v_cvt_pk_bf16_f32 v157, v138, v139\n\t" \
    "v_cvt_pk_bf16_f32 v159, v254, v255\n\t" \
    "v_cvt_pk_bf16_f32 v156, v136, v137\n\t" \
    "v_cvt_pk_bf16_f32 v158, v252, v253\n\t" \
    "v_bfi_b32 v132, s41, v37, v132\n\t" \
    "v_bfi_b32 v248, s41, v37, v248\n\t" \
    "buffer_store_dwordx4 v[156:159], v40, s[4:7], s50 offen offset:0 sc1\n\t" \
    "v_cvt_pk_bf16_f32 v169, v134, v135\n\t" \
    "v_cvt_pk_bf16_f32 v171, v250, v251\n\t" \
    "v_cvt_pk_bf16_f32 v168, v132, v133\n\t" \
    "v_cvt_pk_bf16_f32 v170, v248, v249\n\t" \
    "buffer_store_dwordx4 v[168:171], v40, s[4:7], s50 offen offset:1024 sc1\n\t" \
    "s_cmp_ge_u32 s25, 252\n\t" \
    "s_cbranch_scc1 .Lend_%=\n\t" \
    "ds_read_b128 v[192:195], v10 offset:3072\n\t" \
    "ds_read_b128 v[212:215], v10 offset:4096\n\t" \
    "v_xor_b32 v9, 0x80, v10\n\t" \
    "v_xor_b32 v16, 64, v18\n\t" \
    "s_mov_b32 s20, 0xff00ff00\n\t" \
    "s_mov_b32 s21, 0xff00ff00\n\t" \
    "ds_read_b128 v[200:203], v9 offset:3072\n\t" \
    "ds_read_b128 v[232:235], v9 offset:4096\n\t" \
    "s_waitcnt lgkmcnt(0)\n\t" \
    "s_sub_u32 s37, s16, 300\n\t" \
    "s_mov_b32 s62, 300\n\t" \
    "s_sub_u32 s39, s58, s37\n\t" \
    "s_cmp_gt_u32 s39, 600\n\t" \
    "s_cselect_b32 s39, 400, s39\n\t" \
    "s_max_u32 s62, s62, s39\n\t" \
    "s_sub_u32 s39, s59, s37\n\t" \
    "s_cmp_gt_u32 s39, 600\n\t" \
    "s_cselect_b32 s39, 400, s39\n\t" \
    "s_max_u32 s62, s62, s39\n\t" \
    "s_sub_u32 s39, s60, s37\n\t" \
    "s_cmp_gt_u32 s39, 600\n\t" \
    "s_cselect_b32 s39, 400, s39\n\t" \
    "s_max_u32 s62, s62, s39\n\t" \
    "s_sub_u32 s39, s61, s37\n\t" \
    "s_cmp_gt_u32 s39, 600\n\t" \
    "s_cselect_b32 s39, 400, s39\n\t" \
    "s_max_u32 s62, s62, s39\n\t" \
    "s_sub_u32 s62, s62, 300\n\t" \
    "s_mul_i32 s62, s62, 5\n\t" \
    "s_lshr_b32 s62, s62, 2\n\t" \
    "s_waitcnt vmcnt(2)\n\t" \
    "v_and_b32 v19, v68, v70\n\t" \
    "v_bitop3_b32 v19, v19, v116, v118 bitop3:0x80\n\t" \
    "v_and_b32 v19, 1, v19\n\t" \
    "v_cmp_eq_u32 vcc, 0, v19\n\t" \
    "s_nop 1\n\t" \
    "s_cbranch_vccnz .Ls37_%=\n\t" \
    ".Lb37_%=:\n\t" \
    "ds_write_b128 v43, v[68:71] offset:33024\n\t" \
    "ds_write_b128 v43, v[116:119] offset:33152\n\t" \
    "s_add_u32 s37, s62, 110\n\t" \
    ".Lh44_%=:\n\t" \
    "s_memrealtime s[22:23]\n\t" \
    "s_waitcnt lgkmcnt(0)\n\t" \
    "s_sub_u32 s22, s22, s18\n\t" \
    "s_cmp_lt_u32 s22, s37\n\t" \
    "s_cbranch_scc1 .Lh44_%=\n\t" \
    "s_add_u32 s54, s53, 524288\n\t" \
    "buffer_load_dwordx4 v[236:239], v0, s[4:7], s54 offen offset:0 sc1\n\t" \
    "s_add_u32 s54, s53, 4767744\n\t" \
    "buffer_load_dwordx4 v[220:223], v0, s[4:7], s54 offen offset:0 sc1\n\t" \
    "s_waitcnt lgkmcnt(0)\n\t" \
    "s_barrier\n\t" \
    "ds_read_b64_tr_b16 v[140:141], v41 offset:33024\n\t" \
    "ds_read_b64_tr_b16 v[142:143], v41 offset:33280\n\t" \
    "ds_read_b64_tr_b16 v[216:217], v41 offset:33032\n\t" \
    "ds_read_b64_tr_b16 v[218:219], v41 offset:33288\n\t" \
    "s_waitcnt lgkmcnt(2)\n\t" \
    "v_mfma_f32_16x16x32_bf16 v[12:15], v[140:143], v[192:195], 0\n\t" \
    "s_waitcnt lgkmcnt(0)\n\t" \
    "v_mfma_f32_16x16x32_bf16 v[240:243], v[216:219], v[192:195], 0\n\t" \
    "v_mfma_f32_16x16x32_bf16 v[120:123], v[140:143], v[232:235], 0\n\t" \
    "v_mfma_f32_16x16x32_bf16 v[72:75], v[216:219], v[232:235], 0\n\t" \
    "s_nop 3\n\t" \
    "v_cvt_pk_bf16_f32 v148, v12, v13\n\t" \
    "v_cvt_pk_bf16_f32 v149, v14, v15\n\t" \
    "v_cvt_pk_bf16_f32 v150, v240, v241\n\t" \
    "v_cvt_pk_bf16_f32 v151, v242, v243\n\t" \
    "v_cvt_pk_bf16_f32 v56, v120, v121\n\t" \
    "v_cvt_pk_bf16_f32 v57, v122, v123\n\t" \
    "v_cvt_pk_bf16_f32 v58, v72, v73\n\t" \
    "v_cvt_pk_bf16_f32 v59, v74, v75\n\t" \
    "s_waitcnt vmcnt(0)\n\t" \
    "v_and_b32 v2, v236, v238\n\t" \
    "v_bitop3_b32 v2, v2, v220, v222 bitop3:0x80\n\t" \
    "v_and_b32 v2, 1, v2\n\t" \
    "v_cmp_eq_u32 vcc, 0, v2\n\t" \
    "s_nop 1\n\t" \
    "s_cbranch_vccnz .Ls38_%=\n\t" \
    ".Lb38_%=:\n\t" \
    "ds_write_b128 v43, v[236:239] offset:0\n\t" \
    "ds_write_b128 v43, v[220:223] offset:128\n\t" \
    "s_add_u32 s37, s62, 95\n\t" \
    ".Lh45_%=:\n\t" \
    "s_memrealtime s[22:23]\n\t" \
    "s_waitcnt lgkmcnt(0)\n\t" \
    "s_sub_u32 s22, s22, s56\n\t" \
    "s_cmp_lt_u32 s22, s37\n\t" \
    "s_cbranch_scc1 .Lh45_%=\n\t" \
    "s_add_u32 s54, s53, 1048576\n\t" \
    "buffer_load_dwordx4 v[128:131], v0, s[4:7], s54 offen offset:0 sc1\n\t" \
    "s_add_u32 s54, s53, 5292032\n\t" \
    "buffer_load_dwordx4 v[196:199], v0, s[4:7], s54 offen offset:0 sc1\n\t" \
    "s_waitcnt lgkmcnt(0)\n\t" \
    "s_barrier\n\t" \
    "ds_read_b64_tr_b16 v[208:209], v41 offset:0\n\t" \
    "ds_read_b64_tr_b16 v[210:211], v41 offset:256\n\t" \
    "ds_read_b64_tr_b16 v[28:29], v41 offset:8\n\t" \
    "ds_read_b64_tr_b16 v[30:31], v41 offset:264\n\t" \
    "s_waitcnt lgkmcnt(2)\n\t" \
    "v_mfma_f32_16x16x32_bf16 v[76:79], v[208:211], v[200:203], 0\n\t" \
    "s_waitcnt lgkmcnt(0)\n\t" \
    "v_mfma_f32_16x16x32_bf16 v[88:91], v[28:31], v[200:203], 0\n\t" \
    "v_mfma_f32_16x16x32_bf16 v[52:55], v[208:211], v[212:215], 0\n\t" \
    "v_mfma_f32_16x16x32_bf16 v[84:87], v[28:31], v[212:215], 0\n\t" \
    "s_nop 3\n\t" \
    "v_cvt_pk_bf16_f32 v204, v76, v77\n\t" \
    "v_cvt_pk_bf16_f32 v205, v78, v79\n\t" \
    "v_cvt_pk_bf16_f32 v206, v88, v89\n\t" \
    "v_cvt_pk_bf16_f32 v207, v90, v91\n\t" \
    "v_cvt_pk_bf16_f32 v124, v52, v53\n\t" \
    "v_cvt_pk_bf16_f32 v125, v54, v55\n\t" \
    "v_cvt_pk_bf16_f32 v126, v84, v85\n\t" \
    "v_cvt_pk_bf16_f32 v127, v86, v87\n\t" \
    "v_cndmask_b32 v164, v148, v204, s[20:21]\n\t" \
    "v_cndmask_b32 v165, v149, v205, s[20:21]\n\t" \
    "v_cndmask_b32 v166, v150, v206, s[20:21]\n\t" \
    "v_cndmask_b32 v167, v151, v207, s[20:21]\n\t" \
    "v_cndmask_b32 v100, v204, v148, s[20:21]\n\t" \
    "v_cndmask_b32 v101, v205, v149, s[20:21]\n\t" \
    "buffer_store_dwordx4 v[164:167], v18, s[8:11], 0 offen offset:0 sc1\n\t" \
    "v_cndmask_b32 v102, v206, v150, s[20:21]\n\t" \
    "v_cndmask_b32 v103, v207, v151, s[20:21]\n\t" \
    "buffer_store_dwordx4 v[100:103], v16, s[8:11], 0 offen offset:3072 sc1\n\t" \
    "s_waitcnt vmcnt(2)\n\t" \
    "v_and_b32 v1, v128, v130\n\t" \
    "v_bitop3_b32 v1, v1, v196, v198 bitop3:0x80\n\t" \
    "v_and_b32 v1, 1, v1\n\t" \
    "v_cmp_eq_u32 vcc, 0, v1\n\t" \
    "s_nop 1\n\t" \
    "s_cbranch_vccnz .Ls39_%=\n\t" \
    ".Lb39_%=:\n\t" \
    "ds_write_b128 v43, v[128:131] offset:16512\n\t" \
    "ds_write_b128 v43, v[196:199] offset:16640\n\t" \
    "s_waitcnt lgkmcnt(0)\n\t" \
    "s_barrier\n\t" \
    "ds_read_b64_tr_b16 v[24:25], v41 offset:16512\n\t" \
    "ds_read_b64_tr_b16 v[26:27], v41 offset:16768\n\t" \
    "ds_read_b64_tr_b16 v[176:177], v41 offset:16520\n\t" \
    "ds_read_b64_tr_b16 v[178:179], v41 offset:16776\n\t" \
    "s_waitcnt lgkmcnt(2)\n\t" \
    "v_mfma_f32_16x16x32_bf16 v[228:231], v[24:27], v[192:195], 0\n\t" \
    "s_waitcnt lgkmcnt(0)\n\t" \
    "v_mfma_f32_16x16x32_bf16 v[96:99], v[176:179], v[192:195], 0\n\t" \
    "v_mfma_f32_16x16x32_bf16 v[152:155], v[24:27], v[232:235], 0\n\t" \
    "v_mfma_f32_16x16x32_bf16 v[80:83], v[176:179], v[232:235], 0\n\t" \
    "s_nop 3\n\t" \
    "v_cvt_pk_bf16_f32 v32, v228, v229\n\t" \
    "v_cvt_pk_bf16_f32 v33, v230, v231\n\t" \
    "v_cvt_pk_bf16_f32 v34, v96, v97\n\t" \
    "v_cvt_pk_bf16_f32 v35, v98, v99\n\t" \
    "v_cvt_pk_bf16_f32 v104, v152, v153\n\t" \
    "v_cvt_pk_bf16_f32 v105, v154, v155\n\t" \
    "v_cvt_pk_bf16_f32 v106, v80, v81\n\t" \
    "v_cvt_pk_bf16_f32 v107, v82, v83\n\t" \
    "v_cndmask_b32 v172, v32, v56, s[20:21]\n\t" \
    "v_cndmask_b32 v173, v33, v57, s[20:21]\n\t" \
    "v_cndmask_b32 v174, v34, v58, s[20:21]\n\t" \
    "v_cndmask_b32 v175, v35, v59, s[20:21]\n\t" \
    "v_cndmask_b32 v224, v56, v32, s[20:21]\n\t" \
    "v_cndmask_b32 v225, v57, v33, s[20:21]\n\t" \
    "buffer_store_dwordx4 v[172:175], v18, s[8:11], 0 offen offset:128 sc1\n\t" \
    "v_cndmask_b32 v226, v58, v34, s[20:21]\n\t" \
    "v_cndmask_b32 v227, v59, v35, s[20:21]\n\t" \
    "buffer_store_dwordx4 v[224:227], v16, s[8:11], 0 offen offset:3200 sc1\n\t" \
    "v_cndmask_b32 v92, v124, v104, s[20:21]\n\t" \
    "v_cndmask_b32 v93, v125, v105, s[20:21]\n\t" \
    "v_cndmask_b32 v94, v126, v106, s[20:21]\n\t" \
    "v_cndmask_b32 v95, v127, v107, s[20:21]\n\t" \
    "v_cndmask_b32 v108, v104, v124, s[20:21]\n\t" \
    "v_cndmask_b32 v109, v105, v125, s[20:21]\n\t" \
    "buffer_store_dwordx4 v[92:95], v18, s[8:11], 0 offen offset:256 sc1\n\t" \
    "v_cndmask_b32 v110, v106, v126, s[20:21]\n\t" \
    "v_cndmask_b32 v111, v107, v127, s[20:21]\n\t" \
    "buffer_store_dwordx4 v[108:111], v16, s[8:11], 0 offen offset:3328 sc1\n\t" \
    "s_or_b32 s37, s2, s24\n\t" \
    "s_cbranch_scc1 .Lend_%=\n\t" \
    "s_mov_b32 s37, 34635776\n\t" \
    "v_mov_b32 v3, s34\n\t" \
    "s_mov_b64 exec, 1\n\t" \
    "buffer_store_dword v3, v0, s[4:7], s37 offen sc1\n\t" \
    "s_mov_b64 exec, -1\n\t" \
    ".Lend_%=:\n\t" \
    "s_endpgm\n\t" \
    ".Lq1024_%=:\n\t" \
    "s_mov_b32 s40, 0\n\t" \
    ".Lr1024_%=:\n\t" \
    "s_add_u32 s12, s55, 34603008\n\t" \
    "s_buffer_load_dword s13, s[4:7], s12 glc\n\t" \
    "s_buffer_load_dword s14, s[4:7], s12 offset:0x400 glc\n\t" \
    "s_waitcnt lgkmcnt(0)\n\t" \
    "s_xor_b32 s13, s13, s34\n\t" \
    "s_xor_b32 s14, s14, s34\n\t" \
    "s_or_b32 s13, s13, s14\n\t" \
    "s_cbranch_scc0 .Lb23_%=\n\t" \
    "s_add_u32 s40, s40, 1\n\t" \
    "s_cmp_lt_u32 s40, 1\n\t" \
    "s_cbranch_scc1 .Lr1024_%=\n\t" \
    "s_branch .Ls23_%=\n\t" \
    ".Lq1021_%=:\n\t" \
    "s_mov_b32 s40, 0\n\t" \
    ".Lr1021_%=:\n\t" \
    "s_nop 0\n\t" \
    "v_readlane_b32 s12, v15, 0\n\t" \
    "v_readlane_b32 s13, v15, 1\n\t" \
    "v_readlane_b32 s14, v15, 2\n\t" \
    "v_readlane_b32 s15, v15, 3\n\t" \
    "v_readlane_b32 s16, v15, 4\n\t" \
    "v_readlane_b32 s17, v15, 5\n\t" \
    "v_readlane_b32 s18, v15, 6\n\t" \
    "v_readlane_b32 s19, v15, 7\n\t" \
    "v_readlane_b32 s3, v15, 8\n\t" \
    "s_add_u32 s12, s12, s33\n\t" \
    "s_add_u32 s13, s13, s33\n\t" \
    "s_add_u32 s14, s14, s33\n\t" \
    "s_add_u32 s15, s15, s33\n\t" \
    "s_add_u32 s16, s16, s33\n\t" \
    "s_add_u32 s17, s17, s33\n\t" \
    "s_add_u32 s18, s18, s33\n\t" \
    "s_add_u32 s19, s19, s33\n\t" \
    "s_add_u32 s3, s3, s33\n\t" \
    "s_buffer_load_dword s12, s[4:7], s12 offset:0xc0000 glc\n\t" \
    "s_buffer_load_dword s13, s[4:7], s13 offset:0xc0000 glc\n\t" \
    "s_buffer_load_dword s14, s[4:7], s14 offset:0xc0000 glc\n\t" \
    "s_buffer_load_dword s15, s[4:7], s15 offset:0xc0000 glc\n\t" \
    "s_buffer_load_dword s16, s[4:7], s16 offset:0xc0000 glc\n\t" \
    "s_buffer_load_dword s17, s[4:7], s17 offset:0xc0000 glc\n\t" \
    "s_buffer_load_dword s18, s[4:7], s18 offset:0xc0000 glc\n\t" \
    "s_buffer_load_dword s19, s[4:7], s19 offset:0xc0000 glc\n\t" \
    "s_buffer_load_dword s3, s[4:7], s3 offset:0xc0000 glc\n\t" \
    "s_waitcnt lgkmcnt(0)\n\t" \
    "s_xor_b32 s12, s12, s34\n\t" \
    "s_xor_b32 s13, s13, s34\n\t" \
    "s_xor_b32 s14, s14, s34\n\t" \
    "s_xor_b32 s15, s15, s34\n\t" \
    "s_xor_b32 s16, s16, s34\n\t" \
    "s_xor_b32 s17, s17, s34\n\t" \
    "s_xor_b32 s18, s18, s34\n\t" \
    "s_xor_b32 s19, s19, s34\n\t" \
    "s_xor_b32 s3, s3, s34\n\t" \
    "s_or_b32 s12, s12, s13\n\t" \
    "s_or_b32 s12, s12, s14\n\t" \
    "s_or_b32 s12, s12, s15\n\t" \
    "s_or_b32 s12, s12, s16\n\t" \
    "s_or_b32 s12, s12, s17\n\t" \
    "s_or_b32 s12, s12, s18\n\t" \
    "s_or_b32 s12, s12, s19\n\t" \
    "s_or_b32 s12, s12, s3\n\t" \
    "s_cbranch_scc0 .Lb20_%=\n\t" \
    "s_add_u32 s40, s40, 1\n\t" \
    "s_cmp_lt_u32 s40, 1\n\t" \
    "s_cbranch_scc1 .Lr1021_%=\n\t" \
    "s_branch .Ls20_%=\n\t" \
    ".Lpb_%=:\n\t" \
    "s_sub_u32 s37, s24, 4\n\t" \
    "s_cbranch_scc1 .Lpe_%=\n\t" \
    "s_mul_i32 s38, s37, 0x2100\n\t" \
    "s_and_b32 s38, s38, 0x2300\n\t" \
    "s_add_u32 s38, s38, 34652160\n\t" \
    "s_mov_b32 s60, -1\n\t" \
    "s_mov_b32 s61, -1\n\t" \
    "s_memrealtime s[62:63]\n\t" \
    "s_mov_b64 exec, 0xff\n\t" \
    ".Lpc1_%=:\n\t" \
    "s_memtime s[56:57]\n\t" \
    "s_waitcnt lgkmcnt(0)\n\t" \
    "buffer_store_dwordx4 v[56:59], v0, s[4:7], s38 offen sc1\n\t" \
    "s_waitcnt vmcnt(0)\n\t" \
    "s_memtime s[58:59]\n\t" \
    "s_memrealtime s[22:23]\n\t" \
    "s_waitcnt lgkmcnt(0)\n\t" \
    "s_sub_u32 s56, s58, s56\n\t" \
    "s_min_u32 s56, s56, s60\n\t" \
    "s_mov_b32 s60, s61\n\t" \
    "s_mov_b32 s61, s56\n\t" \
    "s_xor_b32 s38, s38, 0x2000\n\t" \
    "s_add_u32 s38, s38, 0x400\n\t" \
    "s_andn2_b32 s38, s38, 0x1000\n\t" \
    "s_sub_u32 s22, s22, s62\n\t" \
    "s_cmp_lt_u32 s22, 60\n\t" \
    "s_cbranch_scc1 .Lpc1_%=\n\t" \
    "s_mov_b64 exec, -1\n\t" \
    "s_bitcmp1_b32 s38, 13\n\t" \
    "s_cselect_b32 s56, s61, s60\n\t" \
    "s_cselect_b32 s57, s60, s61\n\t" \
    "v_mov_b32 v56, s56\n\t" \
    "v_mov_b32 v57, s57\n\t" \
    "s_lshl3_add_u32 s37, s37, 156992\n\t" \
    "v_mov_b32 v58, s37\n\t" \
    "ds_write_b64 v58, v[56:57]\n\t" \
    "s_branch .Lpe_%=\n\t" \
    ".Lr2_%=:\n\t" \
    "s_add_u32 s40, s40, 1\n\t" \
    "s_cmp_gt_u32 s40, 32\n\t" \
    "s_cbranch_scc1 .Lr3_%=\n\t" \
    "s_sleep 2\n\t" \
    "s_and_b32 s37, s2, 7\n\t" \
    "s_lshl_b32 s37, s37, 10\n\t" \
    "s_add_u32 s37, s37, 34643968\n\t" \
    "v_and_b32 v34, 0x70, v0\n\t" \
    "v_lshlrev_b32 v34, 3, v34\n\t" \
    "buffer_load_dword v39, v34, s[4:7], s37 offen sc1\n\t" \
    "s_waitcnt vmcnt(0)\n\t" \
    "v_readlane_b32 s56, v39, 0\n\t" \
    "v_readlane_b32 s57, v39, 1\n\t" \
    "v_readlane_b32 s58, v39, 2\n\t" \
    "v_readlane_b32 s59, v39, 3\n\t" \
    "v_readlane_b32 s60, v39, 4\n\t" \
    "v_readlane_b32 s61, v39, 5\n\t" \
    "v_readlane_b32 s62, v39, 6\n\t" \
    "v_readlane_b32 s63, v39, 7\n\t" \
    "s_branch .Lr1_%=\n\t" \
    ".Lpp_%=:\n\t" \
    "ds_read_b128 v[112:115], v9 offset:64\n\t" \
    "ds_read_b128 v[116:119], v9 offset:80\n\t" \
    "s_getreg_b32 s37, hwreg(HW_REG_XCC_ID, 0, 2)\n\t" \
    "s_waitcnt lgkmcnt(0)\n\t" \
    "v_min3_u32 v112, v112, v114, v116\n\t" \
    "v_min3_u32 v113, v113, v115, v117\n\t" \
    "v_min_u32 v112, v112, v118\n\t" \
    "v_min_u32 v113, v113, v119\n\t" \
    "v_cmp_gt_u32 vcc, v112, v113\n\t" \
    "s_nop 4\n\t" \
    "s_and_b32 s38, vcc_lo, 4\n\t" \
    "s_add_u32 s37, s37, s38\n\t" \
    "s_add_u32 s37, s37, 1\n\t" \
    "s_and_b32 s37, s37, 7\n\t" \
    "s_lshl_b32 s37, s37, 1\n\t" \
    "s_xor_b32 s37, s37, s34\n\t" \
    "v_mov_b32 v112, s37\n\t" \
    "v_mov_b32 v113, s37\n\t" \
    "v_mov_b32 v114, s37\n\t" \
    "v_mov_b32 v115, s37\n\t" \
    "s_mov_b32 s39, 34636800\n\t" \
    "buffer_store_dwordx4 v[112:115], v0, s[4:7], s39 offen sc1\n\t" \
    "s_nop 1\n\t" \
    "s_branch .Lpq_%=\n\t" \
    ".Lr5_%=:\n\t" \
    "s_add_u32 s40, s40, 1\n\t" \
    "s_cmp_gt_u32 s40, 8192\n\t" \
    "s_cbranch_scc1 .Lr6_%=\n\t" \
    "s_sleep 4\n\t" \
    "v_mov_b32 v37, 0\n\t" \
    "buffer_load_dword v11, v37, s[4:7], s52 offen sc1\n\t" \
    "s_waitcnt vmcnt(0)\n\t" \
    "v_readfirstlane_b32 s36, v11\n\t" \
    "s_branch .Lr4_%=\n\t" \
    ".Ls20_%=:\n\t" \
    "s_mov_b32 s40, 0\n\t" \
    "s_add_u32 s38, s33, 786432\n\t" \
    ".Ll20_%=:\n\t" \
    "s_sleep 2\n\t" \
    "buffer_load_dword v16, v15, s[4:7], s38 offen sc1\n\t" \
    "s_waitcnt vmcnt(0)\n\t" \
    "v_cmp_ne_u32 vcc, s34, v16\n\t" \
    "s_add_u32 s40, s40, 1\n\t" \
    "s_cmp_ge_u32 s40, 8192\n\t" \
    "s_cbranch_scc1 .Lb20_%=\n\t" \
    "s_cbranch_vccnz .Ll20_%=\n\t" \
    "s_branch .Lb20_%=\n\t" \
    ".Ls23_%=:\n\t" \
    "s_mov_b32 s40, 0\n\t" \
    "s_add_u32 s38, s55, 34603008\n\t" \
    ".Ll23_%=:\n\t" \
    "s_sleep 2\n\t" \
    "buffer_load_dword v8, v38, s[4:7], s38 offen sc1\n\t" \
    "s_waitcnt vmcnt(0)\n\t" \
    "v_cmp_ne_u32 vcc, s34, v8\n\t" \
    "s_add_u32 s40, s40, 1\n\t" \
    "s_cmp_ge_u32 s40, 8192\n\t" \
    "s_cbranch_scc1 .Lb23_%=\n\t" \
    "s_cbranch_vccnz .Ll23_%=\n\t" \
    "s_branch .Lb23_%=\n\t" \
    ".Ls37_%=:\n\t" \
    "s_mov_b32 s40, 0\n\t" \
    ".Ll37_%=:\n\t" \
    "s_add_u32 s54, s53, 0\n\t" \
    "buffer_load_dwordx4 v[68:71], v0, s[4:7], s54 offen offset:0 sc1\n\t" \
    "s_add_u32 s54, s53, 4243456\n\t" \
    "buffer_load_dwordx4 v[116:119], v0, s[4:7], s54 offen offset:0 sc1\n\t" \
    "s_waitcnt vmcnt(0)\n\t" \
    "v_and_b32 v19, v68, v70\n\t" \
    "v_bitop3_b32 v19, v19, v116, v118 bitop3:0x80\n\t" \
    "v_and_b32 v19, 1, v19\n\t" \
    "v_cmp_eq_u32 vcc, 0, v19\n\t" \
    "s_add_u32 s40, s40, 1\n\t" \
    "s_cmp_ge_u32 s40, 8192\n\t" \
    "s_cbranch_scc1 .Lb37_%=\n\t" \
    "s_cbranch_vccnz .Ll37_%=\n\t" \
    "s_branch .Lb37_%=\n\t" \
    ".Ls38_%=:\n\t" \
    "s_mov_b32 s40, 0\n\t" \
    ".Ll38_%=:\n\t" \
    "s_add_u32 s54, s53, 524288\n\t" \
    "buffer_load_dwordx4 v[236:239], v0, s[4:7], s54 offen offset:0 sc1\n\t" \
    "s_add_u32 s54, s53, 4767744\n\t" \
    "buffer_load_dwordx4 v[220:223], v0, s[4:7], s54 offen offset:0 sc1\n\t" \
    "s_waitcnt vmcnt(0)\n\t" \
    "v_and_b32 v2, v236, v238\n\t" \
    "v_bitop3_b32 v2, v2, v220, v222 bitop3:0x80\n\t" \
    "v_and_b32 v2, 1, v2\n\t" \
    "v_cmp_eq_u32 vcc, 0, v2\n\t" \
    "s_add_u32 s40, s40, 1\n\t" \
    "s_cmp_ge_u32 s40, 8192\n\t" \
    "s_cbranch_scc1 .Lb38_%=\n\t" \
    "s_cbranch_vccnz .Ll38_%=\n\t" \
    "s_branch .Lb38_%=\n\t" \
    ".Ls39_%=:\n\t" \
    "s_mov_b32 s40, 0\n\t" \
    ".Ll39_%=:\n\t" \
    "s_add_u32 s54, s53, 1048576\n\t" \
    "buffer_load_dwordx4 v[128:131], v0, s[4:7], s54 offen offset:0 sc1\n\t" \
    "s_add_u32 s54, s53, 5292032\n\t" \
    "buffer_load_dwordx4 v[196:199], v0, s[4:7], s54 offen offset:0 sc1\n\t" \
    "s_waitcnt vmcnt(0)\n\t" \
    "v_and_b32 v1, v128, v130\n\t" \
    "v_bitop3_b32 v1, v1, v196, v198 bitop3:0x80\n\t" \
    "v_and_b32 v1, 1, v1\n\t" \
    "v_cmp_eq_u32 vcc, 0, v1\n\t" \
    "s_add_u32 s40, s40, 1\n\t" \
    "s_cmp_ge_u32 s40, 8192\n\t" \
    "s_cbranch_scc1 .Lb39_%=\n\t" \
    "s_cbranch_vccnz .Ll39_%=\n\t" \
    "s_branch .Lb39_%=\n\t" \
    ".Lfin_%=:\n\t"
#define K_CLOBBERS "v1", "v2", "v3", "v4", "v5", "v6", "v7", "v8", "v9", "v10", "v11", "v12", "v13", "v14", "v15", "v16", "v17", "v18", "v19", "v20", "v21", "v22", "v23", "v24", "v25", "v26", "v27", "v28", "v29", "v30", "v31", "v32", "v33", "v34", "v35", "v36", "v37", "v38", "v39", "v40", "v41", "v42", "v43", "v44", "v45", "v46", "v47", "v48", "v49", "v50", "v51", "v52", "v53", "v54", "v55", "v56", "v57", "v58", "v59", "v60", "v61", "v62", "v63", "v64", "v65", "v66", "v67", "v68", "v69", "v70", "v71", "v72", "v73", "v74", "v75", "v76", "v77", "v78", "v79", "v80", "v81", "v82", "v83", "v84", "v85", "v86", "v87", "v88", "v89", "v90", "v91", "v92", "v93", "v94", "v95", "v96", "v97", "v98", "v99", "v100", "v101", "v102", "v103", "v104", "v105", "v106", "v107", "v108", "v109", "v110", "v111", "v112", "v113", "v114", "v115", "v116", "v117", "v118", "v119", "v120", "v121", "v122", "v123", "v124", "v125", "v126", "v127", "v128", "v129", "v130", "v131", "v132", "v133", "v134", "v135", "v136", "v137", "v138", "v139", "v140", "v141", "v142", "v143", "v144", "v145", "v146", "v147", "v148", "v149", "v150", "v151", "v152", "v153", "v154", "v155", "v156", "v157", "v158", "v159", "v160", "v161", "v162", "v163", "v164", "v165", "v166", "v167", "v168", "v169", "v170", "v171", "v172", "v173", "v174", "v175", "v176", "v177", "v178", "v179", "v180", "v181", "v182", "v183", "v184", "v185", "v186", "v187", "v188", "v189", "v190", "v191", "v192", "v193", "v194", "v195", "v196", "v197", "v198", "v199", "v200", "v201", "v202", "v203", "v204", "v205", "v206", "v207", "v208", "v209", "v210", "v211", "v212", "v213", "v214", "v215", "v216", "v217", "v218", "v219", "v220", "v221", "v222", "v223", "v224", "v225", "v226", "v227", "v228", "v229", "v230", "v231", "v232", "v233", "v234", "v235", "v236", "v237", "v238", "v239", "v240", "v241", "v242", "v243", "v244", "v245", "v246", "v247", "v248", "v249", "v250", "v251", "v252", "v253", "v254", "v255", "s3", "s4", "s5", "s6", "s7", "s8", "s9", "s10", "s11", "s12", "s13", "s14", "s15", "s16", "s17", "s18", "s19", "s20", "s21", "s22", "s23", "s24", "s25", "s26", "s27", "s28", "s29", "s30", "s31", "s32", "s33", "s34", "s35", "s36", "s37", "s38", "s39", "s40", "s41", "s42", "s43", "s44", "s45", "s46", "s47", "s48", "s49", "s50", "s51", "s52", "s53", "s54", "s55", "s56", "s57", "s58", "s59", "s60", "s61", "s62", "s63", "s64", "s65"

extern "C" __global__ __launch_bounds__(512, 2)
void k_conv(const char* __restrict__ x, const char* __restrict__ wgt, char* __restrict__ ws, char* __restrict__ out)
{
  __shared__ __attribute__((aligned(256))) char smem[157568];
  unsigned tid = threadIdx.x;
  auto kp = __builtin_amdgcn_kernarg_segment_ptr();
  asm volatile(K_BODY : "+{v0}"(tid), "+{s[0:1]}"(kp)
               : "{s2}"(blockIdx.x), "i"((__attribute__((address_space(3))) char*)smem)
               : "memory", "scc", "vcc", K_CLOBBERS);
}

extern "C" void kernel_launch(void* const* d_in, const int* in_sizes, int n_in,
                              void* d_out, int out_size, void* d_ws, size_t ws_size,
                              hipStream_t stream) {
  k_conv<<<dim3(256), dim3(512), 0, stream>>>((const char*)d_in[0], (const char*)d_in[1], (char*)d_ws, (char*)d_out);
}
```

```cpp
#include <hip/hip_runtime.h>
#include <hip/hip_bf16.h>

#define WS_OFF 0
#define K_BODY \
    "s_load_dwordx2 s[16:17], s[0:1], 0x0\n\t" \
    "s_load_dwordx2 s[4:5], s[0:1], 0x10\n\t" \
    "s_load_dwordx2 s[12:13], s[0:1], 0x8\n\t" \
    "s_load_dwordx2 s[8:9], s[0:1], 0x18\n\t" \
    "s_getpc_b64 s[22:23]\n\t" \
    ".Lpc_%=:\n\t" \
    "s_cmp_ge_u32 s2, 16\n\t" \
    "s_cbranch_scc1 .Le1_%=\n\t" \
    "s_mov_b32 m0, 157312\n\t" \
    "v_lshlrev_b32 v8, 6, v0\n\t" \
    "v_min_u32 v8, .Lfin_%=-.Lpc_%=, v8\n\t" \
    "global_load_lds_dword v8, s[22:23]\n\t" \
    ".Le1_%=:\n\t" \
    "v_readfirstlane_b32 s24, v0\n\t" \
    "s_and_b32 s25, s2, 3\n\t" \
    "s_lshl_b32 s25, s25, 5\n\t" \
    "s_lshr_b32 s37, s2, 3\n\t" \
    "s_add_u32 s25, s25, s37\n\t" \
    "s_min_u32 s28, s25, 125\n\t" \
    "s_mul_i32 s26, s28, 57\n\t" \
    "s_lshr_b32 s26, s26, 9\n\t" \
    "s_mul_i32 s37, s26, 9\n\t" \
    "s_sub_u32 s27, s28, s37\n\t" \
    "s_bfe_u32 s37, s2, 0x10002\n\t" \
    "s_lshl1_add_u32 s26, s26, s37\n\t" \
    "v_and_b32 v0, 63, v0\n\t" \
    "v_lshlrev_b32 v0, 4, v0\n\t" \
    "s_lshr_b32 s24, s24, 6\n\t" \
    "s_sub_u32 s37, s24, 4\n\t" \
    "s_cbranch_scc1 .Le2_%=\n\t" \
    "s_cmp_ge_u32 s25, 126\n\t" \
    "s_cbranch_scc1 .Le2_%=\n\t" \
    "s_mul_i32 m0, s37, 12416\n\t" \
    "s_lshl2_add_u32 s37, s26, s37\n\t" \
    "s_mul_i32 s37, s37, 86016\n\t" \
    "s_mul_i32 s38, s27, 4992\n\t" \
    "s_add_u32 s37, s37, s38\n\t" \
    "s_addk_i32 s37, 896\n\t" \
    "v_add_u32 v9, s37, v0\n\t" \
    "s_cmp_eq_u32 s27, 0\n\t" \
    "s_cselect_b32 s38, 43008, 0\n\t" \
    "s_sub_u32 s39, s38, 2048\n\t" \
    "v_add_u32 v7, s39, v9\n\t" \
    "v_subrev_u32 v8, 0x400, v9\n\t" \
    "s_mov_b64 exec, 0xff\n\t" \
    "v_add_u32 v8, s38, v8\n\t" \
    "s_mov_b64 exec, -1\n\t" \
    "s_mov_b32 s18, 9633792\n\t" \
    "s_mov_b32 s19, 0x20000\n\t" \
    "s_mov_b32 s39, 43008\n\t" \
    "s_waitcnt lgkmcnt(0)\n\t" \
    "buffer_load_dwordx4 v7, s[16:19], 0 offen offset:0 nt lds\n\t" \
    "s_add_u32 m0, m0, 0x400\n\t" \
    "s_nop 0\n\t" \
    "buffer_load_dwordx4 v8, s[16:19], 0 offen offset:0 nt lds\n\t" \
    "s_add_u32 m0, m0, 0x400\n\t" \
    "s_nop 0\n\t" \
    "buffer_load_dwordx4 v9, s[16:19], 0 offen offset:0 nt lds\n\t" \
    "s_add_u32 m0, m0, 32\n\t" \
    "s_nop 0\n\t" \
    "buffer_load_dwordx4 v9, s[16:19], 0 offen offset:1024 nt lds\n\t" \
    "buffer_load_dwordx4 v9, s[16:19], 0 offen offset:2048 nt lds\n\t" \
    "buffer_load_dwordx4 v9, s[16:19], 0 offen offset:3072 nt lds\n\t" \
    "s_add_u32 m0, m0, 0x1020\n\t" \
    "s_nop 0\n\t" \
    "buffer_load_dwordx4 v7, s[16:19], s39 offen offset:0 nt lds\n\t" \
    "s_add_u32 m0, m0, 0x400\n\t" \
    "s_nop 0\n\t" \
    "buffer_load_dwordx4 v8, s[16:19], s39 offen offset:0 nt lds\n\t" \
    "s_add_u32 m0, m0, 0x400\n\t" \
    "s_nop 0\n\t" \
    "buffer_load_dwordx4 v9, s[16:19], s39 offen offset:0 nt lds\n\t" \
    "s_add_u32 m0, m0, 32\n\t" \
    "s_nop 0\n\t" \
    "buffer_load_dwordx4 v9, s[16:19], s39 offen offset:1024 nt lds\n\t" \
    "buffer_load_dwordx4 v9, s[16:19], s39 offen offset:2048 nt lds\n\t" \
    "buffer_load_dwordx4 v9, s[16:19], s39 offen offset:3072 nt lds\n\t" \
    ".Le2_%=:\n\t" \
    "s_mov_b32 s6, 34668544\n\t" \
    "s_mov_b32 s7, 0x20000\n\t" \
    "v_lshrrev_b32 v8, 7, v0\n\t" \
    "v_and_b32 v9, 0x70, v0\n\t" \
    "v_lshl_or_b32 v2, v8, 15, v9\n\t" \
    "s_lshl_b32 s32, s2, 7\n\t" \
    "s_add_u32 s32, s32, 33554432\n\t" \
    "s_cmp_ge_u32 s24, 4\n\t" \
    "s_cbranch_scc1 .Le3_%=\n\t" \
    "v_mov_b32 v12, 0\n\t" \
    "v_mov_b32 v13, 0\n\t" \
    "v_mov_b32 v14, 0\n\t" \
    "v_mov_b32 v15, 0\n\t" \
    "s_lshl_b32 s37, s24, 18\n\t" \
    "s_add_u32 s37, s37, s32\n\t" \
    "s_xor_b32 s38, s24, 2\n\t" \
    "s_lshr_b32 s39, s2, 3\n\t" \
    "s_or_b32 s38, s38, s39\n\t" \
    "s_memrealtime s[22:23]\n\t" \
    "s_waitcnt lgkmcnt(0)\n\t" \
    "s_cbranch_scc1 .Le4_%=\n\t" \
    "v_lshl_or_b32 v8, v8, 10, v9\n\t" \
    "v_mov_b32 v16, s22\n\t" \
    "v_mov_b32 v17, s22\n\t" \
    "v_mov_b32 v18, s22\n\t" \
    "v_mov_b32 v19, s22\n\t" \
    "s_add_u32 s38, s32, 1089536\n\t" \
    "buffer_store_dwordx4 v[16:19], v8, s[4:7], s38 offen sc1\n\t" \
    ".Le4_%=:\n\t" \
    "\n\t" \
    ".Le3_%=:\n\t" \
    "s_bitcmp1_b32 s2, 2\n\t" \
    "s_cselect_b32 s38, 126, 0\n\t" \
    "s_add_u32 s28, s28, s38\n\t" \
    "s_mul_i32 s30, s28, 293\n\t" \
    "s_lshr_b32 s30, s30, 11\n\t" \
    "s_mul_i32 s37, s30, 7\n\t" \
    "s_sub_u32 s28, s28, s37\n\t" \
    "s_mul_i32 s29, s30, 43\n\t" \
    "s_lshr_b32 s29, s29, 7\n\t" \
    "s_mul_i32 s37, s29, 3\n\t" \
    "s_sub_u32 s30, s30, s37\n\t" \
    "s_and_b32 s37, s29, 12\n\t" \
    "s_add_u32 s37, s37, s29\n\t" \
    "s_mul_i32 s31, s37, 768\n\t" \
    "s_lshl_b32 s37, s30, 6\n\t" \
    "s_add_u32 s31, s31, s37\n\t" \
    "s_add_u32 s41, s29, s30\n\t" \
    "s_add_u32 s41, s41, s28\n\t" \
    "s_and_b32 s41, s41, 3\n\t" \
    "s_cmp_lt_u32 s25, 126\n\t" \
    "s_cselect_b32 s14, 903168, 0\n\t" \
    "s_mov_b32 s15, 0x20000\n\t" \
    "v_lshrrev_b32 v10, 6, v0\n\t" \
    "v_lshrrev_b32 v11, 9, v0\n\t" \
    "v_sub_u32 v8, v10, v11\n\t" \
    "v_mul_u32_u24 v8, 18432, v8\n\t" \
    "v_and_b32 v9, 0x30, v0\n\t" \
    "v_lshlrev_b32 v11, 5, v11\n\t" \
    "v_bitop3_b32 v1, v8, v9, v11 bitop3:0xf6\n\t" \
    "s_mov_b32 exec_lo, 0xf0000000\n\t" \
    "s_mov_b32 exec_hi, 0xf0000000\n\t" \
    "v_bfrev_b32 v1, 1\n\t" \
    "s_mov_b64 exec, -1\n\t" \
    "s_cmp_ge_u32 s24, 4\n\t" \
    "s_cbranch_scc1 .Le9_%=\n\t" \
    "v_bfe_u32 v20, v0, 4, 4\n\t" \
    "v_and_b32 v10, 12, v10\n\t" \
    "v_cvt_f32_ubyte0 v21, v10\n\t" \
    "v_and_b32 v22, 1, v20\n\t" \
    "v_cvt_f32_ubyte0 v22, v22\n\t" \
    "v_mul_f32 v22, 0x3e800000, v22\n\t" \
    "v_lshrrev_b32 v23, 1, v20\n\t" \
    "v_mov_b32 v32, 0\n\t" \
    "v_mov_b32 v33, -1\n\t" \
    "v_mov_b32 v31, 0x3e800000\n\t" \
    "s_mov_b32 s65, 0\n\t" \
    "s_mov_b32 s40, 0\n\t" \
    "s_cmp_eq_u32 s24, 1\n\t" \
    "s_cbranch_scc1 .Lm1_%=\n\t" \
    "s_cmp_eq_u32 s24, 2\n\t" \
    "s_cbranch_scc1 .Lm2_%=\n\t" \
    "s_cmp_eq_u32 s24, 3\n\t" \
    "s_cbranch_scc1 .Lm3_%=\n\t" \
    "s_add_u32 s37, s32, 1048576\n\t" \
    "s_mov_b64 exec, 0xff\n\t" \
    "\n\t" \
    "s_mov_b64 exec, -1\n\t" \
    "v_or_b32 v24, 1, v20\n\t" \
    "v_cvt_f32_ubyte0 v24, v24\n\t" \
    "v_mul_f32 v24, 0x3c800000, v24\n\t" \
    "v_lshrrev_b32 v25, 4, v0\n\t" \
    "v_and_b32 v25, 16, v25\n\t" \
    "v_lshrrev_b32 v26, 2, v11\n\t" \
    "v_or_b32 v25, v25, v26\n\t" \
    "v_cvt_f32_ubyte0 v25, v25\n\t" \
    "v_add_f32 v26, 2.0, v25\n\t" \
    "v_add_f32 v27, 4.0, v25\n\t" \
    "v_add_f32 v28, 4.0, v26\n\t" \
    "v_mov_b32 v29, v22\n\t" \
    "v_mov_b32 v30, v22\n\t" \
    "v_mov_b32 v31, v24\n\t" \
    "s_mov_b32 s64, 0x3e800000\n\t" \
    "s_mov_b32 s65, 0x3e800000\n\t" \
    "v_add_u32 v34, 154880, v0\n\t" \
    "s_branch .Ltl_%=\n\t" \
    ".Lm1_%=:\n\t" \
    "v_lshrrev_b32 v24, 5, v0\n\t" \
    "v_and_b32 v24, 12, v24\n\t" \
    "v_and_or_b32 v24, v20, 3, v24\n\t" \
    "v_cvt_f32_ubyte0 v24, v24\n\t" \
    "v_mul_f32 v24, 0x3d924925, v24\n\t" \
    "v_mul_f32 v32, 0x41000000, v24\n\t" \
    "v_lshrrev_b32 v27, 3, v11\n\t" \
    "v_cvt_f32_ubyte0 v27, v27\n\t" \
    "v_add_f32 v25, -2.0, v27\n\t" \
    "v_add_f32 v26, -1.0, v27\n\t" \
    "v_add_f32 v28, 1.0, v27\n\t" \
    "v_and_b32 v29, 4, v20\n\t" \
    "v_cvt_f32_ubyte0 v29, v29\n\t" \
    "v_mul_f32 v29, 0x3d800000, v29\n\t" \
    "v_mov_b32 v30, v29\n\t" \
    "v_mov_b32 v31, 0xbe800000\n\t" \
    "s_mov_b32 s64, 0\n\t" \
    "s_mov_b32 exec_hi, 0\n\t" \
    "v_mov_b32 v33, 0\n\t" \
    "s_mov_b64 exec, -1\n\t" \
    "v_add_u32 v34, 149760, v0\n\t" \
    "s_branch .Ltl_%=\n\t" \
    ".Lm2_%=:\n\t" \
    "v_add_u32 v24, 6, v20\n\t" \
    "v_cvt_f32_ubyte0 v24, v24\n\t" \
    "v_mul_f32 v24, 0x3d000000, v24\n\t" \
    "v_add_f32 v25, 0.5, v21\n\t" \
    "v_add_f32 v26, 1.0, v25\n\t" \
    "v_add_f32 v27, 2.0, v25\n\t" \
    "v_add_f32 v28, 2.0, v26\n\t" \
    "v_mov_b32 v29, 0\n\t" \
    "v_mov_b32 v30, 0\n\t" \
    "s_mov_b32 s64, 0.5\n\t" \
    "v_add_u32 v34, 152832, v0\n\t" \
    "s_branch .Ltl_%=\n\t" \
    ".Lm3_%=:\n\t" \
    "s_mov_b32 s37, 34635776\n\t" \
    "buffer_load_dword v3, v12, s[4:7], s37 offen sc1\n\t" \
    "v_lshrrev_b32 v8, 2, v0\n\t" \
    "v_and_b32 v8, 28, v8\n\t" \
    "v_add_u32 v8, 156928, v8\n\t" \
    "ds_write_b32 v8, v12\n\t" \
    "v_add_u32 v24, 6, v23\n\t" \
    "v_cvt_f32_ubyte0 v24, v24\n\t" \
    "v_mul_f32 v24, 0x3d924925, v24\n\t" \
    "v_mov_b32 v25, v21\n\t" \
    "v_add_f32 v26, 1.0, v21\n\t" \
    "v_add_f32 v27, 2.0, v21\n\t" \
    "v_add_f32 v28, 2.0, v26\n\t" \
    "v_sub_f32 v29, 0, v22\n\t" \
    "v_sub_f32 v30, 0, v22\n\t" \
    "s_mov_b32 exec_lo, 0\n\t" \
    "v_fma_f32 v25, v21, 0.5, 4.0\n\t" \
    "v_add_f32 v27, 1.0, v25\n\t" \
    "v_fmaak_f32 v26, v21, v31, 0x41200000\n\t" \
    "v_mov_b32 v28, v26\n\t" \
    "v_add_f32 v30, v31, v30\n\t" \
    "s_mov_b64 exec, -1\n\t" \
    "s_mov_b32 s40, 0x11\n\t" \
    "v_add_u32 v34, 151808, v0\n\t" \
    ".Ltl_%=:\n\t" \
    "v_fma_f32 v36, v24, v25, v29\n\t" \
    "v_fma_f32 v38, v24, v26, v29\n\t" \
    "v_fma_f32 v40, v24, v27, v29\n\t" \
    "v_fma_f32 v42, v24, v28, v30\n\t" \
    "v_add_f32 v37, v36, v31\n\t" \
    "v_add_f32 v39, v38, v31\n\t" \
    "v_add_f32 v41, v40, v31\n\t" \
    "v_add_f32 v43, v42, v31\n\t" \
    "v_cos_f32 v36, v36\n\t" \
    "v_cos_f32 v37, v37\n\t" \
    "v_cos_f32 v38, v38\n\t" \
    "v_cos_f32 v39, v39\n\t" \
    "v_cos_f32 v40, v40\n\t" \
    "v_cos_f32 v41, v41\n\t" \
    "v_cos_f32 v42, v42\n\t" \
    "v_cos_f32 v43, v43\n\t" \
    "s_bitcmp0_b32 s40, 4\n\t" \
    "s_cbranch_scc1 .Lt2_%=\n\t" \
    "v_mul_f32 v36, 0x3b924925, v36\n\t" \
    "v_mul_f32 v37, 0x3b924925, v37\n\t" \
    "v_mul_f32 v38, 0x3b924925, v38\n\t" \
    "v_mul_f32 v39, 0x3b924925, v39\n\t" \
    "v_mul_f32 v40, 0x3b924925, v40\n\t" \
    "v_mul_f32 v41, 0x3b924925, v41\n\t" \
    "v_mul_f32 v42, 0x3b924925, v42\n\t" \
    "v_mul_f32 v43, 0x3b924925, v43\n\t" \
    ".Lt2_%=:\n\t" \
    "v_cvt_pk_bf16_f32 v44, v36, v37\n\t" \
    "v_cvt_pk_bf16_f32 v45, v38, v39\n\t" \
    "v_cvt_pk_bf16_f32 v46, v40, v41\n\t" \
    "v_cvt_pk_bf16_f32 v47, v42, v43\n\t" \
    "v_and_b32 v44, v33, v44\n\t" \
    "v_and_b32 v45, v33, v45\n\t" \
    "v_mov_b32 v33, -1\n\t" \
    "v_add_f32 v24, s64, v24\n\t" \
    "v_add_f32 v31, s65, v31\n\t" \
    "v_add_f32 v29, v32, v29\n\t" \
    "v_add_f32 v30, v32, v30\n\t" \
    "ds_write_b128 v34, v[44:47]\n\t" \
    "s_add_u32 s40, s40, 1\n\t" \
    "s_bitcmp1_b32 s40, 0\n\t" \
    "v_add_u32 v34, 0x400, v34\n\t" \
    "s_cbranch_scc1 .Ltl_%=\n\t" \
    "s_bitcmp1_b32 s40, 2\n\t" \
    "s_cbranch_scc1 .Lt4_%=\n\t" \
    "s_cmp_lg_u32 s24, 3\n\t" \
    "s_cbranch_scc1 .Lx1_%=\n\t" \
    "v_lshrrev_b32 v8, 7, v0\n\t" \
    "v_mul_u32_u24 v8, 96, v8\n\t" \
    "v_and_b32 v9, 16, v0\n\t" \
    "v_bfe_u32 v10, v0, 5, 2\n\t" \
    "v_add3_u32 v8, v8, v9, v10\n\t" \
    "v_mul_u32_u24 v8, 192, v8\n\t" \
    "s_mul_i32 s37, s28, 129024\n\t" \
    "s_add_u32 s37, s37, s31\n\t" \
    "s_mov_b32 m0, 157312\n\t" \
    "s_mov_b32 exec_hi, 0xffffff\n\t" \
    "buffer_load_dword v8, s[12:15], s37 offen lds\n\t" \
    "s_mov_b64 exec, -1\n\t" \
    ".Lx1_%=:\n\t" \
    "s_mov_b32 s40, 2\n\t" \
    "s_lshl2_add_u32 s37, s28, s24\n\t" \
    "s_lshl_b32 s37, s37, 2\n\t" \
    "v_lshrrev_b32 v25, 2, v20\n\t" \
    "v_add_u32 v25, s37, v25\n\t" \
    "v_mul_u32_u24 v26, 147, v25\n\t" \
    "v_lshrrev_b32 v26, 10, v26\n\t" \
    "v_lshl_or_b32 v24, v26, 1, 1\n\t" \
    "v_cvt_f32_ubyte0 v24, v24\n\t" \
    "v_mul_f32 v24, 0x3c800000, v24\n\t" \
    "v_mul_u32_u24 v26, 7, v26\n\t" \
    "v_sub_u32 v25, v25, v26\n\t" \
    "v_xor_b32 v22, s41, v20\n\t" \
    "v_bfe_u32 v32, v22, 1, 1\n\t" \
    "v_and_b32 v22, 1, v22\n\t" \
    "v_cvt_f32_ubyte0 v22, v22\n\t" \
    "v_mul_f32 v22, 0x3e800000, v22\n\t" \
    "v_lshl_or_b32 v32, v25, 1, v32\n\t" \
    "v_cvt_f32_ubyte0 v32, v32\n\t" \
    "v_mul_f32 v32, 0x3e924925, v32\n\t" \
    "v_mul_f32 v29, 0x3d800000, v21\n\t" \
    "v_fma_f32 v29, v29, v32, v22\n\t" \
    "v_mov_b32 v30, v29\n\t" \
    "v_mov_b32 v31, v24\n\t" \
    "v_mov_b32 v25, 0\n\t" \
    "v_mov_b32 v26, 2.0\n\t" \
    "v_mov_b32 v27, 4.0\n\t" \
    "v_mov_b32 v28, 0x40c00000\n\t" \
    "s_mov_b32 s64, 0\n\t" \
    "s_mov_b32 s65, 0\n\t" \
    "s_lshl_b32 s37, s24, 11\n\t" \
    "s_add_u32 s37, s37, 49664\n\t" \
    "v_add_u32 v34, s37, v0\n\t" \
    "s_branch .Ltl_%=\n\t" \
    ".Lt4_%=:\n\t" \
    ".Lt3_%=:\n\t" \
    ".Le9_%=:\n\t" \
    "s_lshr_b32 s37, s24, 2\n\t" \
    "s_and_b32 s38, s24, 2\n\t" \
    "s_mul_i32 s39, s37, 24832\n\t" \
    "s_lshl4_add_u32 s39, s38, s39\n\t" \
    "v_lshrrev_b32 v4, 6, v0\n\t" \
    "v_and_b32 v5, 12, v4\n\t" \
    "v_add_u32 v6, v4, v5\n\t" \
    "v_lshrrev_b32 v7, 1, v0\n\t" \
    "v_and_b32 v7, 24, v7\n\t" \
    "v_and_b32 v4, 11, v4\n\t" \
    "v_mul_u32_u24 v4, 192, v4\n\t" \
    "v_bfe_u32 v5, v0, 8, 1\n\t" \
    "v_mul_u32_u24 v5, 3104, v5\n\t" \
    "v_add3_u32 v4, v4, v5, v7\n\t" \
    "v_add_u32 v8, s39, v4\n\t" \
    "v_mov_b32 v9, 156928\n\t" \
    "v_add_u32 v10, 149760, v0\n\t" \
    "s_lshl2_add_u32 s37, s37, s38\n\t" \
    "s_lshl_b32 s37, s37, 7\n\t" \
    "s_mul_i32 s53, s27, 28\n\t" \
    "s_add_u32 s53, s53, s26\n\t" \
    "s_andn2_b32 s39, s53, 7\n\t" \
    "s_add_u32 s53, s53, s39\n\t" \
    "s_lshl_b32 s53, s53, 10\n\t" \
    "s_add_u32 s37, s37, s53\n\t" \
    "s_and_b32 s38, s24, 1\n\t" \
    "s_lshl_b32 s39, s38, 13\n\t" \
    "s_add_u32 s37, s37, s39\n\t" \
    "s_cmp_lt_u32 s25, 126\n\t" \
    "s_cselect_b32 s43, 0, 0x80000000\n\t" \
    "s_add_u32 s37, s37, s43\n\t" \
    "v_lshrrev_b32 v11, 8, v0\n\t" \
    "v_and_b32 v12, 0xf0, v0\n\t" \
    "v_mul_u32_u24 v4, 8486912, v11\n\t" \
    "v_add3_u32 v13, v4, v12, s37\n\t" \
    "s_lshl_b32 s39, s38, 10\n\t" \
    "v_add_u32 v14, s39, v10\n\t" \
    "s_lshl_b32 s39, s38, 5\n\t" \
    "s_add_u32 s39, s39, 116736\n\t" \
    "v_lshl_add_u32 v4, v6, 6, v7\n\t" \
    "v_lshrrev_b32 v5, 3, v0\n\t" \
    "v_and_b32 v5, 32, v5\n\t" \
    "v_xor_b32 v5, s39, v5\n\t" \
    "v_add_u32 v15, v4, v5\n\t" \
    "s_and_b32 s37, s24, 6\n\t" \
    "s_lshl_b32 s37, s37, 10\n\t" \
    "v_add_u32 v16, s37, v0\n\t" \
    "s_cmp_ge_u32 s25, 126\n\t" \
    "s_cbranch_scc1 .Lqa_%=\n\t" \
    ".Lqd_%=:\n\t" \
    "s_waitcnt lgkmcnt(0)\n\t" \
    "s_barrier\n\t" \
    "ds_read_b128 v[24:27], v14 offset:5120\n\t" \
    "ds_read_b128 v[28:31], v16 offset:49664\n\t" \
    "ds_read_b128 v[32:35], v16 offset:50688\n\t" \
    "s_cmp_ge_u32 s25, 126\n\t" \
    "s_cbranch_scc1 .Lqb_%=\n\t" \
    ".Lqe_%=:\n\t" \
    "s_and_b32 s33, s2, 7\n\t" \
    "s_lshl_b32 s33, s33, 15\n\t" \
    "s_add_u32 s33, s33, 33554432\n\t" \
    "s_lshr_b32 s37, s29, 2\n\t" \
    "s_mul_i32 s39, s30, 3\n\t" \
    "s_add_u32 s37, s37, s39\n\t" \
    "s_mul_i32 s37, s37, 114688\n\t" \
    "s_and_b32 s39, s29, 3\n\t" \
    "s_lshl_b32 s39, s39, 8\n\t" \
    "s_add_u32 s37, s37, s39\n\t" \
    "s_add_u32 s37, s37, s43\n\t" \
    "s_add_u32 s37, s37, 2097152\n\t" \
    "s_and_b32 s39, s24, 6\n\t" \
    "s_lshl3_add_u32 s39, s28, s39\n\t" \
    "s_lshl_b32 s39, s39, 1\n\t" \
    "v_add_u32 v14, s39, v11\n\t" \
    "v_mul_u32_u24 v17, 147, v14\n\t" \
    "v_lshrrev_b32 v17, 10, v17\n\t" \
    "s_and_b32 s39, s24, 1\n\t" \
    "v_subrev_u32 v18, s39, v17\n\t" \
    "v_mul_i32_i24 v18, 7, v18\n\t" \
    "v_sub_u32 v14, v14, v18\n\t" \
    "v_and_b32 v18, 14, v14\n\t" \
    "v_add_lshl_u32 v14, v14, v18, 12\n\t" \
    "v_and_b32 v18, 7, v17\n\t" \
    "v_mul_u32_u24 v18, 4243456, v18\n\t" \
    "v_add3_u32 v14, v14, v18, v12\n\t" \
    "v_and_b32 v17, 8, v17\n\t" \
    "v_xor_b32 v17, 8, v17\n\t" \
    "v_lshl_add_u32 v14, v17, 10, v14\n\t" \
    "v_add_u32 v19, s37, v14\n\t" \
    "s_lshl_b32 s43, s41, 10\n\t" \
    "s_and_b32 s37, s2, 0x70\n\t" \
    "s_lshl_b32 s37, s37, 1\n\t" \
    "s_lshl2_add_u32 s57, s24, s37\n\t" \
    "s_min_u32 s58, s57, 248\n\t" \
    "s_mul_i32 s59, s58, 147\n\t" \
    "s_lshr_b32 s59, s59, 12\n\t" \
    "s_mul_i32 s37, s59, 28\n\t" \
    "s_sub_u32 s60, s58, s37\n\t" \
    "s_andn2_b32 s39, s58, 7\n\t" \
    "s_add_u32 s51, s58, s39\n\t" \
    "s_lshl_b32 s51, s51, 10\n\t" \
    "s_sub_u32 s37, s60, 1\n\t" \
    "s_cselect_b32 s52, 27, -1\n\t" \
    "s_add_u32 s52, s52, s58\n\t" \
    "s_andn2_b32 s39, s52, 7\n\t" \
    "s_add_u32 s52, s52, s39\n\t" \
    "s_lshl_b32 s52, s52, 10\n\t" \
    "v_lshrrev_b32 v16, 9, v0\n\t" \
    "v_and_b32 v18, 0x2f0, v0\n\t" \
    "v_and_b32 v17, 1, v11\n\t" \
    "v_lshl_add_u32 v14, v17, 11, v18\n\t" \
    "v_add_u32 v36, s51, v14\n\t" \
    "v_add_u32 v37, s52, v0\n\t" \
    "s_mul_i32 s44, s24, 7296\n\t" \
    "s_mul_i32 s37, s60, 344064\n\t" \
    "s_mul_i32 s39, s59, 4992\n\t" \
    "s_add_u32 s37, s37, s39\n\t" \
    "s_cmp_eq_u32 s59, 8\n\t" \
    "s_movk_i32 s39, 2496\n\t" \
    "s_cselect_b32 s39, 1536, s39\n\t" \
    "s_add_u32 s37, s37, s39\n\t" \
    "s_and_b32 s39, s8, 0x1fffff\n\t" \
    "s_add_u32 s37, s37, s39\n\t" \
    "v_bfe_u32 v38, v0, 5, 2\n\t" \
    "v_lshl_add_u32 v38, v16, 4, v38\n\t" \
    "v_mul_u32_u24 v38, 43008, v38\n\t" \
    "v_add_u32 v39, s37, v38\n\t" \
    "s_lshr_b32 s37, s60, 1\n\t" \
    "s_mul_i32 s37, s37, 9\n\t" \
    "s_add_u32 s37, s37, s59\n\t" \
    "v_mad_u32_u24 v38, v16, 9, s37\n\t" \
    "v_lshrrev_b32 v18, 5, v38\n\t" \
    "s_and_b32 s29, s51, 0x1000\n\t" \
    "s_and_b32 s30, s52, 0x1000\n\t" \
    "s_lshl_b32 s37, s24, 2\n\t" \
    "s_lshl1_add_u32 s39, s24, 8\n\t" \
    "s_min_u32 s61, s37, s39\n\t" \
    "s_add_u32 s37, s37, 2\n\t" \
    "s_add_u32 s39, s24, 20\n\t" \
    "s_cmp_lt_u32 s24, 4\n\t" \
    "s_cselect_b32 s62, s37, s39\n\t" \
    "s_bfe_u32 s63, s2, 0x10003\n\t" \
    "s_mul_i32 s47, s63, 28\n\t" \
    "s_add_u32 s45, s47, s61\n\t" \
    "s_add_u32 s46, s47, s62\n\t" \
    "s_andn2_b32 s39, s45, 7\n\t" \
    "s_add_u32 s45, s45, s39\n\t" \
    "s_lshl_b32 s45, s45, 10\n\t" \
    "s_andn2_b32 s39, s46, 7\n\t" \
    "s_add_u32 s46, s46, s39\n\t" \
    "s_lshl_b32 s46, s46, 10\n\t" \
    "v_lshlrev_b32 v17, 4, v11\n\t" \
    "v_lshrrev_b32 v14, 6, v12\n\t" \
    "v_mul_u32_u24 v14, 7296, v14\n\t" \
    "v_and_b32 v38, 16, v0\n\t" \
    "v_mul_u32_u24 v38, 226, v38\n\t" \
    "v_and_b32 v40, 32, v0\n\t" \
    "v_lshl_add_u32 v38, v40, 1, v38\n\t" \
    "v_add3_u32 v14, v14, v38, v17\n\t" \
    "s_lshl_b32 s37, s61, 7\n\t" \
    "v_add_u32 v44, s37, v14\n\t" \
    "s_and_b32 s37, s62, 30\n\t" \
    "s_lshl_b32 s37, s37, 7\n\t" \
    "v_add_u32 v45, s37, v14\n\t" \
    "s_mov_b32 s1, 0\n\t" \
    "s_mov_b32 s41, 0x18000\n\t" \
    "s_lshl_b32 s37, s63, 9\n\t" \
    "s_add_u32 s51, s51, s37\n\t" \
    "s_cmp_lt_u32 s57, 252\n\t" \
    "s_cselect_b32 s51, s51, 0x80000000\n\t" \
    "s_and_b32 s37, s2, 0x87\n\t" \
    "s_lshl4_add_u32 s37, s24, s37\n\t" \
    "s_lshl_b32 s55, s37, 7\n\t" \
    "v_and_b32 v46, 16, v0\n\t" \
    "v_lshlrev_b32 v47, 6, v46\n\t" \
    "v_mad_u32_u24 v38, v12, 12, v17\n\t" \
    "v_and_b32 v14, 0x170, v0\n\t" \
    "v_and_b32 v41, 0x80, v0\n\t" \
    "v_lshl_add_u32 v14, v41, 3, v14\n\t" \
    "v_lshl_add_u32 v14, v16, 11, v14\n\t" \
    "v_add_u32 v42, s51, v14\n\t" \
    "s_mul_i32 s37, s24, 2064\n\t" \
    "v_and_b32 v14, 0x300, v0\n\t" \
    "v_add3_u32 v43, v14, v0, s37\n\t" \
    "s_lshl_b32 s37, s37, 1\n\t" \
    "v_lshlrev_b32 v14, 7, v16\n\t" \
    "v_xor_b32 v14, v14, v0\n\t" \
    "v_add_u32 v40, s37, v14\n\t" \
    "v_lshrrev_b32 v14, 5, v12\n\t" \
    "v_mul_u32_u24 v14, 2064, v14\n\t" \
    "v_lshl_add_u32 v14, v46, 2, v14\n\t" \
    "s_lshl_b32 s37, s24, 8\n\t" \
    "v_add3_u32 v48, v14, v17, s37\n\t" \
    "v_bfe_u32 v14, v0, 7, 1\n\t" \
    "v_lshl_add_u32 v14, v11, 2, v14\n\t" \
    "v_mul_u32_u24 v14, 2064, v14\n\t" \
    "v_lshrrev_b32 v41, 3, v0\n\t" \
    "v_and_or_b32 v14, v41, 8, v14\n\t" \
    "s_and_b32 s37, s24, 3\n\t" \
    "s_lshl_b32 s37, s37, 6\n\t" \
    "s_and_b32 s39, s24, 4\n\t" \
    "s_lshl_b32 s39, s39, 8\n\t" \
    "s_or_b32 s37, s37, s39\n\t" \
    "v_and_or_b32 v41, v0, 32, s37\n\t" \
    "v_mul_u32_u24 v46, 40, v46\n\t" \
    "v_xor_b32 v41, v41, v46\n\t" \
    "v_add_u32 v49, v14, v41\n\t" \
    "ds_read_b128 v[52:55], v10 offset:0\n\t" \
    "ds_read_b128 v[56:59], v10 offset:1024\n\t" \
    "s_and_b32 s37, s24, 1\n\t" \
    "s_lshl_b32 s37, s37, 4\n\t" \
    "s_lshr_b32 s39, s24, 1\n\t" \
    "s_add_u32 s37, s37, s39\n\t" \
    "s_mul_i32 s37, s37, 192\n\t" \
    "s_add_u32 s37, s37, s31\n\t" \
    "s_lshl_b32 s39, s24, 12\n\t" \
    "s_add_u32 m0, s39, 116736\n\t" \
    "s_add_u32 s38, s37, 0\n\t" \
    "buffer_load_dwordx4 v1, s[12:15], s38 offen  lds\n\t" \
    "s_add_u32 m0, s39, 117760\n\t" \
    "s_add_u32 s38, s37, 258048\n\t" \
    "buffer_load_dwordx4 v1, s[12:15], s38 offen  lds\n\t" \
    "s_add_u32 m0, s39, 118784\n\t" \
    "s_add_u32 s38, s37, 516096\n\t" \
    "buffer_load_dwordx4 v1, s[12:15], s38 offen  lds\n\t" \
    "s_add_u32 m0, s39, 119808\n\t" \
    "s_add_u32 s38, s37, 774144\n\t" \
    "buffer_load_dwordx4 v1, s[12:15], s38 offen  lds\n\t" \
    "s_cmp_lg_u32 s24, 3\n\t" \
    "s_cbranch_scc1 .Lr9_%=\n\t" \
    "s_and_b32 s37, s2, 7\n\t" \
    "s_lshl_b32 s37, s37, 10\n\t" \
    "s_add_u32 s37, s37, 34643968\n\t" \
    "s_buffer_load_dword s56, s[4:7], s37 offset:0x0\n\t" \
    "s_buffer_load_dword s57, s[4:7], s37 offset:0x80\n\t" \
    "s_buffer_load_dword s58, s[4:7], s37 offset:0x100\n\t" \
    "s_buffer_load_dword s59, s[4:7], s37 offset:0x180\n\t" \
    "s_buffer_load_dword s60, s[4:7], s37 offset:0x200\n\t" \
    "s_buffer_load_dword s61, s[4:7], s37 offset:0x280\n\t" \
    "s_buffer_load_dword s62, s[4:7], s37 offset:0x300\n\t" \
    "s_buffer_load_dword s63, s[4:7], s37 offset:0x380\n\t" \
    "s_mov_b32 s40, 0\n\t" \
    ".Lr1_%=:\n\t" \
    "s_waitcnt lgkmcnt(0)\n\t" \
    "s_memrealtime s[22:23]\n\t" \
    "s_waitcnt lgkmcnt(0)\n\t" \
    "s_mov_b32 s37, 1000\n\t" \
    "s_mov_b32 s38, 0\n\t" \
    "s_sub_u32 s39, s22, s56\n\t" \
    "s_min_u32 s37, s37, s39\n\t" \
    "s_max_u32 s38, s38, s39\n\t" \
    "s_sub_u32 s39, s22, s57\n\t" \
    "s_min_u32 s37, s37, s39\n\t" \
    "s_max_u32 s38, s38, s39\n\t" \
    "s_sub_u32 s39, s22, s58\n\t" \
    "s_min_u32 s37, s37, s39\n\t" \
    "s_max_u32 s38, s38, s39\n\t" \
    "s_sub_u32 s39, s22, s59\n\t" \
    "s_min_u32 s37, s37, s39\n\t" \
    "s_max_u32 s38, s38, s39\n\t" \
    "s_sub_u32 s39, s22, s60\n\t" \
    "s_min_u32 s37, s37, s39\n\t" \
    "s_max_u32 s38, s38, s39\n\t" \
    "s_sub_u32 s39, s22, s61\n\t" \
    "s_min_u32 s37, s37, s39\n\t" \
    "s_max_u32 s38, s38, s39\n\t" \
    "s_sub_u32 s39, s22, s62\n\t" \
    "s_min_u32 s37, s37, s39\n\t" \
    "s_max_u32 s38, s38, s39\n\t" \
    "s_sub_u32 s39, s22, s63\n\t" \
    "s_min_u32 s37, s37, s39\n\t" \
    "s_max_u32 s38, s38, s39\n\t" \
    "s_cmp_gt_u32 s38, 1000\n\t" \
    "s_cbranch_scc1 .Lr2_%=\n\t" \
    ".Lr3_%=:\n\t" \
    "s_sub_u32 s35, s22, s37\n\t" \
    "s_waitcnt vmcnt(5)\n\t" \
    "v_readfirstlane_b32 s34, v3\n\t" \
    "s_mul_i32 s34, s34, 1664525\n\t" \
    "s_add_u32 s34, s34, 1013904223\n\t" \
    "s_or_b32 s34, s34, 1\n\t" \
    "v_mov_b32 v60, s34\n\t" \
    "v_mov_b32 v61, s35\n\t" \
    "ds_write_b64 v9, v[60:61] offset:32\n\t" \
    ".Lr9_%=:\n\t" \
    "s_cmp_ge_u32 s25, 126\n\t" \
    "s_cbranch_scc1 .Lpb_%=\n\t" \
    ".Lpe_%=:\n\t" \
    "s_waitcnt vmcnt(4)\n\t" \
    "s_waitcnt lgkmcnt(0)\n\t" \
    "s_barrier\n\t" \
    "ds_read_b64_tr_b16 v[64:65], v8 offset:0\n\t" \
    "ds_read_b64_tr_b16 v[66:67], v8 offset:768\n\t" \
    "ds_read_b64_tr_b16 v[68:69], v8 offset:12416\n\t" \
    "ds_read_b64_tr_b16 v[70:71], v8 offset:13184\n\t" \
    "ds_read_b64_tr_b16 v[72:73], v8 offset:6208\n\t" \
    "ds_read_b64_tr_b16 v[74:75], v8 offset:6976\n\t" \
    "ds_read_b64_tr_b16 v[76:77], v8 offset:18624\n\t" \
    "ds_read_b64_tr_b16 v[78:79], v8 offset:19392\n\t" \
    "ds_read_b128 v[80:83], v9 offset:32\n\t" \
    "s_waitcnt lgkmcnt(7)\n\t" \
    "v_mfma_f32_16x16x32_bf16 v[84:87], v[24:27], v[64:67], 0\n\t" \
    "s_waitcnt lgkmcnt(5)\n\t" \
    "v_mfma_f32_16x16x32_bf16 v[88:91], v[24:27], v[68:71], 0\n\t" \
    "s_waitcnt lgkmcnt(3)\n\t" \
    "v_mfma_f32_16x16x32_bf16 v[92:95], v[24:27], v[72:75], 0\n\t" \
    "s_waitcnt lgkmcnt(1)\n\t" \
    "v_mfma_f32_16x16x32_bf16 v[96:99], v[24:27], v[76:79], 0\n\t" \
    "ds_read_b64_tr_b16 v[100:101], v8 offset:64\n\t" \
    "ds_read_b64_tr_b16 v[102:103], v8 offset:832\n\t" \
    "ds_read_b64_tr_b16 v[104:105], v8 offset:12480\n\t" \
    "ds_read_b64_tr_b16 v[106:107], v8 offset:13248\n\t" \
    "ds_read_b64_tr_b16 v[108:109], v8 offset:6272\n\t" \
    "ds_read_b64_tr_b16 v[110:111], v8 offset:7040\n\t" \
    "ds_read_b64_tr_b16 v[112:113], v8 offset:18688\n\t" \
    "ds_read_b64_tr_b16 v[114:115], v8 offset:19456\n\t" \
    "s_waitcnt lgkmcnt(8)\n\t" \
    "v_readfirstlane_b32 s34, v80\n\t" \
    "v_readfirstlane_b32 s35, v81\n\t" \
    "s_lshl_b32 s37, s24, 8\n\t" \
    "s_add_u32 s37, s37, s2\n\t" \
    "s_cmp_eq_u32 s37, 255\n\t" \
    "s_cbranch_scc1 .Lpp_%=\n\t" \
    ".Lpq_%=:\n\t" \
    "v_add_u32 v84, 0x8000, v84\n\t" \
    "v_add_u32 v86, 0x8000, v86\n\t" \
    "v_add_u32 v88, 0x8000, v88\n\t" \
    "v_add_u32 v90, 0x8000, v90\n\t" \
    "v_cvt_pk_bf16_f32 v124, v84, v85\n\t" \
    "v_cvt_pk_bf16_f32 v126, v88, v89\n\t" \
    "v_cvt_pk_bf16_f32 v125, v92, v93\n\t" \
    "v_cvt_pk_bf16_f32 v127, v96, v97\n\t" \
    "v_and_b32 v124, -2, v124\n\t" \
    "v_and_b32 v126, -2, v126\n\t" \
    "v_cvt_pk_bf16_f32 v128, v86, v87\n\t" \
    "v_cvt_pk_bf16_f32 v130, v90, v91\n\t" \
    "v_cvt_pk_bf16_f32 v129, v94, v95\n\t" \
    "v_cvt_pk_bf16_f32 v131, v98, v99\n\t" \
    "v_and_b32 v128, -2, v128\n\t" \
    "v_and_b32 v130, -2, v130\n\t" \
    "ds_read_b64_tr_b16 v[132:133], v8 offset:128\n\t" \
    "ds_read_b64_tr_b16 v[134:135], v8 offset:896\n\t" \
    "ds_read_b64_tr_b16 v[136:137], v8 offset:12544\n\t" \
    "ds_read_b64_tr_b16 v[138:139], v8 offset:13312\n\t" \
    "ds_read_b64_tr_b16 v[140:141], v8 offset:6336\n\t" \
    "ds_read_b64_tr_b16 v[142:143], v8 offset:7104\n\t" \
    "ds_read_b64_tr_b16 v[144:145], v8 offset:18752\n\t" \
    "ds_read_b64_tr_b16 v[146:147], v8 offset:19520\n\t" \
    "v_mov_b32 v148, s34\n\t" \
    "v_mov_b32 v149, s34\n\t" \
    "v_mov_b32 v150, s34\n\t" \
    "v_mov_b32 v151, s34\n\t" \
    "s_and_b32 s37, s2, 243\n\t" \
    "s_cmp_eq_u32 s37, 243\n\t" \
    "s_cbranch_scc1 .Lskip_%=\n\t" \
    ".Lh27_%=:\n\t" \
    "s_memrealtime s[22:23]\n\t" \
    "s_waitcnt lgkmcnt(0)\n\t" \
    "s_sub_u32 s22, s22, s35\n\t" \
    "s_cmp_lt_u32 s22, 205\n\t" \
    "s_cbranch_scc1 .Lh27_%=\n\t" \
    "buffer_store_dwordx4 v[124:127], v13, s[4:7], 0 offen offset:0 sc1\n\t" \
    "s_mov_b32 s42, 4243456\n\t" \
    "buffer_store_dwordx4 v[128:131], v13, s[4:7], s42 offen offset:0 sc1\n\t" \
    "v_mfma_f32_16x16x32_bf16 v[152:155], v[24:27], v[100:103], 0\n\t" \
    "v_mfma_f32_16x16x32_bf16 v[156:159], v[24:27], v[104:107], 0\n\t" \
    "v_mfma_f32_16x16x32_bf16 v[160:163], v[24:27], v[108:111], 0\n\t" \
    "v_mfma_f32_16x16x32_bf16 v[164:167], v[24:27], v[112:115], 0\n\t" \
    "v_mfma_f32_16x16x32_bf16 v[168:171], v[24:27], v[132:135], 0\n\t" \
    "v_mfma_f32_16x16x32_bf16 v[172:175], v[24:27], v[136:139], 0\n\t" \
    "v_mfma_f32_16x16x32_bf16 v[176:179], v[24:27], v[140:143], 0\n\t" \
    "v_mfma_f32_16x16x32_bf16 v[180:183], v[24:27], v[144:147], 0\n\t" \
    "s_nop 0\n\t" \
    "v_add_u32 v152, 0x8000, v152\n\t" \
    "v_add_u32 v154, 0x8000, v154\n\t" \
    "v_add_u32 v156, 0x8000, v156\n\t" \
    "v_add_u32 v158, 0x8000, v158\n\t" \
    "v_cvt_pk_bf16_f32 v184, v152, v153\n\t" \
    "v_cvt_pk_bf16_f32 v186, v156, v157\n\t" \
    "v_cvt_pk_bf16_f32 v185, v160, v161\n\t" \
    "v_cvt_pk_bf16_f32 v187, v164, v165\n\t" \
    "v_and_b32 v184, -2, v184\n\t" \
    "v_and_b32 v186, -2, v186\n\t" \
    "v_cvt_pk_bf16_f32 v188, v154, v155\n\t" \
    "v_cvt_pk_bf16_f32 v190, v158, v159\n\t" \
    "v_cvt_pk_bf16_f32 v189, v162, v163\n\t" \
    "v_cvt_pk_bf16_f32 v191, v166, v167\n\t" \
    "v_and_b32 v188, -2, v188\n\t" \
    "v_and_b32 v190, -2, v190\n\t" \
    "v_add_u32 v168, 0x8000, v168\n\t" \
    "v_add_u32 v170, 0x8000, v170\n\t" \
    "v_add_u32 v172, 0x8000, v172\n\t" \
    "v_add_u32 v174, 0x8000, v174\n\t" \
    "v_cvt_pk_bf16_f32 v4, v168, v169\n\t" \
    "v_cvt_pk_bf16_f32 v6, v172, v173\n\t" \
    "v_cvt_pk_bf16_f32 v5, v176, v177\n\t" \
    "v_cvt_pk_bf16_f32 v7, v180, v181\n\t" \
    "v_and_b32 v4, -2, v4\n\t" \
    "v_and_b32 v6, -2, v6\n\t" \
    "v_cvt_pk_bf16_f32 v60, v170, v171\n\t" \
    "v_cvt_pk_bf16_f32 v62, v174, v175\n\t" \
    "v_cvt_pk_bf16_f32 v61, v178, v179\n\t" \
    "v_cvt_pk_bf16_f32 v63, v182, v183\n\t" \
    "v_and_b32 v60, -2, v60\n\t" \
    "v_and_b32 v62, -2, v62\n\t" \
    ".Lh28_%=:\n\t" \
    "s_memrealtime s[22:23]\n\t" \
    "s_waitcnt lgkmcnt(0)\n\t" \
    "s_sub_u32 s22, s22, s35\n\t" \
    "s_cmp_lt_u32 s22, 255\n\t" \
    "s_cbranch_scc1 .Lh28_%=\n\t" \
    "s_mov_b32 s42, 524288\n\t" \
    "buffer_store_dwordx4 v[184:187], v13, s[4:7], s42 offen offset:0 sc1\n\t" \
    "s_mov_b32 s42, 4767744\n\t" \
    "buffer_store_dwordx4 v[188:191], v13, s[4:7], s42 offen offset:0 sc1\n\t" \
    "s_waitcnt vmcnt(4)\n\t" \
    "; wait\n\t" \
    "s_waitcnt lgkmcnt(0)\n\t" \
    "s_barrier\n\t" \
    "s_lshl_b32 s37, s24, 18\n\t" \
    "s_add_u32 s37, s37, s32\n\t" \
    "s_bitcmp1_b32 29, s24\n\t" \
    "s_cselect_b32 s39, 0, 0x80000000\n\t" \
    "v_add_u32 v16, s39, v2\n\t" \
    "v_mov_b32 v20, 0\n\t" \
    "v_mov_b32 v21, 0\n\t" \
    "v_mov_b32 v22, 0\n\t" \
    "v_mov_b32 v23, 0\n\t" \
    "s_cmp_eq_u32 s24, 4\n\t" \
    "s_cselect_b64 exec, 0xff, -1\n\t" \
    "buffer_store_dwordx4 v[20:23], v16, s[4:7], s37 offen sc1\n\t" \
    "s_mov_b64 exec, -1\n\t" \
    ".Lh29_%=:\n\t" \
    "s_memrealtime s[22:23]\n\t" \
    "s_waitcnt lgkmcnt(0)\n\t" \
    "s_sub_u32 s22, s22, s35\n\t" \
    "s_cmp_lt_u32 s22, 305\n\t" \
    "s_cbranch_scc1 .Lh29_%=\n\t" \
    "s_mov_b32 s42, 1048576\n\t" \
    "buffer_store_dwordx4 v[4:7], v13, s[4:7], s42 offen offset:0 sc1\n\t" \
    "s_mov_b32 s42, 5292032\n\t" \
    "buffer_store_dwordx4 v[60:63], v13, s[4:7], s42 offen offset:0 sc1\n\t" \
    "ds_read_b64_tr_b16 v[64:65], v15 offset:0\n\t" \
    "ds_read_b64_tr_b16 v[66:67], v15 offset:256\n\t" \
    "ds_read_b64_tr_b16 v[68:69], v15 offset:2048\n\t" \
    "ds_read_b64_tr_b16 v[70:71], v15 offset:2304\n\t" \
    "ds_read_b64_tr_b16 v[72:73], v15 offset:4096\n\t" \
    "ds_read_b64_tr_b16 v[74:75], v15 offset:4352\n\t" \
    "ds_read_b64_tr_b16 v[76:77], v15 offset:6144\n\t" \
    "ds_read_b64_tr_b16 v[78:79], v15 offset:6400\n\t" \
    "ds_read_b64_tr_b16 v[80:81], v15 offset:8192\n\t" \
    "ds_read_b64_tr_b16 v[82:83], v15 offset:8448\n\t" \
    "ds_read_b64_tr_b16 v[116:117], v15 offset:10240\n\t" \
    "ds_read_b64_tr_b16 v[118:119], v15 offset:10496\n\t" \
    "ds_read_b64_tr_b16 v[120:121], v15 offset:12288\n\t" \
    "ds_read_b64_tr_b16 v[122:123], v15 offset:12544\n\t" \
    "s_waitcnt lgkmcnt(12)\n\t" \
    "v_mfma_f32_16x16x32_bf16 v[84:87], v[28:31], v[64:67], 0\n\t" \
    "ds_read_b64_tr_b16 v[88:89], v15 offset:14336\n\t" \
    "ds_read_b64_tr_b16 v[90:91], v15 offset:14592\n\t" \
    "s_waitcnt lgkmcnt(12)\n\t" \
    "v_mfma_f32_16x16x32_bf16 v[84:87], v[32:35], v[68:71], v[84:87]\n\t" \
    "ds_read_b64_tr_b16 v[92:93], v15 offset:16384\n\t" \
    "ds_read_b64_tr_b16 v[94:95], v15 offset:16640\n\t" \
    "s_waitcnt lgkmcnt(12)\n\t" \
    "v_mfma_f32_16x16x32_bf16 v[96:99], v[28:31], v[72:75], 0\n\t" \
    "ds_read_b64_tr_b16 v[124:125], v15 offset:18432\n\t" \
    "ds_read_b64_tr_b16 v[126:127], v15 offset:18688\n\t" \
    "s_waitcnt lgkmcnt(12)\n\t" \
    "v_mfma_f32_16x16x32_bf16 v[96:99], v[32:35], v[76:79], v[96:99]\n\t" \
    "ds_read_b64_tr_b16 v[128:129], v15 offset:20480\n\t" \
    "ds_read_b64_tr_b16 v[130:131], v15 offset:20736\n\t" \
    "s_waitcnt lgkmcnt(12)\n\t" \
    "v_mfma_f32_16x16x32_bf16 v[100:103], v[28:31], v[80:83], 0\n\t" \
    "ds_read_b64_tr_b16 v[104:105], v15 offset:22528\n\t" \
    "ds_read_b64_tr_b16 v[106:107], v15 offset:22784\n\t" \
    "s_waitcnt lgkmcnt(12)\n\t" \
    "v_mfma_f32_16x16x32_bf16 v[100:103], v[32:35], v[116:119], v[100:103]\n\t" \
    "ds_read_b64_tr_b16 v[108:109], v15 offset:24576\n\t" \
    "ds_read_b64_tr_b16 v[110:111], v15 offset:24832\n\t" \
    "s_waitcnt lgkmcnt(12)\n\t" \
    "v_mfma_f32_16x16x32_bf16 v[112:115], v[28:31], v[120:123], 0\n\t" \
    "ds_read_b64_tr_b16 v[132:133], v15 offset:26624\n\t" \
    "ds_read_b64_tr_b16 v[134:135], v15 offset:26880\n\t" \
    "s_waitcnt lgkmcnt(12)\n\t" \
    "v_mfma_f32_16x16x32_bf16 v[112:115], v[32:35], v[88:91], v[112:115]\n\t" \
    "ds_read_b64_tr_b16 v[136:137], v15 offset:28672\n\t" \
    "ds_read_b64_tr_b16 v[138:139], v15 offset:28928\n\t" \
    "s_waitcnt lgkmcnt(12)\n\t" \
    "v_mfma_f32_16x16x32_bf16 v[140:143], v[28:31], v[92:95], 0\n\t" \
    "ds_read_b64_tr_b16 v[24:25], v15 offset:30720\n\t" \
    "ds_read_b64_tr_b16 v[26:27], v15 offset:30976\n\t" \
    "s_waitcnt lgkmcnt(12)\n\t" \
    "v_mfma_f32_16x16x32_bf16 v[140:143], v[32:35], v[124:127], v[140:143]\n\t" \
    "v_cvt_pk_bf16_f32 v144, v84, v96\n\t" \
    "v_cvt_pk_bf16_f32 v152, v85, v97\n\t" \
    "s_waitcnt lgkmcnt(10)\n\t" \
    "v_mfma_f32_16x16x32_bf16 v[156:159], v[28:31], v[128:131], 0\n\t" \
    "v_cvt_pk_bf16_f32 v160, v86, v98\n\t" \
    "v_cvt_pk_bf16_f32 v164, v87, v99\n\t" \
    "s_waitcnt lgkmcnt(8)\n\t" \
    "v_mfma_f32_16x16x32_bf16 v[156:159], v[32:35], v[104:107], v[156:159]\n\t" \
    "v_cvt_pk_bf16_f32 v145, v100, v112\n\t" \
    "v_cvt_pk_bf16_f32 v153, v101, v113\n\t" \
    "s_waitcnt lgkmcnt(6)\n\t" \
    "v_mfma_f32_16x16x32_bf16 v[168:171], v[28:31], v[108:111], 0\n\t" \
    "v_cvt_pk_bf16_f32 v161, v102, v114\n\t" \
    "v_cvt_pk_bf16_f32 v165, v103, v115\n\t" \
    "s_waitcnt lgkmcnt(4)\n\t" \
    "v_mfma_f32_16x16x32_bf16 v[168:171], v[32:35], v[132:135], v[168:171]\n\t" \
    "s_waitcnt lgkmcnt(2)\n\t" \
    "v_mfma_f32_16x16x32_bf16 v[172:175], v[28:31], v[136:139], 0\n\t" \
    "s_waitcnt lgkmcnt(0)\n\t" \
    "v_mfma_f32_16x16x32_bf16 v[172:175], v[32:35], v[24:27], v[172:175]\n\t" \
    "v_cvt_pk_bf16_f32 v146, v140, v156\n\t" \
    "v_cvt_pk_bf16_f32 v154, v141, v157\n\t" \
    "v_cvt_pk_bf16_f32 v162, v142, v158\n\t" \
    "v_cvt_pk_bf16_f32 v166, v143, v159\n\t" \
    "s_nop 3\n\t" \
    "v_cvt_pk_bf16_f32 v147, v168, v172\n\t" \
    "v_cvt_pk_bf16_f32 v155, v169, v173\n\t" \
    "v_cvt_pk_bf16_f32 v163, v170, v174\n\t" \
    "v_cvt_pk_bf16_f32 v167, v171, v175\n\t" \
    ".Lh30_%=:\n\t" \
    "s_memrealtime s[22:23]\n\t" \
    "s_waitcnt lgkmcnt(0)\n\t" \
    "s_sub_u32 s22, s22, s35\n\t" \
    "s_cmp_lt_u32 s22, 370\n\t" \
    "s_cbranch_scc1 .Lh30_%=\n\t" \
    "s_cmp_eq_u32 s24, 0\n\t" \
    "s_cbranch_scc1 .Lj32_%=\n\t" \
    ".Lh31_%=:\n\t" \
    "s_memrealtime s[22:23]\n\t" \
    "s_waitcnt lgkmcnt(0)\n\t" \
    "s_sub_u32 s22, s22, s35\n\t" \
    "s_cmp_lt_u32 s22, 385\n\t" \
    "s_cbranch_scc1 .Lh31_%=\n\t" \
    ".Lj32_%=:\n\t" \
    "s_waitcnt lgkmcnt(0)\n\t" \
    "s_xor_b32 s42, s43, 0\n\t" \
    "buffer_store_dwordx4 v[144:147], v19, s[4:7], s42 offen offset:0 sc1\n\t" \
    "s_waitcnt vmcnt(1)\n\t" \
    "s_mov_b64 exec, 1\n\t" \
    "ds_add_rtn_u32 v17, v9, v9 offset:8\n\t" \
    "s_add_u32 s37, s32, 524288\n\t" \
    "s_waitcnt lgkmcnt(0)\n\t" \
    "v_cmp_eq_u32 vcc, 1098496, v17\n\t" \
    "s_mov_b64 exec, -1\n\t" \
    "s_nop 0\n\t" \
    "s_cbranch_vccz .La13_%=\n\t" \
    "buffer_store_dwordx4 v[148:151], v2, s[4:7], s37 offen sc1\n\t" \
    ".La13_%=:\n\t" \
    "s_mov_b64 exec, -1\n\t" \
    "s_xor_b32 s42, s43, 1024\n\t" \
    "buffer_store_dwordx4 v[152:155], v19, s[4:7], s42 offen offset:0 sc1\n\t" \
    "s_cmp_eq_u32 s24, 0\n\t" \
    "s_cbranch_scc1 .Lj34_%=\n\t" \
    ".Lh33_%=:\n\t" \
    "s_memrealtime s[22:23]\n\t" \
    "s_waitcnt lgkmcnt(0)\n\t" \
    "s_sub_u32 s22, s22, s35\n\t" \
    "s_cmp_lt_u32 s22, 435\n\t" \
    "s_cbranch_scc1 .Lh33_%=\n\t" \
    ".Lj34_%=:\n\t" \
    "s_waitcnt lgkmcnt(0)\n\t" \
    "s_xor_b32 s42, s43, 2048\n\t" \
    "buffer_store_dwordx4 v[160:163], v19, s[4:7], s42 offen offset:0 sc1\n\t" \
    "s_xor_b32 s42, s43, 3072\n\t" \
    "buffer_store_dwordx4 v[164:167], v19, s[4:7], s42 offen offset:0 sc1\n\t" \
    ".Lskip_%=:\n\t" \
    "s_waitcnt lgkmcnt(0)\n\t" \
    "s_nop 7\n\t" \
    ".Lh35_%=:\n\t" \
    "s_memrealtime s[22:23]\n\t" \
    "s_waitcnt lgkmcnt(0)\n\t" \
    "s_sub_u32 s22, s22, s35\n\t" \
    "s_cmp_lt_u32 s22, 390\n\t" \
    "s_cbranch_scc1 .Lh35_%=\n\t" \
    "s_and_b32 s52, s2, 7\n\t" \
    "s_lshl_b32 s52, s52, 7\n\t" \
    "s_add_u32 s52, s52, 34636800\n\t" \
    "s_buffer_load_dword s36, s[4:7], s52\n\t" \
    "s_and_b32 s37, s24, 3\n\t" \
    "s_mul_i32 s37, s37, 8486912\n\t" \
    "s_add_u32 s53, s53, s37\n\t" \
    "s_and_b32 s37, s24, 4\n\t" \
    "s_lshl_b32 s39, s37, 11\n\t" \
    "s_add_u32 s53, s53, s39\n\t" \
    "s_add_u32 s37, s37, s24\n\t" \
    "s_andn2_b32 s39, s26, 1\n\t" \
    "s_lshl3_add_u32 s37, s39, s37\n\t" \
    "s_mul_i32 s37, s37, 43008\n\t" \
    "s_mul_i32 s39, s27, 4992\n\t" \
    "s_add_u32 s51, s37, s39\n\t" \
    "s_cmp_eq_u32 s27, 8\n\t" \
    "s_movk_i32 s10, 4992\n\t" \
    "s_cselect_b32 s10, 3072, s10\n\t" \
    "s_mov_b32 s11, 0x20000\n\t" \
    "s_and_b32 s37, s2, 0x70\n\t" \
    "s_lshl_b32 s37, s37, 1\n\t" \
    "v_lshrrev_b32 v11, 7, v0\n\t" \
    "v_lshl_add_u32 v11, v11, 2, s37\n\t" \
    "v_min_u32 v11, 248, v11\n\t" \
    "v_mul_u32_u24 v46, 147, v11\n\t" \
    "v_lshrrev_b32 v46, 12, v46\n\t" \
    "v_mul_u32_u24 v41, 28, v46\n\t" \
    "v_sub_u32 v11, v11, v41\n\t" \
    "v_bfe_u32 v41, v0, 4, 3\n\t" \
    "v_min_u32 v41, 4, v41\n\t" \
    "v_add3_u32 v11, v11, v41, -1\n\t" \
    "v_add_u32 v41, 28, v11\n\t" \
    "v_min_u32 v11, v11, v41\n\t" \
    "v_and_b32 v1, 1, v11\n\t" \
    "v_lshrrev_b32 v11, 1, v11\n\t" \
    "v_mad_u32_u24 v11, v11, 9, v46\n\t" \
    "v_lshl_add_u32 v11, v1, 7, v11\n\t" \
    "v_and_b32 v3, 31, v11\n\t" \
    "v_lshrrev_b32 v50, 5, v11\n\t" \
    "v_lshlrev_b32 v50, 7, v50\n\t" \
    "v_lshl_or_b32 v51, v3, 10, v50\n\t" \
    "s_mov_b32 s40, 0\n\t" \
    "s_waitcnt lgkmcnt(0)\n\t" \
    ".Lr4_%=:\n\t" \
    "s_xor_b32 s36, s36, s34\n\t" \
    "s_andn2_b32 s37, s36, 14\n\t" \
    "s_cbranch_scc1 .Lr5_%=\n\t" \
    ".Lr6_%=:\n\t" \
    "s_bfe_u32 s36, s36, 0x30001\n\t" \
    "s_add_u32 s56, s2, s36\n\t" \
    "s_and_b32 s56, s56, 7\n\t" \
    "s_lshr_b32 s37, s2, 7\n\t" \
    "s_lshl1_add_u32 s56, s56, s37\n\t" \
    "s_and_b32 s37, s56, 7\n\t" \
    "s_mul_i32 s48, s37, 4243456\n\t" \
    "s_and_b32 s37, s56, 8\n\t" \
    "s_lshl_b32 s37, s37, 10\n\t" \
    "s_add_u32 s48, s48, s37\n\t" \
    "s_add_u32 s49, s48, 524288\n\t" \
    "s_add_u32 s50, s48, 1048576\n\t" \
    "s_xor_b32 s37, s48, 0x2000\n\t" \
    "s_add_u32 s37, s37, 2097152\n\t" \
    "s_add_u32 s45, s45, s37\n\t" \
    "s_add_u32 s46, s46, s37\n\t" \
    "s_cmp_lt_u32 s24, 4\n\t" \
    "s_cselect_b32 s47, s46, 0x80000000\n\t" \
    "v_add_u32 v17, s45, v0\n\t" \
    "v_add_u32 v19, s46, v0\n\t" \
    "v_add_u32 v41, s47, v0\n\t" \
    "s_lshr_b32 s37, s24, 2\n\t" \
    "s_mul_i32 s37, s37, 6\n\t" \
    "s_mul_i32 s39, s56, 7\n\t" \
    "s_add_u32 s37, s37, s39\n\t" \
    "s_lshr_b32 s37, s37, 4\n\t" \
    "s_and_b32 s39, s24, 3\n\t" \
    "s_mul_i32 s39, s39, 63\n\t" \
    "s_add_u32 s37, s37, s39\n\t" \
    "v_lshrrev_b32 v46, 4, v0\n\t" \
    "v_min_u32 v46, 8, v46\n\t" \
    "v_mad_u32_u24 v46, v46, 7, s37\n\t" \
    "v_add_u32 v1, 2, v46\n\t" \
    "v_lshrrev_b32 v1, 7, v1\n\t" \
    "v_lshl_add_u32 v46, v1, 1, v46\n\t" \
    "v_and_b32 v1, 31, v46\n\t" \
    "v_lshrrev_b32 v11, 5, v46\n\t" \
    "v_lshlrev_b32 v11, 7, v11\n\t" \
    "v_lshl_or_b32 v3, v1, 10, v11\n\t" \
    "v_add_u32 v18, s36, v18\n\t" \
    "v_lshlrev_b32 v18, 8, v18\n\t" \
    "v_and_b32 v18, 0x400, v18\n\t" \
    "v_xor_b32 v42, v42, v18\n\t" \
    "s_sub_u32 s37, s24, s36\n\t" \
    "s_and_b32 s37, s37, 7\n\t" \
    "s_mul_i32 s39, s27, 28\n\t" \
    "s_add_u32 s39, s39, s26\n\t" \
    "s_lshr_b32 s39, s39, 5\n\t" \
    "s_lshl4_add_u32 s37, s39, s37\n\t" \
    "s_lshl_b32 s52, s37, 7\n\t" \
    "s_and_b32 s37, s24, 6\n\t" \
    "s_lshl_b32 s37, s37, 8\n\t" \
    "s_and_b32 s39, s2, 243\n\t" \
    "s_add_u32 s37, s37, s39\n\t" \
    "s_cmp_eq_u32 s37, 1267\n\t" \
    "s_cbranch_scc1 .Lqp_%=\n\t" \
    ".Lqq_%=:\n\t" \
    "s_nop 0\n\t" \
    "v_readlane_b32 s12, v3, 0\n\t" \
    "v_readlane_b32 s13, v3, 1\n\t" \
    "v_readlane_b32 s14, v3, 2\n\t" \
    "v_readlane_b32 s15, v3, 3\n\t" \
    "v_readlane_b32 s16, v3, 4\n\t" \
    "v_readlane_b32 s17, v3, 5\n\t" \
    "v_readlane_b32 s18, v3, 6\n\t" \
    "v_readlane_b32 s19, v3, 7\n\t" \
    "v_readlane_b32 s3, v3, 8\n\t" \
    "s_add_u32 s12, s12, s33\n\t" \
    "s_add_u32 s13, s13, s33\n\t" \
    "s_add_u32 s14, s14, s33\n\t" \
    "s_add_u32 s15, s15, s33\n\t" \
    "s_add_u32 s16, s16, s33\n\t" \
    "s_add_u32 s17, s17, s33\n\t" \
    "s_add_u32 s18, s18, s33\n\t" \
    "s_add_u32 s19, s19, s33\n\t" \
    "s_add_u32 s3, s3, s33\n\t" \
    "s_waitcnt vmcnt(0)\n\t" \
    "s_mov_b64 exec, 1\n\t" \
    "ds_add_rtn_u32 v16, v9, v9 offset:12\n\t" \
    "s_add_u32 s37, s32, 786432\n\t" \
    "s_waitcnt lgkmcnt(0)\n\t" \
    "v_cmp_eq_u32 vcc, 1098496, v16\n\t" \
    "s_mov_b64 exec, -1\n\t" \
    "s_nop 0\n\t" \
    "s_cbranch_vccz .La20_%=\n\t" \
    "buffer_store_dwordx4 v[148:151], v2, s[4:7], s37 offen sc1\n\t" \
    ".La20_%=:\n\t" \
    "s_mov_b64 exec, -1\n\t" \
    ".Lh36_%=:\n\t" \
    "s_memrealtime s[22:23]\n\t" \
    "s_waitcnt lgkmcnt(0)\n\t" \
    "s_sub_u32 s22, s22, s35\n\t" \
    "s_cmp_lt_u32 s22, 482\n\t" \
    "s_cbranch_scc1 .Lh36_%=\n\t" \
    "s_cmp_lg_u32 s24, 0\n\t" \
    "s_cbranch_scc1 .Lb15_%=\n\t" \
    "s_mov_b32 s40, 0\n\t" \
    "s_add_u32 s38, s33, 524288\n\t" \
    ".Ll15_%=:\n\t" \
    "buffer_load_dword v46, v51, s[4:7], s38 offen sc1\n\t" \
    "s_waitcnt vmcnt(0)\n\t" \
    "v_cmp_ne_u32 vcc, s34, v46\n\t" \
    "s_add_u32 s40, s40, 1\n\t" \
    "s_cmp_ge_u32 s40, 8192\n\t" \
    "s_cbranch_scc1 .Lb15_%=\n\t" \
    "s_cbranch_vccz .Lb15_%=\n\t" \
    "s_sleep 2\n\t" \
    "s_branch .Ll15_%=\n\t" \
    ".Lb15_%=:\n\t" \
    "s_waitcnt lgkmcnt(0)\n\t" \
    "s_barrier\n\t" \
    "buffer_load_dwordx4 v[176:179], v36, s[4:7], s48 offen offset:0 sc1\n\t" \
    "buffer_load_dwordx4 v[180:183], v36, s[4:7], s48 offen offset:1024 sc1\n\t" \
    "buffer_load_dwordx4 v[184:187], v36, s[4:7], s48 offen offset:256 sc1\n\t" \
    "buffer_load_dwordx4 v[188:191], v36, s[4:7], s48 offen offset:1280 sc1\n\t" \
    "buffer_load_dwordx4 v[20:23], v37, s[4:7], s48 offen offset:0 sc1\n\t" \
    "s_mov_b32 s37, 34637824\n\t" \
    "s_buffer_load_dword s56, s[4:7], s37 offset:0x0\n\t" \
    "s_buffer_load_dword s57, s[4:7], s37 offset:0x80\n\t" \
    "s_buffer_load_dword s58, s[4:7], s37 offset:0x100\n\t" \
    "s_buffer_load_dword s59, s[4:7], s37 offset:0x180\n\t" \
    "s_buffer_load_dword s60, s[4:7], s37 offset:0x200\n\t" \
    "s_buffer_load_dword s61, s[4:7], s37 offset:0x280\n\t" \
    "s_cmp_lt_u32 s24, 4\n\t" \
    "s_cbranch_scc1 .Lp37_%=\n\t" \
    "s_setprio 1\n\t" \
    ".Lp37_%=:\n\t" \
    "s_waitcnt vmcnt(4)\n\t" \
    "v_mfma_f32_32x32x16_bf16 v[192:207], v[52:55], v[176:179], 0\n\t" \
    "buffer_load_dwordx4 v[4:7], v36, s[4:7], s49 offen offset:0 sc1\n\t" \
    "s_waitcnt vmcnt(4)\n\t" \
    "v_mfma_f32_32x32x16_bf16 v[192:207], v[56:59], v[180:183], v[192:207]\n\t" \
    "buffer_load_dwordx4 v[60:63], v36, s[4:7], s49 offen offset:1024 sc1\n\t" \
    "s_waitcnt vmcnt(4)\n\t" \
    "v_mfma_f32_32x32x16_bf16 v[208:223], v[52:55], v[184:187], 0\n\t" \
    "buffer_load_dwordx4 v[64:67], v36, s[4:7], s49 offen offset:256 sc1\n\t" \
    "s_waitcnt vmcnt(4)\n\t" \
    "v_mfma_f32_32x32x16_bf16 v[208:223], v[56:59], v[188:191], v[208:223]\n\t" \
    "buffer_load_dwordx4 v[68:71], v36, s[4:7], s49 offen offset:1280 sc1\n\t" \
    "v_mfma_f32_32x32x16_bf16 v[224:239], v[56:59], v[176:179], 0\n\t" \
    "s_add_u32 m0, s44, 0\n\t" \
    "s_nop 8\n\t" \
    "v_cvt_pk_bf16_f32 v1, v192, v208\n\t" \
    "v_cvt_pk_bf16_f32 v11, v193, v209\n\t" \
    "v_cvt_pk_bf16_f32 v18, v194, v210\n\t" \
    "v_cvt_pk_bf16_f32 v50, v195, v211\n\t" \
    "v_cvt_pk_bf16_f32 v8, v196, v212\n\t" \
    "s_waitcnt vmcnt(4)\n\t" \
    "v_permlane16_swap_b32 v20, v180\n\t" \
    "v_permlane16_swap_b32 v21, v181\n\t" \
    "ds_write_addtid_b32 v1 offset:3616\n\t" \
    "ds_write_addtid_b32 v11 offset:3872\n\t" \
    "ds_write_addtid_b32 v18 offset:4128\n\t" \
    "ds_write_addtid_b32 v50 offset:4384\n\t" \
    "ds_write_addtid_b32 v8 offset:4640\n\t" \
    "v_permlane16_swap_b32 v22, v182\n\t" \
    "v_permlane16_swap_b32 v23, v183\n\t" \
    "v_cvt_pk_bf16_f32 v9, v197, v213\n\t" \
    "v_cvt_pk_bf16_f32 v16, v198, v214\n\t" \
    "v_cvt_pk_bf16_f32 v51, v199, v215\n\t" \
    "v_cvt_pk_bf16_f32 v46, v200, v216\n\t" \
    "v_cvt_pk_bf16_f32 v1, v201, v217\n\t" \
    "v_mfma_f32_32x32x16_bf16 v[240:255], v[56:59], v[184:187], 0\n\t" \
    "v_permlane16_swap_b32 v180, v188\n\t" \
    "buffer_load_dwordx4 v[72:75], v37, s[4:7], s49 offen offset:0 sc1\n\t" \
    "ds_write_addtid_b32 v9 offset:4896\n\t" \
    "ds_write_addtid_b32 v16 offset:5152\n\t" \
    "ds_write_addtid_b32 v51 offset:5408\n\t" \
    "ds_write_addtid_b32 v46 offset:5664\n\t" \
    "ds_write_addtid_b32 v1 offset:5920\n\t" \
    "v_permlane16_swap_b32 v181, v189\n\t" \
    "v_permlane16_swap_b32 v182, v190\n\t" \
    "v_cvt_pk_bf16_f32 v11, v202, v218\n\t" \
    "v_cvt_pk_bf16_f32 v18, v203, v219\n\t" \
    "v_cvt_pk_bf16_f32 v50, v204, v220\n\t" \
    "v_cvt_pk_bf16_f32 v8, v205, v221\n\t" \
    "v_permlane16_swap_b32 v183, v191\n\t" \
    "v_mfma_f32_32x32x16_bf16 v[224:239], v[52:55], v[20:23], v[224:239]\n\t" \
    "ds_write_addtid_b32 v11 offset:6176\n\t" \
    "ds_write_addtid_b32 v18 offset:6432\n\t" \
    "ds_write_addtid_b32 v50 offset:6688\n\t" \
    "ds_write_addtid_b32 v8 offset:6944\n\t" \
    "v_mfma_f32_32x32x16_bf16 v[240:255], v[52:55], v[180:183], v[240:255]\n\t" \
    "s_mov_b32 s40, 0\n\t" \
    "s_waitcnt lgkmcnt(0)\n\t" \
    ".Lq4_%=:\n\t" \
    "s_mov_b32 s37, 0\n\t" \
    "s_mov_b32 s0, 0\n\t" \
    "s_xor_b32 s61, s61, s34\n\t" \
    "s_or_b32 s37, s37, s61\n\t" \
    "s_lshl1_add_u32 s0, s0, s61\n\t" \
    "s_xor_b32 s60, s60, s34\n\t" \
    "s_or_b32 s37, s37, s60\n\t" \
    "s_lshl1_add_u32 s0, s0, s60\n\t" \
    "s_xor_b32 s59, s59, s34\n\t" \
    "s_or_b32 s37, s37, s59\n\t" \
    "s_lshl1_add_u32 s0, s0, s59\n\t" \
    "s_xor_b32 s58, s58, s34\n\t" \
    "s_or_b32 s37, s37, s58\n\t" \
    "s_lshl1_add_u32 s0, s0, s58\n\t" \
    "s_xor_b32 s57, s57, s34\n\t" \
    "s_or_b32 s37, s37, s57\n\t" \
    "s_lshl1_add_u32 s0, s0, s57\n\t" \
    "s_xor_b32 s56, s56, s34\n\t" \
    "s_or_b32 s37, s37, s56\n\t" \
    "s_lshl1_add_u32 s0, s0, s56\n\t" \
    "s_andn2_b32 s37, s37, 2\n\t" \
    "s_cbranch_scc1 .Lq5_%=\n\t" \
    ".Lq6_%=:\n\t" \
    "s_lshr_b32 s0, s0, 1\n\t" \
    "v_add_u32 v51, 344064, v39\n\t" \
    "v_lshrrev_b32 v46, 21, v51\n\t" \
    "v_bfe_u32 v46, s0, v46, 1\n\t" \
    "v_lshrrev_b32 v1, 3, v51\n\t" \
    "v_lshlrev_b32 v46, 10, v46\n\t" \
    "v_and_b32 v1, 0x400, v1\n\t" \
    "v_xor_b32 v46, v46, v1\n\t" \
    "v_xor_b32 v11, v42, v46\n\t" \
    "v_lshrrev_b32 v46, 21, v39\n\t" \
    "v_bfe_u32 v46, s0, v46, 1\n\t" \
    "v_lshrrev_b32 v1, 3, v39\n\t" \
    "v_lshlrev_b32 v46, 10, v46\n\t" \
    "v_and_b32 v1, 0x400, v1\n\t" \
    "v_xor_b32 v46, v46, v1\n\t" \
    "v_xor_b32 v42, v42, v46\n\t" \
    "s_lshr_b32 s37, s10, 1\n\t" \
    "s_add_u32 s37, s37, s51\n\t" \
    "s_and_b32 s39, s8, 0x1fffff\n\t" \
    "s_add_u32 s37, s37, s39\n\t" \
    "s_lshr_b32 s39, s37, 21\n\t" \
    "s_lshr_b32 s39, s0, s39\n\t" \
    "s_xor_b32 s37, s37, s48\n\t" \
    "s_lshr_b32 s37, s37, 13\n\t" \
    "s_xor_b32 s37, s37, s39\n\t" \
    "s_and_b32 s37, s37, 1\n\t" \
    "s_mul_i32 s37, s37, 172032\n\t" \
    "s_add_u32 s37, s37, s51\n\t" \
    "s_add_u32 s8, s8, s37\n\t" \
    "s_addc_u32 s9, s9, 0\n\t" \
    ".Lh38_%=:\n\t" \
    "s_memrealtime s[22:23]\n\t" \
    "s_waitcnt lgkmcnt(0)\n\t" \
    "s_sub_u32 s22, s22, s35\n\t" \
    "s_cmp_lt_u32 s22, 605\n\t" \
    "s_cbranch_scc1 .Lh38_%=\n\t" \
    "s_buffer_load_dword s12, s[4:7], s12 offset:0xc0000\n\t" \
    "s_buffer_load_dword s13, s[4:7], s13 offset:0xc0000\n\t" \
    "s_buffer_load_dword s14, s[4:7], s14 offset:0xc0000\n\t" \
    "s_buffer_load_dword s15, s[4:7], s15 offset:0xc0000\n\t" \
    "s_buffer_load_dword s16, s[4:7], s16 offset:0xc0000\n\t" \
    "s_buffer_load_dword s17, s[4:7], s17 offset:0xc0000\n\t" \
    "s_buffer_load_dword s18, s[4:7], s18 offset:0xc0000\n\t" \
    "s_buffer_load_dword s19, s[4:7], s19 offset:0xc0000\n\t" \
    "s_buffer_load_dword s3, s[4:7], s3 offset:0xc0000\n\t" \
    "s_waitcnt vmcnt(4)\n\t" \
    "v_mfma_f32_32x32x16_bf16 v[192:207], v[52:55], v[4:7], 0\n\t" \
    "v_cvt_pk_bf16_f32 v18, v224, v240\n\t" \
    "v_cvt_pk_bf16_f32 v50, v225, v241\n\t" \
    "v_cvt_pk_bf16_f32 v8, v226, v242\n\t" \
    "v_cvt_pk_bf16_f32 v9, v227, v243\n\t" \
    "v_cvt_pk_bf16_f32 v16, v228, v244\n\t" \
    "buffer_load_dwordx4 v[76:79], v36, s[4:7], s50 offen offset:0 sc1\n\t" \
    "ds_write_addtid_b32 v18 offset:0\n\t" \
    "ds_write_addtid_b32 v50 offset:256\n\t" \
    "ds_write_addtid_b32 v8 offset:512\n\t" \
    "ds_write_addtid_b32 v9 offset:768\n\t" \
    "ds_write_addtid_b32 v16 offset:1024\n\t" \
    "s_waitcnt vmcnt(4)\n\t" \
    "v_mfma_f32_32x32x16_bf16 v[192:207], v[56:59], v[60:63], v[192:207]\n\t" \
    "buffer_load_dwordx4 v[80:83], v36, s[4:7], s50 offen offset:1024 sc1\n\t" \
    "v_cvt_pk_bf16_f32 v51, v229, v245\n\t" \
    "v_cvt_pk_bf16_f32 v39, v230, v246\n\t" \
    "v_cvt_pk_bf16_f32 v1, v231, v247\n\t" \
    "v_cvt_pk_bf16_f32 v46, v232, v248\n\t" \
    "v_cvt_pk_bf16_f32 v18, v233, v249\n\t" \
    "s_waitcnt vmcnt(4)\n\t" \
    "v_mfma_f32_32x32x16_bf16 v[208:223], v[52:55], v[64:67], 0\n\t" \
    "buffer_load_dwordx4 v[116:119], v36, s[4:7], s50 offen offset:256 sc1\n\t" \
    "ds_write_addtid_b32 v51 offset:1280\n\t" \
    "ds_write_addtid_b32 v39 offset:1536\n\t" \
    "ds_write_addtid_b32 v1 offset:1792\n\t" \
    "ds_write_addtid_b32 v46 offset:2048\n\t" \
    "ds_write_addtid_b32 v18 offset:2304\n\t" \
    "v_cvt_pk_bf16_f32 v50, v234, v250\n\t" \
    "v_cvt_pk_bf16_f32 v8, v235, v251\n\t" \
    "v_cvt_pk_bf16_f32 v9, v236, v252\n\t" \
    "v_cvt_pk_bf16_f32 v16, v237, v253\n\t" \
    "buffer_load_dwordx4 v[120:123], v36, s[4:7], s50 offen offset:1280 sc1\n\t" \
    "s_waitcnt vmcnt(5)\n\t" \
    "v_mfma_f32_32x32x16_bf16 v[208:223], v[56:59], v[68:71], v[208:223]\n\t" \
    "ds_write_addtid_b32 v50 offset:2560\n\t" \
    "ds_write_addtid_b32 v8 offset:2816\n\t" \
    "ds_write_addtid_b32 v9 offset:3072\n\t" \
    "ds_write_addtid_b32 v16 offset:3328\n\t" \
    "buffer_load_dwordx4 v[88:91], v37, s[4:7], s50 offen offset:0 sc1\n\t" \
    "s_waitcnt lgkmcnt(0)\n\t" \
    "s_xor_b32 s12, s12, s34\n\t" \
    "s_xor_b32 s13, s13, s34\n\t" \
    "s_xor_b32 s14, s14, s34\n\t" \
    "s_xor_b32 s15, s15, s34\n\t" \
    "s_xor_b32 s16, s16, s34\n\t" \
    "s_xor_b32 s17, s17, s34\n\t" \
    "s_xor_b32 s18, s18, s34\n\t" \
    "s_xor_b32 s19, s19, s34\n\t" \
    "s_xor_b32 s3, s3, s34\n\t" \
    "s_or_b32 s12, s12, s13\n\t" \
    "s_or_b32 s12, s12, s14\n\t" \
    "s_or_b32 s12, s12, s15\n\t" \
    "s_or_b32 s12, s12, s16\n\t" \
    "s_or_b32 s12, s12, s17\n\t" \
    "s_or_b32 s12, s12, s18\n\t" \
    "s_or_b32 s12, s12, s19\n\t" \
    "s_or_b32 s12, s12, s3\n\t" \
    "s_cbranch_scc1 .Lq1023_%=\n\t" \
    ".Lb22_%=:\n\t" \
    "s_waitcnt lgkmcnt(0)\n\t" \
    "s_barrier\n\t" \
    "ds_read_b128 v[92:95], v44 offset:0\n\t" \
    "s_mov_b32 s45, 0\n\t" \
    "buffer_load_dwordx4 v[12:15], v17, s[4:7], s45 offen offset:0 sc1\n\t" \
    "ds_read_b128 v[124:127], v44 offset:128\n\t" \
    "ds_read_b128 v[128:131], v44 offset:29184\n\t" \
    "ds_read_b128 v[84:87], v44 offset:29312\n\t" \
    "ds_read_b128 v[96:99], v45 offset:0\n\t" \
    "buffer_load_dwordx4 v[104:107], v17, s[4:7], s45 offen offset:1024 sc1\n\t" \
    "ds_read_b128 v[108:111], v45 offset:128\n\t" \
    "ds_read_b128 v[100:103], v45 offset:29184\n\t" \
    "ds_read_b128 v[112:115], v45 offset:29312\n\t" \
    "v_mfma_f32_32x32x16_bf16 v[224:239], v[56:59], v[4:7], 0\n\t" \
    "v_cvt_pk_bf16_f32 v39, v192, v208\n\t" \
    "v_cvt_pk_bf16_f32 v1, v193, v209\n\t" \
    "v_cvt_pk_bf16_f32 v46, v194, v210\n\t" \
    "v_cvt_pk_bf16_f32 v18, v195, v211\n\t" \
    "v_cvt_pk_bf16_f32 v36, v196, v212\n\t" \
    "s_waitcnt vmcnt(7)\n\t" \
    "v_permlane16_swap_b32 v72, v60\n\t" \
    "v_permlane16_swap_b32 v73, v61\n\t" \
    "s_mov_b32 s46, 114688\n\t" \
    "buffer_load_dwordx4 v[132:135], v17, s[4:7], s46 offen offset:0 sc1\n\t" \
    "ds_write_addtid_b32 v39 offset:61984\n\t" \
    "ds_write_addtid_b32 v1 offset:62240\n\t" \
    "ds_write_addtid_b32 v46 offset:62496\n\t" \
    "ds_write_addtid_b32 v18 offset:62752\n\t" \
    "ds_write_addtid_b32 v36 offset:63008\n\t" \
    "v_permlane16_swap_b32 v74, v62\n\t" \
    "v_permlane16_swap_b32 v75, v63\n\t" \
    "v_cvt_pk_bf16_f32 v50, v197, v213\n\t" \
    "v_cvt_pk_bf16_f32 v8, v198, v214\n\t" \
    "v_cvt_pk_bf16_f32 v9, v199, v215\n\t" \
    "v_cvt_pk_bf16_f32 v16, v200, v216\n\t" \
    "v_cvt_pk_bf16_f32 v37, v201, v217\n\t" \
    "v_mfma_f32_32x32x16_bf16 v[240:255], v[56:59], v[64:67], 0\n\t" \
    "v_permlane16_swap_b32 v60, v68\n\t" \
    "buffer_load_dwordx4 v[28:31], v17, s[4:7], s46 offen offset:1024 sc1\n\t" \
    "ds_write_addtid_b32 v50 offset:63264\n\t" \
    "ds_write_addtid_b32 v8 offset:63520\n\t" \
    "ds_write_addtid_b32 v9 offset:63776\n\t" \
    "ds_write_addtid_b32 v16 offset:64032\n\t" \
    "ds_write_addtid_b32 v37 offset:64288\n\t" \
    "v_permlane16_swap_b32 v61, v69\n\t" \
    "v_permlane16_swap_b32 v62, v70\n\t" \
    "v_cvt_pk_bf16_f32 v3, v202, v218\n\t" \
    "v_cvt_pk_bf16_f32 v51, v203, v219\n\t" \
    "v_cvt_pk_bf16_f32 v39, v204, v220\n\t" \
    "v_cvt_pk_bf16_f32 v1, v205, v221\n\t" \
    "v_permlane16_swap_b32 v63, v71\n\t" \
    "v_mfma_f32_32x32x16_bf16 v[224:239], v[52:55], v[72:75], v[224:239]\n\t" \
    "buffer_load_dwordx4 v[136:139], v19, s[4:7], s45 offen offset:0 sc1\n\t" \
    "ds_write_addtid_b32 v3 offset:64544\n\t" \
    "ds_write_addtid_b32 v51 offset:64800\n\t" \
    "ds_write_addtid_b32 v39 offset:65056\n\t" \
    "ds_write_addtid_b32 v1 offset:65312\n\t" \
    "v_mfma_f32_32x32x16_bf16 v[240:255], v[52:55], v[60:63], v[240:255]\n\t" \
    "s_waitcnt vmcnt(9)\n\t" \
    "v_mfma_f32_32x32x16_bf16 v[192:207], v[52:55], v[76:79], 0\n\t" \
    "s_nop 9\n\t" \
    "v_cvt_pk_bf16_f32 v46, v224, v240\n\t" \
    "v_cvt_pk_bf16_f32 v18, v225, v241\n\t" \
    "v_cvt_pk_bf16_f32 v36, v226, v242\n\t" \
    "v_cvt_pk_bf16_f32 v50, v227, v243\n\t" \
    "v_cvt_pk_bf16_f32 v8, v228, v244\n\t" \
    "buffer_load_dwordx4 v[24:27], v19, s[4:7], s46 offen offset:0 sc1\n\t" \
    "ds_write_addtid_b32 v46 offset:58368\n\t" \
    "ds_write_addtid_b32 v18 offset:58624\n\t" \
    "ds_write_addtid_b32 v36 offset:58880\n\t" \
    "ds_write_addtid_b32 v50 offset:59136\n\t" \
    "ds_write_addtid_b32 v8 offset:59392\n\t" \
    "s_waitcnt vmcnt(9)\n\t" \
    "v_mfma_f32_32x32x16_bf16 v[192:207], v[56:59], v[80:83], v[192:207]\n\t" \
    "v_cvt_pk_bf16_f32 v9, v229, v245\n\t" \
    "v_cvt_pk_bf16_f32 v16, v230, v246\n\t" \
    "v_cvt_pk_bf16_f32 v37, v231, v247\n\t" \
    "v_cvt_pk_bf16_f32 v3, v232, v248\n\t" \
    "v_cvt_pk_bf16_f32 v51, v233, v249\n\t" \
    "s_waitcnt vmcnt(8)\n\t" \
    "v_mfma_f32_32x32x16_bf16 v[208:223], v[52:55], v[116:119], 0\n\t" \
    "s_mov_b32 s47, 229376\n\t" \
    "buffer_load_dwordx4 v[32:35], v17, s[4:7], s47 offen offset:0 sc1\n\t" \
    "ds_write_addtid_b32 v9 offset:59648\n\t" \
    "ds_write_addtid_b32 v16 offset:59904\n\t" \
    "ds_write_addtid_b32 v37 offset:60160\n\t" \
    "ds_write_addtid_b32 v3 offset:60416\n\t" \
    "ds_write_addtid_b32 v51 offset:60672\n\t" \
    "v_cvt_pk_bf16_f32 v39, v234, v250\n\t" \
    "v_cvt_pk_bf16_f32 v1, v235, v251\n\t" \
    "v_cvt_pk_bf16_f32 v46, v236, v252\n\t" \
    "v_cvt_pk_bf16_f32 v18, v237, v253\n\t" \
    "s_waitcnt vmcnt(8)\n\t" \
    "v_mfma_f32_32x32x16_bf16 v[208:223], v[56:59], v[120:123], v[208:223]\n\t" \
    "buffer_load_dwordx4 v[140:143], v17, s[4:7], s47 offen offset:1024 sc1\n\t" \
    "ds_write_addtid_b32 v39 offset:60928\n\t" \
    "ds_write_addtid_b32 v1 offset:61184\n\t" \
    "ds_write_addtid_b32 v46 offset:61440\n\t" \
    "ds_write_addtid_b32 v18 offset:61696\n\t" \
    "s_waitcnt vmcnt(8)\n\t" \
    "; wait\n\t" \
    "s_waitcnt lgkmcnt(0)\n\t" \
    "s_barrier\n\t" \
    "s_memrealtime s[64:65]\n\t" \
    "s_cmp_lg_u32 s24, 7\n\t" \
    "s_cbranch_scc1 .Lar_%=\n\t" \
    "s_waitcnt lgkmcnt(0)\n\t" \
    "v_mov_b32 v149, s64\n\t" \
    "s_add_u32 s37, s32, 1048576\n\t" \
    "s_mov_b64 exec, 0xff\n\t" \
    "buffer_store_dwordx4 v[148:151], v0, s[4:7], s37 offen sc1\n\t" \
    "s_mov_b64 exec, -1\n\t" \
    ".Lar_%=:\n\t" \
    "v_add_u32 v44, 58368, v44\n\t" \
    "buffer_load_dwordx4 v[156:159], v19, s[4:7], s47 offen offset:0 sc1\n\t" \
    "v_add_u32 v45, 58368, v45\n\t" \
    "ds_read_b128 v[168:171], v44 offset:0\n\t" \
    "ds_read_b128 v[172:175], v44 offset:128\n\t" \
    "ds_read_b128 v[144:147], v44 offset:29184\n\t" \
    "ds_read_b128 v[152:155], v44 offset:29312\n\t" \
    "buffer_load_dwordx4 v[160:163], v41, s[4:7], s45 offen offset:1024 sc1\n\t" \
    "ds_read_b128 v[164:167], v45 offset:0\n\t" \
    "ds_read_b128 v[176:179], v45 offset:128\n\t" \
    "ds_read_b128 v[184:187], v45 offset:29184\n\t" \
    "ds_read_b128 v[188:191], v45 offset:29312\n\t" \
    "v_mfma_f32_32x32x16_bf16 v[224:239], v[56:59], v[76:79], 0\n\t" \
    "v_cvt_pk_bf16_f32 v36, v192, v208\n\t" \
    "v_cvt_pk_bf16_f32 v50, v193, v209\n\t" \
    "v_cvt_pk_bf16_f32 v8, v194, v210\n\t" \
    "v_cvt_pk_bf16_f32 v9, v195, v211\n\t" \
    "v_cvt_pk_bf16_f32 v16, v196, v212\n\t" \
    "s_waitcnt vmcnt(9)\n\t" \
    "v_mfma_f32_16x16x32_bf16 v[20:23], v[12:15], v[92:95], 0\n\t" \
    "v_permlane16_swap_b32 v88, v80\n\t" \
    "v_permlane16_swap_b32 v89, v81\n\t" \
    "buffer_load_dwordx4 v[180:183], v41, s[4:7], s46 offen offset:1024 sc1\n\t" \
    "ds_write_addtid_b32 v36 offset:3616\n\t" \
    "ds_write_addtid_b32 v50 offset:3872\n\t" \
    "ds_write_addtid_b32 v8 offset:4128\n\t" \
    "ds_write_addtid_b32 v9 offset:4384\n\t" \
    "ds_write_addtid_b32 v16 offset:4640\n\t" \
    "v_mfma_f32_16x16x32_bf16 v[4:7], v[12:15], v[124:127], 0\n\t" \
    "v_permlane16_swap_b32 v90, v82\n\t" \
    "v_mfma_f32_16x16x32_bf16 v[64:67], v[12:15], v[128:131], 0\n\t" \
    "v_permlane16_swap_b32 v91, v83\n\t" \
    "v_cvt_pk_bf16_f32 v37, v197, v213\n\t" \
    "v_cvt_pk_bf16_f32 v3, v198, v214\n\t" \
    "v_cvt_pk_bf16_f32 v51, v199, v215\n\t" \
    "v_cvt_pk_bf16_f32 v39, v200, v216\n\t" \
    "v_cvt_pk_bf16_f32 v1, v201, v217\n\t" \
    "v_mfma_f32_32x32x16_bf16 v[240:255], v[56:59], v[116:119], 0\n\t" \
    "v_mfma_f32_16x16x32_bf16 v[68:71], v[12:15], v[84:87], 0\n\t" \
    "v_permlane16_swap_b32 v80, v120\n\t" \
    "buffer_load_dwordx4 v[72:75], v41, s[4:7], s47 offen offset:1024 sc1\n\t" \
    "ds_write_addtid_b32 v37 offset:4896\n\t" \
    "ds_write_addtid_b32 v3 offset:5152\n\t" \
    "ds_write_addtid_b32 v51 offset:5408\n\t" \
    "ds_write_addtid_b32 v39 offset:5664\n\t" \
    "ds_write_addtid_b32 v1 offset:5920\n\t" \
    "s_waitcnt vmcnt(10)\n\t" \
    "v_mfma_f32_16x16x32_bf16 v[60:63], v[104:107], v[124:127], 0\n\t" \
    "v_permlane16_swap_b32 v81, v121\n\t" \
    "v_permlane16_swap_b32 v82, v122\n\t" \
    "v_cvt_pk_bf16_f32 v46, v202, v218\n\t" \
    "v_cvt_pk_bf16_f32 v18, v203, v219\n\t" \
    "v_cvt_pk_bf16_f32 v36, v204, v220\n\t" \
    "v_cvt_pk_bf16_f32 v50, v205, v221\n\t" \
    "v_mfma_f32_16x16x32_bf16 v[4:7], v[104:107], v[92:95], v[4:7]\n\t" \
    "v_permlane16_swap_b32 v83, v123\n\t" \
    "v_mfma_f32_16x16x32_bf16 v[148:151], v[104:107], v[84:87], 0\n\t" \
    "v_mfma_f32_32x32x16_bf16 v[224:239], v[52:55], v[88:91], v[224:239]\n\t" \
    "s_mov_b32 s45, 344064\n\t" \
    "buffer_load_dwordx4 v[76:79], v17, s[4:7], s45 offen offset:0 sc1\n\t" \
    "ds_write_addtid_b32 v46 offset:6176\n\t" \
    "ds_write_addtid_b32 v18 offset:6432\n\t" \
    "ds_write_addtid_b32 v36 offset:6688\n\t" \
    "ds_write_addtid_b32 v50 offset:6944\n\t" \
    "v_mfma_f32_32x32x16_bf16 v[240:255], v[52:55], v[80:83], v[240:255]\n\t" \
    "v_mfma_f32_16x16x32_bf16 v[68:71], v[104:107], v[128:131], v[68:71]\n\t" \
    "s_nop 10\n\t" \
    "v_cvt_pk_bf16_f32 v8, v224, v240\n\t" \
    "v_cvt_pk_bf16_f32 v9, v225, v241\n\t" \
    "v_cvt_pk_bf16_f32 v16, v226, v242\n\t" \
    "v_cvt_pk_bf16_f32 v37, v227, v243\n\t" \
    "v_cvt_pk_bf16_f32 v3, v228, v244\n\t" \
    "s_waitcnt vmcnt(10) lgkmcnt(15)\n\t" \
    "v_mfma_f32_16x16x32_bf16 v[20:23], v[132:135], v[168:171], v[20:23]\n\t" \
    "buffer_load_dwordx4 v[56:59], v17, s[4:7], s45 offen offset:1024 sc1\n\t" \
    "v_mfma_f32_16x16x32_bf16 v[4:7], v[132:135], v[172:175], v[4:7]\n\t" \
    "ds_write_addtid_b32 v8 offset:0\n\t" \
    "ds_write_addtid_b32 v9 offset:256\n\t" \
    "ds_write_addtid_b32 v16 offset:512\n\t" \
    "ds_write_addtid_b32 v37 offset:768\n\t" \
    "ds_write_addtid_b32 v3 offset:1024\n\t" \
    "v_mfma_f32_16x16x32_bf16 v[64:67], v[132:135], v[144:147], v[64:67]\n\t" \
    "v_cvt_pk_bf16_f32 v51, v229, v245\n\t" \
    "v_cvt_pk_bf16_f32 v39, v230, v246\n\t" \
    "v_cvt_pk_bf16_f32 v1, v231, v247\n\t" \
    "v_cvt_pk_bf16_f32 v46, v232, v248\n\t" \
    "v_cvt_pk_bf16_f32 v18, v233, v249\n\t" \
    "s_mov_b32 s46, 458752\n\t" \
    "buffer_load_dwordx4 v[116:119], v17, s[4:7], s46 offen offset:0 sc1\n\t" \
    "v_mfma_f32_16x16x32_bf16 v[68:71], v[132:135], v[152:155], v[68:71]\n\t" \
    "ds_write_addtid_b32 v51 offset:1280\n\t" \
    "ds_write_addtid_b32 v39 offset:1536\n\t" \
    "ds_write_addtid_b32 v1 offset:1792\n\t" \
    "ds_write_addtid_b32 v46 offset:2048\n\t" \
    "ds_write_addtid_b32 v18 offset:2304\n\t" \
    "s_waitcnt vmcnt(11)\n\t" \
    "v_mfma_f32_16x16x32_bf16 v[60:63], v[28:31], v[172:175], v[60:63]\n\t" \
    "buffer_load_dwordx4 v[12:15], v17, s[4:7], s46 offen offset:1024 sc1\n\t" \
    "v_mfma_f32_16x16x32_bf16 v[4:7], v[28:31], v[168:171], v[4:7]\n\t" \
    "v_cvt_pk_bf16_f32 v36, v234, v250\n\t" \
    "v_cvt_pk_bf16_f32 v50, v235, v251\n\t" \
    "v_cvt_pk_bf16_f32 v8, v236, v252\n\t" \
    "v_cvt_pk_bf16_f32 v9, v237, v253\n\t" \
    "v_mfma_f32_16x16x32_bf16 v[148:151], v[28:31], v[152:155], v[148:151]\n\t" \
    "ds_write_addtid_b32 v36 offset:2560\n\t" \
    "ds_write_addtid_b32 v50 offset:2816\n\t" \
    "ds_write_addtid_b32 v8 offset:3072\n\t" \
    "ds_write_addtid_b32 v9 offset:3328\n\t" \
    "s_mov_b32 s47, 573440\n\t" \
    "buffer_load_dwordx4 v[192:195], v17, s[4:7], s47 offen offset:0 sc1\n\t" \
    "v_mfma_f32_16x16x32_bf16 v[68:71], v[28:31], v[144:147], v[68:71]\n\t" \
    "s_waitcnt lgkmcnt(0)\n\t" \
    "s_barrier\n\t" \
    "ds_read_b128 v[196:199], v10 offset:2048\n\t" \
    "v_mov_b32 v16, 0x10000\n\t" \
    "s_waitcnt vmcnt(12)\n\t" \
    "v_mfma_f32_16x16x32_bf16 v[200:203], v[136:139], v[96:99], 0\n\t" \
    "v_subrev_u32 v44, 58368, v44\n\t" \
    "v_mfma_f32_16x16x32_bf16 v[204:207], v[136:139], v[108:111], 0\n\t" \
    "v_subrev_u32 v45, 58368, v45\n\t" \
    "v_mfma_f32_16x16x32_bf16 v[208:211], v[136:139], v[100:103], 0\n\t" \
    "ds_read_b128 v[212:215], v44 offset:0\n\t" \
    "buffer_load_dwordx4 v[216:219], v17, s[4:7], s47 offen offset:1024 sc1\n\t" \
    "v_mfma_f32_16x16x32_bf16 v[220:223], v[136:139], v[112:115], 0\n\t" \
    "ds_read_b128 v[120:123], v44 offset:128\n\t" \
    "s_waitcnt vmcnt(12)\n\t" \
    "v_mfma_f32_16x16x32_bf16 v[200:203], v[24:27], v[164:167], v[200:203]\n\t" \
    "ds_read_b128 v[88:91], v44 offset:29184\n\t" \
    "v_mfma_f32_16x16x32_bf16 v[204:207], v[24:27], v[176:179], v[204:207]\n\t" \
    "ds_read_b128 v[52:55], v44 offset:29312\n\t" \
    "buffer_load_dwordx4 v[80:83], v19, s[4:7], s45 offen offset:0 sc1\n\t" \
    "v_mfma_f32_16x16x32_bf16 v[208:211], v[24:27], v[184:187], v[208:211]\n\t" \
    "ds_read_b128 v[104:107], v45 offset:0\n\t" \
    "v_mfma_f32_16x16x32_bf16 v[220:223], v[24:27], v[188:191], v[220:223]\n\t" \
    "ds_read_b128 v[132:135], v45 offset:128\n\t" \
    "s_waitcnt vmcnt(12) lgkmcnt(5)\n\t" \
    "v_mfma_f32_16x16x32_bf16 v[20:23], v[32:35], v[212:215], v[20:23]\n\t" \
    "ds_read_b128 v[224:227], v45 offset:29184\n\t" \
    "buffer_load_dwordx4 v[228:231], v19, s[4:7], s46 offen offset:0 sc1\n\t" \
    "s_waitcnt lgkmcnt(5)\n\t" \
    "v_mfma_f32_16x16x32_bf16 v[4:7], v[32:35], v[120:123], v[4:7]\n\t" \
    "ds_read_b128 v[232:235], v45 offset:29312\n\t" \
    "s_waitcnt lgkmcnt(5)\n\t" \
    "v_mfma_f32_16x16x32_bf16 v[64:67], v[32:35], v[88:91], v[64:67]\n\t" \
    "s_waitcnt lgkmcnt(4)\n\t" \
    "v_mfma_f32_16x16x32_bf16 v[68:71], v[32:35], v[52:55], v[68:71]\n\t" \
    "buffer_load_dwordx4 v[236:239], v19, s[4:7], s47 offen offset:0 sc1\n\t" \
    "s_waitcnt vmcnt(13)\n\t" \
    "v_mfma_f32_16x16x32_bf16 v[60:63], v[140:143], v[120:123], v[60:63]\n\t" \
    "s_waitcnt lgkmcnt(0)\n\t" \
    "s_add_u32 s12, s55, 34603008\n\t" \
    "s_buffer_load_dword s13, s[4:7], s12\n\t" \
    "s_buffer_load_dword s14, s[4:7], s12 offset:0x400\n\t" \
    "v_mfma_f32_16x16x32_bf16 v[4:7], v[140:143], v[212:215], v[4:7]\n\t" \
    "v_mfma_f32_16x16x32_bf16 v[148:151], v[140:143], v[52:55], v[148:151]\n\t" \
    "buffer_load_dwordx4 v[240:243], v41, s[4:7], s45 offen offset:1024 sc1\n\t" \
    "v_mfma_f32_16x16x32_bf16 v[68:71], v[140:143], v[88:91], v[68:71]\n\t" \
    "s_waitcnt vmcnt(13)\n\t" \
    "v_mfma_f32_16x16x32_bf16 v[200:203], v[156:159], v[104:107], v[200:203]\n\t" \
    "v_mfma_f32_16x16x32_bf16 v[204:207], v[156:159], v[132:135], v[204:207]\n\t" \
    "buffer_load_dwordx4 v[244:247], v41, s[4:7], s46 offen offset:1024 sc1\n\t" \
    "v_mfma_f32_16x16x32_bf16 v[208:211], v[156:159], v[224:227], v[208:211]\n\t" \
    "v_sub_f32 v20, v20, v60\n\t" \
    "v_sub_f32 v21, v21, v61\n\t" \
    "v_sub_f32 v22, v22, v62\n\t" \
    "v_mfma_f32_16x16x32_bf16 v[220:223], v[156:159], v[232:235], v[220:223]\n\t" \
    "v_sub_f32 v23, v23, v63\n\t" \
    "v_sub_f32 v64, v64, v148\n\t" \
    "v_sub_f32 v65, v65, v149\n\t" \
    "v_sub_f32 v66, v66, v150\n\t" \
    "v_sub_f32 v67, v67, v151\n\t" \
    "v_cvt_pk_bf16_f32 v250, v4, v5\n\t" \
    "v_cvt_pk_bf16_f32 v251, v6, v7\n\t" \
    "v_cvt_pk_bf16_f32 v248, v20, v21\n\t" \
    "v_cvt_pk_bf16_f32 v249, v22, v23\n\t" \
    "v_cvt_pk_bf16_f32 v254, v68, v69\n\t" \
    "v_cvt_pk_bf16_f32 v255, v70, v71\n\t" \
    "ds_write_b128 v40, v[248:251] offset:58368\n\t" \
    "v_cvt_pk_bf16_f32 v252, v64, v65\n\t" \
    "v_cvt_pk_bf16_f32 v253, v66, v67\n\t" \
    "ds_write_b128 v40, v[252:255] offset:59392\n\t" \
    "s_waitcnt vmcnt(11)\n\t" \
    "s_cmp_ge_u32 s24, 4\n\t" \
    "s_cbranch_scc1 .Lk42_%=\n\t" \
    "v_mfma_f32_16x16x32_bf16 v[28:31], v[160:163], v[108:111], 0\n\t" \
    "v_mfma_f32_16x16x32_bf16 v[28:31], v[180:183], v[176:179], v[28:31]\n\t" \
    "v_mfma_f32_16x16x32_bf16 v[28:31], v[72:75], v[132:135], v[28:31]\n\t" \
    "v_mfma_f32_16x16x32_bf16 v[136:139], v[160:163], v[112:115], 0\n\t" \
    "v_mfma_f32_16x16x32_bf16 v[136:139], v[180:183], v[188:191], v[136:139]\n\t" \
    "v_mfma_f32_16x16x32_bf16 v[136:139], v[72:75], v[232:235], v[136:139]\n\t" \
    "v_mfma_f32_16x16x32_bf16 v[204:207], v[160:163], v[96:99], v[204:207]\n\t" \
    "v_mfma_f32_16x16x32_bf16 v[204:207], v[180:183], v[164:167], v[204:207]\n\t" \
    "v_mfma_f32_16x16x32_bf16 v[204:207], v[72:75], v[104:107], v[204:207]\n\t" \
    "v_mfma_f32_16x16x32_bf16 v[220:223], v[160:163], v[100:103], v[220:223]\n\t" \
    "s_nop 0\n\t" \
    "v_sub_f32 v200, v200, v28\n\t" \
    "v_sub_f32 v201, v201, v29\n\t" \
    "v_mfma_f32_16x16x32_bf16 v[220:223], v[180:183], v[184:187], v[220:223]\n\t" \
    "v_sub_f32 v202, v202, v30\n\t" \
    "v_sub_f32 v203, v203, v31\n\t" \
    "v_mfma_f32_16x16x32_bf16 v[220:223], v[72:75], v[224:227], v[220:223]\n\t" \
    "v_sub_f32 v208, v208, v136\n\t" \
    "v_sub_f32 v209, v209, v137\n\t" \
    "v_sub_f32 v210, v210, v138\n\t" \
    "v_sub_f32 v211, v211, v139\n\t" \
    ".Lk42_%=:\n\t" \
    "s_waitcnt vmcnt(10)\n\t" \
    "v_mfma_f32_16x16x32_bf16 v[24:27], v[76:79], v[92:95], 0\n\t" \
    "buffer_load_dwordx4 v[32:35], v41, s[4:7], s47 offen offset:1024 sc1\n\t" \
    "v_mfma_f32_16x16x32_bf16 v[140:143], v[76:79], v[124:127], 0\n\t" \
    "v_cvt_pk_bf16_f32 v158, v204, v205\n\t" \
    "v_cvt_pk_bf16_f32 v159, v206, v207\n\t" \
    "v_mfma_f32_16x16x32_bf16 v[60:63], v[76:79], v[128:131], 0\n\t" \
    "v_cvt_pk_bf16_f32 v156, v200, v201\n\t" \
    "v_cvt_pk_bf16_f32 v157, v202, v203\n\t" \
    "v_mfma_f32_16x16x32_bf16 v[148:151], v[76:79], v[84:87], 0\n\t" \
    "v_cvt_pk_bf16_f32 v6, v220, v221\n\t" \
    "v_cvt_pk_bf16_f32 v7, v222, v223\n\t" \
    "s_mov_b32 s45, 688128\n\t" \
    "buffer_load_dwordx4 v[20:23], v17, s[4:7], s45 offen offset:0 sc1\n\t" \
    "s_waitcnt vmcnt(11)\n\t" \
    "v_mfma_f32_16x16x32_bf16 v[68:71], v[56:59], v[124:127], 0\n\t" \
    "ds_write_b128 v40, v[156:159] offset:60432\n\t" \
    "v_cvt_pk_bf16_f32 v4, v208, v209\n\t" \
    "v_mfma_f32_16x16x32_bf16 v[140:143], v[56:59], v[92:95], v[140:143]\n\t" \
    "v_cvt_pk_bf16_f32 v5, v210, v211\n\t" \
    "v_mfma_f32_16x16x32_bf16 v[248:251], v[56:59], v[84:87], 0\n\t" \
    "ds_write_b128 v40, v[4:7] offset:61456\n\t" \
    "buffer_load_dwordx4 v[64:67], v17, s[4:7], s45 offen offset:1024 sc1\n\t" \
    "v_mfma_f32_16x16x32_bf16 v[148:151], v[56:59], v[128:131], v[148:151]\n\t" \
    "s_waitcnt vmcnt(11)\n\t" \
    "v_mfma_f32_16x16x32_bf16 v[24:27], v[116:119], v[168:171], v[24:27]\n\t" \
    "v_mfma_f32_16x16x32_bf16 v[140:143], v[116:119], v[172:175], v[140:143]\n\t" \
    "s_waitcnt lgkmcnt(0)\n\t" \
    "s_xor_b32 s13, s13, s34\n\t" \
    "s_xor_b32 s14, s14, s34\n\t" \
    "s_or_b32 s13, s13, s14\n\t" \
    "s_cbranch_scc1 .Lq1026_%=\n\t" \
    ".Lb25_%=:\n\t" \
    "s_waitcnt lgkmcnt(0)\n\t" \
    "s_barrier\n\t" \
    "s_memrealtime s[16:17]\n\t" \
    "ds_read_b64_tr_b16 v[252:253], v49 offset:58368\n\t" \
    "ds_read_b64_tr_b16 v[254:255], v49 offset:62496\n\t" \
    "s_mov_b32 s46, 802816\n\t" \
    "buffer_load_dwordx4 v[160:163], v17, s[4:7], s46 offen offset:0 sc1\n\t" \
    "v_mfma_f32_16x16x32_bf16 v[60:63], v[116:119], v[144:147], v[60:63]\n\t" \
    "ds_read_b64_tr_b16 v[180:181], v49 offset:58624\n\t" \
    "ds_read_b64_tr_b16 v[182:183], v49 offset:62752\n\t" \
    "v_mfma_f32_16x16x32_bf16 v[148:151], v[116:119], v[152:155], v[148:151]\n\t" \
    "ds_read_b64_tr_b16 v[28:29], v49 offset:58384\n\t" \
    "ds_read_b64_tr_b16 v[30:31], v49 offset:62512\n\t" \
    "s_waitcnt vmcnt(11)\n\t" \
    "v_mfma_f32_16x16x32_bf16 v[68:71], v[12:15], v[172:175], v[68:71]\n\t" \
    "ds_read_b64_tr_b16 v[72:73], v49 offset:58640\n\t" \
    "ds_read_b64_tr_b16 v[74:75], v49 offset:62768\n\t" \
    "buffer_load_dwordx4 v[136:139], v17, s[4:7], s46 offen offset:1024 sc1\n\t" \
    "v_mfma_f32_16x16x32_bf16 v[140:143], v[12:15], v[168:171], v[140:143]\n\t" \
    "v_mfma_f32_16x16x32_bf16 v[248:251], v[12:15], v[152:155], v[248:251]\n\t" \
    "v_mfma_f32_16x16x32_bf16 v[148:151], v[12:15], v[144:147], v[148:151]\n\t" \
    "s_waitcnt lgkmcnt(6)\n\t" \
    "v_mfma_f32_16x16x32_bf16 v[204:207], v[252:255], v[196:199], 0\n\t" \
    "s_waitcnt lgkmcnt(4)\n\t" \
    "v_mfma_f32_16x16x32_bf16 v[200:203], v[180:183], v[196:199], 0\n\t" \
    "s_mov_b32 s47, 917504\n\t" \
    "buffer_load_dwordx4 v[76:79], v17, s[4:7], s47 offen offset:0 sc1\n\t" \
    "s_waitcnt vmcnt(12)\n\t" \
    "v_mfma_f32_16x16x32_bf16 v[24:27], v[192:195], v[212:215], v[24:27]\n\t" \
    "s_waitcnt lgkmcnt(2)\n\t" \
    "v_mfma_f32_16x16x32_bf16 v[220:223], v[28:31], v[196:199], 0\n\t" \
    "s_waitcnt lgkmcnt(0)\n\t" \
    "v_mfma_f32_16x16x32_bf16 v[156:159], v[72:75], v[196:199], 0\n\t" \
    "v_mfma_f32_16x16x32_bf16 v[140:143], v[192:195], v[120:123], v[140:143]\n\t" \
    "s_cmp_lg_u32 s24, 4\n\t" \
    "s_cbranch_scc1 .Lph_%=\n\t" \
    "s_waitcnt lgkmcnt(0)\n\t" \
    "v_mov_b32 v208, s16\n\t" \
    "v_mov_b32 v209, s16\n\t" \
    "v_mov_b32 v210, s16\n\t" \
    "v_mov_b32 v211, s16\n\t" \
    "buffer_store_dwordx4 v[208:211], v2, s[4:7], s32 offen sc1\n\t" \
    "s_nop 0\n\t" \
    ".Lph_%=:\n\t" \
    "v_mfma_f32_16x16x32_bf16 v[60:63], v[192:195], v[88:91], v[60:63]\n\t" \
    "buffer_load_dwordx4 v[4:7], v17, s[4:7], s47 offen offset:1024 sc1\n\t" \
    "v_mfma_f32_16x16x32_bf16 v[148:151], v[192:195], v[52:55], v[148:151]\n\t" \
    "v_bfi_b32 v204, s41, v16, v204\n\t" \
    "v_bfi_b32 v200, s41, v16, v200\n\t" \
    "s_waitcnt vmcnt(12)\n\t" \
    "v_mfma_f32_16x16x32_bf16 v[68:71], v[216:219], v[120:123], v[68:71]\n\t" \
    "v_cvt_pk_bf16_f32 v57, v206, v207\n\t" \
    "v_cvt_pk_bf16_f32 v59, v202, v203\n\t" \
    "v_mfma_f32_16x16x32_bf16 v[140:143], v[216:219], v[212:215], v[140:143]\n\t" \
    "v_cvt_pk_bf16_f32 v56, v204, v205\n\t" \
    "v_cvt_pk_bf16_f32 v58, v200, v201\n\t" \
    "v_mfma_f32_16x16x32_bf16 v[248:251], v[216:219], v[52:55], v[248:251]\n\t" \
    "v_bfi_b32 v220, s41, v16, v220\n\t" \
    "v_bfi_b32 v156, s41, v16, v156\n\t" \
    "v_mfma_f32_16x16x32_bf16 v[148:151], v[216:219], v[88:91], v[148:151]\n\t" \
    "buffer_store_dwordx4 v[56:59], v42, s[4:7], s48 offen offset:0 sc1\n\t" \
    "v_cvt_pk_bf16_f32 v45, v222, v223\n\t" \
    "s_waitcnt vmcnt(12)\n\t" \
    "v_mfma_f32_16x16x32_bf16 v[116:119], v[80:83], v[96:99], 0\n\t" \
    "v_cvt_pk_bf16_f32 v47, v158, v159\n\t" \
    "v_cvt_pk_bf16_f32 v44, v220, v221\n\t" \
    "v_mfma_f32_16x16x32_bf16 v[12:15], v[80:83], v[108:111], 0\n\t" \
    "v_cvt_pk_bf16_f32 v46, v156, v157\n\t" \
    "buffer_store_dwordx4 v[44:47], v11, s[4:7], s48 offen offset:128 sc1\n\t" \
    "v_mfma_f32_16x16x32_bf16 v[252:255], v[80:83], v[100:103], 0\n\t" \
    "v_sub_f32 v24, v24, v68\n\t" \
    "v_sub_f32 v25, v25, v69\n\t" \
    "v_mfma_f32_16x16x32_bf16 v[180:183], v[80:83], v[112:115], 0\n\t" \
    "v_sub_f32 v26, v26, v70\n\t" \
    "v_sub_f32 v27, v27, v71\n\t" \
    "s_waitcnt vmcnt(12)\n\t" \
    "v_mfma_f32_16x16x32_bf16 v[116:119], v[228:231], v[164:167], v[116:119]\n\t" \
    "v_sub_f32 v60, v60, v248\n\t" \
    "v_sub_f32 v61, v61, v249\n\t" \
    "v_mfma_f32_16x16x32_bf16 v[12:15], v[228:231], v[176:179], v[12:15]\n\t" \
    "v_sub_f32 v62, v62, v250\n\t" \
    "v_sub_f32 v63, v63, v251\n\t" \
    "v_mfma_f32_16x16x32_bf16 v[252:255], v[228:231], v[184:187], v[252:255]\n\t" \
    "v_cvt_pk_bf16_f32 v30, v140, v141\n\t" \
    "v_cvt_pk_bf16_f32 v31, v142, v143\n\t" \
    "buffer_load_dwordx4 v[72:75], v19, s[4:7], s45 offen offset:0 sc1\n\t" \
    "v_mfma_f32_16x16x32_bf16 v[180:183], v[228:231], v[188:191], v[180:183]\n\t" \
    "v_cvt_pk_bf16_f32 v28, v24, v25\n\t" \
    "v_cvt_pk_bf16_f32 v29, v26, v27\n\t" \
    "s_waitcnt vmcnt(12)\n\t" \
    "v_mfma_f32_16x16x32_bf16 v[116:119], v[236:239], v[104:107], v[116:119]\n\t" \
    "v_cvt_pk_bf16_f32 v210, v148, v149\n\t" \
    "v_cvt_pk_bf16_f32 v211, v150, v151\n\t" \
    "v_mfma_f32_16x16x32_bf16 v[12:15], v[236:239], v[132:135], v[12:15]\n\t" \
    "ds_write_b128 v40, v[28:31] offset:0\n\t" \
    "v_cvt_pk_bf16_f32 v208, v60, v61\n\t" \
    "buffer_load_dwordx4 v[192:195], v19, s[4:7], s46 offen offset:0 sc1\n\t" \
    "v_mfma_f32_16x16x32_bf16 v[252:255], v[236:239], v[224:227], v[252:255]\n\t" \
    "v_cvt_pk_bf16_f32 v209, v62, v63\n\t" \
    "ds_write_b128 v40, v[208:211] offset:1024\n\t" \
    "v_mfma_f32_16x16x32_bf16 v[180:183], v[236:239], v[232:235], v[180:183]\n\t" \
    "s_waitcnt vmcnt(10)\n\t" \
    "s_cmp_ge_u32 s24, 4\n\t" \
    "s_cbranch_scc1 .Lk43_%=\n\t" \
    "v_mfma_f32_16x16x32_bf16 v[204:207], v[240:243], v[108:111], 0\n\t" \
    "v_mfma_f32_16x16x32_bf16 v[204:207], v[244:247], v[176:179], v[204:207]\n\t" \
    "v_mfma_f32_16x16x32_bf16 v[204:207], v[32:35], v[132:135], v[204:207]\n\t" \
    "v_mfma_f32_16x16x32_bf16 v[200:203], v[240:243], v[112:115], 0\n\t" \
    "v_mfma_f32_16x16x32_bf16 v[200:203], v[244:247], v[188:191], v[200:203]\n\t" \
    "v_mfma_f32_16x16x32_bf16 v[200:203], v[32:35], v[232:235], v[200:203]\n\t" \
    "v_mfma_f32_16x16x32_bf16 v[12:15], v[240:243], v[96:99], v[12:15]\n\t" \
    "v_mfma_f32_16x16x32_bf16 v[12:15], v[244:247], v[164:167], v[12:15]\n\t" \
    "v_mfma_f32_16x16x32_bf16 v[12:15], v[32:35], v[104:107], v[12:15]\n\t" \
    "v_mfma_f32_16x16x32_bf16 v[180:183], v[240:243], v[100:103], v[180:183]\n\t" \
    "s_nop 0\n\t" \
    "v_sub_f32 v116, v116, v204\n\t" \
    "v_sub_f32 v117, v117, v205\n\t" \
    "v_mfma_f32_16x16x32_bf16 v[180:183], v[244:247], v[184:187], v[180:183]\n\t" \
    "v_sub_f32 v118, v118, v206\n\t" \
    "v_sub_f32 v119, v119, v207\n\t" \
    "v_mfma_f32_16x16x32_bf16 v[180:183], v[32:35], v[224:227], v[180:183]\n\t" \
    "v_sub_f32 v252, v252, v200\n\t" \
    "v_sub_f32 v253, v253, v201\n\t" \
    "v_sub_f32 v254, v254, v202\n\t" \
    "v_sub_f32 v255, v255, v203\n\t" \
    ".Lk43_%=:\n\t" \
    "s_waitcnt vmcnt(9)\n\t" \
    "v_mfma_f32_16x16x32_bf16 v[216:219], v[20:23], v[92:95], 0\n\t" \
    "buffer_load_dwordx4 v[56:59], v19, s[4:7], s47 offen offset:0 sc1\n\t" \
    "v_mfma_f32_16x16x32_bf16 v[220:223], v[20:23], v[124:127], 0\n\t" \
    "v_cvt_pk_bf16_f32 v158, v12, v13\n\t" \
    "v_cvt_pk_bf16_f32 v159, v14, v15\n\t" \
    "v_mfma_f32_16x16x32_bf16 v[44:47], v[20:23], v[128:131], 0\n\t" \
    "v_cvt_pk_bf16_f32 v156, v116, v117\n\t" \
    "v_cvt_pk_bf16_f32 v157, v118, v119\n\t" \
    "v_mfma_f32_16x16x32_bf16 v[80:83], v[20:23], v[84:87], 0\n\t" \
    "v_cvt_pk_bf16_f32 v70, v180, v181\n\t" \
    "v_cvt_pk_bf16_f32 v71, v182, v183\n\t" \
    "buffer_load_dwordx4 v[248:251], v41, s[4:7], s45 offen offset:1024 sc1\n\t" \
    "s_waitcnt vmcnt(10)\n\t" \
    "v_mfma_f32_16x16x32_bf16 v[140:143], v[64:67], v[124:127], 0\n\t" \
    "ds_write_b128 v40, v[156:159] offset:2064\n\t" \
    "v_cvt_pk_bf16_f32 v68, v252, v253\n\t" \
    "v_mfma_f32_16x16x32_bf16 v[220:223], v[64:67], v[92:95], v[220:223]\n\t" \
    "v_cvt_pk_bf16_f32 v69, v254, v255\n\t" \
    "v_mfma_f32_16x16x32_bf16 v[228:231], v[64:67], v[84:87], 0\n\t" \
    "ds_write_b128 v40, v[68:71] offset:3088\n\t" \
    "buffer_load_dwordx4 v[24:27], v41, s[4:7], s46 offen offset:1024 sc1\n\t" \
    "v_mfma_f32_16x16x32_bf16 v[80:83], v[64:67], v[128:131], v[80:83]\n\t" \
    "s_waitcnt vmcnt(10)\n\t" \
    "v_mfma_f32_16x16x32_bf16 v[216:219], v[160:163], v[168:171], v[216:219]\n\t" \
    "v_mfma_f32_16x16x32_bf16 v[220:223], v[160:163], v[172:175], v[220:223]\n\t" \
    "s_waitcnt lgkmcnt(0)\n\t" \
    "s_barrier\n\t" \
    "s_memrealtime s[18:19]\n\t" \
    "s_setprio 2\n\t" \
    "ds_read_b64_tr_b16 v[148:149], v49 offset:0\n\t" \
    "ds_read_b64_tr_b16 v[150:151], v49 offset:4128\n\t" \
    "buffer_load_dwordx4 v[28:31], v41, s[4:7], s47 offen offset:1024 sc1\n\t" \
    "v_mfma_f32_16x16x32_bf16 v[44:47], v[160:163], v[144:147], v[44:47]\n\t" \
    "ds_read_b64_tr_b16 v[60:61], v49 offset:256\n\t" \
    "ds_read_b64_tr_b16 v[62:63], v49 offset:4384\n\t" \
    "v_mfma_f32_16x16x32_bf16 v[80:83], v[160:163], v[152:155], v[80:83]\n\t" \
    "ds_read_b64_tr_b16 v[208:209], v49 offset:16\n\t" \
    "ds_read_b64_tr_b16 v[210:211], v49 offset:4144\n\t" \
    "s_waitcnt vmcnt(10)\n\t" \
    "v_mfma_f32_16x16x32_bf16 v[140:143], v[136:139], v[172:175], v[140:143]\n\t" \
    "ds_read_b64_tr_b16 v[236:237], v49 offset:272\n\t" \
    "ds_read_b64_tr_b16 v[238:239], v49 offset:4400\n\t" \
    "v_mfma_f32_16x16x32_bf16 v[220:223], v[136:139], v[168:171], v[220:223]\n\t" \
    "v_mfma_f32_16x16x32_bf16 v[228:231], v[136:139], v[152:155], v[228:231]\n\t" \
    "v_mfma_f32_16x16x32_bf16 v[80:83], v[136:139], v[144:147], v[80:83]\n\t" \
    "s_waitcnt lgkmcnt(6)\n\t" \
    "v_mfma_f32_16x16x32_bf16 v[240:243], v[148:151], v[196:199], 0\n\t" \
    "s_waitcnt lgkmcnt(4)\n\t" \
    "v_mfma_f32_16x16x32_bf16 v[244:247], v[60:63], v[196:199], 0\n\t" \
    "s_waitcnt vmcnt(9)\n\t" \
    "v_mfma_f32_16x16x32_bf16 v[216:219], v[76:79], v[212:215], v[216:219]\n\t" \
    "s_waitcnt lgkmcnt(2)\n\t" \
    "v_mfma_f32_16x16x32_bf16 v[204:207], v[208:211], v[196:199], 0\n\t" \
    "s_waitcnt lgkmcnt(0)\n\t" \
    "v_mfma_f32_16x16x32_bf16 v[32:35], v[236:239], v[196:199], 0\n\t" \
    "v_mfma_f32_16x16x32_bf16 v[220:223], v[76:79], v[120:123], v[220:223]\n\t" \
    "v_mfma_f32_16x16x32_bf16 v[44:47], v[76:79], v[88:91], v[44:47]\n\t" \
    "s_add_u32 s37, s52, s33\n\t" \
    "s_buffer_load_dword s58, s[4:7], s37 offset:0x0\n\t" \
    "s_buffer_load_dword s59, s[4:7], s37 offset:0x400\n\t" \
    "s_buffer_load_dword s60, s[4:7], s37 offset:0x4000\n\t" \
    "s_buffer_load_dword s61, s[4:7], s37 offset:0x4400\n\t" \
    "v_mfma_f32_16x16x32_bf16 v[80:83], v[76:79], v[52:55], v[80:83]\n\t" \
    "v_bfi_b32 v240, s41, v16, v240\n\t" \
    "v_bfi_b32 v244, s41, v16, v244\n\t" \
    "s_waitcnt vmcnt(8)\n\t" \
    "v_mfma_f32_16x16x32_bf16 v[140:143], v[4:7], v[120:123], v[140:143]\n\t" \
    "v_cvt_pk_bf16_f32 v201, v242, v243\n\t" \
    "v_cvt_pk_bf16_f32 v203, v246, v247\n\t" \
    "v_add_u32 v40, 91392, v40\n\t" \
    "v_mfma_f32_16x16x32_bf16 v[220:223], v[4:7], v[212:215], v[220:223]\n\t" \
    "v_cvt_pk_bf16_f32 v200, v240, v241\n\t" \
    "v_cvt_pk_bf16_f32 v202, v244, v245\n\t" \
    "v_mfma_f32_16x16x32_bf16 v[228:231], v[4:7], v[52:55], v[228:231]\n\t" \
    "v_bfi_b32 v204, s41, v16, v204\n\t" \
    "v_bfi_b32 v32, s41, v16, v32\n\t" \
    "v_mfma_f32_16x16x32_bf16 v[80:83], v[4:7], v[88:91], v[80:83]\n\t" \
    "buffer_store_dwordx4 v[200:203], v42, s[4:7], s49 offen offset:0 sc1\n\t" \
    "v_cvt_pk_bf16_f32 v13, v206, v207\n\t" \
    "s_waitcnt vmcnt(6)\n\t" \
    "v_mfma_f32_16x16x32_bf16 v[116:119], v[72:75], v[96:99], 0\n\t" \
    "v_cvt_pk_bf16_f32 v15, v34, v35\n\t" \
    "v_cvt_pk_bf16_f32 v12, v204, v205\n\t" \
    "v_mfma_f32_16x16x32_bf16 v[20:23], v[72:75], v[108:111], 0\n\t" \
    "v_cvt_pk_bf16_f32 v14, v32, v33\n\t" \
    "buffer_store_dwordx4 v[12:15], v11, s[4:7], s49 offen offset:128 sc1\n\t" \
    "v_mfma_f32_16x16x32_bf16 v[180:183], v[72:75], v[100:103], 0\n\t" \
    "s_setprio 0\n\t" \
    "s_cmp_lt_u32 s24, 4\n\t" \
    "s_cbranch_scc1 .Lp44_%=\n\t" \
    "s_setprio 1\n\t" \
    ".Lp44_%=:\n\t" \
    "v_sub_f32 v216, v216, v140\n\t" \
    "v_sub_f32 v217, v217, v141\n\t" \
    "v_mfma_f32_16x16x32_bf16 v[124:127], v[72:75], v[112:115], 0\n\t" \
    "v_sub_f32 v218, v218, v142\n\t" \
    "v_sub_f32 v219, v219, v143\n\t" \
    "s_waitcnt vmcnt(6)\n\t" \
    "v_mfma_f32_16x16x32_bf16 v[116:119], v[192:195], v[164:167], v[116:119]\n\t" \
    "v_sub_f32 v44, v44, v228\n\t" \
    "v_sub_f32 v45, v45, v229\n\t" \
    "v_mfma_f32_16x16x32_bf16 v[20:23], v[192:195], v[176:179], v[20:23]\n\t" \
    "v_sub_f32 v46, v46, v230\n\t" \
    "v_sub_f32 v47, v47, v231\n\t" \
    "v_mfma_f32_16x16x32_bf16 v[180:183], v[192:195], v[184:187], v[180:183]\n\t" \
    "v_cvt_pk_bf16_f32 v158, v220, v221\n\t" \
    "v_cvt_pk_bf16_f32 v159, v222, v223\n\t" \
    "v_mfma_f32_16x16x32_bf16 v[124:127], v[192:195], v[188:191], v[124:127]\n\t" \
    "v_cvt_pk_bf16_f32 v156, v216, v217\n\t" \
    "v_cvt_pk_bf16_f32 v157, v218, v219\n\t" \
    "s_waitcnt vmcnt(5)\n\t" \
    "v_mfma_f32_16x16x32_bf16 v[116:119], v[56:59], v[104:107], v[116:119]\n\t" \
    "v_cvt_pk_bf16_f32 v94, v80, v81\n\t" \
    "v_cvt_pk_bf16_f32 v95, v82, v83\n\t" \
    "v_mfma_f32_16x16x32_bf16 v[20:23], v[56:59], v[132:135], v[20:23]\n\t" \
    "ds_write_b128 v40, v[156:159] offset:0\n\t" \
    "v_cvt_pk_bf16_f32 v92, v44, v45\n\t" \
    "v_mfma_f32_16x16x32_bf16 v[180:183], v[56:59], v[224:227], v[180:183]\n\t" \
    "v_cvt_pk_bf16_f32 v93, v46, v47\n\t" \
    "ds_write_b128 v40, v[92:95] offset:1024\n\t" \
    "v_mfma_f32_16x16x32_bf16 v[124:127], v[56:59], v[232:235], v[124:127]\n\t" \
    "s_waitcnt vmcnt(2)\n\t" \
    "s_cmp_ge_u32 s24, 4\n\t" \
    "s_cbranch_scc1 .Lk45_%=\n\t" \
    "v_mfma_f32_16x16x32_bf16 v[252:255], v[248:251], v[108:111], 0\n\t" \
    "v_mfma_f32_16x16x32_bf16 v[252:255], v[24:27], v[176:179], v[252:255]\n\t" \
    "v_mfma_f32_16x16x32_bf16 v[252:255], v[28:31], v[132:135], v[252:255]\n\t" \
    "v_mfma_f32_16x16x32_bf16 v[84:87], v[248:251], v[112:115], 0\n\t" \
    "v_mfma_f32_16x16x32_bf16 v[84:87], v[24:27], v[188:191], v[84:87]\n\t" \
    "v_mfma_f32_16x16x32_bf16 v[84:87], v[28:31], v[232:235], v[84:87]\n\t" \
    "v_mfma_f32_16x16x32_bf16 v[20:23], v[248:251], v[96:99], v[20:23]\n\t" \
    "v_mfma_f32_16x16x32_bf16 v[20:23], v[24:27], v[164:167], v[20:23]\n\t" \
    "v_mfma_f32_16x16x32_bf16 v[20:23], v[28:31], v[104:107], v[20:23]\n\t" \
    "v_mfma_f32_16x16x32_bf16 v[124:127], v[248:251], v[100:103], v[124:127]\n\t" \
    "s_nop 0\n\t" \
    "v_sub_f32 v116, v116, v252\n\t" \
    "v_sub_f32 v117, v117, v253\n\t" \
    "v_mfma_f32_16x16x32_bf16 v[124:127], v[24:27], v[184:187], v[124:127]\n\t" \
    "v_sub_f32 v118, v118, v254\n\t" \
    "v_sub_f32 v119, v119, v255\n\t" \
    "v_mfma_f32_16x16x32_bf16 v[124:127], v[28:31], v[224:227], v[124:127]\n\t" \
    "v_sub_f32 v180, v180, v84\n\t" \
    "v_sub_f32 v181, v181, v85\n\t" \
    "v_sub_f32 v182, v182, v86\n\t" \
    "v_sub_f32 v183, v183, v87\n\t" \
    ".Lk45_%=:\n\t" \
    "v_cvt_pk_bf16_f32 v70, v20, v21\n\t" \
    "v_cvt_pk_bf16_f32 v71, v22, v23\n\t" \
    "v_cvt_pk_bf16_f32 v68, v116, v117\n\t" \
    "v_cvt_pk_bf16_f32 v69, v118, v119\n\t" \
    "s_nop 1\n\t" \
    "v_cvt_pk_bf16_f32 v66, v124, v125\n\t" \
    "v_cvt_pk_bf16_f32 v67, v126, v127\n\t" \
    "ds_write_b128 v40, v[68:71] offset:2064\n\t" \
    "v_cvt_pk_bf16_f32 v64, v180, v181\n\t" \
    "v_cvt_pk_bf16_f32 v65, v182, v183\n\t" \
    "v_add_u32 v49, 91392, v49\n\t" \
    "ds_write_b128 v40, v[64:67] offset:3088\n\t" \
    "s_waitcnt lgkmcnt(0)\n\t" \
    "s_barrier\n\t" \
    "s_memrealtime s[56:57]\n\t" \
    "s_add_u32 s54, s53, 0\n\t" \
    "buffer_load_dwordx4 v[128:131], v0, s[4:7], s54 offen offset:0 sc1\n\t" \
    "s_add_u32 s54, s53, 4243456\n\t" \
    "buffer_load_dwordx4 v[160:163], v0, s[4:7], s54 offen offset:0 sc1\n\t" \
    "ds_read_b64_tr_b16 v[172:173], v49 offset:0\n\t" \
    "ds_read_b64_tr_b16 v[174:175], v49 offset:4128\n\t" \
    "ds_read_b64_tr_b16 v[168:169], v49 offset:256\n\t" \
    "ds_read_b64_tr_b16 v[170:171], v49 offset:4384\n\t" \
    "ds_read_b64_tr_b16 v[152:153], v49 offset:16\n\t" \
    "ds_read_b64_tr_b16 v[154:155], v49 offset:4144\n\t" \
    "ds_read_b64_tr_b16 v[136:137], v49 offset:272\n\t" \
    "ds_read_b64_tr_b16 v[138:139], v49 offset:4400\n\t" \
    "s_waitcnt lgkmcnt(6)\n\t" \
    "v_mfma_f32_16x16x32_bf16 v[144:147], v[172:175], v[196:199], 0\n\t" \
    "s_waitcnt lgkmcnt(4)\n\t" \
    "v_mfma_f32_16x16x32_bf16 v[148:151], v[168:171], v[196:199], 0\n\t" \
    "s_waitcnt lgkmcnt(2)\n\t" \
    "v_mfma_f32_16x16x32_bf16 v[60:63], v[152:155], v[196:199], 0\n\t" \
    "s_waitcnt lgkmcnt(0)\n\t" \
    "v_mfma_f32_16x16x32_bf16 v[208:211], v[136:139], v[196:199], 0\n\t" \
    "s_nop 1\n\t" \
    "v_bfi_b32 v144, s41, v16, v144\n\t" \
    "s_nop 0\n\t" \
    "v_bfi_b32 v148, s41, v16, v148\n\t" \
    "v_cvt_pk_bf16_f32 v237, v146, v147\n\t" \
    "v_cvt_pk_bf16_f32 v239, v150, v151\n\t" \
    "v_cvt_pk_bf16_f32 v236, v144, v145\n\t" \
    "v_cvt_pk_bf16_f32 v238, v148, v149\n\t" \
    "v_bfi_b32 v60, s41, v16, v60\n\t" \
    "v_bfi_b32 v208, s41, v16, v208\n\t" \
    "buffer_store_dwordx4 v[236:239], v42, s[4:7], s50 offen offset:0 sc1\n\t" \
    "v_cvt_pk_bf16_f32 v77, v62, v63\n\t" \
    "v_cvt_pk_bf16_f32 v79, v210, v211\n\t" \
    "v_cvt_pk_bf16_f32 v76, v60, v61\n\t" \
    "v_cvt_pk_bf16_f32 v78, v208, v209\n\t" \
    "buffer_store_dwordx4 v[76:79], v11, s[4:7], s50 offen offset:128 sc1\n\t" \
    "s_cmp_ge_u32 s25, 126\n\t" \
    "s_cbranch_scc1 .Lend_%=\n\t" \
    "ds_read_b128 v[120:123], v10 offset:3072\n\t" \
    "ds_read_b128 v[212:215], v10 offset:4096\n\t" \
    "s_waitcnt lgkmcnt(0)\n\t" \
    "s_sub_u32 s37, s16, 300\n\t" \
    "s_mov_b32 s62, 300\n\t" \
    "s_sub_u32 s39, s58, s37\n\t" \
    "s_cmp_gt_u32 s39, 600\n\t" \
    "s_cselect_b32 s39, 400, s39\n\t" \
    "s_max_u32 s62, s62, s39\n\t" \
    "s_sub_u32 s39, s59, s37\n\t" \
    "s_cmp_gt_u32 s39, 600\n\t" \
    "s_cselect_b32 s39, 400, s39\n\t" \
    "s_max_u32 s62, s62, s39\n\t" \
    "s_sub_u32 s39, s60, s37\n\t" \
    "s_cmp_gt_u32 s39, 600\n\t" \
    "s_cselect_b32 s39, 400, s39\n\t" \
    "s_max_u32 s62, s62, s39\n\t" \
    "s_sub_u32 s39, s61, s37\n\t" \
    "s_cmp_gt_u32 s39, 600\n\t" \
    "s_cselect_b32 s39, 400, s39\n\t" \
    "s_max_u32 s62, s62, s39\n\t" \
    "s_sub_u32 s62, s62, 300\n\t" \
    "s_mul_i32 s62, s62, 5\n\t" \
    "s_lshr_b32 s62, s62, 2\n\t" \
    "s_waitcnt vmcnt(2)\n\t" \
    "v_and_b32 v3, v128, v130\n\t" \
    "v_bitop3_b32 v3, v3, v160, v162 bitop3:0x80\n\t" \
    "v_and_b32 v3, 1, v3\n\t" \
    "v_cmp_eq_u32 vcc, 0, v3\n\t" \
    "s_nop 1\n\t" \
    "s_cbranch_vccnz .Ls39_%=\n\t" \
    ".Lb39_%=:\n\t" \
    "ds_write_b128 v48, v[128:131] offset:33024\n\t" \
    "ds_write_b128 v48, v[160:163] offset:33152\n\t" \
    "s_add_u32 s37, s62, 150\n\t" \
    ".Lh46_%=:\n\t" \
    "s_memrealtime s[22:23]\n\t" \
    "s_waitcnt lgkmcnt(0)\n\t" \
    "s_sub_u32 s22, s22, s18\n\t" \
    "s_cmp_lt_u32 s22, s37\n\t" \
    "s_cbranch_scc1 .Lh46_%=\n\t" \
    "s_add_u32 s54, s53, 524288\n\t" \
    "buffer_load_dwordx4 v[240:243], v0, s[4:7], s54 offen offset:0 sc1\n\t" \
    "s_add_u32 s54, s53, 4767744\n\t" \
    "buffer_load_dwordx4 v[244:247], v0, s[4:7], s54 offen offset:0 sc1\n\t" \
    "s_waitcnt lgkmcnt(0)\n\t" \
    "s_barrier\n\t" \
    "ds_read_b64_tr_b16 v[52:53], v43 offset:33024\n\t" \
    "ds_read_b64_tr_b16 v[54:55], v43 offset:33280\n\t" \
    "ds_read_b64_tr_b16 v[4:5], v43 offset:33032\n\t" \
    "ds_read_b64_tr_b16 v[6:7], v43 offset:33288\n\t" \
    "s_waitcnt lgkmcnt(2)\n\t" \
    "v_mfma_f32_16x16x32_bf16 v[88:91], v[52:55], v[120:123], 0\n\t" \
    "s_waitcnt lgkmcnt(0)\n\t" \
    "v_mfma_f32_16x16x32_bf16 v[200:203], v[4:7], v[120:123], 0\n\t" \
    "v_mfma_f32_16x16x32_bf16 v[204:207], v[52:55], v[212:215], 0\n\t" \
    "v_mfma_f32_16x16x32_bf16 v[32:35], v[4:7], v[212:215], 0\n\t" \
    "s_nop 3\n\t" \
    "v_cvt_pk_bf16_f32 v12, v88, v89\n\t" \
    "v_cvt_pk_bf16_f32 v13, v90, v91\n\t" \
    "v_cvt_pk_bf16_f32 v14, v200, v201\n\t" \
    "v_cvt_pk_bf16_f32 v15, v202, v203\n\t" \
    "buffer_store_dwordx4 v[12:15], v38, s[8:11], 0 offen offset:0 sc1\n\t" \
    "v_cvt_pk_bf16_f32 v72, v204, v205\n\t" \
    "v_cvt_pk_bf16_f32 v73, v206, v207\n\t" \
    "v_cvt_pk_bf16_f32 v74, v32, v33\n\t" \
    "v_cvt_pk_bf16_f32 v75, v34, v35\n\t" \
    "buffer_store_dwordx4 v[72:75], v38, s[8:11], 0 offen offset:3072 sc1\n\t" \
    "s_waitcnt vmcnt(2)\n\t" \
    "v_and_b32 v51, v240, v242\n\t" \
    "v_bitop3_b32 v51, v51, v244, v246 bitop3:0x80\n\t" \
    "v_and_b32 v51, 1, v51\n\t" \
    "v_cmp_eq_u32 vcc, 0, v51\n\t" \
    "s_nop 1\n\t" \
    "s_cbranch_vccnz .Ls40_%=\n\t" \
    ".Lb40_%=:\n\t" \
    "ds_write_b128 v48, v[240:243] offset:0\n\t" \
    "ds_write_b128 v48, v[244:247] offset:128\n\t" \
    "s_add_u32 s37, s62, 120\n\t" \
    ".Lh47_%=:\n\t" \
    "s_memrealtime s[22:23]\n\t" \
    "s_waitcnt lgkmcnt(0)\n\t" \
    "s_sub_u32 s22, s22, s56\n\t" \
    "s_cmp_lt_u32 s22, s37\n\t" \
    "s_cbranch_scc1 .Lh47_%=\n\t" \
    "s_add_u32 s54, s53, 1048576\n\t" \
    "buffer_load_dwordx4 v[140:143], v0, s[4:7], s54 offen offset:0 sc1\n\t" \
    "s_add_u32 s54, s53, 5292032\n\t" \
    "buffer_load_dwordx4 v[228:231], v0, s[4:7], s54 offen offset:0 sc1\n\t" \
    "s_waitcnt lgkmcnt(0)\n\t" \
    "s_barrier\n\t" \
    "ds_read_b64_tr_b16 v[220:221], v43 offset:0\n\t" \
    "ds_read_b64_tr_b16 v[222:223], v43 offset:256\n\t" \
    "ds_read_b64_tr_b16 v[192:193], v43 offset:8\n\t" \
    "ds_read_b64_tr_b16 v[194:195], v43 offset:264\n\t" \
    "s_waitcnt lgkmcnt(2)\n\t" \
    "v_mfma_f32_16x16x32_bf16 v[216:219], v[220:223], v[120:123], 0\n\t" \
    "s_waitcnt lgkmcnt(0)\n\t" \
    "v_mfma_f32_16x16x32_bf16 v[80:83], v[192:195], v[120:123], 0\n\t" \
    "v_mfma_f32_16x16x32_bf16 v[156:159], v[220:223], v[212:215], 0\n\t" \
    "v_mfma_f32_16x16x32_bf16 v[44:47], v[192:195], v[212:215], 0\n\t" \
    "s_nop 3\n\t" \
    "v_cvt_pk_bf16_f32 v92, v216, v217\n\t" \
    "v_cvt_pk_bf16_f32 v93, v218, v219\n\t" \
    "v_cvt_pk_bf16_f32 v94, v80, v81\n\t" \
    "v_cvt_pk_bf16_f32 v95, v82, v83\n\t" \
    "buffer_store_dwordx4 v[92:95], v38, s[8:11], 0 offen offset:64 sc1\n\t" \
    "v_cvt_pk_bf16_f32 v56, v156, v157\n\t" \
    "v_cvt_pk_bf16_f32 v57, v158, v159\n\t" \
    "v_cvt_pk_bf16_f32 v58, v44, v45\n\t" \
    "v_cvt_pk_bf16_f32 v59, v46, v47\n\t" \
    "buffer_store_dwordx4 v[56:59], v38, s[8:11], 0 offen offset:3136 sc1\n\t" \
    "s_waitcnt vmcnt(2)\n\t" \
    "v_and_b32 v39, v140, v142\n\t" \
    "v_bitop3_b32 v39, v39, v228, v230 bitop3:0x80\n\t" \
    "v_and_b32 v39, 1, v39\n\t" \
    "v_cmp_eq_u32 vcc, 0, v39\n\t" \
    "s_nop 1\n\t" \
    "s_cbranch_vccnz .Ls41_%=\n\t" \
    ".Lb41_%=:\n\t" \
    "ds_write_b128 v48, v[140:143] offset:16512\n\t" \
    "ds_write_b128 v48, v[228:231] offset:16640\n\t" \
    "s_waitcnt lgkmcnt(0)\n\t" \
    "s_barrier\n\t" \
    "ds_read_b64_tr_b16 v[108:109], v43 offset:16512\n\t" \
    "ds_read_b64_tr_b16 v[110:111], v43 offset:16768\n\t" \
    "ds_read_b64_tr_b16 v[176:177], v43 offset:16520\n\t" \
    "ds_read_b64_tr_b16 v[178:179], v43 offset:16776\n\t" \
    "s_waitcnt lgkmcnt(2)\n\t" \
    "v_mfma_f32_16x16x32_bf16 v[132:135], v[108:111], v[120:123], 0\n\t" \
    "s_waitcnt lgkmcnt(0)\n\t" \
    "v_mfma_f32_16x16x32_bf16 v[112:115], v[176:179], v[120:123], 0\n\t" \
    "v_mfma_f32_16x16x32_bf16 v[188:191], v[108:111], v[212:215], 0\n\t" \
    "v_mfma_f32_16x16x32_bf16 v[232:235], v[176:179], v[212:215], 0\n\t" \
    "s_nop 3\n\t" \
    "v_cvt_pk_bf16_f32 v96, v132, v133\n\t" \
    "v_cvt_pk_bf16_f32 v97, v134, v135\n\t" \
    "v_cvt_pk_bf16_f32 v98, v112, v113\n\t" \
    "v_cvt_pk_bf16_f32 v99, v114, v115\n\t" \
    "buffer_store_dwordx4 v[96:99], v38, s[8:11], 0 offen offset:128 sc1\n\t" \
    "v_cvt_pk_bf16_f32 v164, v188, v189\n\t" \
    "v_cvt_pk_bf16_f32 v165, v190, v191\n\t" \
    "v_cvt_pk_bf16_f32 v166, v232, v233\n\t" \
    "v_cvt_pk_bf16_f32 v167, v234, v235\n\t" \
    "buffer_store_dwordx4 v[164:167], v38, s[8:11], 0 offen offset:3200 sc1\n\t" \
    "s_or_b32 s37, s2, s24\n\t" \
    "s_cbranch_scc1 .Lend_%=\n\t" \
    "s_mov_b32 s37, 34635776\n\t" \
    "v_mov_b32 v1, s34\n\t" \
    "s_mov_b64 exec, 1\n\t" \
    "buffer_store_dword v1, v0, s[4:7], s37 offen sc1\n\t" \
    "s_mov_b64 exec, -1\n\t" \
    ".Lend_%=:\n\t" \
    "s_endpgm\n\t" \
    ".Lq1026_%=:\n\t" \
    "s_mov_b32 s40, 0\n\t" \
    ".Lr1026_%=:\n\t" \
    "s_add_u32 s12, s55, 34603008\n\t" \
    "s_buffer_load_dword s13, s[4:7], s12 glc\n\t" \
    "s_buffer_load_dword s14, s[4:7], s12 offset:0x400 glc\n\t" \
    "s_waitcnt lgkmcnt(0)\n\t" \
    "s_xor_b32 s13, s13, s34\n\t" \
    "s_xor_b32 s14, s14, s34\n\t" \
    "s_or_b32 s13, s13, s14\n\t" \
    "s_cbranch_scc0 .Lb25_%=\n\t" \
    "s_add_u32 s40, s40, 1\n\t" \
    "s_cmp_lt_u32 s40, 1\n\t" \
    "s_cbranch_scc1 .Lr1026_%=\n\t" \
    "s_branch .Ls25_%=\n\t" \
    ".Lq1023_%=:\n\t" \
    "s_mov_b32 s40, 0\n\t" \
    ".Lr1023_%=:\n\t" \
    "s_nop 0\n\t" \
    "v_readlane_b32 s12, v3, 0\n\t" \
    "v_readlane_b32 s13, v3, 1\n\t" \
    "v_readlane_b32 s14, v3, 2\n\t" \
    "v_readlane_b32 s15, v3, 3\n\t" \
    "v_readlane_b32 s16, v3, 4\n\t" \
    "v_readlane_b32 s17, v3, 5\n\t" \
    "v_readlane_b32 s18, v3, 6\n\t" \
    "v_readlane_b32 s19, v3, 7\n\t" \
    "v_readlane_b32 s3, v3, 8\n\t" \
    "s_add_u32 s12, s12, s33\n\t" \
    "s_add_u32 s13, s13, s33\n\t" \
    "s_add_u32 s14, s14, s33\n\t" \
    "s_add_u32 s15, s15, s33\n\t" \
    "s_add_u32 s16, s16, s33\n\t" \
    "s_add_u32 s17, s17, s33\n\t" \
    "s_add_u32 s18, s18, s33\n\t" \
    "s_add_u32 s19, s19, s33\n\t" \
    "s_add_u32 s3, s3, s33\n\t" \
    "s_buffer_load_dword s12, s[4:7], s12 offset:0xc0000 glc\n\t" \
    "s_buffer_load_dword s13, s[4:7], s13 offset:0xc0000 glc\n\t" \
    "s_buffer_load_dword s14, s[4:7], s14 offset:0xc0000 glc\n\t" \
    "s_buffer_load_dword s15, s[4:7], s15 offset:0xc0000 glc\n\t" \
    "s_buffer_load_dword s16, s[4:7], s16 offset:0xc0000 glc\n\t" \
    "s_buffer_load_dword s17, s[4:7], s17 offset:0xc0000 glc\n\t" \
    "s_buffer_load_dword s18, s[4:7], s18 offset:0xc0000 glc\n\t" \
    "s_buffer_load_dword s19, s[4:7], s19 offset:0xc0000 glc\n\t" \
    "s_buffer_load_dword s3, s[4:7], s3 offset:0xc0000 glc\n\t" \
    "s_waitcnt lgkmcnt(0)\n\t" \
    "s_xor_b32 s12, s12, s34\n\t" \
    "s_xor_b32 s13, s13, s34\n\t" \
    "s_xor_b32 s14, s14, s34\n\t" \
    "s_xor_b32 s15, s15, s34\n\t" \
    "s_xor_b32 s16, s16, s34\n\t" \
    "s_xor_b32 s17, s17, s34\n\t" \
    "s_xor_b32 s18, s18, s34\n\t" \
    "s_xor_b32 s19, s19, s34\n\t" \
    "s_xor_b32 s3, s3, s34\n\t" \
    "s_or_b32 s12, s12, s13\n\t" \
    "s_or_b32 s12, s12, s14\n\t" \
    "s_or_b32 s12, s12, s15\n\t" \
    "s_or_b32 s12, s12, s16\n\t" \
    "s_or_b32 s12, s12, s17\n\t" \
    "s_or_b32 s12, s12, s18\n\t" \
    "s_or_b32 s12, s12, s19\n\t" \
    "s_or_b32 s12, s12, s3\n\t" \
    "s_cbranch_scc0 .Lb22_%=\n\t" \
    "s_add_u32 s40, s40, 1\n\t" \
    "s_cmp_lt_u32 s40, 1\n\t" \
    "s_cbranch_scc1 .Lr1023_%=\n\t" \
    "s_branch .Ls22_%=\n\t" \
    ".Lqa_%=:\n\t" \
    "s_sub_u32 s37, s24, 4\n\t" \
    "s_bfe_u32 s38, s2, 0x20002\n\t" \
    "s_lshl1_add_u32 s38, s38, s37\n\t" \
    "s_lshl_b32 s42, s38, 21\n\t" \
    "s_cmp_eq_u32 s2, 255\n\t" \
    "s_cbranch_scc1 .Lqd_%=\n\t" \
    "s_cmp_ge_u32 s37, 2\n\t" \
    "s_cbranch_scc1 .Lqd_%=\n\t" \
    "s_waitcnt lgkmcnt(0)\n\t" \
    "s_sleep 4\n\t" \
    "s_lshl_b32 s3, s38, 10\n\t" \
    "s_and_b32 s37, s8, 0x1fffff\n\t" \
    "s_add_u32 s38, s37, 16383\n\t" \
    "s_andn2_b32 s38, s38, 16383\n\t" \
    "s_add_u32 s39, s37, 9617408\n\t" \
    "s_andn2_b32 s39, s39, 16383\n\t" \
    "s_max_u32 s42, s42, s38\n\t" \
    "s_min_u32 s42, s42, s39\n\t" \
    "s_sub_u32 s42, s42, s37\n\t" \
    "s_add_u32 s3, s3, s42\n\t" \
    "s_mov_b64 s[16:17], s[8:9]\n\t" \
    "s_mov_b32 s18, 9633792\n\t" \
    "s_mov_b32 s19, 0x20000\n\t" \
    "s_add_u32 s37, s3, 0x2000\n\t" \
    "buffer_store_dwordx4 v[20:23], v0, s[16:19], s3 offen sc1\n\t" \
    "buffer_store_dwordx4 v[20:23], v0, s[16:19], s37 offen sc1\n\t" \
    "s_buffer_load_dword s0, s[16:19], s3 offset:0x380\n\t" \
    "s_branch .Lqd_%=\n\t" \
    ".Lpb_%=:\n\t" \
    "s_sub_u32 s37, s24, 4\n\t" \
    "s_cbranch_scc1 .Lpe_%=\n\t" \
    "s_cmp_lg_u32 s2, 255\n\t" \
    "s_cbranch_scc1 .Lpe_%=\n\t" \
    "s_mul_i32 s38, s37, 0x2100\n\t" \
    "s_and_b32 s38, s38, 0x2300\n\t" \
    "s_add_u32 s38, s38, 34652160\n\t" \
    "s_mov_b32 s60, -1\n\t" \
    "s_mov_b32 s61, -1\n\t" \
    "s_memrealtime s[62:63]\n\t" \
    "s_mov_b64 exec, 0xff\n\t" \
    ".Lpc1_%=:\n\t" \
    "s_memtime s[56:57]\n\t" \
    "s_waitcnt lgkmcnt(0)\n\t" \
    "buffer_store_dwordx4 v[20:23], v0, s[4:7], s38 offen sc1\n\t" \
    "s_waitcnt vmcnt(0)\n\t" \
    "s_memtime s[58:59]\n\t" \
    "s_memrealtime s[22:23]\n\t" \
    "s_waitcnt lgkmcnt(0)\n\t" \
    "s_sub_u32 s56, s58, s56\n\t" \
    "s_min_u32 s56, s56, s60\n\t" \
    "s_mov_b32 s60, s61\n\t" \
    "s_mov_b32 s61, s56\n\t" \
    "s_xor_b32 s38, s38, 0x2000\n\t" \
    "s_add_u32 s38, s38, 0x400\n\t" \
    "s_andn2_b32 s38, s38, 0x1000\n\t" \
    "s_sub_u32 s22, s22, s62\n\t" \
    "s_cmp_lt_u32 s22, 60\n\t" \
    "s_cbranch_scc1 .Lpc1_%=\n\t" \
    "s_mov_b64 exec, -1\n\t" \
    "s_bitcmp1_b32 s38, 13\n\t" \
    "s_cselect_b32 s56, s61, s60\n\t" \
    "s_cselect_b32 s57, s60, s61\n\t" \
    "v_mov_b32 v20, s56\n\t" \
    "v_mov_b32 v21, s57\n\t" \
    "s_lshl3_add_u32 s37, s37, 156992\n\t" \
    "v_mov_b32 v22, s37\n\t" \
    "ds_write_b64 v22, v[20:21]\n\t" \
    "s_branch .Lpe_%=\n\t" \
    ".Lqb_%=:\n\t" \
    "s_cmp_eq_u32 s2, 255\n\t" \
    "s_cbranch_scc1 .Lqe_%=\n\t" \
    "s_sub_u32 s37, s24, 4\n\t" \
    "s_cmp_ge_u32 s37, 2\n\t" \
    "s_cbranch_scc1 .Lqe_%=\n\t" \
    "s_mov_b32 s60, -1\n\t" \
    "s_mov_b32 s61, -1\n\t" \
    "s_mov_b32 s40, 0\n\t" \
    "s_waitcnt vmcnt(0)\n\t" \
    ".Lqc_%=:\n\t" \
    "s_and_b32 s37, s40, 1\n\t" \
    "s_mul_i32 s37, s37, 0x1fc0\n\t" \
    "s_lshl_b32 s38, s40, 6\n\t" \
    "s_add_u32 s37, s37, s38\n\t" \
    "s_add_u32 s37, s37, s3\n\t" \
    "s_memtime s[56:57]\n\t" \
    "s_waitcnt lgkmcnt(0)\n\t" \
    "s_buffer_load_dword s38, s[16:19], s37\n\t" \
    "s_waitcnt lgkmcnt(0)\n\t" \
    "s_memtime s[58:59]\n\t" \
    "s_waitcnt lgkmcnt(0)\n\t" \
    "s_sub_u32 s56, s58, s56\n\t" \
    "s_min_u32 s56, s56, s60\n\t" \
    "s_mov_b32 s60, s61\n\t" \
    "s_mov_b32 s61, s56\n\t" \
    "s_add_u32 s40, s40, 1\n\t" \
    "s_cmp_lt_u32 s40, 6\n\t" \
    "s_cbranch_scc1 .Lqc_%=\n\t" \
    "s_cmp_gt_u32 s60, s61\n\t" \
    "s_cselect_b32 s0, 2, 0\n\t" \
    "s_branch .Lqe_%=\n\t" \
    ".Lr2_%=:\n\t" \
    "s_add_u32 s40, s40, 1\n\t" \
    "s_cmp_gt_u32 s40, 32\n\t" \
    "s_cbranch_scc1 .Lr3_%=\n\t" \
    "s_sleep 2\n\t" \
    "s_and_b32 s37, s2, 7\n\t" \
    "s_lshl_b32 s37, s37, 10\n\t" \
    "s_add_u32 s37, s37, 34643968\n\t" \
    "v_and_b32 v51, 0x70, v0\n\t" \
    "v_lshlrev_b32 v51, 3, v51\n\t" \
    "buffer_load_dword v50, v51, s[4:7], s37 offen sc1\n\t" \
    "s_waitcnt vmcnt(0)\n\t" \
    "v_readlane_b32 s56, v50, 0\n\t" \
    "v_readlane_b32 s57, v50, 1\n\t" \
    "v_readlane_b32 s58, v50, 2\n\t" \
    "v_readlane_b32 s59, v50, 3\n\t" \
    "v_readlane_b32 s60, v50, 4\n\t" \
    "v_readlane_b32 s61, v50, 5\n\t" \
    "v_readlane_b32 s62, v50, 6\n\t" \
    "v_readlane_b32 s63, v50, 7\n\t" \
    "s_branch .Lr1_%=\n\t" \
    ".Lpp_%=:\n\t" \
    "ds_read_b128 v[116:119], v9 offset:64\n\t" \
    "ds_read_b128 v[120:123], v9 offset:80\n\t" \
    "s_getreg_b32 s37, hwreg(HW_REG_XCC_ID, 0, 2)\n\t" \
    "s_waitcnt lgkmcnt(0)\n\t" \
    "v_min3_u32 v116, v116, v118, v120\n\t" \
    "v_min3_u32 v117, v117, v119, v121\n\t" \
    "v_min_u32 v116, v116, v122\n\t" \
    "v_min_u32 v117, v117, v123\n\t" \
    "v_cmp_gt_u32 vcc, v116, v117\n\t" \
    "s_nop 4\n\t" \
    "s_and_b32 s38, vcc_lo, 4\n\t" \
    "s_add_u32 s37, s37, s38\n\t" \
    "s_add_u32 s37, s37, 1\n\t" \
    "s_and_b32 s37, s37, 7\n\t" \
    "s_lshl_b32 s37, s37, 1\n\t" \
    "s_xor_b32 s37, s37, s34\n\t" \
    "v_mov_b32 v116, s37\n\t" \
    "v_mov_b32 v117, s37\n\t" \
    "v_mov_b32 v118, s37\n\t" \
    "v_mov_b32 v119, s37\n\t" \
    "s_mov_b32 s39, 34636800\n\t" \
    "buffer_store_dwordx4 v[116:119], v0, s[4:7], s39 offen sc1\n\t" \
    "s_nop 1\n\t" \
    "s_branch .Lpq_%=\n\t" \
    ".Lqp_%=:\n\t" \
    "s_lshr_b32 s37, s56, 2\n\t" \
    "s_and_b32 s37, s37, 2\n\t" \
    "s_and_b32 s0, s0, 2\n\t" \
    "s_xor_b32 s37, s37, s0\n\t" \
    "s_xor_b32 s37, s37, s34\n\t" \
    "v_mov_b32 v50, s37\n\t" \
    "v_mov_b32 v8, 0\n\t" \
    "s_bfe_u32 s37, s2, 0x20002\n\t" \
    "s_lshl1_add_u32 s37, s37, s24\n\t" \
    "s_lshl_b32 s37, s37, 7\n\t" \
    "s_add_u32 s37, s37, 34637312\n\t" \
    "v_lshrrev_b32 v8, 2, v0\n\t" \
    "s_mov_b32 exec_hi, 0\n\t" \
    "buffer_store_dword v50, v8, s[4:7], s37 offen sc1\n\t" \
    "s_mov_b64 exec, -1\n\t" \
    "s_branch .Lqq_%=\n\t" \
    ".Lq5_%=:\n\t" \
    "s_add_u32 s40, s40, 1\n\t" \
    "s_cmp_gt_u32 s40, 8192\n\t" \
    "s_cbranch_scc1 .Lq6_%=\n\t" \
    "s_sleep 4\n\t" \
    "v_and_b32 v16, 0x70, v0\n\t" \
    "v_lshlrev_b32 v16, 3, v16\n\t" \
    "s_mov_b32 s37, 34637824\n\t" \
    "buffer_load_dword v9, v16, s[4:7], s37 offen sc1\n\t" \
    "s_waitcnt vmcnt(0)\n\t" \
    "v_readlane_b32 s56, v9, 0\n\t" \
    "v_readlane_b32 s57, v9, 1\n\t" \
    "v_readlane_b32 s58, v9, 2\n\t" \
    "v_readlane_b32 s59, v9, 3\n\t" \
    "v_readlane_b32 s60, v9, 4\n\t" \
    "v_readlane_b32 s61, v9, 5\n\t" \
    "s_branch .Lq4_%=\n\t" \
    ".Lr5_%=:\n\t" \
    "s_add_u32 s40, s40, 1\n\t" \
    "s_cmp_gt_u32 s40, 8192\n\t" \
    "s_cbranch_scc1 .Lr6_%=\n\t" \
    "s_sleep 4\n\t" \
    "v_mov_b32 v16, 0\n\t" \
    "buffer_load_dword v8, v16, s[4:7], s52 offen sc1\n\t" \
    "s_waitcnt vmcnt(0)\n\t" \
    "v_readfirstlane_b32 s36, v8\n\t" \
    "s_branch .Lr4_%=\n\t" \
    ".Ls22_%=:\n\t" \
    "s_mov_b32 s40, 0\n\t" \
    "s_add_u32 s38, s33, 786432\n\t" \
    ".Ll22_%=:\n\t" \
    "s_sleep 2\n\t" \
    "buffer_load_dword v51, v3, s[4:7], s38 offen sc1\n\t" \
    "s_waitcnt vmcnt(0)\n\t" \
    "v_cmp_ne_u32 vcc, s34, v51\n\t" \
    "s_add_u32 s40, s40, 1\n\t" \
    "s_cmp_ge_u32 s40, 8192\n\t" \
    "s_cbranch_scc1 .Lb22_%=\n\t" \
    "s_cbranch_vccnz .Ll22_%=\n\t" \
    "s_branch .Lb22_%=\n\t" \
    ".Ls25_%=:\n\t" \
    "s_mov_b32 s40, 0\n\t" \
    "s_add_u32 s38, s55, 34603008\n\t" \
    ".Ll25_%=:\n\t" \
    "s_sleep 2\n\t" \
    "buffer_load_dword v37, v47, s[4:7], s38 offen sc1\n\t" \
    "s_waitcnt vmcnt(0)\n\t" \
    "v_cmp_ne_u32 vcc, s34, v37\n\t" \
    "s_add_u32 s40, s40, 1\n\t" \
    "s_cmp_ge_u32 s40, 8192\n\t" \
    "s_cbranch_scc1 .Lb25_%=\n\t" \
    "s_cbranch_vccnz .Ll25_%=\n\t" \
    "s_branch .Lb25_%=\n\t" \
    ".Ls39_%=:\n\t" \
    "s_mov_b32 s40, 0\n\t" \
    ".Ll39_%=:\n\t" \
    "s_add_u32 s54, s53, 0\n\t" \
    "buffer_load_dwordx4 v[128:131], v0, s[4:7], s54 offen offset:0 sc1\n\t" \
    "s_add_u32 s54, s53, 4243456\n\t" \
    "buffer_load_dwordx4 v[160:163], v0, s[4:7], s54 offen offset:0 sc1\n\t" \
    "s_waitcnt vmcnt(0)\n\t" \
    "v_and_b32 v3, v128, v130\n\t" \
    "v_bitop3_b32 v3, v3, v160, v162 bitop3:0x80\n\t" \
    "v_and_b32 v3, 1, v3\n\t" \
    "v_cmp_eq_u32 vcc, 0, v3\n\t" \
    "s_add_u32 s40, s40, 1\n\t" \
    "s_cmp_ge_u32 s40, 8192\n\t" \
    "s_cbranch_scc1 .Lb39_%=\n\t" \
    "s_cbranch_vccnz .Ll39_%=\n\t" \
    "s_branch .Lb39_%=\n\t" \
    ".Ls40_%=:\n\t" \
    "s_mov_b32 s40, 0\n\t" \
    ".Ll40_%=:\n\t" \
    "s_add_u32 s54, s53, 524288\n\t" \
    "buffer_load_dwordx4 v[240:243], v0, s[4:7], s54 offen offset:0 sc1\n\t" \
    "s_add_u32 s54, s53, 4767744\n\t" \
    "buffer_load_dwordx4 v[244:247], v0, s[4:7], s54 offen offset:0 sc1\n\t" \
    "s_waitcnt vmcnt(0)\n\t" \
    "v_and_b32 v51, v240, v242\n\t" \
    "v_bitop3_b32 v51, v51, v244, v246 bitop3:0x80\n\t" \
    "v_and_b32 v51, 1, v51\n\t" \
    "v_cmp_eq_u32 vcc, 0, v51\n\t" \
    "s_add_u32 s40, s40, 1\n\t" \
    "s_cmp_ge_u32 s40, 8192\n\t" \
    "s_cbranch_scc1 .Lb40_%=\n\t" \
    "s_cbranch_vccnz .Ll40_%=\n\t" \
    "s_branch .Lb40_%=\n\t" \
    ".Ls41_%=:\n\t" \
    "s_mov_b32 s40, 0\n\t" \
    ".Ll41_%=:\n\t" \
    "s_add_u32 s54, s53, 1048576\n\t" \
    "buffer_load_dwordx4 v[140:143], v0, s[4:7], s54 offen offset:0 sc1\n\t" \
    "s_add_u32 s54, s53, 5292032\n\t" \
    "buffer_load_dwordx4 v[228:231], v0, s[4:7], s54 offen offset:0 sc1\n\t" \
    "s_waitcnt vmcnt(0)\n\t" \
    "v_and_b32 v39, v140, v142\n\t" \
    "v_bitop3_b32 v39, v39, v228, v230 bitop3:0x80\n\t" \
    "v_and_b32 v39, 1, v39\n\t" \
    "v_cmp_eq_u32 vcc, 0, v39\n\t" \
    "s_add_u32 s40, s40, 1\n\t" \
    "s_cmp_ge_u32 s40, 8192\n\t" \
    "s_cbranch_scc1 .Lb41_%=\n\t" \
    "s_cbranch_vccnz .Ll41_%=\n\t" \
    "s_branch .Lb41_%=\n\t" \
    ".Lfin_%=:\n\t"
#define K_CLOBBERS "v1", "v2", "v3", "v4", "v5", "v6", "v7", "v8", "v9", "v10", "v11", "v12", "v13", "v14", "v15", "v16", "v17", "v18", "v19", "v20", "v21", "v22", "v23", "v24", "v25", "v26", "v27", "v28", "v29", "v30", "v31", "v32", "v33", "v34", "v35", "v36", "v37", "v38", "v39", "v40", "v41", "v42", "v43", "v44", "v45", "v46", "v47", "v48", "v49", "v50", "v51", "v52", "v53", "v54", "v55", "v56", "v57", "v58", "v59", "v60", "v61", "v62", "v63", "v64", "v65", "v66", "v67", "v68", "v69", "v70", "v71", "v72", "v73", "v74", "v75", "v76", "v77", "v78", "v79", "v80", "v81", "v82", "v83", "v84", "v85", "v86", "v87", "v88", "v89", "v90", "v91", "v92", "v93", "v94", "v95", "v96", "v97", "v98", "v99", "v100", "v101", "v102", "v103", "v104", "v105", "v106", "v107", "v108", "v109", "v110", "v111", "v112", "v113", "v114", "v115", "v116", "v117", "v118", "v119", "v120", "v121", "v122", "v123", "v124", "v125", "v126", "v127", "v128", "v129", "v130", "v131", "v132", "v133", "v134", "v135", "v136", "v137", "v138", "v139", "v140", "v141", "v142", "v143", "v144", "v145", "v146", "v147", "v148", "v149", "v150", "v151", "v152", "v153", "v154", "v155", "v156", "v157", "v158", "v159", "v160", "v161", "v162", "v163", "v164", "v165", "v166", "v167", "v168", "v169", "v170", "v171", "v172", "v173", "v174", "v175", "v176", "v177", "v178", "v179", "v180", "v181", "v182", "v183", "v184", "v185", "v186", "v187", "v188", "v189", "v190", "v191", "v192", "v193", "v194", "v195", "v196", "v197", "v198", "v199", "v200", "v201", "v202", "v203", "v204", "v205", "v206", "v207", "v208", "v209", "v210", "v211", "v212", "v213", "v214", "v215", "v216", "v217", "v218", "v219", "v220", "v221", "v222", "v223", "v224", "v225", "v226", "v227", "v228", "v229", "v230", "v231", "v232", "v233", "v234", "v235", "v236", "v237", "v238", "v239", "v240", "v241", "v242", "v243", "v244", "v245", "v246", "v247", "v248", "v249", "v250", "v251", "v252", "v253", "v254", "v255", "s3", "s4", "s5", "s6", "s7", "s8", "s9", "s10", "s11", "s12", "s13", "s14", "s15", "s16", "s17", "s18", "s19", "s20", "s21", "s22", "s23", "s24", "s25", "s26", "s27", "s28", "s29", "s30", "s31", "s32", "s33", "s34", "s35", "s36", "s37", "s38", "s39", "s40", "s41", "s42", "s43", "s44", "s45", "s46", "s47", "s48", "s49", "s50", "s51", "s52", "s53", "s54", "s55", "s56", "s57", "s58", "s59", "s60", "s61", "s62", "s63", "s64", "s65"

extern "C" __global__ __launch_bounds__(512, 2)
void k_conv(const char* __restrict__ x, const char* __restrict__ wgt, char* __restrict__ ws, char* __restrict__ out)
{
  __shared__ __attribute__((aligned(256))) char smem[157568];
  unsigned tid = threadIdx.x;
  auto kp = __builtin_amdgcn_kernarg_segment_ptr();
  asm volatile(K_BODY : "+{v0}"(tid), "+{s[0:1]}"(kp)
               : "{s2}"(blockIdx.x), "i"((__attribute__((address_space(3))) char*)smem)
               : "memory", "scc", "vcc", K_CLOBBERS);
}

extern "C" void kernel_launch(void* const* d_in, const int* in_sizes, int n_in,
                              void* d_out, int out_size, void* d_ws, size_t ws_size,
                              hipStream_t stream) {
  k_conv<<<dim3(256), dim3(512), 0, stream>>>((const char*)d_in[0], (const char*)d_in[1], (char*)d_ws + WS_OFF, (char*)d_out);
}
```

```cpp
#include <hip/hip_runtime.h>
#include <hip/hip_bf16.h>

#define WS_OFF 0
#define K_BODY \
    "s_load_dwordx2 s[16:17], s[0:1], 0x0\n\t" \
    "s_load_dwordx2 s[4:5], s[0:1], 0x10\n\t" \
    "s_load_dwordx2 s[12:13], s[0:1], 0x8\n\t" \
    "s_load_dwordx2 s[8:9], s[0:1], 0x18\n\t" \
    "s_getpc_b64 s[22:23]\n\t" \
    ".Lpc_%=:\n\t" \
    "s_cmp_ge_u32 s2, 16\n\t" \
    "s_cbranch_scc1 .Le1_%=\n\t" \
    "s_mov_b32 m0, 157312\n\t" \
    "v_lshlrev_b32 v8, 6, v0\n\t" \
    "v_min_u32 v8, .Lfin_%=-.Lpc_%=, v8\n\t" \
    "global_load_lds_dword v8, s[22:23]\n\t" \
    ".Le1_%=:\n\t" \
    "v_readfirstlane_b32 s24, v0\n\t" \
    "s_and_b32 s25, s2, 3\n\t" \
    "s_lshl_b32 s25, s25, 5\n\t" \
    "s_lshr_b32 s37, s2, 3\n\t" \
    "s_add_u32 s25, s25, s37\n\t" \
    "s_min_u32 s28, s25, 125\n\t" \
    "s_mul_i32 s26, s28, 57\n\t" \
    "s_lshr_b32 s26, s26, 9\n\t" \
    "s_mul_i32 s37, s26, 9\n\t" \
    "s_sub_u32 s27, s28, s37\n\t" \
    "s_bfe_u32 s37, s2, 0x10002\n\t" \
    "s_lshl1_add_u32 s26, s26, s37\n\t" \
    "v_and_b32 v0, 63, v0\n\t" \
    "v_lshlrev_b32 v0, 4, v0\n\t" \
    "s_lshr_b32 s24, s24, 6\n\t" \
    "s_sub_u32 s37, s24, 4\n\t" \
    "s_cbranch_scc1 .Le2_%=\n\t" \
    "s_cmp_ge_u32 s25, 126\n\t" \
    "s_cbranch_scc1 .Le2_%=\n\t" \
    "s_mul_i32 m0, s37, 12416\n\t" \
    "s_lshl2_add_u32 s37, s26, s37\n\t" \
    "s_mul_i32 s37, s37, 86016\n\t" \
    "s_mul_i32 s38, s27, 4992\n\t" \
    "s_add_u32 s37, s37, s38\n\t" \
    "s_addk_i32 s37, 896\n\t" \
    "v_add_u32 v9, s37, v0\n\t" \
    "s_cmp_eq_u32 s27, 0\n\t" \
    "s_cselect_b32 s38, 43008, 0\n\t" \
    "s_sub_u32 s39, s38, 2048\n\t" \
    "v_add_u32 v7, s39, v9\n\t" \
    "v_subrev_u32 v8, 0x400, v9\n\t" \
    "s_mov_b64 exec, 0xff\n\t" \
    "v_add_u32 v8, s38, v8\n\t" \
    "s_mov_b64 exec, -1\n\t" \
    "s_mov_b32 s18, 9633792\n\t" \
    "s_mov_b32 s19, 0x20000\n\t" \
    "s_mov_b32 s39, 43008\n\t" \
    "s_waitcnt lgkmcnt(0)\n\t" \
    "buffer_load_dwordx4 v7, s[16:19], 0 offen offset:0 sc1 nt lds\n\t" \
    "s_add_u32 m0, m0, 2080\n\t" \
    "s_nop 0\n\t" \
    "buffer_load_dwordx4 v9, s[16:19], 0 offen offset:3072 sc1 nt lds\n\t" \
    "buffer_load_dwordx4 v9, s[16:19], 0 offen offset:2048 sc1 nt lds\n\t" \
    "s_add_u32 m0, m0, 4128\n\t" \
    "s_nop 0\n\t" \
    "buffer_load_dwordx4 v7, s[16:19], s39 offen offset:0 sc1 nt lds\n\t" \
    "s_add_u32 m0, m0, 2080\n\t" \
    "s_nop 0\n\t" \
    "buffer_load_dwordx4 v9, s[16:19], s39 offen offset:3072 sc1 nt lds\n\t" \
    "buffer_load_dwordx4 v9, s[16:19], s39 offen offset:2048 sc1 nt lds\n\t" \
    "buffer_load_dwordx4 v9, s[16:19], s39 offen offset:1024 sc1 nt lds\n\t" \
    "s_add_u32 m0, m0, 4294966240\n\t" \
    "s_nop 0\n\t" \
    "buffer_load_dwordx4 v8, s[16:19], s39 offen offset:0 sc1 nt lds\n\t" \
    "s_add_u32 m0, m0, 1024\n\t" \
    "s_nop 0\n\t" \
    "buffer_load_dwordx4 v9, s[16:19], s39 offen offset:0 sc1 nt lds\n\t" \
    "s_add_u32 m0, m0, 4294960064\n\t" \
    "s_nop 0\n\t" \
    "buffer_load_dwordx4 v8, s[16:19], 0 offen offset:0 sc1 nt lds\n\t" \
    "s_add_u32 m0, m0, 1024\n\t" \
    "s_nop 0\n\t" \
    "buffer_load_dwordx4 v9, s[16:19], 0 offen offset:0 sc1 nt lds\n\t" \
    "s_add_u32 m0, m0, 32\n\t" \
    "s_nop 0\n\t" \
    "buffer_load_dwordx4 v9, s[16:19], 0 offen offset:1024 sc1 nt lds\n\t" \
    ".Le2_%=:\n\t" \
    "s_mov_b32 s6, 34668544\n\t" \
    "s_mov_b32 s7, 0x20000\n\t" \
    "v_lshrrev_b32 v8, 7, v0\n\t" \
    "v_and_b32 v9, 0x70, v0\n\t" \
    "v_lshl_or_b32 v2, v8, 15, v9\n\t" \
    "s_lshl_b32 s32, s2, 7\n\t" \
    "s_add_u32 s32, s32, 33554432\n\t" \
    "s_cmp_ge_u32 s24, 4\n\t" \
    "s_cbranch_scc1 .Le3_%=\n\t" \
    "v_mov_b32 v12, 0\n\t" \
    "v_mov_b32 v13, 0\n\t" \
    "v_mov_b32 v14, 0\n\t" \
    "v_mov_b32 v15, 0\n\t" \
    "s_lshl_b32 s37, s24, 18\n\t" \
    "s_add_u32 s37, s37, s32\n\t" \
    "s_xor_b32 s38, s24, 2\n\t" \
    "s_lshr_b32 s39, s2, 3\n\t" \
    "s_or_b32 s38, s38, s39\n\t" \
    "s_memrealtime s[22:23]\n\t" \
    "s_waitcnt lgkmcnt(0)\n\t" \
    "s_cbranch_scc1 .Le4_%=\n\t" \
    "v_lshl_or_b32 v8, v8, 10, v9\n\t" \
    "v_mov_b32 v16, s22\n\t" \
    "v_mov_b32 v17, s22\n\t" \
    "v_mov_b32 v18, s22\n\t" \
    "v_mov_b32 v19, s22\n\t" \
    "s_add_u32 s38, s32, 1089536\n\t" \
    "buffer_store_dwordx4 v[16:19], v8, s[4:7], s38 offen sc1\n\t" \
    ".Le4_%=:\n\t" \
    "\n\t" \
    ".Le3_%=:\n\t" \
    "s_bitcmp1_b32 s2, 2\n\t" \
    "s_cselect_b32 s38, 126, 0\n\t" \
    "s_add_u32 s28, s28, s38\n\t" \
    "s_mul_i32 s30, s28, 293\n\t" \
    "s_lshr_b32 s30, s30, 11\n\t" \
    "s_mul_i32 s37, s30, 7\n\t" \
    "s_sub_u32 s28, s28, s37\n\t" \
    "s_mul_i32 s29, s30, 43\n\t" \
    "s_lshr_b32 s29, s29, 7\n\t" \
    "s_mul_i32 s37, s29, 3\n\t" \
    "s_sub_u32 s30, s30, s37\n\t" \
    "s_and_b32 s37, s29, 12\n\t" \
    "s_add_u32 s37, s37, s29\n\t" \
    "s_mul_i32 s31, s37, 768\n\t" \
    "s_lshl_b32 s37, s30, 6\n\t" \
    "s_add_u32 s31, s31, s37\n\t" \
    "s_add_u32 s41, s29, s30\n\t" \
    "s_add_u32 s41, s41, s28\n\t" \
    "s_and_b32 s41, s41, 3\n\t" \
    "s_cmp_lt_u32 s25, 126\n\t" \
    "s_cselect_b32 s14, 903168, 0\n\t" \
    "s_mov_b32 s15, 0x20000\n\t" \
    "v_lshrrev_b32 v10, 6, v0\n\t" \
    "v_lshrrev_b32 v11, 9, v0\n\t" \
    "v_sub_u32 v8, v10, v11\n\t" \
    "v_mul_u32_u24 v8, 18432, v8\n\t" \
    "v_and_b32 v9, 0x30, v0\n\t" \
    "v_lshlrev_b32 v11, 5, v11\n\t" \
    "v_bitop3_b32 v1, v8, v9, v11 bitop3:0xf6\n\t" \
    "s_mov_b32 exec_lo, 0xf0000000\n\t" \
    "s_mov_b32 exec_hi, 0xf0000000\n\t" \
    "v_bfrev_b32 v1, 1\n\t" \
    "s_mov_b64 exec, -1\n\t" \
    "s_cmp_ge_u32 s24, 4\n\t" \
    "s_cbranch_scc1 .Le9_%=\n\t" \
    "v_bfe_u32 v20, v0, 4, 4\n\t" \
    "v_and_b32 v10, 12, v10\n\t" \
    "v_cvt_f32_ubyte0 v21, v10\n\t" \
    "v_and_b32 v22, 1, v20\n\t" \
    "v_cvt_f32_ubyte0 v22, v22\n\t" \
    "v_mul_f32 v22, 0x3e800000, v22\n\t" \
    "v_lshrrev_b32 v23, 1, v20\n\t" \
    "v_mov_b32 v32, 0\n\t" \
    "v_mov_b32 v33, -1\n\t" \
    "v_mov_b32 v31, 0x3e800000\n\t" \
    "s_mov_b32 s65, 0\n\t" \
    "s_mov_b32 s40, 0\n\t" \
    "s_cmp_eq_u32 s24, 1\n\t" \
    "s_cbranch_scc1 .Lm1_%=\n\t" \
    "s_cmp_eq_u32 s24, 2\n\t" \
    "s_cbranch_scc1 .Lm2_%=\n\t" \
    "s_cmp_eq_u32 s24, 3\n\t" \
    "s_cbranch_scc1 .Lm3_%=\n\t" \
    "s_add_u32 s37, s32, 1048576\n\t" \
    "s_mov_b64 exec, 0xff\n\t" \
    "\n\t" \
    "s_mov_b64 exec, -1\n\t" \
    "v_or_b32 v24, 1, v20\n\t" \
    "v_cvt_f32_ubyte0 v24, v24\n\t" \
    "v_mul_f32 v24, 0x3c800000, v24\n\t" \
    "v_lshrrev_b32 v25, 4, v0\n\t" \
    "v_and_b32 v25, 16, v25\n\t" \
    "v_lshrrev_b32 v26, 2, v11\n\t" \
    "v_or_b32 v25, v25, v26\n\t" \
    "v_cvt_f32_ubyte0 v25, v25\n\t" \
    "v_add_f32 v26, 2.0, v25\n\t" \
    "v_add_f32 v27, 4.0, v25\n\t" \
    "v_add_f32 v28, 4.0, v26\n\t" \
    "v_mov_b32 v29, v22\n\t" \
    "v_mov_b32 v30, v22\n\t" \
    "v_mov_b32 v31, v24\n\t" \
    "s_mov_b32 s64, 0x3e800000\n\t" \
    "s_mov_b32 s65, 0x3e800000\n\t" \
    "v_add_u32 v34, 154880, v0\n\t" \
    "s_branch .Ltl_%=\n\t" \
    ".Lm1_%=:\n\t" \
    "v_lshrrev_b32 v24, 5, v0\n\t" \
    "v_and_b32 v24, 12, v24\n\t" \
    "v_and_or_b32 v24, v20, 3, v24\n\t" \
    "v_cvt_f32_ubyte0 v24, v24\n\t" \
    "v_mul_f32 v24, 0x3d924925, v24\n\t" \
    "v_mul_f32 v32, 0x41000000, v24\n\t" \
    "v_lshrrev_b32 v27, 3, v11\n\t" \
    "v_cvt_f32_ubyte0 v27, v27\n\t" \
    "v_add_f32 v25, -2.0, v27\n\t" \
    "v_add_f32 v26, -1.0, v27\n\t" \
    "v_add_f32 v28, 1.0, v27\n\t" \
    "v_and_b32 v29, 4, v20\n\t" \
    "v_cvt_f32_ubyte0 v29, v29\n\t" \
    "v_mul_f32 v29, 0x3d800000, v29\n\t" \
    "v_mov_b32 v30, v29\n\t" \
    "v_mov_b32 v31, 0xbe800000\n\t" \
    "s_mov_b32 s64, 0\n\t" \
    "s_mov_b32 exec_hi, 0\n\t" \
    "v_mov_b32 v33, 0\n\t" \
    "s_mov_b64 exec, -1\n\t" \
    "v_add_u32 v34, 149760, v0\n\t" \
    "s_branch .Ltl_%=\n\t" \
    ".Lm2_%=:\n\t" \
    "v_add_u32 v24, 6, v20\n\t" \
    "v_cvt_f32_ubyte0 v24, v24\n\t" \
    "v_mul_f32 v24, 0x3d000000, v24\n\t" \
    "v_add_f32 v25, 0.5, v21\n\t" \
    "v_add_f32 v26, 1.0, v25\n\t" \
    "v_add_f32 v27, 2.0, v25\n\t" \
    "v_add_f32 v28, 2.0, v26\n\t" \
    "v_mov_b32 v29, 0\n\t" \
    "v_mov_b32 v30, 0\n\t" \
    "s_mov_b32 s64, 0.5\n\t" \
    "v_add_u32 v34, 152832, v0\n\t" \
    "s_branch .Ltl_%=\n\t" \
    ".Lm3_%=:\n\t" \
    "s_mov_b32 s37, 34635776\n\t" \
    "buffer_load_dword v3, v12, s[4:7], s37 offen sc1\n\t" \
    "v_lshrrev_b32 v8, 2, v0\n\t" \
    "v_and_b32 v8, 28, v8\n\t" \
    "v_add_u32 v8, 156928, v8\n\t" \
    "ds_write_b32 v8, v12\n\t" \
    "v_add_u32 v24, 6, v23\n\t" \
    "v_cvt_f32_ubyte0 v24, v24\n\t" \
    "v_mul_f32 v24, 0x3d924925, v24\n\t" \
    "v_mov_b32 v25, v21\n\t" \
    "v_add_f32 v26, 1.0, v21\n\t" \
    "v_add_f32 v27, 2.0, v21\n\t" \
    "v_add_f32 v28, 2.0, v26\n\t" \
    "v_sub_f32 v29, 0, v22\n\t" \
    "v_sub_f32 v30, 0, v22\n\t" \
    "s_mov_b32 exec_lo, 0\n\t" \
    "v_fma_f32 v25, v21, 0.5, 4.0\n\t" \
    "v_add_f32 v27, 1.0, v25\n\t" \
    "v_fmaak_f32 v26, v21, v31, 0x41200000\n\t" \
    "v_mov_b32 v28, v26\n\t" \
    "v_add_f32 v30, v31, v30\n\t" \
    "s_mov_b64 exec, -1\n\t" \
    "s_mov_b32 s40, 0x11\n\t" \
    "v_add_u32 v34, 151808, v0\n\t" \
    ".Ltl_%=:\n\t" \
    "v_fma_f32 v36, v24, v25, v29\n\t" \
    "v_fma_f32 v38, v24, v26, v29\n\t" \
    "v_fma_f32 v40, v24, v27, v29\n\t" \
    "v_fma_f32 v42, v24, v28, v30\n\t" \
    "v_add_f32 v37, v36, v31\n\t" \
    "v_add_f32 v39, v38, v31\n\t" \
    "v_add_f32 v41, v40, v31\n\t" \
    "v_add_f32 v43, v42, v31\n\t" \
    "v_cos_f32 v36, v36\n\t" \
    "v_cos_f32 v37, v37\n\t" \
    "v_cos_f32 v38, v38\n\t" \
    "v_cos_f32 v39, v39\n\t" \
    "v_cos_f32 v40, v40\n\t" \
    "v_cos_f32 v41, v41\n\t" \
    "v_cos_f32 v42, v42\n\t" \
    "v_cos_f32 v43, v43\n\t" \
    "s_bitcmp0_b32 s40, 4\n\t" \
    "s_cbranch_scc1 .Lt2_%=\n\t" \
    "v_mul_f32 v36, 0x3b924925, v36\n\t" \
    "v_mul_f32 v37, 0x3b924925, v37\n\t" \
    "v_mul_f32 v38, 0x3b924925, v38\n\t" \
    "v_mul_f32 v39, 0x3b924925, v39\n\t" \
    "v_mul_f32 v40, 0x3b924925, v40\n\t" \
    "v_mul_f32 v41, 0x3b924925, v41\n\t" \
    "v_mul_f32 v42, 0x3b924925, v42\n\t" \
    "v_mul_f32 v43, 0x3b924925, v43\n\t" \
    ".Lt2_%=:\n\t" \
    "v_cvt_pk_bf16_f32 v44, v36, v37\n\t" \
    "v_cvt_pk_bf16_f32 v45, v38, v39\n\t" \
    "v_cvt_pk_bf16_f32 v46, v40, v41\n\t" \
    "v_cvt_pk_bf16_f32 v47, v42, v43\n\t" \
    "v_and_b32 v44, v33, v44\n\t" \
    "v_and_b32 v45, v33, v45\n\t" \
    "v_mov_b32 v33, -1\n\t" \
    "v_add_f32 v24, s64, v24\n\t" \
    "v_add_f32 v31, s65, v31\n\t" \
    "v_add_f32 v29, v32, v29\n\t" \
    "v_add_f32 v30, v32, v30\n\t" \
    "ds_write_b128 v34, v[44:47]\n\t" \
    "s_add_u32 s40, s40, 1\n\t" \
    "s_bitcmp1_b32 s40, 0\n\t" \
    "v_add_u32 v34, 0x400, v34\n\t" \
    "s_cbranch_scc1 .Ltl_%=\n\t" \
    "s_bitcmp1_b32 s40, 2\n\t" \
    "s_cbranch_scc1 .Lt4_%=\n\t" \
    "s_mov_b32 s40, 2\n\t" \
    "s_lshl2_add_u32 s37, s28, s24\n\t" \
    "s_lshl_b32 s37, s37, 2\n\t" \
    "v_lshrrev_b32 v25, 2, v20\n\t" \
    "v_add_u32 v25, s37, v25\n\t" \
    "v_mul_u32_u24 v26, 147, v25\n\t" \
    "v_lshrrev_b32 v26, 10, v26\n\t" \
    "v_lshl_or_b32 v24, v26, 1, 1\n\t" \
    "v_cvt_f32_ubyte0 v24, v24\n\t" \
    "v_mul_f32 v24, 0x3c800000, v24\n\t" \
    "v_mul_u32_u24 v26, 7, v26\n\t" \
    "v_sub_u32 v25, v25, v26\n\t" \
    "v_xor_b32 v22, s41, v20\n\t" \
    "v_bfe_u32 v32, v22, 1, 1\n\t" \
    "v_and_b32 v22, 1, v22\n\t" \
    "v_cvt_f32_ubyte0 v22, v22\n\t" \
    "v_mul_f32 v22, 0x3e800000, v22\n\t" \
    "v_lshl_or_b32 v32, v25, 1, v32\n\t" \
    "v_cvt_f32_ubyte0 v32, v32\n\t" \
    "v_mul_f32 v32, 0x3e924925, v32\n\t" \
    "v_mul_f32 v29, 0x3d800000, v21\n\t" \
    "v_fma_f32 v29, v29, v32, v22\n\t" \
    "v_mov_b32 v30, v29\n\t" \
    "v_mov_b32 v31, v24\n\t" \
    "v_mov_b32 v25, 0\n\t" \
    "v_mov_b32 v26, 2.0\n\t" \
    "v_mov_b32 v27, 4.0\n\t" \
    "v_mov_b32 v28, 0x40c00000\n\t" \
    "s_mov_b32 s64, 0\n\t" \
    "s_mov_b32 s65, 0\n\t" \
    "s_lshl_b32 s37, s24, 11\n\t" \
    "s_add_u32 s37, s37, 49664\n\t" \
    "v_add_u32 v34, s37, v0\n\t" \
    "s_branch .Ltl_%=\n\t" \
    ".Lt4_%=:\n\t" \
    ".Lt3_%=:\n\t" \
    "s_cmp_lg_u32 s24, 3\n\t" \
    "s_cbranch_scc1 .Lx2_%=\n\t" \
    "v_lshrrev_b32 v8, 7, v0\n\t" \
    "v_mul_u32_u24 v8, 96, v8\n\t" \
    "v_and_b32 v9, 16, v0\n\t" \
    "v_bfe_u32 v10, v0, 5, 2\n\t" \
    "v_add3_u32 v8, v8, v9, v10\n\t" \
    "v_mul_u32_u24 v8, 192, v8\n\t" \
    "s_mul_i32 s37, s28, 129024\n\t" \
    "s_add_u32 s37, s37, s31\n\t" \
    "s_mov_b32 m0, 157312\n\t" \
    "s_mov_b32 exec_hi, 0xffffff\n\t" \
    "buffer_load_dword v8, s[12:15], s37 offen lds\n\t" \
    "s_mov_b64 exec, -1\n\t" \
    ".Lx2_%=:\n\t" \
    ".Le9_%=:\n\t" \
    "s_lshr_b32 s37, s24, 2\n\t" \
    "s_and_b32 s38, s24, 2\n\t" \
    "s_mul_i32 s39, s37, 24832\n\t" \
    "s_lshl4_add_u32 s39, s38, s39\n\t" \
    "v_lshrrev_b32 v4, 6, v0\n\t" \
    "v_and_b32 v5, 12, v4\n\t" \
    "v_add_u32 v6, v4, v5\n\t" \
    "v_lshrrev_b32 v7, 1, v0\n\t" \
    "v_and_b32 v7, 24, v7\n\t" \
    "v_and_b32 v4, 11, v4\n\t" \
    "v_mul_u32_u24 v4, 192, v4\n\t" \
    "v_bfe_u32 v5, v0, 8, 1\n\t" \
    "v_mul_u32_u24 v5, 3104, v5\n\t" \
    "v_add3_u32 v4, v4, v5, v7\n\t" \
    "v_add_u32 v8, s39, v4\n\t" \
    "v_mov_b32 v9, 156928\n\t" \
    "v_add_u32 v10, 149760, v0\n\t" \
    "s_lshl2_add_u32 s37, s37, s38\n\t" \
    "s_lshl_b32 s37, s37, 7\n\t" \
    "s_mul_i32 s53, s27, 28\n\t" \
    "s_add_u32 s53, s53, s26\n\t" \
    "s_andn2_b32 s39, s53, 7\n\t" \
    "s_add_u32 s53, s53, s39\n\t" \
    "s_lshl_b32 s53, s53, 10\n\t" \
    "s_add_u32 s37, s37, s53\n\t" \
    "s_and_b32 s38, s24, 1\n\t" \
    "s_lshl_b32 s39, s38, 13\n\t" \
    "s_add_u32 s37, s37, s39\n\t" \
    "s_cmp_lt_u32 s25, 126\n\t" \
    "s_cselect_b32 s43, 0, 0x80000000\n\t" \
    "s_add_u32 s37, s37, s43\n\t" \
    "v_lshrrev_b32 v11, 8, v0\n\t" \
    "v_and_b32 v12, 0xf0, v0\n\t" \
    "v_mul_u32_u24 v4, 8486912, v11\n\t" \
    "v_add3_u32 v13, v4, v12, s37\n\t" \
    "s_lshl_b32 s39, s38, 10\n\t" \
    "v_add_u32 v14, s39, v10\n\t" \
    "s_lshl_b32 s39, s38, 5\n\t" \
    "s_add_u32 s39, s39, 116736\n\t" \
    "v_lshl_add_u32 v4, v6, 6, v7\n\t" \
    "v_lshrrev_b32 v5, 3, v0\n\t" \
    "v_and_b32 v5, 32, v5\n\t" \
    "v_xor_b32 v5, s39, v5\n\t" \
    "v_add_u32 v15, v4, v5\n\t" \
    "s_and_b32 s37, s24, 6\n\t" \
    "s_lshl_b32 s37, s37, 10\n\t" \
    "v_add_u32 v16, s37, v0\n\t" \
    "s_cmp_ge_u32 s25, 126\n\t" \
    "s_cbranch_scc1 .Lqa_%=\n\t" \
    ".Lqd_%=:\n\t" \
    "s_waitcnt lgkmcnt(0)\n\t" \
    "s_barrier\n\t" \
    "ds_read_b128 v[24:27], v14 offset:5120\n\t" \
    "ds_read_b128 v[28:31], v16 offset:49664\n\t" \
    "ds_read_b128 v[32:35], v16 offset:50688\n\t" \
    "s_and_b32 s33, s2, 7\n\t" \
    "s_lshl_b32 s33, s33, 15\n\t" \
    "s_add_u32 s33, s33, 33554432\n\t" \
    "s_lshr_b32 s37, s29, 2\n\t" \
    "s_mul_i32 s39, s30, 3\n\t" \
    "s_add_u32 s37, s37, s39\n\t" \
    "s_mul_i32 s37, s37, 114688\n\t" \
    "s_and_b32 s39, s29, 3\n\t" \
    "s_lshl_b32 s39, s39, 8\n\t" \
    "s_add_u32 s37, s37, s39\n\t" \
    "s_add_u32 s37, s37, s43\n\t" \
    "s_add_u32 s37, s37, 2097152\n\t" \
    "s_and_b32 s39, s24, 6\n\t" \
    "s_lshl3_add_u32 s39, s28, s39\n\t" \
    "s_lshl_b32 s39, s39, 1\n\t" \
    "v_add_u32 v14, s39, v11\n\t" \
    "v_mul_u32_u24 v17, 147, v14\n\t" \
    "v_lshrrev_b32 v17, 10, v17\n\t" \
    "s_and_b32 s39, s24, 1\n\t" \
    "v_subrev_u32 v18, s39, v17\n\t" \
    "v_mul_i32_i24 v18, 7, v18\n\t" \
    "v_sub_u32 v14, v14, v18\n\t" \
    "v_and_b32 v18, 14, v14\n\t" \
    "v_add_lshl_u32 v14, v14, v18, 12\n\t" \
    "v_and_b32 v18, 7, v17\n\t" \
    "v_mul_u32_u24 v18, 4243456, v18\n\t" \
    "v_add3_u32 v14, v14, v18, v12\n\t" \
    "v_and_b32 v17, 8, v17\n\t" \
    "v_xor_b32 v17, 8, v17\n\t" \
    "v_lshl_add_u32 v14, v17, 10, v14\n\t" \
    "v_add_u32 v19, s37, v14\n\t" \
    "s_lshl_b32 s43, s41, 10\n\t" \
    "s_and_b32 s37, s2, 0x70\n\t" \
    "s_lshl_b32 s37, s37, 1\n\t" \
    "s_lshl2_add_u32 s57, s24, s37\n\t" \
    "s_min_u32 s58, s57, 248\n\t" \
    "s_mul_i32 s59, s58, 147\n\t" \
    "s_lshr_b32 s59, s59, 12\n\t" \
    "s_mul_i32 s37, s59, 28\n\t" \
    "s_sub_u32 s60, s58, s37\n\t" \
    "s_andn2_b32 s39, s58, 7\n\t" \
    "s_add_u32 s51, s58, s39\n\t" \
    "s_lshl_b32 s51, s51, 10\n\t" \
    "s_sub_u32 s37, s60, 1\n\t" \
    "s_cselect_b32 s52, 27, -1\n\t" \
    "s_add_u32 s52, s52, s58\n\t" \
    "s_andn2_b32 s39, s52, 7\n\t" \
    "s_add_u32 s52, s52, s39\n\t" \
    "s_lshl_b32 s52, s52, 10\n\t" \
    "v_lshrrev_b32 v16, 9, v0\n\t" \
    "v_and_b32 v18, 0x2f0, v0\n\t" \
    "v_and_b32 v17, 1, v11\n\t" \
    "v_lshl_add_u32 v14, v17, 11, v18\n\t" \
    "v_add_u32 v36, s51, v14\n\t" \
    "v_add_u32 v37, s52, v0\n\t" \
    "s_mul_i32 s44, s24, 7296\n\t" \
    "s_mul_i32 s37, s60, 344064\n\t" \
    "s_mul_i32 s39, s59, 4992\n\t" \
    "s_add_u32 s37, s37, s39\n\t" \
    "s_cmp_eq_u32 s59, 8\n\t" \
    "s_movk_i32 s39, 2496\n\t" \
    "s_cselect_b32 s39, 1536, s39\n\t" \
    "s_add_u32 s37, s37, s39\n\t" \
    "s_and_b32 s39, s8, 0x1fffff\n\t" \
    "s_add_u32 s37, s37, s39\n\t" \
    "v_bfe_u32 v38, v0, 5, 2\n\t" \
    "v_lshl_add_u32 v38, v16, 4, v38\n\t" \
    "v_mul_u32_u24 v38, 43008, v38\n\t" \
    "v_add_u32 v39, s37, v38\n\t" \
    "s_lshr_b32 s37, s60, 1\n\t" \
    "s_mul_i32 s37, s37, 9\n\t" \
    "s_add_u32 s37, s37, s59\n\t" \
    "v_mad_u32_u24 v38, v16, 9, s37\n\t" \
    "v_lshrrev_b32 v18, 5, v38\n\t" \
    "s_and_b32 s29, s51, 0x1000\n\t" \
    "s_and_b32 s30, s52, 0x1000\n\t" \
    "s_lshl_b32 s37, s24, 2\n\t" \
    "s_lshl1_add_u32 s39, s24, 8\n\t" \
    "s_min_u32 s61, s37, s39\n\t" \
    "s_add_u32 s37, s37, 2\n\t" \
    "s_add_u32 s39, s24, 20\n\t" \
    "s_cmp_lt_u32 s24, 4\n\t" \
    "s_cselect_b32 s62, s37, s39\n\t" \
    "s_bfe_u32 s63, s2, 0x10003\n\t" \
    "s_mul_i32 s47, s63, 28\n\t" \
    "s_add_u32 s45, s47, s61\n\t" \
    "s_add_u32 s46, s47, s62\n\t" \
    "s_andn2_b32 s39, s45, 7\n\t" \
    "s_add_u32 s45, s45, s39\n\t" \
    "s_lshl_b32 s45, s45, 10\n\t" \
    "s_andn2_b32 s39, s46, 7\n\t" \
    "s_add_u32 s46, s46, s39\n\t" \
    "s_lshl_b32 s46, s46, 10\n\t" \
    "v_lshlrev_b32 v17, 4, v11\n\t" \
    "v_lshrrev_b32 v14, 6, v12\n\t" \
    "v_mul_u32_u24 v14, 7296, v14\n\t" \
    "v_and_b32 v38, 16, v0\n\t" \
    "v_mul_u32_u24 v38, 226, v38\n\t" \
    "v_and_b32 v40, 32, v0\n\t" \
    "v_lshl_add_u32 v38, v40, 1, v38\n\t" \
    "v_add3_u32 v14, v14, v38, v17\n\t" \
    "s_lshl_b32 s37, s61, 7\n\t" \
    "v_add_u32 v44, s37, v14\n\t" \
    "s_and_b32 s37, s62, 30\n\t" \
    "s_lshl_b32 s37, s37, 7\n\t" \
    "v_add_u32 v45, s37, v14\n\t" \
    "s_mov_b32 s1, 0\n\t" \
    "s_mov_b32 s41, 0x18000\n\t" \
    "s_lshl_b32 s37, s63, 9\n\t" \
    "s_add_u32 s51, s51, s37\n\t" \
    "s_cmp_lt_u32 s57, 252\n\t" \
    "s_cselect_b32 s51, s51, 0x80000000\n\t" \
    "s_and_b32 s37, s2, 0x87\n\t" \
    "s_lshl4_add_u32 s37, s24, s37\n\t" \
    "s_lshl_b32 s55, s37, 7\n\t" \
    "v_and_b32 v46, 16, v0\n\t" \
    "v_lshlrev_b32 v47, 6, v46\n\t" \
    "v_mad_u32_u24 v38, v12, 12, v17\n\t" \
    "v_and_b32 v14, 0x170, v0\n\t" \
    "v_and_b32 v41, 0x80, v0\n\t" \
    "v_lshl_add_u32 v14, v41, 3, v14\n\t" \
    "v_lshl_add_u32 v14, v16, 11, v14\n\t" \
    "v_add_u32 v42, s51, v14\n\t" \
    "s_mul_i32 s37, s24, 2064\n\t" \
    "v_and_b32 v14, 0x300, v0\n\t" \
    "v_add3_u32 v43, v14, v0, s37\n\t" \
    "s_lshl_b32 s37, s37, 1\n\t" \
    "v_lshlrev_b32 v14, 7, v16\n\t" \
    "v_xor_b32 v14, v14, v0\n\t" \
    "v_add_u32 v40, s37, v14\n\t" \
    "v_lshrrev_b32 v14, 5, v12\n\t" \
    "v_mul_u32_u24 v14, 2064, v14\n\t" \
    "v_lshl_add_u32 v14, v46, 2, v14\n\t" \
    "s_lshl_b32 s37, s24, 8\n\t" \
    "v_add3_u32 v48, v14, v17, s37\n\t" \
    "v_bfe_u32 v14, v0, 7, 1\n\t" \
    "v_lshl_add_u32 v14, v11, 2, v14\n\t" \
    "v_mul_u32_u24 v14, 2064, v14\n\t" \
    "v_lshrrev_b32 v41, 3, v0\n\t" \
    "v_and_or_b32 v14, v41, 8, v14\n\t" \
    "s_and_b32 s37, s24, 3\n\t" \
    "s_lshl_b32 s37, s37, 6\n\t" \
    "s_and_b32 s39, s24, 4\n\t" \
    "s_lshl_b32 s39, s39, 8\n\t" \
    "s_or_b32 s37, s37, s39\n\t" \
    "v_and_or_b32 v41, v0, 32, s37\n\t" \
    "v_mul_u32_u24 v46, 40, v46\n\t" \
    "v_xor_b32 v41, v41, v46\n\t" \
    "v_add_u32 v49, v14, v41\n\t" \
    "ds_read_b128 v[52:55], v10 offset:0\n\t" \
    "ds_read_b128 v[56:59], v10 offset:1024\n\t" \
    "s_sleep 6\n\t" \
    "s_and_b32 s37, s24, 1\n\t" \
    "s_lshl_b32 s37, s37, 4\n\t" \
    "s_lshr_b32 s39, s24, 1\n\t" \
    "s_add_u32 s37, s37, s39\n\t" \
    "s_mul_i32 s37, s37, 192\n\t" \
    "s_add_u32 s37, s37, s31\n\t" \
    "s_lshl_b32 s39, s24, 12\n\t" \
    "s_add_u32 m0, s39, 116736\n\t" \
    "s_add_u32 s38, s37, 0\n\t" \
    "buffer_load_dwordx4 v1, s[12:15], s38 offen  lds\n\t" \
    "s_add_u32 m0, s39, 117760\n\t" \
    "s_add_u32 s38, s37, 258048\n\t" \
    "buffer_load_dwordx4 v1, s[12:15], s38 offen  lds\n\t" \
    "s_add_u32 m0, s39, 118784\n\t" \
    "s_add_u32 s38, s37, 516096\n\t" \
    "buffer_load_dwordx4 v1, s[12:15], s38 offen  lds\n\t" \
    "s_add_u32 m0, s39, 119808\n\t" \
    "s_add_u32 s38, s37, 774144\n\t" \
    "buffer_load_dwordx4 v1, s[12:15], s38 offen  lds\n\t" \
    "s_cmp_lg_u32 s24, 3\n\t" \
    "s_cbranch_scc1 .Lr9_%=\n\t" \
    "s_and_b32 s37, s2, 7\n\t" \
    "s_lshl_b32 s37, s37, 10\n\t" \
    "s_add_u32 s37, s37, 34643968\n\t" \
    "s_buffer_load_dword s56, s[4:7], s37 offset:0x0\n\t" \
    "s_buffer_load_dword s57, s[4:7], s37 offset:0x80\n\t" \
    "s_buffer_load_dword s58, s[4:7], s37 offset:0x100\n\t" \
    "s_buffer_load_dword s59, s[4:7], s37 offset:0x180\n\t" \
    "s_buffer_load_dword s60, s[4:7], s37 offset:0x200\n\t" \
    "s_buffer_load_dword s61, s[4:7], s37 offset:0x280\n\t" \
    "s_buffer_load_dword s62, s[4:7], s37 offset:0x300\n\t" \
    "s_buffer_load_dword s63, s[4:7], s37 offset:0x380\n\t" \
    "s_mov_b32 s40, 0\n\t" \
    ".Lr1_%=:\n\t" \
    "s_waitcnt lgkmcnt(0)\n\t" \
    "s_memrealtime s[22:23]\n\t" \
    "s_waitcnt lgkmcnt(0)\n\t" \
    "s_mov_b32 s37, 1000\n\t" \
    "s_mov_b32 s38, 0\n\t" \
    "s_sub_u32 s39, s22, s56\n\t" \
    "s_min_u32 s37, s37, s39\n\t" \
    "s_max_u32 s38, s38, s39\n\t" \
    "s_sub_u32 s39, s22, s57\n\t" \
    "s_min_u32 s37, s37, s39\n\t" \
    "s_max_u32 s38, s38, s39\n\t" \
    "s_sub_u32 s39, s22, s58\n\t" \
    "s_min_u32 s37, s37, s39\n\t" \
    "s_max_u32 s38, s38, s39\n\t" \
    "s_sub_u32 s39, s22, s59\n\t" \
    "s_min_u32 s37, s37, s39\n\t" \
    "s_max_u32 s38, s38, s39\n\t" \
    "s_sub_u32 s39, s22, s60\n\t" \
    "s_min_u32 s37, s37, s39\n\t" \
    "s_max_u32 s38, s38, s39\n\t" \
    "s_sub_u32 s39, s22, s61\n\t" \
    "s_min_u32 s37, s37, s39\n\t" \
    "s_max_u32 s38, s38, s39\n\t" \
    "s_sub_u32 s39, s22, s62\n\t" \
    "s_min_u32 s37, s37, s39\n\t" \
    "s_max_u32 s38, s38, s39\n\t" \
    "s_sub_u32 s39, s22, s63\n\t" \
    "s_min_u32 s37, s37, s39\n\t" \
    "s_max_u32 s38, s38, s39\n\t" \
    "s_cmp_gt_u32 s38, 1000\n\t" \
    "s_cbranch_scc1 .Lr2_%=\n\t" \
    ".Lr3_%=:\n\t" \
    "s_sub_u32 s35, s22, s37\n\t" \
    "s_waitcnt vmcnt(5)\n\t" \
    "v_readfirstlane_b32 s34, v3\n\t" \
    "s_mul_i32 s34, s34, 1664525\n\t" \
    "s_add_u32 s34, s34, 1013904223\n\t" \
    "s_or_b32 s34, s34, 1\n\t" \
    "v_mov_b32 v60, s34\n\t" \
    "v_mov_b32 v61, s35\n\t" \
    "ds_write_b64 v9, v[60:61] offset:32\n\t" \
    ".Lr9_%=:\n\t" \
    "s_cmp_ge_u32 s25, 126\n\t" \
    "s_cbranch_scc1 .Lpb_%=\n\t" \
    ".Lpe_%=:\n\t" \
    "s_cmp_lt_u32 s24, 4\n\t" \
    "s_cbranch_scc1 .Lw_%=\n\t" \
    "s_waitcnt vmcnt(4)\n\t" \
    ".Lw_%=:\n\t" \
    "s_waitcnt lgkmcnt(0)\n\t" \
    "s_barrier\n\t" \
    "ds_read_b64_tr_b16 v[64:65], v8 offset:0\n\t" \
    "ds_read_b64_tr_b16 v[66:67], v8 offset:768\n\t" \
    "ds_read_b64_tr_b16 v[68:69], v8 offset:12416\n\t" \
    "ds_read_b64_tr_b16 v[70:71], v8 offset:13184\n\t" \
    "ds_read_b64_tr_b16 v[72:73], v8 offset:6208\n\t" \
    "ds_read_b64_tr_b16 v[74:75], v8 offset:6976\n\t" \
    "ds_read_b64_tr_b16 v[76:77], v8 offset:18624\n\t" \
    "ds_read_b64_tr_b16 v[78:79], v8 offset:19392\n\t" \
    "ds_read_b128 v[80:83], v9 offset:32\n\t" \
    "s_waitcnt lgkmcnt(7)\n\t" \
    "v_mfma_f32_16x16x32_bf16 v[84:87], v[24:27], v[64:67], 0\n\t" \
    "s_waitcnt lgkmcnt(5)\n\t" \
    "v_mfma_f32_16x16x32_bf16 v[88:91], v[24:27], v[68:71], 0\n\t" \
    "s_waitcnt lgkmcnt(3)\n\t" \
    "v_mfma_f32_16x16x32_bf16 v[92:95], v[24:27], v[72:75], 0\n\t" \
    "s_waitcnt lgkmcnt(1)\n\t" \
    "v_mfma_f32_16x16x32_bf16 v[96:99], v[24:27], v[76:79], 0\n\t" \
    "s_waitcnt lgkmcnt(0)\n\t" \
    "v_readfirstlane_b32 s34, v80\n\t" \
    "v_readfirstlane_b32 s35, v81\n\t" \
    "s_lshl_b32 s37, s24, 8\n\t" \
    "s_add_u32 s37, s37, s2\n\t" \
    "s_cmp_eq_u32 s37, 255\n\t" \
    "s_cbranch_scc1 .Lpp_%=\n\t" \
    ".Lpq_%=:\n\t" \
    "v_add_u32 v84, 0x8000, v84\n\t" \
    "v_add_u32 v86, 0x8000, v86\n\t" \
    "v_add_u32 v88, 0x8000, v88\n\t" \
    "v_add_u32 v90, 0x8000, v90\n\t" \
    "v_cvt_pk_bf16_f32 v108, v84, v85\n\t" \
    "v_cvt_pk_bf16_f32 v110, v88, v89\n\t" \
    "v_cvt_pk_bf16_f32 v109, v92, v93\n\t" \
    "v_cvt_pk_bf16_f32 v111, v96, v97\n\t" \
    "v_and_b32 v108, -2, v108\n\t" \
    "v_and_b32 v110, -2, v110\n\t" \
    "v_cvt_pk_bf16_f32 v112, v86, v87\n\t" \
    "v_cvt_pk_bf16_f32 v114, v90, v91\n\t" \
    "v_cvt_pk_bf16_f32 v113, v94, v95\n\t" \
    "v_cvt_pk_bf16_f32 v115, v98, v99\n\t" \
    "v_and_b32 v112, -2, v112\n\t" \
    "v_and_b32 v114, -2, v114\n\t" \
    ".Lh27_%=:\n\t" \
    "s_memrealtime s[22:23]\n\t" \
    "s_waitcnt lgkmcnt(0)\n\t" \
    "s_sub_u32 s22, s22, s35\n\t" \
    "s_cmp_lt_u32 s22, 205\n\t" \
    "s_cbranch_scc1 .Lh27_%=\n\t" \
    "buffer_store_dwordx4 v[108:111], v13, s[4:7], 0 offen offset:0 sc1\n\t" \
    "s_mov_b32 s42, 4243456\n\t" \
    "buffer_store_dwordx4 v[112:115], v13, s[4:7], s42 offen offset:0 sc1\n\t" \
    "ds_read_b64_tr_b16 v[116:117], v8 offset:64\n\t" \
    "ds_read_b64_tr_b16 v[118:119], v8 offset:832\n\t" \
    "ds_read_b64_tr_b16 v[120:121], v8 offset:12480\n\t" \
    "ds_read_b64_tr_b16 v[122:123], v8 offset:13248\n\t" \
    "ds_read_b64_tr_b16 v[124:125], v8 offset:6272\n\t" \
    "ds_read_b64_tr_b16 v[126:127], v8 offset:7040\n\t" \
    "ds_read_b64_tr_b16 v[128:129], v8 offset:18688\n\t" \
    "ds_read_b64_tr_b16 v[130:131], v8 offset:19456\n\t" \
    "ds_read_b64_tr_b16 v[132:133], v8 offset:128\n\t" \
    "ds_read_b64_tr_b16 v[134:135], v8 offset:896\n\t" \
    "ds_read_b64_tr_b16 v[136:137], v8 offset:12544\n\t" \
    "ds_read_b64_tr_b16 v[138:139], v8 offset:13312\n\t" \
    "ds_read_b64_tr_b16 v[140:141], v8 offset:6336\n\t" \
    "ds_read_b64_tr_b16 v[142:143], v8 offset:7104\n\t" \
    "ds_read_b64_tr_b16 v[144:145], v8 offset:18752\n\t" \
    "ds_read_b64_tr_b16 v[146:147], v8 offset:19520\n\t" \
    "v_mov_b32 v148, s34\n\t" \
    "v_mov_b32 v149, s34\n\t" \
    "v_mov_b32 v150, s34\n\t" \
    "v_mov_b32 v151, s34\n\t" \
    "s_and_b32 s37, s2, 243\n\t" \
    "s_cmp_eq_u32 s37, 243\n\t" \
    "s_cbranch_scc1 .Lskip_%=\n\t" \
    "s_waitcnt lgkmcnt(14)\n\t" \
    "v_mfma_f32_16x16x32_bf16 v[152:155], v[24:27], v[116:119], 0\n\t" \
    "s_waitcnt lgkmcnt(12)\n\t" \
    "v_mfma_f32_16x16x32_bf16 v[156:159], v[24:27], v[120:123], 0\n\t" \
    "s_waitcnt lgkmcnt(10)\n\t" \
    "v_mfma_f32_16x16x32_bf16 v[160:163], v[24:27], v[124:127], 0\n\t" \
    "s_waitcnt lgkmcnt(8)\n\t" \
    "v_mfma_f32_16x16x32_bf16 v[164:167], v[24:27], v[128:131], 0\n\t" \
    "s_waitcnt lgkmcnt(6)\n\t" \
    "v_mfma_f32_16x16x32_bf16 v[168:171], v[24:27], v[132:135], 0\n\t" \
    "s_waitcnt lgkmcnt(4)\n\t" \
    "v_mfma_f32_16x16x32_bf16 v[172:175], v[24:27], v[136:139], 0\n\t" \
    "s_waitcnt lgkmcnt(2)\n\t" \
    "v_mfma_f32_16x16x32_bf16 v[176:179], v[24:27], v[140:143], 0\n\t" \
    "s_waitcnt lgkmcnt(0)\n\t" \
    "v_mfma_f32_16x16x32_bf16 v[180:183], v[24:27], v[144:147], 0\n\t" \
    "v_add_u32 v152, 0x8000, v152\n\t" \
    "v_add_u32 v154, 0x8000, v154\n\t" \
    "v_add_u32 v156, 0x8000, v156\n\t" \
    "v_add_u32 v158, 0x8000, v158\n\t" \
    "v_cvt_pk_bf16_f32 v184, v152, v153\n\t" \
    "v_cvt_pk_bf16_f32 v186, v156, v157\n\t" \
    "v_cvt_pk_bf16_f32 v185, v160, v161\n\t" \
    "v_cvt_pk_bf16_f32 v187, v164, v165\n\t" \
    "v_and_b32 v184, -2, v184\n\t" \
    "v_and_b32 v186, -2, v186\n\t" \
    "v_cvt_pk_bf16_f32 v188, v154, v155\n\t" \
    "v_cvt_pk_bf16_f32 v190, v158, v159\n\t" \
    "v_cvt_pk_bf16_f32 v189, v162, v163\n\t" \
    "v_cvt_pk_bf16_f32 v191, v166, v167\n\t" \
    "v_and_b32 v188, -2, v188\n\t" \
    "v_and_b32 v190, -2, v190\n\t" \
    "v_add_u32 v168, 0x8000, v168\n\t" \
    "v_add_u32 v170, 0x8000, v170\n\t" \
    "v_add_u32 v172, 0x8000, v172\n\t" \
    "v_add_u32 v174, 0x8000, v174\n\t" \
    "v_cvt_pk_bf16_f32 v4, v168, v169\n\t" \
    "v_cvt_pk_bf16_f32 v6, v172, v173\n\t" \
    "v_cvt_pk_bf16_f32 v5, v176, v177\n\t" \
    "v_cvt_pk_bf16_f32 v7, v180, v181\n\t" \
    "v_and_b32 v4, -2, v4\n\t" \
    "v_and_b32 v6, -2, v6\n\t" \
    "v_cvt_pk_bf16_f32 v60, v170, v171\n\t" \
    "v_cvt_pk_bf16_f32 v62, v174, v175\n\t" \
    "v_cvt_pk_bf16_f32 v61, v178, v179\n\t" \
    "v_cvt_pk_bf16_f32 v63, v182, v183\n\t" \
    "v_and_b32 v60, -2, v60\n\t" \
    "v_and_b32 v62, -2, v62\n\t" \
    ".Lh28_%=:\n\t" \
    "s_memrealtime s[22:23]\n\t" \
    "s_waitcnt lgkmcnt(0)\n\t" \
    "s_sub_u32 s22, s22, s35\n\t" \
    "s_cmp_lt_u32 s22, 255\n\t" \
    "s_cbranch_scc1 .Lh28_%=\n\t" \
    "s_mov_b32 s42, 524288\n\t" \
    "buffer_store_dwordx4 v[184:187], v13, s[4:7], s42 offen offset:0 sc1\n\t" \
    "s_mov_b32 s42, 4767744\n\t" \
    "buffer_store_dwordx4 v[188:191], v13, s[4:7], s42 offen offset:0 sc1\n\t" \
    "s_waitcnt vmcnt(4)\n\t" \
    "; wait\n\t" \
    "s_waitcnt lgkmcnt(0)\n\t" \
    "s_barrier\n\t" \
    "s_lshl_b32 s37, s24, 18\n\t" \
    "s_add_u32 s37, s37, s32\n\t" \
    "s_bitcmp1_b32 29, s24\n\t" \
    "s_cselect_b32 s39, 0, 0x80000000\n\t" \
    "v_add_u32 v16, s39, v2\n\t" \
    "v_mov_b32 v20, 0\n\t" \
    "v_mov_b32 v21, 0\n\t" \
    "v_mov_b32 v22, 0\n\t" \
    "v_mov_b32 v23, 0\n\t" \
    "s_cmp_eq_u32 s24, 4\n\t" \
    "s_cselect_b64 exec, 0xff, -1\n\t" \
    "buffer_store_dwordx4 v[20:23], v16, s[4:7], s37 offen sc1\n\t" \
    "s_mov_b64 exec, -1\n\t" \
    ".Lh29_%=:\n\t" \
    "s_memrealtime s[22:23]\n\t" \
    "s_waitcnt lgkmcnt(0)\n\t" \
    "s_sub_u32 s22, s22, s35\n\t" \
    "s_cmp_lt_u32 s22, 305\n\t" \
    "s_cbranch_scc1 .Lh29_%=\n\t" \
    "s_mov_b32 s42, 1048576\n\t" \
    "buffer_store_dwordx4 v[4:7], v13, s[4:7], s42 offen offset:0 sc1\n\t" \
    "s_mov_b32 s42, 5292032\n\t" \
    "buffer_store_dwordx4 v[60:63], v13, s[4:7], s42 offen offset:0 sc1\n\t" \
    "ds_read_b64_tr_b16 v[64:65], v15 offset:0\n\t" \
    "ds_read_b64_tr_b16 v[66:67], v15 offset:256\n\t" \
    "ds_read_b64_tr_b16 v[68:69], v15 offset:2048\n\t" \
    "ds_read_b64_tr_b16 v[70:71], v15 offset:2304\n\t" \
    "ds_read_b64_tr_b16 v[72:73], v15 offset:4096\n\t" \
    "ds_read_b64_tr_b16 v[74:75], v15 offset:4352\n\t" \
    "ds_read_b64_tr_b16 v[76:77], v15 offset:6144\n\t" \
    "ds_read_b64_tr_b16 v[78:79], v15 offset:6400\n\t" \
    "ds_read_b64_tr_b16 v[80:81], v15 offset:8192\n\t" \
    "ds_read_b64_tr_b16 v[82:83], v15 offset:8448\n\t" \
    "ds_read_b64_tr_b16 v[100:101], v15 offset:10240\n\t" \
    "ds_read_b64_tr_b16 v[102:103], v15 offset:10496\n\t" \
    "ds_read_b64_tr_b16 v[104:105], v15 offset:12288\n\t" \
    "ds_read_b64_tr_b16 v[106:107], v15 offset:12544\n\t" \
    "s_waitcnt lgkmcnt(12)\n\t" \
    "v_mfma_f32_16x16x32_bf16 v[84:87], v[28:31], v[64:67], 0\n\t" \
    "ds_read_b64_tr_b16 v[88:89], v15 offset:14336\n\t" \
    "ds_read_b64_tr_b16 v[90:91], v15 offset:14592\n\t" \
    "s_waitcnt lgkmcnt(12)\n\t" \
    "v_mfma_f32_16x16x32_bf16 v[84:87], v[32:35], v[68:71], v[84:87]\n\t" \
    "ds_read_b64_tr_b16 v[92:93], v15 offset:16384\n\t" \
    "ds_read_b64_tr_b16 v[94:95], v15 offset:16640\n\t" \
    "s_waitcnt lgkmcnt(12)\n\t" \
    "v_mfma_f32_16x16x32_bf16 v[96:99], v[28:31], v[72:75], 0\n\t" \
    "ds_read_b64_tr_b16 v[108:109], v15 offset:18432\n\t" \
    "ds_read_b64_tr_b16 v[110:111], v15 offset:18688\n\t" \
    "s_waitcnt lgkmcnt(12)\n\t" \
    "v_mfma_f32_16x16x32_bf16 v[96:99], v[32:35], v[76:79], v[96:99]\n\t" \
    "ds_read_b64_tr_b16 v[112:113], v15 offset:20480\n\t" \
    "ds_read_b64_tr_b16 v[114:115], v15 offset:20736\n\t" \
    "s_waitcnt lgkmcnt(12)\n\t" \
    "v_mfma_f32_16x16x32_bf16 v[116:119], v[28:31], v[80:83], 0\n\t" \
    "ds_read_b64_tr_b16 v[120:121], v15 offset:22528\n\t" \
    "ds_read_b64_tr_b16 v[122:123], v15 offset:22784\n\t" \
    "s_waitcnt lgkmcnt(12)\n\t" \
    "v_mfma_f32_16x16x32_bf16 v[116:119], v[32:35], v[100:103], v[116:119]\n\t" \
    "ds_read_b64_tr_b16 v[124:125], v15 offset:24576\n\t" \
    "ds_read_b64_tr_b16 v[126:127], v15 offset:24832\n\t" \
    "s_waitcnt lgkmcnt(12)\n\t" \
    "v_mfma_f32_16x16x32_bf16 v[128:131], v[28:31], v[104:107], 0\n\t" \
    "ds_read_b64_tr_b16 v[132:133], v15 offset:26624\n\t" \
    "ds_read_b64_tr_b16 v[134:135], v15 offset:26880\n\t" \
    "s_waitcnt lgkmcnt(12)\n\t" \
    "v_mfma_f32_16x16x32_bf16 v[128:131], v[32:35], v[88:91], v[128:131]\n\t" \
    "ds_read_b64_tr_b16 v[136:137], v15 offset:28672\n\t" \
    "ds_read_b64_tr_b16 v[138:139], v15 offset:28928\n\t" \
    "s_waitcnt lgkmcnt(12)\n\t" \
    "v_mfma_f32_16x16x32_bf16 v[140:143], v[28:31], v[92:95], 0\n\t" \
    "ds_read_b64_tr_b16 v[24:25], v15 offset:30720\n\t" \
    "ds_read_b64_tr_b16 v[26:27], v15 offset:30976\n\t" \
    "s_waitcnt lgkmcnt(12)\n\t" \
    "v_mfma_f32_16x16x32_bf16 v[140:143], v[32:35], v[108:111], v[140:143]\n\t" \
    "v_cvt_pk_bf16_f32 v144, v84, v96\n\t" \
    "v_cvt_pk_bf16_f32 v152, v85, v97\n\t" \
    "s_waitcnt lgkmcnt(10)\n\t" \
    "v_mfma_f32_16x16x32_bf16 v[156:159], v[28:31], v[112:115], 0\n\t" \
    "v_cvt_pk_bf16_f32 v160, v86, v98\n\t" \
    "v_cvt_pk_bf16_f32 v164, v87, v99\n\t" \
    "s_waitcnt lgkmcnt(8)\n\t" \
    "v_mfma_f32_16x16x32_bf16 v[156:159], v[32:35], v[120:123], v[156:159]\n\t" \
    "v_cvt_pk_bf16_f32 v145, v116, v128\n\t" \
    "v_cvt_pk_bf16_f32 v153, v117, v129\n\t" \
    "s_waitcnt lgkmcnt(6)\n\t" \
    "v_mfma_f32_16x16x32_bf16 v[168:171], v[28:31], v[124:127], 0\n\t" \
    "v_cvt_pk_bf16_f32 v161, v118, v130\n\t" \
    "v_cvt_pk_bf16_f32 v165, v119, v131\n\t" \
    "s_waitcnt lgkmcnt(4)\n\t" \
    "v_mfma_f32_16x16x32_bf16 v[168:171], v[32:35], v[132:135], v[168:171]\n\t" \
    "s_waitcnt lgkmcnt(2)\n\t" \
    "v_mfma_f32_16x16x32_bf16 v[172:175], v[28:31], v[136:139], 0\n\t" \
    "s_waitcnt lgkmcnt(0)\n\t" \
    "v_mfma_f32_16x16x32_bf16 v[172:175], v[32:35], v[24:27], v[172:175]\n\t" \
    "v_cvt_pk_bf16_f32 v146, v140, v156\n\t" \
    "v_cvt_pk_bf16_f32 v154, v141, v157\n\t" \
    "v_cvt_pk_bf16_f32 v162, v142, v158\n\t" \
    "v_cvt_pk_bf16_f32 v166, v143, v159\n\t" \
    "s_nop 3\n\t" \
    "v_cvt_pk_bf16_f32 v147, v168, v172\n\t" \
    "v_cvt_pk_bf16_f32 v155, v169, v173\n\t" \
    "v_cvt_pk_bf16_f32 v163, v170, v174\n\t" \
    "v_cvt_pk_bf16_f32 v167, v171, v175\n\t" \
    ".Lh30_%=:\n\t" \
    "s_memrealtime s[22:23]\n\t" \
    "s_waitcnt lgkmcnt(0)\n\t" \
    "s_sub_u32 s22, s22, s35\n\t" \
    "s_cmp_lt_u32 s22, 370\n\t" \
    "s_cbranch_scc1 .Lh30_%=\n\t" \
    "s_cmp_eq_u32 s24, 0\n\t" \
    "s_cbranch_scc1 .Lj32_%=\n\t" \
    ".Lh31_%=:\n\t" \
    "s_memrealtime s[22:23]\n\t" \
    "s_waitcnt lgkmcnt(0)\n\t" \
    "s_sub_u32 s22, s22, s35\n\t" \
    "s_cmp_lt_u32 s22, 385\n\t" \
    "s_cbranch_scc1 .Lh31_%=\n\t" \
    ".Lj32_%=:\n\t" \
    "s_waitcnt lgkmcnt(0)\n\t" \
    "s_xor_b32 s42, s43, 0\n\t" \
    "buffer_store_dwordx4 v[144:147], v19, s[4:7], s42 offen offset:0 sc1\n\t" \
    "s_waitcnt vmcnt(1)\n\t" \
    "s_mov_b64 exec, 1\n\t" \
    "ds_add_rtn_u32 v17, v9, v9 offset:8\n\t" \
    "s_add_u32 s37, s32, 524288\n\t" \
    "s_waitcnt lgkmcnt(0)\n\t" \
    "v_cmp_eq_u32 vcc, 1098496, v17\n\t" \
    "s_mov_b64 exec, -1\n\t" \
    "s_nop 0\n\t" \
    "s_cbranch_vccz .La13_%=\n\t" \
    "buffer_store_dwordx4 v[148:151], v2, s[4:7], s37 offen sc1\n\t" \
    ".La13_%=:\n\t" \
    "s_mov_b64 exec, -1\n\t" \
    "s_xor_b32 s42, s43, 1024\n\t" \
    "buffer_store_dwordx4 v[152:155], v19, s[4:7], s42 offen offset:0 sc1\n\t" \
    "s_cmp_eq_u32 s24, 0\n\t" \
    "s_cbranch_scc1 .Lj34_%=\n\t" \
    ".Lh33_%=:\n\t" \
    "s_memrealtime s[22:23]\n\t" \
    "s_waitcnt lgkmcnt(0)\n\t" \
    "s_sub_u32 s22, s22, s35\n\t" \
    "s_cmp_lt_u32 s22, 435\n\t" \
    "s_cbranch_scc1 .Lh33_%=\n\t" \
    ".Lj34_%=:\n\t" \
    "s_waitcnt lgkmcnt(0)\n\t" \
    "s_xor_b32 s42, s43, 2048\n\t" \
    "buffer_store_dwordx4 v[160:163], v19, s[4:7], s42 offen offset:0 sc1\n\t" \
    "s_xor_b32 s42, s43, 3072\n\t" \
    "buffer_store_dwordx4 v[164:167], v19, s[4:7], s42 offen offset:0 sc1\n\t" \
    ".Lskip_%=:\n\t" \
    "s_waitcnt lgkmcnt(0)\n\t" \
    "s_nop 7\n\t" \
    ".Lh35_%=:\n\t" \
    "s_memrealtime s[22:23]\n\t" \
    "s_waitcnt lgkmcnt(0)\n\t" \
    "s_sub_u32 s22, s22, s35\n\t" \
    "s_cmp_lt_u32 s22, 390\n\t" \
    "s_cbranch_scc1 .Lh35_%=\n\t" \
    "s_and_b32 s52, s2, 7\n\t" \
    "s_lshl_b32 s52, s52, 7\n\t" \
    "s_add_u32 s52, s52, 34636800\n\t" \
    "s_buffer_load_dword s36, s[4:7], s52\n\t" \
    "s_and_b32 s37, s24, 3\n\t" \
    "s_mul_i32 s37, s37, 8486912\n\t" \
    "s_add_u32 s53, s53, s37\n\t" \
    "s_and_b32 s37, s24, 4\n\t" \
    "s_lshl_b32 s39, s37, 11\n\t" \
    "s_add_u32 s53, s53, s39\n\t" \
    "s_add_u32 s37, s37, s24\n\t" \
    "s_andn2_b32 s39, s26, 1\n\t" \
    "s_lshl3_add_u32 s37, s39, s37\n\t" \
    "s_mul_i32 s37, s37, 43008\n\t" \
    "s_mul_i32 s39, s27, 4992\n\t" \
    "s_add_u32 s51, s37, s39\n\t" \
    "s_cmp_eq_u32 s27, 8\n\t" \
    "s_movk_i32 s10, 4992\n\t" \
    "s_cselect_b32 s10, 3072, s10\n\t" \
    "s_mov_b32 s11, 0x20000\n\t" \
    "s_and_b32 s37, s2, 0x70\n\t" \
    "s_lshl_b32 s37, s37, 1\n\t" \
    "v_lshrrev_b32 v11, 7, v0\n\t" \
    "v_lshl_add_u32 v11, v11, 2, s37\n\t" \
    "v_min_u32 v11, 248, v11\n\t" \
    "v_mul_u32_u24 v46, 147, v11\n\t" \
    "v_lshrrev_b32 v46, 12, v46\n\t" \
    "v_mul_u32_u24 v41, 28, v46\n\t" \
    "v_sub_u32 v11, v11, v41\n\t" \
    "v_bfe_u32 v41, v0, 4, 3\n\t" \
    "v_min_u32 v41, 4, v41\n\t" \
    "v_add3_u32 v11, v11, v41, -1\n\t" \
    "v_add_u32 v41, 28, v11\n\t" \
    "v_min_u32 v11, v11, v41\n\t" \
    "v_and_b32 v1, 1, v11\n\t" \
    "v_lshrrev_b32 v11, 1, v11\n\t" \
    "v_mad_u32_u24 v11, v11, 9, v46\n\t" \
    "v_lshl_add_u32 v11, v1, 7, v11\n\t" \
    "v_and_b32 v3, 31, v11\n\t" \
    "v_lshrrev_b32 v50, 5, v11\n\t" \
    "v_lshlrev_b32 v50, 7, v50\n\t" \
    "v_lshl_or_b32 v51, v3, 10, v50\n\t" \
    "s_mov_b32 s40, 0\n\t" \
    "s_waitcnt lgkmcnt(0)\n\t" \
    ".Lr4_%=:\n\t" \
    "s_xor_b32 s36, s36, s34\n\t" \
    "s_andn2_b32 s37, s36, 14\n\t" \
    "s_cbranch_scc1 .Lr5_%=\n\t" \
    ".Lr6_%=:\n\t" \
    "s_bfe_u32 s36, s36, 0x30001\n\t" \
    "s_add_u32 s56, s2, s36\n\t" \
    "s_and_b32 s56, s56, 7\n\t" \
    "s_lshr_b32 s37, s2, 7\n\t" \
    "s_lshl1_add_u32 s56, s56, s37\n\t" \
    "s_and_b32 s37, s56, 7\n\t" \
    "s_mul_i32 s48, s37, 4243456\n\t" \
    "s_and_b32 s37, s56, 8\n\t" \
    "s_lshl_b32 s37, s37, 10\n\t" \
    "s_add_u32 s48, s48, s37\n\t" \
    "s_add_u32 s49, s48, 524288\n\t" \
    "s_add_u32 s50, s48, 1048576\n\t" \
    "s_xor_b32 s37, s48, 0x2000\n\t" \
    "s_add_u32 s37, s37, 2097152\n\t" \
    "s_add_u32 s45, s45, s37\n\t" \
    "s_add_u32 s46, s46, s37\n\t" \
    "s_cmp_lt_u32 s24, 4\n\t" \
    "s_cselect_b32 s47, s46, 0x80000000\n\t" \
    "v_add_u32 v17, s45, v0\n\t" \
    "v_add_u32 v19, s46, v0\n\t" \
    "v_add_u32 v41, s47, v0\n\t" \
    "s_lshr_b32 s37, s24, 2\n\t" \
    "s_mul_i32 s37, s37, 6\n\t" \
    "s_mul_i32 s39, s56, 7\n\t" \
    "s_add_u32 s37, s37, s39\n\t" \
    "s_lshr_b32 s37, s37, 4\n\t" \
    "s_and_b32 s39, s24, 3\n\t" \
    "s_mul_i32 s39, s39, 63\n\t" \
    "s_add_u32 s37, s37, s39\n\t" \
    "v_lshrrev_b32 v46, 4, v0\n\t" \
    "v_min_u32 v46, 8, v46\n\t" \
    "v_mad_u32_u24 v46, v46, 7, s37\n\t" \
    "v_add_u32 v1, 2, v46\n\t" \
    "v_lshrrev_b32 v1, 7, v1\n\t" \
    "v_lshl_add_u32 v46, v1, 1, v46\n\t" \
    "v_and_b32 v1, 31, v46\n\t" \
    "v_lshrrev_b32 v11, 5, v46\n\t" \
    "v_lshlrev_b32 v11, 7, v11\n\t" \
    "v_lshl_or_b32 v3, v1, 10, v11\n\t" \
    "v_add_u32 v18, s36, v18\n\t" \
    "v_lshlrev_b32 v18, 8, v18\n\t" \
    "v_and_b32 v18, 0x400, v18\n\t" \
    "v_xor_b32 v42, v42, v18\n\t" \
    "s_sub_u32 s37, s24, s36\n\t" \
    "s_and_b32 s37, s37, 7\n\t" \
    "s_mul_i32 s39, s27, 28\n\t" \
    "s_add_u32 s39, s39, s26\n\t" \
    "s_lshr_b32 s39, s39, 5\n\t" \
    "s_lshl4_add_u32 s37, s39, s37\n\t" \
    "s_lshl_b32 s52, s37, 7\n\t" \
    "s_nop 0\n\t" \
    "v_readlane_b32 s12, v3, 0\n\t" \
    "v_readlane_b32 s13, v3, 1\n\t" \
    "v_readlane_b32 s14, v3, 2\n\t" \
    "v_readlane_b32 s15, v3, 3\n\t" \
    "v_readlane_b32 s16, v3, 4\n\t" \
    "v_readlane_b32 s17, v3, 5\n\t" \
    "v_readlane_b32 s18, v3, 6\n\t" \
    "v_readlane_b32 s19, v3, 7\n\t" \
    "v_readlane_b32 s3, v3, 8\n\t" \
    "s_add_u32 s12, s12, s33\n\t" \
    "s_add_u32 s13, s13, s33\n\t" \
    "s_add_u32 s14, s14, s33\n\t" \
    "s_add_u32 s15, s15, s33\n\t" \
    "s_add_u32 s16, s16, s33\n\t" \
    "s_add_u32 s17, s17, s33\n\t" \
    "s_add_u32 s18, s18, s33\n\t" \
    "s_add_u32 s19, s19, s33\n\t" \
    "s_add_u32 s3, s3, s33\n\t" \
    "s_waitcnt vmcnt(0)\n\t" \
    "s_mov_b64 exec, 1\n\t" \
    "ds_add_rtn_u32 v50, v9, v9 offset:12\n\t" \
    "s_add_u32 s37, s32, 786432\n\t" \
    "s_waitcnt lgkmcnt(0)\n\t" \
    "v_cmp_eq_u32 vcc, 1098496, v50\n\t" \
    "s_mov_b64 exec, -1\n\t" \
    "s_nop 0\n\t" \
    "s_cbranch_vccz .La20_%=\n\t" \
    "buffer_store_dwordx4 v[148:151], v2, s[4:7], s37 offen sc1\n\t" \
    ".La20_%=:\n\t" \
    "s_mov_b64 exec, -1\n\t" \
    ".Lh36_%=:\n\t" \
    "s_memrealtime s[22:23]\n\t" \
    "s_waitcnt lgkmcnt(0)\n\t" \
    "s_sub_u32 s22, s22, s35\n\t" \
    "s_cmp_lt_u32 s22, 490\n\t" \
    "s_cbranch_scc1 .Lh36_%=\n\t" \
    "s_mov_b32 s37, 34637824\n\t" \
    "s_buffer_load_dword s56, s[4:7], s37 offset:0x0\n\t" \
    "s_buffer_load_dword s57, s[4:7], s37 offset:0x80\n\t" \
    "s_buffer_load_dword s58, s[4:7], s37 offset:0x100\n\t" \
    "s_buffer_load_dword s59, s[4:7], s37 offset:0x180\n\t" \
    "s_buffer_load_dword s60, s[4:7], s37 offset:0x200\n\t" \
    "s_buffer_load_dword s61, s[4:7], s37 offset:0x280\n\t" \
    "s_cmp_lg_u32 s24, 0\n\t" \
    "s_cbranch_scc1 .Lb15_%=\n\t" \
    "s_mov_b32 s40, 0\n\t" \
    "s_add_u32 s38, s33, 524288\n\t" \
    ".Ll15_%=:\n\t" \
    "buffer_load_dword v8, v51, s[4:7], s38 offen sc1\n\t" \
    "s_waitcnt vmcnt(0)\n\t" \
    "v_cmp_ne_u32 vcc, s34, v8\n\t" \
    "s_add_u32 s40, s40, 1\n\t" \
    "s_cmp_ge_u32 s40, 8192\n\t" \
    "s_cbranch_scc1 .Lb15_%=\n\t" \
    "s_cbranch_vccz .Lb15_%=\n\t" \
    "s_sleep 2\n\t" \
    "s_branch .Ll15_%=\n\t" \
    ".Lb15_%=:\n\t" \
    "s_waitcnt lgkmcnt(0)\n\t" \
    "s_barrier\n\t" \
    "buffer_load_dwordx4 v[176:179], v36, s[4:7], s48 offen offset:0 sc1\n\t" \
    "buffer_load_dwordx4 v[180:183], v36, s[4:7], s48 offen offset:1024 sc1\n\t" \
    "buffer_load_dwordx4 v[184:187], v36, s[4:7], s48 offen offset:256 sc1\n\t" \
    "buffer_load_dwordx4 v[188:191], v36, s[4:7], s48 offen offset:1280 sc1\n\t" \
    "buffer_load_dwordx4 v[20:23], v37, s[4:7], s48 offen offset:0 sc1\n\t" \
    "s_cmp_lt_u32 s24, 4\n\t" \
    "s_cbranch_scc1 .Lp37_%=\n\t" \
    "s_setprio 1\n\t" \
    ".Lp37_%=:\n\t" \
    "s_mov_b32 s40, 0\n\t" \
    "s_waitcnt lgkmcnt(0)\n\t" \
    ".Lq4_%=:\n\t" \
    "s_mov_b32 s37, 0\n\t" \
    "s_mov_b32 s0, 0\n\t" \
    "s_xor_b32 s61, s61, s34\n\t" \
    "s_or_b32 s37, s37, s61\n\t" \
    "s_lshl1_add_u32 s0, s0, s61\n\t" \
    "s_xor_b32 s60, s60, s34\n\t" \
    "s_or_b32 s37, s37, s60\n\t" \
    "s_lshl1_add_u32 s0, s0, s60\n\t" \
    "s_xor_b32 s59, s59, s34\n\t" \
    "s_or_b32 s37, s37, s59\n\t" \
    "s_lshl1_add_u32 s0, s0, s59\n\t" \
    "s_xor_b32 s58, s58, s34\n\t" \
    "s_or_b32 s37, s37, s58\n\t" \
    "s_lshl1_add_u32 s0, s0, s58\n\t" \
    "s_xor_b32 s57, s57, s34\n\t" \
    "s_or_b32 s37, s37, s57\n\t" \
    "s_lshl1_add_u32 s0, s0, s57\n\t" \
    "s_xor_b32 s56, s56, s34\n\t" \
    "s_or_b32 s37, s37, s56\n\t" \
    "s_lshl1_add_u32 s0, s0, s56\n\t" \
    "s_andn2_b32 s37, s37, 2\n\t" \
    "s_cbranch_scc1 .Lq5_%=\n\t" \
    ".Lq6_%=:\n\t" \
    "s_lshr_b32 s0, s0, 1\n\t" \
    "s_add_u32 s37, s36, 3\n\t" \
    "s_bitcmp1_b32 s37, 2\n\t" \
    "s_cselect_b32 s37, 51, 12\n\t" \
    "s_xor_b32 s0, s0, s37\n\t" \
    "v_add_u32 v1, 344064, v39\n\t" \
    "v_lshrrev_b32 v11, 21, v1\n\t" \
    "v_bfe_u32 v11, s0, v11, 1\n\t" \
    "v_lshrrev_b32 v18, 3, v1\n\t" \
    "v_lshlrev_b32 v11, 10, v11\n\t" \
    "v_and_b32 v18, 0x400, v18\n\t" \
    "v_xor_b32 v11, v11, v18\n\t" \
    "v_xor_b32 v9, v42, v11\n\t" \
    "v_lshrrev_b32 v11, 21, v39\n\t" \
    "v_bfe_u32 v11, s0, v11, 1\n\t" \
    "v_lshrrev_b32 v18, 3, v39\n\t" \
    "v_lshlrev_b32 v11, 10, v11\n\t" \
    "v_and_b32 v18, 0x400, v18\n\t" \
    "v_xor_b32 v11, v11, v18\n\t" \
    "v_xor_b32 v42, v42, v11\n\t" \
    "s_lshr_b32 s37, s10, 1\n\t" \
    "s_add_u32 s37, s37, s51\n\t" \
    "s_and_b32 s39, s8, 0x1fffff\n\t" \
    "s_add_u32 s37, s37, s39\n\t" \
    "s_lshr_b32 s39, s37, 21\n\t" \
    "s_lshr_b32 s39, s0, s39\n\t" \
    "s_xor_b32 s37, s37, s48\n\t" \
    "s_lshr_b32 s37, s37, 13\n\t" \
    "s_xor_b32 s37, s37, s39\n\t" \
    "s_and_b32 s37, s37, 1\n\t" \
    "s_mul_i32 s37, s37, 172032\n\t" \
    "s_add_u32 s37, s37, s51\n\t" \
    "s_add_u32 s8, s8, s37\n\t" \
    "s_addc_u32 s9, s9, 0\n\t" \
    "s_waitcnt vmcnt(4)\n\t" \
    "v_mfma_f32_32x32x16_bf16 v[192:207], v[52:55], v[176:179], 0\n\t" \
    "buffer_load_dwordx4 v[4:7], v36, s[4:7], s49 offen offset:0 sc1\n\t" \
    "s_waitcnt vmcnt(4)\n\t" \
    "v_mfma_f32_32x32x16_bf16 v[192:207], v[56:59], v[180:183], v[192:207]\n\t" \
    "buffer_load_dwordx4 v[60:63], v36, s[4:7], s49 offen offset:1024 sc1\n\t" \
    "s_waitcnt vmcnt(4)\n\t" \
    "v_mfma_f32_32x32x16_bf16 v[208:223], v[52:55], v[184:187], 0\n\t" \
    "buffer_load_dwordx4 v[64:67], v36, s[4:7], s49 offen offset:256 sc1\n\t" \
    "s_waitcnt vmcnt(4)\n\t" \
    "v_mfma_f32_32x32x16_bf16 v[208:223], v[56:59], v[188:191], v[208:223]\n\t" \
    "buffer_load_dwordx4 v[68:71], v36, s[4:7], s49 offen offset:1280 sc1\n\t" \
    "v_mfma_f32_32x32x16_bf16 v[224:239], v[56:59], v[176:179], 0\n\t" \
    "s_add_u32 m0, s44, 0\n\t" \
    "s_nop 8\n\t" \
    "v_cvt_pk_bf16_f32 v50, v192, v208\n\t" \
    "v_cvt_pk_bf16_f32 v51, v193, v209\n\t" \
    "v_cvt_pk_bf16_f32 v8, v194, v210\n\t" \
    "v_cvt_pk_bf16_f32 v16, v195, v211\n\t" \
    "v_cvt_pk_bf16_f32 v46, v196, v212\n\t" \
    "s_waitcnt vmcnt(4)\n\t" \
    "v_permlane16_swap_b32 v20, v180\n\t" \
    "v_permlane16_swap_b32 v21, v181\n\t" \
    "ds_write_addtid_b32 v50 offset:3616\n\t" \
    "ds_write_addtid_b32 v51 offset:3872\n\t" \
    "ds_write_addtid_b32 v8 offset:4128\n\t" \
    "ds_write_addtid_b32 v16 offset:4384\n\t" \
    "ds_write_addtid_b32 v46 offset:4640\n\t" \
    "v_permlane16_swap_b32 v22, v182\n\t" \
    "v_permlane16_swap_b32 v23, v183\n\t" \
    "v_cvt_pk_bf16_f32 v1, v197, v213\n\t" \
    "v_cvt_pk_bf16_f32 v39, v198, v214\n\t" \
    "v_cvt_pk_bf16_f32 v18, v199, v215\n\t" \
    "v_cvt_pk_bf16_f32 v11, v200, v216\n\t" \
    "v_cvt_pk_bf16_f32 v50, v201, v217\n\t" \
    "v_mfma_f32_32x32x16_bf16 v[240:255], v[56:59], v[184:187], 0\n\t" \
    "v_permlane16_swap_b32 v180, v188\n\t" \
    "buffer_load_dwordx4 v[72:75], v37, s[4:7], s49 offen offset:0 sc1\n\t" \
    "ds_write_addtid_b32 v1 offset:4896\n\t" \
    "ds_write_addtid_b32 v39 offset:5152\n\t" \
    "ds_write_addtid_b32 v18 offset:5408\n\t" \
    "ds_write_addtid_b32 v11 offset:5664\n\t" \
    "ds_write_addtid_b32 v50 offset:5920\n\t" \
    "v_permlane16_swap_b32 v181, v189\n\t" \
    "v_permlane16_swap_b32 v182, v190\n\t" \
    "v_cvt_pk_bf16_f32 v51, v202, v218\n\t" \
    "v_cvt_pk_bf16_f32 v8, v203, v219\n\t" \
    "v_cvt_pk_bf16_f32 v16, v204, v220\n\t" \
    "v_cvt_pk_bf16_f32 v46, v205, v221\n\t" \
    "v_permlane16_swap_b32 v183, v191\n\t" \
    "v_mfma_f32_32x32x16_bf16 v[224:239], v[52:55], v[20:23], v[224:239]\n\t" \
    "ds_write_addtid_b32 v51 offset:6176\n\t" \
    "ds_write_addtid_b32 v8 offset:6432\n\t" \
    "ds_write_addtid_b32 v16 offset:6688\n\t" \
    "ds_write_addtid_b32 v46 offset:6944\n\t" \
    "v_mfma_f32_32x32x16_bf16 v[240:255], v[52:55], v[180:183], v[240:255]\n\t" \
    ".Lh38_%=:\n\t" \
    "s_memrealtime s[22:23]\n\t" \
    "s_waitcnt lgkmcnt(0)\n\t" \
    "s_sub_u32 s22, s22, s35\n\t" \
    "s_cmp_lt_u32 s22, 603\n\t" \
    "s_cbranch_scc1 .Lh38_%=\n\t" \
    "s_buffer_load_dword s12, s[4:7], s12 offset:0xc0000\n\t" \
    "s_buffer_load_dword s13, s[4:7], s13 offset:0xc0000\n\t" \
    "s_buffer_load_dword s14, s[4:7], s14 offset:0xc0000\n\t" \
    "s_buffer_load_dword s15, s[4:7], s15 offset:0xc0000\n\t" \
    "s_buffer_load_dword s16, s[4:7], s16 offset:0xc0000\n\t" \
    "s_buffer_load_dword s17, s[4:7], s17 offset:0xc0000\n\t" \
    "s_buffer_load_dword s18, s[4:7], s18 offset:0xc0000\n\t" \
    "s_buffer_load_dword s19, s[4:7], s19 offset:0xc0000\n\t" \
    "s_buffer_load_dword s3, s[4:7], s3 offset:0xc0000\n\t" \
    "s_waitcnt vmcnt(4)\n\t" \
    "v_mfma_f32_32x32x16_bf16 v[192:207], v[52:55], v[4:7], 0\n\t" \
    "v_cvt_pk_bf16_f32 v1, v224, v240\n\t" \
    "v_cvt_pk_bf16_f32 v39, v225, v241\n\t" \
    "v_cvt_pk_bf16_f32 v18, v226, v242\n\t" \
    "v_cvt_pk_bf16_f32 v11, v227, v243\n\t" \
    "v_cvt_pk_bf16_f32 v50, v228, v244\n\t" \
    "buffer_load_dwordx4 v[76:79], v36, s[4:7], s50 offen offset:0 sc1\n\t" \
    "ds_write_addtid_b32 v1 offset:0\n\t" \
    "ds_write_addtid_b32 v39 offset:256\n\t" \
    "ds_write_addtid_b32 v18 offset:512\n\t" \
    "ds_write_addtid_b32 v11 offset:768\n\t" \
    "ds_write_addtid_b32 v50 offset:1024\n\t" \
    "s_waitcnt vmcnt(4)\n\t" \
    "v_mfma_f32_32x32x16_bf16 v[192:207], v[56:59], v[60:63], v[192:207]\n\t" \
    "buffer_load_dwordx4 v[80:83], v36, s[4:7], s50 offen offset:1024 sc1\n\t" \
    "v_cvt_pk_bf16_f32 v51, v229, v245\n\t" \
    "v_cvt_pk_bf16_f32 v8, v230, v246\n\t" \
    "v_cvt_pk_bf16_f32 v16, v231, v247\n\t" \
    "v_cvt_pk_bf16_f32 v46, v232, v248\n\t" \
    "v_cvt_pk_bf16_f32 v1, v233, v249\n\t" \
    "s_waitcnt vmcnt(4)\n\t" \
    "v_mfma_f32_32x32x16_bf16 v[208:223], v[52:55], v[64:67], 0\n\t" \
    "buffer_load_dwordx4 v[100:103], v36, s[4:7], s50 offen offset:256 sc1\n\t" \
    "ds_write_addtid_b32 v51 offset:1280\n\t" \
    "ds_write_addtid_b32 v8 offset:1536\n\t" \
    "ds_write_addtid_b32 v16 offset:1792\n\t" \
    "ds_write_addtid_b32 v46 offset:2048\n\t" \
    "ds_write_addtid_b32 v1 offset:2304\n\t" \
    "v_cvt_pk_bf16_f32 v39, v234, v250\n\t" \
    "v_cvt_pk_bf16_f32 v18, v235, v251\n\t" \
    "v_cvt_pk_bf16_f32 v11, v236, v252\n\t" \
    "v_cvt_pk_bf16_f32 v50, v237, v253\n\t" \
    "buffer_load_dwordx4 v[104:107], v36, s[4:7], s50 offen offset:1280 sc1\n\t" \
    "s_waitcnt vmcnt(5)\n\t" \
    "v_mfma_f32_32x32x16_bf16 v[208:223], v[56:59], v[68:71], v[208:223]\n\t" \
    "ds_write_addtid_b32 v39 offset:2560\n\t" \
    "ds_write_addtid_b32 v18 offset:2816\n\t" \
    "ds_write_addtid_b32 v11 offset:3072\n\t" \
    "ds_write_addtid_b32 v50 offset:3328\n\t" \
    "buffer_load_dwordx4 v[88:91], v37, s[4:7], s50 offen offset:0 sc1\n\t" \
    "s_waitcnt lgkmcnt(0)\n\t" \
    "s_xor_b32 s12, s12, s34\n\t" \
    "s_xor_b32 s13, s13, s34\n\t" \
    "s_xor_b32 s14, s14, s34\n\t" \
    "s_xor_b32 s15, s15, s34\n\t" \
    "s_xor_b32 s16, s16, s34\n\t" \
    "s_xor_b32 s17, s17, s34\n\t" \
    "s_xor_b32 s18, s18, s34\n\t" \
    "s_xor_b32 s19, s19, s34\n\t" \
    "s_xor_b32 s3, s3, s34\n\t" \
    "s_or_b32 s12, s12, s13\n\t" \
    "s_or_b32 s12, s12, s14\n\t" \
    "s_or_b32 s12, s12, s15\n\t" \
    "s_or_b32 s12, s12, s16\n\t" \
    "s_or_b32 s12, s12, s17\n\t" \
    "s_or_b32 s12, s12, s18\n\t" \
    "s_or_b32 s12, s12, s19\n\t" \
    "s_or_b32 s12, s12, s3\n\t" \
    "s_cbranch_scc1 .Lq1023_%=\n\t" \
    ".Lb22_%=:\n\t" \
    "s_waitcnt lgkmcnt(0)\n\t" \
    "s_barrier\n\t" \
    "ds_read_b128 v[92:95], v44 offset:0\n\t" \
    "s_mov_b32 s45, 0\n\t" \
    "buffer_load_dwordx4 v[12:15], v17, s[4:7], s45 offen offset:0 sc1\n\t" \
    "ds_read_b128 v[108:111], v44 offset:128\n\t" \
    "ds_read_b128 v[112:115], v44 offset:29184\n\t" \
    "ds_read_b128 v[84:87], v44 offset:29312\n\t" \
    "ds_read_b128 v[96:99], v45 offset:0\n\t" \
    "buffer_load_dwordx4 v[120:123], v17, s[4:7], s45 offen offset:1024 sc1\n\t" \
    "ds_read_b128 v[124:127], v45 offset:128\n\t" \
    "ds_read_b128 v[116:119], v45 offset:29184\n\t" \
    "ds_read_b128 v[128:131], v45 offset:29312\n\t" \
    "v_mfma_f32_32x32x16_bf16 v[224:239], v[56:59], v[4:7], 0\n\t" \
    "v_cvt_pk_bf16_f32 v8, v192, v208\n\t" \
    "v_cvt_pk_bf16_f32 v16, v193, v209\n\t" \
    "v_cvt_pk_bf16_f32 v46, v194, v210\n\t" \
    "v_cvt_pk_bf16_f32 v1, v195, v211\n\t" \
    "v_cvt_pk_bf16_f32 v36, v196, v212\n\t" \
    "s_waitcnt vmcnt(7)\n\t" \
    "v_permlane16_swap_b32 v72, v60\n\t" \
    "v_permlane16_swap_b32 v73, v61\n\t" \
    "s_mov_b32 s46, 114688\n\t" \
    "buffer_load_dwordx4 v[132:135], v17, s[4:7], s46 offen offset:0 sc1\n\t" \
    "ds_write_addtid_b32 v8 offset:61984\n\t" \
    "ds_write_addtid_b32 v16 offset:62240\n\t" \
    "ds_write_addtid_b32 v46 offset:62496\n\t" \
    "ds_write_addtid_b32 v1 offset:62752\n\t" \
    "ds_write_addtid_b32 v36 offset:63008\n\t" \
    "v_permlane16_swap_b32 v74, v62\n\t" \
    "v_permlane16_swap_b32 v75, v63\n\t" \
    "v_cvt_pk_bf16_f32 v39, v197, v213\n\t" \
    "v_cvt_pk_bf16_f32 v18, v198, v214\n\t" \
    "v_cvt_pk_bf16_f32 v11, v199, v215\n\t" \
    "v_cvt_pk_bf16_f32 v50, v200, v216\n\t" \
    "v_cvt_pk_bf16_f32 v37, v201, v217\n\t" \
    "v_mfma_f32_32x32x16_bf16 v[240:255], v[56:59], v[64:67], 0\n\t" \
    "v_permlane16_swap_b32 v60, v68\n\t" \
    "buffer_load_dwordx4 v[28:31], v17, s[4:7], s46 offen offset:1024 sc1\n\t" \
    "ds_write_addtid_b32 v39 offset:63264\n\t" \
    "ds_write_addtid_b32 v18 offset:63520\n\t" \
    "ds_write_addtid_b32 v11 offset:63776\n\t" \
    "ds_write_addtid_b32 v50 offset:64032\n\t" \
    "ds_write_addtid_b32 v37 offset:64288\n\t" \
    "v_permlane16_swap_b32 v61, v69\n\t" \
    "v_permlane16_swap_b32 v62, v70\n\t" \
    "v_cvt_pk_bf16_f32 v3, v202, v218\n\t" \
    "v_cvt_pk_bf16_f32 v51, v203, v219\n\t" \
    "v_cvt_pk_bf16_f32 v8, v204, v220\n\t" \
    "v_cvt_pk_bf16_f32 v16, v205, v221\n\t" \
    "v_permlane16_swap_b32 v63, v71\n\t" \
    "v_mfma_f32_32x32x16_bf16 v[224:239], v[52:55], v[72:75], v[224:239]\n\t" \
    "buffer_load_dwordx4 v[136:139], v19, s[4:7], s45 offen offset:0 sc1\n\t" \
    "ds_write_addtid_b32 v3 offset:64544\n\t" \
    "ds_write_addtid_b32 v51 offset:64800\n\t" \
    "ds_write_addtid_b32 v8 offset:65056\n\t" \
    "ds_write_addtid_b32 v16 offset:65312\n\t" \
    "v_mfma_f32_32x32x16_bf16 v[240:255], v[52:55], v[60:63], v[240:255]\n\t" \
    "s_waitcnt vmcnt(9)\n\t" \
    "v_mfma_f32_32x32x16_bf16 v[192:207], v[52:55], v[76:79], 0\n\t" \
    "s_nop 9\n\t" \
    "v_cvt_pk_bf16_f32 v46, v224, v240\n\t" \
    "v_cvt_pk_bf16_f32 v1, v225, v241\n\t" \
    "v_cvt_pk_bf16_f32 v36, v226, v242\n\t" \
    "v_cvt_pk_bf16_f32 v39, v227, v243\n\t" \
    "v_cvt_pk_bf16_f32 v18, v228, v244\n\t" \
    "buffer_load_dwordx4 v[24:27], v19, s[4:7], s46 offen offset:0 sc1\n\t" \
    "ds_write_addtid_b32 v46 offset:58368\n\t" \
    "ds_write_addtid_b32 v1 offset:58624\n\t" \
    "ds_write_addtid_b32 v36 offset:58880\n\t" \
    "ds_write_addtid_b32 v39 offset:59136\n\t" \
    "ds_write_addtid_b32 v18 offset:59392\n\t" \
    "s_waitcnt vmcnt(9)\n\t" \
    "v_mfma_f32_32x32x16_bf16 v[192:207], v[56:59], v[80:83], v[192:207]\n\t" \
    "v_cvt_pk_bf16_f32 v11, v229, v245\n\t" \
    "v_cvt_pk_bf16_f32 v50, v230, v246\n\t" \
    "v_cvt_pk_bf16_f32 v37, v231, v247\n\t" \
    "v_cvt_pk_bf16_f32 v3, v232, v248\n\t" \
    "v_cvt_pk_bf16_f32 v51, v233, v249\n\t" \
    "s_mov_b32 s47, 229376\n\t" \
    "buffer_load_dwordx4 v[32:35], v17, s[4:7], s47 offen offset:0 sc1\n\t" \
    "s_waitcnt vmcnt(9)\n\t" \
    "v_mfma_f32_32x32x16_bf16 v[208:223], v[52:55], v[100:103], 0\n\t" \
    "ds_write_addtid_b32 v11 offset:59648\n\t" \
    "ds_write_addtid_b32 v50 offset:59904\n\t" \
    "ds_write_addtid_b32 v37 offset:60160\n\t" \
    "ds_write_addtid_b32 v3 offset:60416\n\t" \
    "ds_write_addtid_b32 v51 offset:60672\n\t" \
    "buffer_load_dwordx4 v[140:143], v17, s[4:7], s47 offen offset:1024 sc1\n\t" \
    "v_cvt_pk_bf16_f32 v8, v234, v250\n\t" \
    "v_cvt_pk_bf16_f32 v16, v235, v251\n\t" \
    "v_cvt_pk_bf16_f32 v46, v236, v252\n\t" \
    "v_cvt_pk_bf16_f32 v1, v237, v253\n\t" \
    "s_waitcnt vmcnt(9)\n\t" \
    "v_mfma_f32_32x32x16_bf16 v[208:223], v[56:59], v[104:107], v[208:223]\n\t" \
    "ds_write_addtid_b32 v8 offset:60928\n\t" \
    "ds_write_addtid_b32 v16 offset:61184\n\t" \
    "ds_write_addtid_b32 v46 offset:61440\n\t" \
    "ds_write_addtid_b32 v1 offset:61696\n\t" \
    "buffer_load_dwordx4 v[156:159], v19, s[4:7], s47 offen offset:0 sc1\n\t" \
    "s_waitcnt vmcnt(9)\n\t" \
    "; wait\n\t" \
    "s_waitcnt lgkmcnt(0)\n\t" \
    "s_barrier\n\t" \
    "s_memrealtime s[64:65]\n\t" \
    "s_cmp_lg_u32 s24, 7\n\t" \
    "s_cbranch_scc1 .Lar_%=\n\t" \
    "s_waitcnt lgkmcnt(0)\n\t" \
    "v_mov_b32 v149, s64\n\t" \
    "s_add_u32 s37, s32, 1048576\n\t" \
    "s_mov_b64 exec, 0xff\n\t" \
    "buffer_store_dwordx4 v[148:151], v0, s[4:7], s37 offen sc1\n\t" \
    "s_mov_b64 exec, -1\n\t" \
    ".Lar_%=:\n\t" \
    "v_add_u32 v44, 58368, v44\n\t" \
    "buffer_load_dwordx4 v[168:171], v41, s[4:7], s45 offen offset:1024 sc1\n\t" \
    "v_add_u32 v45, 58368, v45\n\t" \
    "ds_read_b128 v[172:175], v44 offset:0\n\t" \
    "ds_read_b128 v[144:147], v44 offset:128\n\t" \
    "ds_read_b128 v[152:155], v44 offset:29184\n\t" \
    "ds_read_b128 v[160:163], v44 offset:29312\n\t" \
    "buffer_load_dwordx4 v[164:167], v41, s[4:7], s46 offen offset:1024 sc1\n\t" \
    "ds_read_b128 v[176:179], v45 offset:0\n\t" \
    "ds_read_b128 v[184:187], v45 offset:128\n\t" \
    "ds_read_b128 v[188:191], v45 offset:29184\n\t" \
    "ds_read_b128 v[20:23], v45 offset:29312\n\t" \
    "v_mfma_f32_32x32x16_bf16 v[224:239], v[56:59], v[76:79], 0\n\t" \
    "v_cvt_pk_bf16_f32 v36, v192, v208\n\t" \
    "v_cvt_pk_bf16_f32 v39, v193, v209\n\t" \
    "v_cvt_pk_bf16_f32 v18, v194, v210\n\t" \
    "v_cvt_pk_bf16_f32 v11, v195, v211\n\t" \
    "v_cvt_pk_bf16_f32 v50, v196, v212\n\t" \
    "s_waitcnt vmcnt(10)\n\t" \
    "v_mfma_f32_16x16x32_bf16 v[180:183], v[12:15], v[92:95], 0\n\t" \
    "v_permlane16_swap_b32 v88, v80\n\t" \
    "buffer_load_dwordx4 v[4:7], v41, s[4:7], s47 offen offset:1024 sc1\n\t" \
    "v_permlane16_swap_b32 v89, v81\n\t" \
    "ds_write_addtid_b32 v36 offset:3616\n\t" \
    "ds_write_addtid_b32 v39 offset:3872\n\t" \
    "ds_write_addtid_b32 v18 offset:4128\n\t" \
    "ds_write_addtid_b32 v11 offset:4384\n\t" \
    "ds_write_addtid_b32 v50 offset:4640\n\t" \
    "v_mfma_f32_16x16x32_bf16 v[64:67], v[12:15], v[108:111], 0\n\t" \
    "v_permlane16_swap_b32 v90, v82\n\t" \
    "v_mfma_f32_16x16x32_bf16 v[68:71], v[12:15], v[112:115], 0\n\t" \
    "v_permlane16_swap_b32 v91, v83\n\t" \
    "v_cvt_pk_bf16_f32 v37, v197, v213\n\t" \
    "v_cvt_pk_bf16_f32 v3, v198, v214\n\t" \
    "v_cvt_pk_bf16_f32 v51, v199, v215\n\t" \
    "v_cvt_pk_bf16_f32 v8, v200, v216\n\t" \
    "v_cvt_pk_bf16_f32 v16, v201, v217\n\t" \
    "s_mov_b32 s45, 344064\n\t" \
    "buffer_load_dwordx4 v[72:75], v17, s[4:7], s45 offen offset:0 sc1\n\t" \
    "v_mfma_f32_32x32x16_bf16 v[240:255], v[56:59], v[100:103], 0\n\t" \
    "v_mfma_f32_16x16x32_bf16 v[60:63], v[12:15], v[84:87], 0\n\t" \
    "v_permlane16_swap_b32 v80, v104\n\t" \
    "ds_write_addtid_b32 v37 offset:4896\n\t" \
    "ds_write_addtid_b32 v3 offset:5152\n\t" \
    "ds_write_addtid_b32 v51 offset:5408\n\t" \
    "ds_write_addtid_b32 v8 offset:5664\n\t" \
    "ds_write_addtid_b32 v16 offset:5920\n\t" \
    "s_waitcnt vmcnt(11)\n\t" \
    "v_mfma_f32_16x16x32_bf16 v[148:151], v[120:123], v[108:111], 0\n\t" \
    "v_permlane16_swap_b32 v81, v105\n\t" \
    "buffer_load_dwordx4 v[76:79], v17, s[4:7], s45 offen offset:1024 sc1\n\t" \
    "v_permlane16_swap_b32 v82, v106\n\t" \
    "v_cvt_pk_bf16_f32 v46, v202, v218\n\t" \
    "v_cvt_pk_bf16_f32 v1, v203, v219\n\t" \
    "v_cvt_pk_bf16_f32 v36, v204, v220\n\t" \
    "v_cvt_pk_bf16_f32 v39, v205, v221\n\t" \
    "v_mfma_f32_16x16x32_bf16 v[64:67], v[120:123], v[92:95], v[64:67]\n\t" \
    "v_permlane16_swap_b32 v83, v107\n\t" \
    "v_mfma_f32_16x16x32_bf16 v[56:59], v[120:123], v[84:87], 0\n\t" \
    "v_mfma_f32_32x32x16_bf16 v[224:239], v[52:55], v[88:91], v[224:239]\n\t" \
    "ds_write_addtid_b32 v46 offset:6176\n\t" \
    "ds_write_addtid_b32 v1 offset:6432\n\t" \
    "ds_write_addtid_b32 v36 offset:6688\n\t" \
    "ds_write_addtid_b32 v39 offset:6944\n\t" \
    "s_mov_b32 s46, 458752\n\t" \
    "buffer_load_dwordx4 v[100:103], v17, s[4:7], s46 offen offset:0 sc1\n\t" \
    "v_mfma_f32_32x32x16_bf16 v[240:255], v[52:55], v[80:83], v[240:255]\n\t" \
    "v_mfma_f32_16x16x32_bf16 v[60:63], v[120:123], v[112:115], v[60:63]\n\t" \
    "s_nop 10\n\t" \
    "v_cvt_pk_bf16_f32 v18, v224, v240\n\t" \
    "v_cvt_pk_bf16_f32 v11, v225, v241\n\t" \
    "v_cvt_pk_bf16_f32 v50, v226, v242\n\t" \
    "v_cvt_pk_bf16_f32 v37, v227, v243\n\t" \
    "v_cvt_pk_bf16_f32 v3, v228, v244\n\t" \
    "s_waitcnt vmcnt(12) lgkmcnt(15)\n\t" \
    "v_mfma_f32_16x16x32_bf16 v[180:183], v[132:135], v[172:175], v[180:183]\n\t" \
    "buffer_load_dwordx4 v[12:15], v17, s[4:7], s46 offen offset:1024 sc1\n\t" \
    "v_mfma_f32_16x16x32_bf16 v[64:67], v[132:135], v[144:147], v[64:67]\n\t" \
    "ds_write_addtid_b32 v18 offset:0\n\t" \
    "ds_write_addtid_b32 v11 offset:256\n\t" \
    "ds_write_addtid_b32 v50 offset:512\n\t" \
    "ds_write_addtid_b32 v37 offset:768\n\t" \
    "ds_write_addtid_b32 v3 offset:1024\n\t" \
    "v_mfma_f32_16x16x32_bf16 v[68:71], v[132:135], v[152:155], v[68:71]\n\t" \
    "v_cvt_pk_bf16_f32 v51, v229, v245\n\t" \
    "v_cvt_pk_bf16_f32 v8, v230, v246\n\t" \
    "v_cvt_pk_bf16_f32 v16, v231, v247\n\t" \
    "v_cvt_pk_bf16_f32 v46, v232, v248\n\t" \
    "v_cvt_pk_bf16_f32 v1, v233, v249\n\t" \
    "s_mov_b32 s47, 573440\n\t" \
    "buffer_load_dwordx4 v[192:195], v17, s[4:7], s47 offen offset:0 sc1\n\t" \
    "v_mfma_f32_16x16x32_bf16 v[60:63], v[132:135], v[160:163], v[60:63]\n\t" \
    "ds_write_addtid_b32 v51 offset:1280\n\t" \
    "ds_write_addtid_b32 v8 offset:1536\n\t" \
    "ds_write_addtid_b32 v16 offset:1792\n\t" \
    "ds_write_addtid_b32 v46 offset:2048\n\t" \
    "ds_write_addtid_b32 v1 offset:2304\n\t" \
    "s_waitcnt vmcnt(13)\n\t" \
    "v_mfma_f32_16x16x32_bf16 v[148:151], v[28:31], v[144:147], v[148:151]\n\t" \
    "buffer_load_dwordx4 v[196:199], v17, s[4:7], s47 offen offset:1024 sc1\n\t" \
    "v_mfma_f32_16x16x32_bf16 v[64:67], v[28:31], v[172:175], v[64:67]\n\t" \
    "v_cvt_pk_bf16_f32 v36, v234, v250\n\t" \
    "v_cvt_pk_bf16_f32 v39, v235, v251\n\t" \
    "v_cvt_pk_bf16_f32 v18, v236, v252\n\t" \
    "v_cvt_pk_bf16_f32 v11, v237, v253\n\t" \
    "v_mfma_f32_16x16x32_bf16 v[56:59], v[28:31], v[160:163], v[56:59]\n\t" \
    "ds_write_addtid_b32 v36 offset:2560\n\t" \
    "ds_write_addtid_b32 v39 offset:2816\n\t" \
    "ds_write_addtid_b32 v18 offset:3072\n\t" \
    "ds_write_addtid_b32 v11 offset:3328\n\t" \
    "buffer_load_dwordx4 v[200:203], v19, s[4:7], s45 offen offset:0 sc1\n\t" \
    "v_mfma_f32_16x16x32_bf16 v[60:63], v[28:31], v[152:155], v[60:63]\n\t" \
    "s_waitcnt lgkmcnt(0)\n\t" \
    "s_barrier\n\t" \
    "ds_read_b128 v[204:207], v10 offset:2048\n\t" \
    "v_mov_b32 v50, 0x10000\n\t" \
    "s_waitcnt vmcnt(14)\n\t" \
    "v_mfma_f32_16x16x32_bf16 v[208:211], v[136:139], v[96:99], 0\n\t" \
    "v_subrev_u32 v44, 58368, v44\n\t" \
    "v_mfma_f32_16x16x32_bf16 v[212:215], v[136:139], v[124:127], 0\n\t" \
    "v_subrev_u32 v45, 58368, v45\n\t" \
    "v_mfma_f32_16x16x32_bf16 v[216:219], v[136:139], v[116:119], 0\n\t" \
    "ds_read_b128 v[220:223], v44 offset:0\n\t" \
    "buffer_load_dwordx4 v[104:107], v19, s[4:7], s46 offen offset:0 sc1\n\t" \
    "v_mfma_f32_16x16x32_bf16 v[88:91], v[136:139], v[128:131], 0\n\t" \
    "ds_read_b128 v[52:55], v44 offset:128\n\t" \
    "s_waitcnt vmcnt(14)\n\t" \
    "v_mfma_f32_16x16x32_bf16 v[208:211], v[24:27], v[176:179], v[208:211]\n\t" \
    "ds_read_b128 v[80:83], v44 offset:29184\n\t" \
    "v_mfma_f32_16x16x32_bf16 v[212:215], v[24:27], v[184:187], v[212:215]\n\t" \
    "ds_read_b128 v[120:123], v44 offset:29312\n\t" \
    "buffer_load_dwordx4 v[132:135], v19, s[4:7], s47 offen offset:0 sc1\n\t" \
    "v_mfma_f32_16x16x32_bf16 v[216:219], v[24:27], v[188:191], v[216:219]\n\t" \
    "ds_read_b128 v[224:227], v45 offset:0\n\t" \
    "v_mfma_f32_16x16x32_bf16 v[88:91], v[24:27], v[20:23], v[88:91]\n\t" \
    "ds_read_b128 v[228:231], v45 offset:128\n\t" \
    "s_waitcnt vmcnt(14) lgkmcnt(5)\n\t" \
    "v_mfma_f32_16x16x32_bf16 v[180:183], v[32:35], v[220:223], v[180:183]\n\t" \
    "ds_read_b128 v[232:235], v45 offset:29184\n\t" \
    "buffer_load_dwordx4 v[236:239], v41, s[4:7], s45 offen offset:1024 sc1\n\t" \
    "s_waitcnt lgkmcnt(5)\n\t" \
    "v_mfma_f32_16x16x32_bf16 v[64:67], v[32:35], v[52:55], v[64:67]\n\t" \
    "ds_read_b128 v[240:243], v45 offset:29312\n\t" \
    "s_waitcnt lgkmcnt(5)\n\t" \
    "v_mfma_f32_16x16x32_bf16 v[68:71], v[32:35], v[80:83], v[68:71]\n\t" \
    "s_waitcnt lgkmcnt(4)\n\t" \
    "v_mfma_f32_16x16x32_bf16 v[60:63], v[32:35], v[120:123], v[60:63]\n\t" \
    "buffer_load_dwordx4 v[244:247], v41, s[4:7], s46 offen offset:1024 sc1\n\t" \
    "s_waitcnt vmcnt(15)\n\t" \
    "v_mfma_f32_16x16x32_bf16 v[148:151], v[140:143], v[52:55], v[148:151]\n\t" \
    "s_waitcnt lgkmcnt(0)\n\t" \
    "s_add_u32 s12, s55, 34603008\n\t" \
    "s_buffer_load_dword s13, s[4:7], s12\n\t" \
    "s_buffer_load_dword s14, s[4:7], s12 offset:0x400\n\t" \
    "v_mfma_f32_16x16x32_bf16 v[64:67], v[140:143], v[220:223], v[64:67]\n\t" \
    "v_mfma_f32_16x16x32_bf16 v[56:59], v[140:143], v[120:123], v[56:59]\n\t" \
    "buffer_load_dwordx4 v[248:251], v41, s[4:7], s47 offen offset:1024 sc1\n\t" \
    "v_mfma_f32_16x16x32_bf16 v[60:63], v[140:143], v[80:83], v[60:63]\n\t" \
    "s_waitcnt vmcnt(15)\n\t" \
    "v_mfma_f32_16x16x32_bf16 v[208:211], v[156:159], v[224:227], v[208:211]\n\t" \
    "v_mfma_f32_16x16x32_bf16 v[212:215], v[156:159], v[228:231], v[212:215]\n\t" \
    "s_mov_b32 s45, 688128\n\t" \
    "buffer_load_dwordx4 v[252:255], v17, s[4:7], s45 offen offset:0 sc1\n\t" \
    "v_mfma_f32_16x16x32_bf16 v[216:219], v[156:159], v[232:235], v[216:219]\n\t" \
    "v_sub_f32 v180, v180, v148\n\t" \
    "v_sub_f32 v181, v181, v149\n\t" \
    "v_sub_f32 v182, v182, v150\n\t" \
    "v_mfma_f32_16x16x32_bf16 v[88:91], v[156:159], v[240:243], v[88:91]\n\t" \
    "v_sub_f32 v183, v183, v151\n\t" \
    "v_sub_f32 v68, v68, v56\n\t" \
    "v_sub_f32 v69, v69, v57\n\t" \
    "v_sub_f32 v70, v70, v58\n\t" \
    "v_sub_f32 v71, v71, v59\n\t" \
    "v_cvt_pk_bf16_f32 v30, v64, v65\n\t" \
    "v_cvt_pk_bf16_f32 v31, v66, v67\n\t" \
    "v_cvt_pk_bf16_f32 v28, v180, v181\n\t" \
    "v_cvt_pk_bf16_f32 v29, v182, v183\n\t" \
    "v_cvt_pk_bf16_f32 v138, v60, v61\n\t" \
    "v_cvt_pk_bf16_f32 v139, v62, v63\n\t" \
    "ds_write_b128 v40, v[28:31] offset:58368\n\t" \
    "v_cvt_pk_bf16_f32 v136, v68, v69\n\t" \
    "v_cvt_pk_bf16_f32 v137, v70, v71\n\t" \
    "ds_write_b128 v40, v[136:139] offset:59392\n\t" \
    "s_waitcnt vmcnt(13)\n\t" \
    "s_cmp_ge_u32 s24, 4\n\t" \
    "s_cbranch_scc1 .Lk42_%=\n\t" \
    "v_mfma_f32_16x16x32_bf16 v[24:27], v[168:171], v[124:127], 0\n\t" \
    "v_mfma_f32_16x16x32_bf16 v[24:27], v[164:167], v[184:187], v[24:27]\n\t" \
    "v_mfma_f32_16x16x32_bf16 v[24:27], v[4:7], v[228:231], v[24:27]\n\t" \
    "v_mfma_f32_16x16x32_bf16 v[32:35], v[168:171], v[128:131], 0\n\t" \
    "v_mfma_f32_16x16x32_bf16 v[32:35], v[164:167], v[20:23], v[32:35]\n\t" \
    "v_mfma_f32_16x16x32_bf16 v[32:35], v[4:7], v[240:243], v[32:35]\n\t" \
    "v_mfma_f32_16x16x32_bf16 v[212:215], v[168:171], v[96:99], v[212:215]\n\t" \
    "v_mfma_f32_16x16x32_bf16 v[212:215], v[164:167], v[176:179], v[212:215]\n\t" \
    "v_mfma_f32_16x16x32_bf16 v[212:215], v[4:7], v[224:227], v[212:215]\n\t" \
    "v_mfma_f32_16x16x32_bf16 v[88:91], v[168:171], v[116:119], v[88:91]\n\t" \
    "s_nop 0\n\t" \
    "v_sub_f32 v208, v208, v24\n\t" \
    "v_sub_f32 v209, v209, v25\n\t" \
    "v_mfma_f32_16x16x32_bf16 v[88:91], v[164:167], v[188:191], v[88:91]\n\t" \
    "v_sub_f32 v210, v210, v26\n\t" \
    "v_sub_f32 v211, v211, v27\n\t" \
    "v_mfma_f32_16x16x32_bf16 v[88:91], v[4:7], v[232:235], v[88:91]\n\t" \
    "v_sub_f32 v216, v216, v32\n\t" \
    "v_sub_f32 v217, v217, v33\n\t" \
    "v_sub_f32 v218, v218, v34\n\t" \
    "v_sub_f32 v219, v219, v35\n\t" \
    ".Lk42_%=:\n\t" \
    "s_waitcnt vmcnt(12)\n\t" \
    "v_mfma_f32_16x16x32_bf16 v[140:143], v[72:75], v[92:95], 0\n\t" \
    "buffer_load_dwordx4 v[156:159], v17, s[4:7], s45 offen offset:1024 sc1\n\t" \
    "v_mfma_f32_16x16x32_bf16 v[148:151], v[72:75], v[108:111], 0\n\t" \
    "v_cvt_pk_bf16_f32 v58, v212, v213\n\t" \
    "v_cvt_pk_bf16_f32 v59, v214, v215\n\t" \
    "v_mfma_f32_16x16x32_bf16 v[64:67], v[72:75], v[112:115], 0\n\t" \
    "v_cvt_pk_bf16_f32 v56, v208, v209\n\t" \
    "v_cvt_pk_bf16_f32 v57, v210, v211\n\t" \
    "v_mfma_f32_16x16x32_bf16 v[180:183], v[72:75], v[84:87], 0\n\t" \
    "v_cvt_pk_bf16_f32 v62, v88, v89\n\t" \
    "v_cvt_pk_bf16_f32 v63, v90, v91\n\t" \
    "s_mov_b32 s46, 802816\n\t" \
    "buffer_load_dwordx4 v[28:31], v17, s[4:7], s46 offen offset:0 sc1\n\t" \
    "s_waitcnt vmcnt(13)\n\t" \
    "v_mfma_f32_16x16x32_bf16 v[68:71], v[76:79], v[108:111], 0\n\t" \
    "ds_write_b128 v40, v[56:59] offset:60432\n\t" \
    "v_cvt_pk_bf16_f32 v60, v216, v217\n\t" \
    "v_mfma_f32_16x16x32_bf16 v[148:151], v[76:79], v[92:95], v[148:151]\n\t" \
    "v_cvt_pk_bf16_f32 v61, v218, v219\n\t" \
    "v_mfma_f32_16x16x32_bf16 v[136:139], v[76:79], v[84:87], 0\n\t" \
    "ds_write_b128 v40, v[60:63] offset:61456\n\t" \
    "buffer_load_dwordx4 v[168:171], v17, s[4:7], s46 offen offset:1024 sc1\n\t" \
    "v_mfma_f32_16x16x32_bf16 v[180:183], v[76:79], v[112:115], v[180:183]\n\t" \
    "s_waitcnt vmcnt(13)\n\t" \
    "v_mfma_f32_16x16x32_bf16 v[140:143], v[100:103], v[172:175], v[140:143]\n\t" \
    "v_mfma_f32_16x16x32_bf16 v[148:151], v[100:103], v[144:147], v[148:151]\n\t" \
    "s_waitcnt lgkmcnt(0)\n\t" \
    "s_xor_b32 s13, s13, s34\n\t" \
    "s_xor_b32 s14, s14, s34\n\t" \
    "s_or_b32 s13, s13, s14\n\t" \
    "s_cbranch_scc1 .Lq1026_%=\n\t" \
    ".Lb25_%=:\n\t" \
    "s_waitcnt lgkmcnt(0)\n\t" \
    "s_barrier\n\t" \
    "s_memrealtime s[16:17]\n\t" \
    "ds_read_b64_tr_b16 v[164:165], v49 offset:58368\n\t" \
    "ds_read_b64_tr_b16 v[166:167], v49 offset:62496\n\t" \
    "s_mov_b32 s47, 917504\n\t" \
    "buffer_load_dwordx4 v[24:27], v17, s[4:7], s47 offen offset:0 sc1\n\t" \
    "v_mfma_f32_16x16x32_bf16 v[64:67], v[100:103], v[152:155], v[64:67]\n\t" \
    "ds_read_b64_tr_b16 v[4:5], v49 offset:58624\n\t" \
    "ds_read_b64_tr_b16 v[6:7], v49 offset:62752\n\t" \
    "v_mfma_f32_16x16x32_bf16 v[180:183], v[100:103], v[160:163], v[180:183]\n\t" \
    "ds_read_b64_tr_b16 v[32:33], v49 offset:58384\n\t" \
    "ds_read_b64_tr_b16 v[34:35], v49 offset:62512\n\t" \
    "s_waitcnt vmcnt(13)\n\t" \
    "v_mfma_f32_16x16x32_bf16 v[68:71], v[12:15], v[144:147], v[68:71]\n\t" \
    "ds_read_b64_tr_b16 v[212:213], v49 offset:58640\n\t" \
    "ds_read_b64_tr_b16 v[214:215], v49 offset:62768\n\t" \
    "buffer_load_dwordx4 v[208:211], v17, s[4:7], s47 offen offset:1024 sc1\n\t" \
    "v_mfma_f32_16x16x32_bf16 v[148:151], v[12:15], v[172:175], v[148:151]\n\t" \
    "v_mfma_f32_16x16x32_bf16 v[136:139], v[12:15], v[160:163], v[136:139]\n\t" \
    "v_mfma_f32_16x16x32_bf16 v[180:183], v[12:15], v[152:155], v[180:183]\n\t" \
    "s_waitcnt lgkmcnt(6)\n\t" \
    "v_mfma_f32_16x16x32_bf16 v[72:75], v[164:167], v[204:207], 0\n\t" \
    "s_waitcnt lgkmcnt(4)\n\t" \
    "v_mfma_f32_16x16x32_bf16 v[88:91], v[4:7], v[204:207], 0\n\t" \
    "buffer_load_dwordx4 v[56:59], v19, s[4:7], s45 offen offset:0 sc1\n\t" \
    "s_waitcnt vmcnt(14)\n\t" \
    "v_mfma_f32_16x16x32_bf16 v[140:143], v[192:195], v[220:223], v[140:143]\n\t" \
    "s_waitcnt lgkmcnt(2)\n\t" \
    "v_mfma_f32_16x16x32_bf16 v[216:219], v[32:35], v[204:207], 0\n\t" \
    "s_waitcnt lgkmcnt(0)\n\t" \
    "v_mfma_f32_16x16x32_bf16 v[60:63], v[212:215], v[204:207], 0\n\t" \
    "v_mfma_f32_16x16x32_bf16 v[148:151], v[192:195], v[52:55], v[148:151]\n\t" \
    "s_cmp_lg_u32 s24, 4\n\t" \
    "s_cbranch_scc1 .Lph_%=\n\t" \
    "s_waitcnt lgkmcnt(0)\n\t" \
    "v_mov_b32 v76, s16\n\t" \
    "v_mov_b32 v77, s16\n\t" \
    "v_mov_b32 v78, s16\n\t" \
    "v_mov_b32 v79, s16\n\t" \
    "buffer_store_dwordx4 v[76:79], v2, s[4:7], s32 offen sc1\n\t" \
    "s_nop 0\n\t" \
    ".Lph_%=:\n\t" \
    "v_mfma_f32_16x16x32_bf16 v[64:67], v[192:195], v[80:83], v[64:67]\n\t" \
    "buffer_load_dwordx4 v[44:47], v19, s[4:7], s46 offen offset:0 sc1\n\t" \
    "v_mfma_f32_16x16x32_bf16 v[180:183], v[192:195], v[120:123], v[180:183]\n\t" \
    "v_bfi_b32 v72, s41, v50, v72\n\t" \
    "v_bfi_b32 v88, s41, v50, v88\n\t" \
    "s_waitcnt vmcnt(14)\n\t" \
    "v_mfma_f32_16x16x32_bf16 v[68:71], v[196:199], v[52:55], v[68:71]\n\t" \
    "v_cvt_pk_bf16_f32 v101, v74, v75\n\t" \
    "v_cvt_pk_bf16_f32 v103, v90, v91\n\t" \
    "v_mfma_f32_16x16x32_bf16 v[148:151], v[196:199], v[220:223], v[148:151]\n\t" \
    "v_cvt_pk_bf16_f32 v100, v72, v73\n\t" \
    "v_cvt_pk_bf16_f32 v102, v88, v89\n\t" \
    "v_mfma_f32_16x16x32_bf16 v[136:139], v[196:199], v[120:123], v[136:139]\n\t" \
    "v_bfi_b32 v216, s41, v50, v216\n\t" \
    "v_bfi_b32 v60, s41, v50, v60\n\t" \
    "v_mfma_f32_16x16x32_bf16 v[180:183], v[196:199], v[80:83], v[180:183]\n\t" \
    "buffer_store_dwordx4 v[100:103], v42, s[4:7], s48 offen offset:0 sc1\n\t" \
    "v_cvt_pk_bf16_f32 v13, v218, v219\n\t" \
    "s_waitcnt vmcnt(14)\n\t" \
    "v_mfma_f32_16x16x32_bf16 v[164:167], v[200:203], v[96:99], 0\n\t" \
    "v_cvt_pk_bf16_f32 v15, v62, v63\n\t" \
    "v_cvt_pk_bf16_f32 v12, v216, v217\n\t" \
    "v_mfma_f32_16x16x32_bf16 v[4:7], v[200:203], v[124:127], 0\n\t" \
    "v_cvt_pk_bf16_f32 v14, v60, v61\n\t" \
    "buffer_store_dwordx4 v[12:15], v9, s[4:7], s48 offen offset:128 sc1\n\t" \
    "v_mfma_f32_16x16x32_bf16 v[32:35], v[200:203], v[116:119], 0\n\t" \
    "v_sub_f32 v140, v140, v68\n\t" \
    "v_sub_f32 v141, v141, v69\n\t" \
    "v_mfma_f32_16x16x32_bf16 v[212:215], v[200:203], v[128:131], 0\n\t" \
    "v_sub_f32 v142, v142, v70\n\t" \
    "v_sub_f32 v143, v143, v71\n\t" \
    "s_waitcnt vmcnt(14)\n\t" \
    "v_mfma_f32_16x16x32_bf16 v[164:167], v[104:107], v[176:179], v[164:167]\n\t" \
    "v_sub_f32 v64, v64, v136\n\t" \
    "v_sub_f32 v65, v65, v137\n\t" \
    "v_mfma_f32_16x16x32_bf16 v[4:7], v[104:107], v[184:187], v[4:7]\n\t" \
    "v_sub_f32 v66, v66, v138\n\t" \
    "v_sub_f32 v67, v67, v139\n\t" \
    "v_mfma_f32_16x16x32_bf16 v[32:35], v[104:107], v[188:191], v[32:35]\n\t" \
    "v_cvt_pk_bf16_f32 v78, v148, v149\n\t" \
    "v_cvt_pk_bf16_f32 v79, v150, v151\n\t" \
    "v_mfma_f32_16x16x32_bf16 v[212:215], v[104:107], v[20:23], v[212:215]\n\t" \
    "v_cvt_pk_bf16_f32 v76, v140, v141\n\t" \
    "v_cvt_pk_bf16_f32 v77, v142, v143\n\t" \
    "s_waitcnt vmcnt(13)\n\t" \
    "v_mfma_f32_16x16x32_bf16 v[164:167], v[132:135], v[224:227], v[164:167]\n\t" \
    "v_cvt_pk_bf16_f32 v194, v180, v181\n\t" \
    "v_cvt_pk_bf16_f32 v195, v182, v183\n\t" \
    "v_mfma_f32_16x16x32_bf16 v[4:7], v[132:135], v[228:231], v[4:7]\n\t" \
    "ds_write_b128 v40, v[76:79] offset:0\n\t" \
    "v_cvt_pk_bf16_f32 v192, v64, v65\n\t" \
    "buffer_load_dwordx4 v[72:75], v19, s[4:7], s47 offen offset:0 sc1\n\t" \
    "v_mfma_f32_16x16x32_bf16 v[32:35], v[132:135], v[232:235], v[32:35]\n\t" \
    "v_cvt_pk_bf16_f32 v193, v66, v67\n\t" \
    "ds_write_b128 v40, v[192:195] offset:1024\n\t" \
    "v_mfma_f32_16x16x32_bf16 v[212:215], v[132:135], v[240:243], v[212:215]\n\t" \
    "s_waitcnt vmcnt(11)\n\t" \
    "s_cmp_ge_u32 s24, 4\n\t" \
    "s_cbranch_scc1 .Lk43_%=\n\t" \
    "v_mfma_f32_16x16x32_bf16 v[88:91], v[236:239], v[124:127], 0\n\t" \
    "v_mfma_f32_16x16x32_bf16 v[88:91], v[244:247], v[184:187], v[88:91]\n\t" \
    "v_mfma_f32_16x16x32_bf16 v[88:91], v[248:251], v[228:231], v[88:91]\n\t" \
    "v_mfma_f32_16x16x32_bf16 v[196:199], v[236:239], v[128:131], 0\n\t" \
    "v_mfma_f32_16x16x32_bf16 v[196:199], v[244:247], v[20:23], v[196:199]\n\t" \
    "v_mfma_f32_16x16x32_bf16 v[196:199], v[248:251], v[240:243], v[196:199]\n\t" \
    "v_mfma_f32_16x16x32_bf16 v[4:7], v[236:239], v[96:99], v[4:7]\n\t" \
    "v_mfma_f32_16x16x32_bf16 v[4:7], v[244:247], v[176:179], v[4:7]\n\t" \
    "v_mfma_f32_16x16x32_bf16 v[4:7], v[248:251], v[224:227], v[4:7]\n\t" \
    "v_mfma_f32_16x16x32_bf16 v[212:215], v[236:239], v[116:119], v[212:215]\n\t" \
    "s_nop 0\n\t" \
    "v_sub_f32 v164, v164, v88\n\t" \
    "v_sub_f32 v165, v165, v89\n\t" \
    "v_mfma_f32_16x16x32_bf16 v[212:215], v[244:247], v[188:191], v[212:215]\n\t" \
    "v_sub_f32 v166, v166, v90\n\t" \
    "v_sub_f32 v167, v167, v91\n\t" \
    "v_mfma_f32_16x16x32_bf16 v[212:215], v[248:251], v[232:235], v[212:215]\n\t" \
    "v_sub_f32 v32, v32, v196\n\t" \
    "v_sub_f32 v33, v33, v197\n\t" \
    "v_sub_f32 v34, v34, v198\n\t" \
    "v_sub_f32 v35, v35, v199\n\t" \
    ".Lk43_%=:\n\t" \
    "s_waitcnt vmcnt(10)\n\t" \
    "v_mfma_f32_16x16x32_bf16 v[100:103], v[252:255], v[92:95], 0\n\t" \
    "buffer_load_dwordx4 v[216:219], v41, s[4:7], s45 offen offset:1024 sc1\n\t" \
    "v_mfma_f32_16x16x32_bf16 v[60:63], v[252:255], v[108:111], 0\n\t" \
    "v_cvt_pk_bf16_f32 v14, v4, v5\n\t" \
    "v_cvt_pk_bf16_f32 v15, v6, v7\n\t" \
    "v_mfma_f32_16x16x32_bf16 v[200:203], v[252:255], v[112:115], 0\n\t" \
    "v_cvt_pk_bf16_f32 v12, v164, v165\n\t" \
    "v_cvt_pk_bf16_f32 v13, v166, v167\n\t" \
    "v_mfma_f32_16x16x32_bf16 v[68:71], v[252:255], v[84:87], 0\n\t" \
    "v_cvt_pk_bf16_f32 v138, v212, v213\n\t" \
    "v_cvt_pk_bf16_f32 v139, v214, v215\n\t" \
    "buffer_load_dwordx4 v[148:151], v41, s[4:7], s46 offen offset:1024 sc1\n\t" \
    "s_waitcnt vmcnt(11)\n\t" \
    "v_mfma_f32_16x16x32_bf16 v[104:107], v[156:159], v[108:111], 0\n\t" \
    "ds_write_b128 v40, v[12:15] offset:2064\n\t" \
    "v_cvt_pk_bf16_f32 v136, v32, v33\n\t" \
    "v_mfma_f32_16x16x32_bf16 v[60:63], v[156:159], v[92:95], v[60:63]\n\t" \
    "v_cvt_pk_bf16_f32 v137, v34, v35\n\t" \
    "v_mfma_f32_16x16x32_bf16 v[140:143], v[156:159], v[84:87], 0\n\t" \
    "ds_write_b128 v40, v[136:139] offset:3088\n\t" \
    "buffer_load_dwordx4 v[180:183], v41, s[4:7], s47 offen offset:1024 sc1\n\t" \
    "v_mfma_f32_16x16x32_bf16 v[68:71], v[156:159], v[112:115], v[68:71]\n\t" \
    "s_waitcnt vmcnt(11)\n\t" \
    "v_mfma_f32_16x16x32_bf16 v[100:103], v[28:31], v[172:175], v[100:103]\n\t" \
    "v_mfma_f32_16x16x32_bf16 v[60:63], v[28:31], v[144:147], v[60:63]\n\t" \
    "s_waitcnt lgkmcnt(0)\n\t" \
    "s_barrier\n\t" \
    "s_memrealtime s[18:19]\n\t" \
    "s_setprio 2\n\t" \
    "ds_read_b64_tr_b16 v[76:77], v49 offset:0\n\t" \
    "ds_read_b64_tr_b16 v[78:79], v49 offset:4128\n\t" \
    "v_mfma_f32_16x16x32_bf16 v[200:203], v[28:31], v[152:155], v[200:203]\n\t" \
    "ds_read_b64_tr_b16 v[16:17], v49 offset:256\n\t" \
    "ds_read_b64_tr_b16 v[18:19], v49 offset:4384\n\t" \
    "v_mfma_f32_16x16x32_bf16 v[68:71], v[28:31], v[160:163], v[68:71]\n\t" \
    "ds_read_b64_tr_b16 v[64:65], v49 offset:16\n\t" \
    "ds_read_b64_tr_b16 v[66:67], v49 offset:4144\n\t" \
    "s_waitcnt vmcnt(10)\n\t" \
    "v_mfma_f32_16x16x32_bf16 v[104:107], v[168:171], v[144:147], v[104:107]\n\t" \
    "ds_read_b64_tr_b16 v[192:193], v49 offset:272\n\t" \
    "ds_read_b64_tr_b16 v[194:195], v49 offset:4400\n\t" \
    "v_mfma_f32_16x16x32_bf16 v[60:63], v[168:171], v[172:175], v[60:63]\n\t" \
    "v_mfma_f32_16x16x32_bf16 v[140:143], v[168:171], v[160:163], v[140:143]\n\t" \
    "v_mfma_f32_16x16x32_bf16 v[68:71], v[168:171], v[152:155], v[68:71]\n\t" \
    "s_waitcnt lgkmcnt(6)\n\t" \
    "v_mfma_f32_16x16x32_bf16 v[132:135], v[76:79], v[204:207], 0\n\t" \
    "s_waitcnt lgkmcnt(4)\n\t" \
    "v_mfma_f32_16x16x32_bf16 v[236:239], v[16:19], v[204:207], 0\n\t" \
    "s_waitcnt vmcnt(9)\n\t" \
    "v_mfma_f32_16x16x32_bf16 v[100:103], v[24:27], v[220:223], v[100:103]\n\t" \
    "s_waitcnt lgkmcnt(2)\n\t" \
    "v_mfma_f32_16x16x32_bf16 v[244:247], v[64:67], v[204:207], 0\n\t" \
    "s_waitcnt lgkmcnt(0)\n\t" \
    "v_mfma_f32_16x16x32_bf16 v[88:91], v[192:195], v[204:207], 0\n\t" \
    "v_mfma_f32_16x16x32_bf16 v[60:63], v[24:27], v[52:55], v[60:63]\n\t" \
    "v_mfma_f32_16x16x32_bf16 v[200:203], v[24:27], v[80:83], v[200:203]\n\t" \
    "s_add_u32 s37, s52, s33\n\t" \
    "s_buffer_load_dword s58, s[4:7], s37 offset:0x0\n\t" \
    "s_buffer_load_dword s59, s[4:7], s37 offset:0x400\n\t" \
    "s_buffer_load_dword s60, s[4:7], s37 offset:0x4000\n\t" \
    "s_buffer_load_dword s61, s[4:7], s37 offset:0x4400\n\t" \
    "v_mfma_f32_16x16x32_bf16 v[68:71], v[24:27], v[120:123], v[68:71]\n\t" \
    "v_bfi_b32 v132, s41, v50, v132\n\t" \
    "v_bfi_b32 v236, s41, v50, v236\n\t" \
    "s_waitcnt vmcnt(8)\n\t" \
    "v_mfma_f32_16x16x32_bf16 v[104:107], v[208:211], v[52:55], v[104:107]\n\t" \
    "v_cvt_pk_bf16_f32 v249, v134, v135\n\t" \
    "v_cvt_pk_bf16_f32 v251, v238, v239\n\t" \
    "v_add_u32 v40, 91392, v40\n\t" \
    "v_mfma_f32_16x16x32_bf16 v[60:63], v[208:211], v[220:223], v[60:63]\n\t" \
    "v_cvt_pk_bf16_f32 v248, v132, v133\n\t" \
    "v_cvt_pk_bf16_f32 v250, v236, v237\n\t" \
    "v_mfma_f32_16x16x32_bf16 v[140:143], v[208:211], v[120:123], v[140:143]\n\t" \
    "v_bfi_b32 v244, s41, v50, v244\n\t" \
    "v_bfi_b32 v88, s41, v50, v88\n\t" \
    "v_mfma_f32_16x16x32_bf16 v[68:71], v[208:211], v[80:83], v[68:71]\n\t" \
    "buffer_store_dwordx4 v[248:251], v42, s[4:7], s49 offen offset:0 sc1\n\t" \
    "v_cvt_pk_bf16_f32 v197, v246, v247\n\t" \
    "s_waitcnt vmcnt(8)\n\t" \
    "v_mfma_f32_16x16x32_bf16 v[4:7], v[56:59], v[96:99], 0\n\t" \
    "v_cvt_pk_bf16_f32 v199, v90, v91\n\t" \
    "v_cvt_pk_bf16_f32 v196, v244, v245\n\t" \
    "v_mfma_f32_16x16x32_bf16 v[164:167], v[56:59], v[124:127], 0\n\t" \
    "v_cvt_pk_bf16_f32 v198, v88, v89\n\t" \
    "buffer_store_dwordx4 v[196:199], v9, s[4:7], s49 offen offset:128 sc1\n\t" \
    "v_mfma_f32_16x16x32_bf16 v[252:255], v[56:59], v[116:119], 0\n\t" \
    "s_setprio 0\n\t" \
    "s_cmp_lt_u32 s24, 4\n\t" \
    "s_cbranch_scc1 .Lp44_%=\n\t" \
    "s_setprio 1\n\t" \
    ".Lp44_%=:\n\t" \
    "v_sub_f32 v100, v100, v104\n\t" \
    "v_sub_f32 v101, v101, v105\n\t" \
    "v_mfma_f32_16x16x32_bf16 v[212:215], v[56:59], v[128:131], 0\n\t" \
    "v_sub_f32 v102, v102, v106\n\t" \
    "v_sub_f32 v103, v103, v107\n\t" \
    "s_waitcnt vmcnt(8)\n\t" \
    "v_mfma_f32_16x16x32_bf16 v[4:7], v[44:47], v[176:179], v[4:7]\n\t" \
    "v_sub_f32 v200, v200, v140\n\t" \
    "v_sub_f32 v201, v201, v141\n\t" \
    "v_mfma_f32_16x16x32_bf16 v[164:167], v[44:47], v[184:187], v[164:167]\n\t" \
    "v_sub_f32 v202, v202, v142\n\t" \
    "v_sub_f32 v203, v203, v143\n\t" \
    "v_mfma_f32_16x16x32_bf16 v[252:255], v[44:47], v[188:191], v[252:255]\n\t" \
    "v_cvt_pk_bf16_f32 v110, v60, v61\n\t" \
    "v_cvt_pk_bf16_f32 v111, v62, v63\n\t" \
    "v_mfma_f32_16x16x32_bf16 v[212:215], v[44:47], v[20:23], v[212:215]\n\t" \
    "v_cvt_pk_bf16_f32 v108, v100, v101\n\t" \
    "v_cvt_pk_bf16_f32 v109, v102, v103\n\t" \
    "s_waitcnt vmcnt(5)\n\t" \
    "v_mfma_f32_16x16x32_bf16 v[4:7], v[72:75], v[224:227], v[4:7]\n\t" \
    "v_cvt_pk_bf16_f32 v14, v68, v69\n\t" \
    "v_cvt_pk_bf16_f32 v15, v70, v71\n\t" \
    "v_mfma_f32_16x16x32_bf16 v[164:167], v[72:75], v[228:231], v[164:167]\n\t" \
    "ds_write_b128 v40, v[108:111] offset:0\n\t" \
    "v_cvt_pk_bf16_f32 v12, v200, v201\n\t" \
    "v_mfma_f32_16x16x32_bf16 v[252:255], v[72:75], v[232:235], v[252:255]\n\t" \
    "v_cvt_pk_bf16_f32 v13, v202, v203\n\t" \
    "ds_write_b128 v40, v[12:15] offset:1024\n\t" \
    "v_mfma_f32_16x16x32_bf16 v[212:215], v[72:75], v[240:243], v[212:215]\n\t" \
    "s_waitcnt vmcnt(2)\n\t" \
    "s_cmp_ge_u32 s24, 4\n\t" \
    "s_cbranch_scc1 .Lk45_%=\n\t" \
    "v_mfma_f32_16x16x32_bf16 v[92:95], v[216:219], v[124:127], 0\n\t" \
    "v_mfma_f32_16x16x32_bf16 v[92:95], v[148:151], v[184:187], v[92:95]\n\t" \
    "v_mfma_f32_16x16x32_bf16 v[92:95], v[180:183], v[228:231], v[92:95]\n\t" \
    "v_mfma_f32_16x16x32_bf16 v[32:35], v[216:219], v[128:131], 0\n\t" \
    "v_mfma_f32_16x16x32_bf16 v[32:35], v[148:151], v[20:23], v[32:35]\n\t" \
    "v_mfma_f32_16x16x32_bf16 v[32:35], v[180:183], v[240:243], v[32:35]\n\t" \
    "v_mfma_f32_16x16x32_bf16 v[164:167], v[216:219], v[96:99], v[164:167]\n\t" \
    "v_mfma_f32_16x16x32_bf16 v[164:167], v[148:151], v[176:179], v[164:167]\n\t" \
    "v_mfma_f32_16x16x32_bf16 v[164:167], v[180:183], v[224:227], v[164:167]\n\t" \
    "v_mfma_f32_16x16x32_bf16 v[212:215], v[216:219], v[116:119], v[212:215]\n\t" \
    "s_nop 0\n\t" \
    "v_sub_f32 v4, v4, v92\n\t" \
    "v_sub_f32 v5, v5, v93\n\t" \
    "v_mfma_f32_16x16x32_bf16 v[212:215], v[148:151], v[188:191], v[212:215]\n\t" \
    "v_sub_f32 v6, v6, v94\n\t" \
    "v_sub_f32 v7, v7, v95\n\t" \
    "v_mfma_f32_16x16x32_bf16 v[212:215], v[180:183], v[232:235], v[212:215]\n\t" \
    "v_sub_f32 v252, v252, v32\n\t" \
    "v_sub_f32 v253, v253, v33\n\t" \
    "v_sub_f32 v254, v254, v34\n\t" \
    "v_sub_f32 v255, v255, v35\n\t" \
    ".Lk45_%=:\n\t" \
    "v_cvt_pk_bf16_f32 v86, v164, v165\n\t" \
    "v_cvt_pk_bf16_f32 v87, v166, v167\n\t" \
    "v_cvt_pk_bf16_f32 v84, v4, v5\n\t" \
    "v_cvt_pk_bf16_f32 v85, v6, v7\n\t" \
    "s_nop 1\n\t" \
    "v_cvt_pk_bf16_f32 v138, v212, v213\n\t" \
    "v_cvt_pk_bf16_f32 v139, v214, v215\n\t" \
    "ds_write_b128 v40, v[84:87] offset:2064\n\t" \
    "v_cvt_pk_bf16_f32 v136, v252, v253\n\t" \
    "v_cvt_pk_bf16_f32 v137, v254, v255\n\t" \
    "v_add_u32 v49, 91392, v49\n\t" \
    "ds_write_b128 v40, v[136:139] offset:3088\n\t" \
    "s_waitcnt lgkmcnt(0)\n\t" \
    "s_barrier\n\t" \
    "s_memrealtime s[56:57]\n\t" \
    "s_add_u32 s54, s53, 0\n\t" \
    "buffer_load_dwordx4 v[112:115], v0, s[4:7], s54 offen offset:0 sc1\n\t" \
    "s_add_u32 s54, s53, 4243456\n\t" \
    "buffer_load_dwordx4 v[156:159], v0, s[4:7], s54 offen offset:0 sc1\n\t" \
    "ds_read_b64_tr_b16 v[28:29], v49 offset:0\n\t" \
    "ds_read_b64_tr_b16 v[30:31], v49 offset:4128\n\t" \
    "ds_read_b64_tr_b16 v[144:145], v49 offset:256\n\t" \
    "ds_read_b64_tr_b16 v[146:147], v49 offset:4384\n\t" \
    "ds_read_b64_tr_b16 v[172:173], v49 offset:16\n\t" \
    "ds_read_b64_tr_b16 v[174:175], v49 offset:4144\n\t" \
    "ds_read_b64_tr_b16 v[160:161], v49 offset:272\n\t" \
    "ds_read_b64_tr_b16 v[162:163], v49 offset:4400\n\t" \
    "s_waitcnt lgkmcnt(6)\n\t" \
    "v_mfma_f32_16x16x32_bf16 v[152:155], v[28:31], v[204:207], 0\n\t" \
    "s_waitcnt lgkmcnt(4)\n\t" \
    "v_mfma_f32_16x16x32_bf16 v[168:171], v[144:147], v[204:207], 0\n\t" \
    "s_waitcnt lgkmcnt(2)\n\t" \
    "v_mfma_f32_16x16x32_bf16 v[76:79], v[172:175], v[204:207], 0\n\t" \
    "s_waitcnt lgkmcnt(0)\n\t" \
    "v_mfma_f32_16x16x32_bf16 v[16:19], v[160:163], v[204:207], 0\n\t" \
    "s_nop 1\n\t" \
    "v_bfi_b32 v152, s41, v50, v152\n\t" \
    "s_nop 0\n\t" \
    "v_bfi_b32 v168, s41, v50, v168\n\t" \
    "v_cvt_pk_bf16_f32 v65, v154, v155\n\t" \
    "v_cvt_pk_bf16_f32 v67, v170, v171\n\t" \
    "v_cvt_pk_bf16_f32 v64, v152, v153\n\t" \
    "v_cvt_pk_bf16_f32 v66, v168, v169\n\t" \
    "v_bfi_b32 v76, s41, v50, v76\n\t" \
    "v_bfi_b32 v16, s41, v50, v16\n\t" \
    "buffer_store_dwordx4 v[64:67], v42, s[4:7], s50 offen offset:0 sc1\n\t" \
    "v_cvt_pk_bf16_f32 v193, v78, v79\n\t" \
    "v_cvt_pk_bf16_f32 v195, v18, v19\n\t" \
    "v_cvt_pk_bf16_f32 v192, v76, v77\n\t" \
    "v_cvt_pk_bf16_f32 v194, v16, v17\n\t" \
    "buffer_store_dwordx4 v[192:195], v9, s[4:7], s50 offen offset:128 sc1\n\t" \
    "s_cmp_ge_u32 s25, 126\n\t" \
    "s_cbranch_scc1 .Lend_%=\n\t" \
    "ds_read_b128 v[24:27], v10 offset:3072\n\t" \
    "ds_read_b128 v[52:55], v10 offset:4096\n\t" \
    "s_waitcnt lgkmcnt(0)\n\t" \
    "s_sub_u32 s37, s16, 300\n\t" \
    "s_mov_b32 s62, 300\n\t" \
    "s_sub_u32 s39, s58, s37\n\t" \
    "s_cmp_gt_u32 s39, 600\n\t" \
    "s_cselect_b32 s39, 400, s39\n\t" \
    "s_max_u32 s62, s62, s39\n\t" \
    "s_sub_u32 s39, s59, s37\n\t" \
    "s_cmp_gt_u32 s39, 600\n\t" \
    "s_cselect_b32 s39, 400, s39\n\t" \
    "s_max_u32 s62, s62, s39\n\t" \
    "s_sub_u32 s39, s60, s37\n\t" \
    "s_cmp_gt_u32 s39, 600\n\t" \
    "s_cselect_b32 s39, 400, s39\n\t" \
    "s_max_u32 s62, s62, s39\n\t" \
    "s_sub_u32 s39, s61, s37\n\t" \
    "s_cmp_gt_u32 s39, 600\n\t" \
    "s_cselect_b32 s39, 400, s39\n\t" \
    "s_max_u32 s62, s62, s39\n\t" \
    "s_sub_u32 s62, s62, 300\n\t" \
    "s_mul_i32 s62, s62, 5\n\t" \
    "s_lshr_b32 s62, s62, 2\n\t" \
    "s_waitcnt vmcnt(2)\n\t" \
    "v_and_b32 v3, v112, v114\n\t" \
    "v_bitop3_b32 v3, v3, v156, v158 bitop3:0x80\n\t" \
    "v_and_b32 v3, 1, v3\n\t" \
    "v_cmp_eq_u32 vcc, 0, v3\n\t" \
    "s_nop 1\n\t" \
    "s_cbranch_vccnz .Ls39_%=\n\t" \
    ".Lb39_%=:\n\t" \
    "ds_write_b128 v48, v[112:115] offset:33024\n\t" \
    "ds_write_b128 v48, v[156:159] offset:33152\n\t" \
    "s_add_u32 s37, s62, 150\n\t" \
    ".Lh46_%=:\n\t" \
    "s_memrealtime s[22:23]\n\t" \
    "s_waitcnt lgkmcnt(0)\n\t" \
    "s_sub_u32 s22, s22, s18\n\t" \
    "s_cmp_lt_u32 s22, s37\n\t" \
    "s_cbranch_scc1 .Lh46_%=\n\t" \
    "s_add_u32 s54, s53, 524288\n\t" \
    "buffer_load_dwordx4 v[220:223], v0, s[4:7], s54 offen offset:0 sc1\n\t" \
    "s_add_u32 s54, s53, 4767744\n\t" \
    "buffer_load_dwordx4 v[132:135], v0, s[4:7], s54 offen offset:0 sc1\n\t" \
    "s_waitcnt lgkmcnt(0)\n\t" \
    "s_barrier\n\t" \
    "ds_read_b64_tr_b16 v[236:237], v43 offset:33024\n\t" \
    "ds_read_b64_tr_b16 v[238:239], v43 offset:33280\n\t" \
    "ds_read_b64_tr_b16 v[120:121], v43 offset:33032\n\t" \
    "ds_read_b64_tr_b16 v[122:123], v43 offset:33288\n\t" \
    "s_waitcnt lgkmcnt(2)\n\t" \
    "v_mfma_f32_16x16x32_bf16 v[80:83], v[236:239], v[24:27], 0\n\t" \
    "s_waitcnt lgkmcnt(0)\n\t" \
    "v_mfma_f32_16x16x32_bf16 v[208:211], v[120:123], v[24:27], 0\n\t" \
    "v_mfma_f32_16x16x32_bf16 v[248:251], v[236:239], v[52:55], 0\n\t" \
    "v_mfma_f32_16x16x32_bf16 v[244:247], v[120:123], v[52:55], 0\n\t" \
    "s_nop 3\n\t" \
    "v_cvt_pk_bf16_f32 v88, v80, v81\n\t" \
    "v_cvt_pk_bf16_f32 v89, v82, v83\n\t" \
    "v_cvt_pk_bf16_f32 v90, v208, v209\n\t" \
    "v_cvt_pk_bf16_f32 v91, v210, v211\n\t" \
    "buffer_store_dwordx4 v[88:91], v38, s[8:11], 0 offen offset:0 sc1\n\t" \
    "v_cvt_pk_bf16_f32 v196, v248, v249\n\t" \
    "v_cvt_pk_bf16_f32 v197, v250, v251\n\t" \
    "v_cvt_pk_bf16_f32 v198, v244, v245\n\t" \
    "v_cvt_pk_bf16_f32 v199, v246, v247\n\t" \
    "buffer_store_dwordx4 v[196:199], v38, s[8:11], 0 offen offset:3072 sc1\n\t" \
    "s_waitcnt vmcnt(2)\n\t" \
    "v_and_b32 v51, v220, v222\n\t" \
    "v_bitop3_b32 v51, v51, v132, v134 bitop3:0x80\n\t" \
    "v_and_b32 v51, 1, v51\n\t" \
    "v_cmp_eq_u32 vcc, 0, v51\n\t" \
    "s_nop 1\n\t" \
    "s_cbranch_vccnz .Ls40_%=\n\t" \
    ".Lb40_%=:\n\t" \
    "ds_write_b128 v48, v[220:223] offset:0\n\t" \
    "ds_write_b128 v48, v[132:135] offset:128\n\t" \
    "s_add_u32 s37, s62, 120\n\t" \
    ".Lh47_%=:\n\t" \
    "s_memrealtime s[22:23]\n\t" \
    "s_waitcnt lgkmcnt(0)\n\t" \
    "s_sub_u32 s22, s22, s56\n\t" \
    "s_cmp_lt_u32 s22, s37\n\t" \
    "s_cbranch_scc1 .Lh47_%=\n\t" \
    "s_add_u32 s54, s53, 1048576\n\t" \
    "buffer_load_dwordx4 v[56:59], v0, s[4:7], s54 offen offset:0 sc1\n\t" \
    "s_add_u32 s54, s53, 5292032\n\t" \
    "buffer_load_dwordx4 v[104:107], v0, s[4:7], s54 offen offset:0 sc1\n\t" \
    "s_waitcnt lgkmcnt(0)\n\t" \
    "s_barrier\n\t" \
    "ds_read_b64_tr_b16 v[140:141], v43 offset:0\n\t" \
    "ds_read_b64_tr_b16 v[142:143], v43 offset:256\n\t" \
    "ds_read_b64_tr_b16 v[60:61], v43 offset:8\n\t" \
    "ds_read_b64_tr_b16 v[62:63], v43 offset:264\n\t" \
    "s_waitcnt lgkmcnt(2)\n\t" \
    "v_mfma_f32_16x16x32_bf16 v[44:47], v[140:143], v[24:27], 0\n\t" \
    "s_waitcnt lgkmcnt(0)\n\t" \
    "v_mfma_f32_16x16x32_bf16 v[100:103], v[60:63], v[24:27], 0\n\t" \
    "v_mfma_f32_16x16x32_bf16 v[68:71], v[140:143], v[52:55], 0\n\t" \
    "v_mfma_f32_16x16x32_bf16 v[108:111], v[60:63], v[52:55], 0\n\t" \
    "s_nop 3\n\t" \
    "v_cvt_pk_bf16_f32 v200, v44, v45\n\t" \
    "v_cvt_pk_bf16_f32 v201, v46, v47\n\t" \
    "v_cvt_pk_bf16_f32 v202, v100, v101\n\t" \
    "v_cvt_pk_bf16_f32 v203, v102, v103\n\t" \
    "buffer_store_dwordx4 v[200:203], v38, s[8:11], 0 offen offset:64 sc1\n\t" \
    "v_cvt_pk_bf16_f32 v12, v68, v69\n\t" \
    "v_cvt_pk_bf16_f32 v13, v70, v71\n\t" \
    "v_cvt_pk_bf16_f32 v14, v108, v109\n\t" \
    "v_cvt_pk_bf16_f32 v15, v110, v111\n\t" \
    "buffer_store_dwordx4 v[12:15], v38, s[8:11], 0 offen offset:3136 sc1\n\t" \
    "s_waitcnt vmcnt(2)\n\t" \
    "v_and_b32 v1, v56, v58\n\t" \
    "v_bitop3_b32 v1, v1, v104, v106 bitop3:0x80\n\t" \
    "v_and_b32 v1, 1, v1\n\t" \
    "v_cmp_eq_u32 vcc, 0, v1\n\t" \
    "s_nop 1\n\t" \
    "s_cbranch_vccnz .Ls41_%=\n\t" \
    ".Lb41_%=:\n\t" \
    "ds_write_b128 v48, v[56:59] offset:16512\n\t" \
    "ds_write_b128 v48, v[104:107] offset:16640\n\t" \
    "s_waitcnt lgkmcnt(0)\n\t" \
    "s_barrier\n\t" \
    "ds_read_b64_tr_b16 v[72:73], v43 offset:16512\n\t" \
    "ds_read_b64_tr_b16 v[74:75], v43 offset:16768\n\t" \
    "ds_read_b64_tr_b16 v[124:125], v43 offset:16520\n\t" \
    "ds_read_b64_tr_b16 v[126:127], v43 offset:16776\n\t" \
    "s_waitcnt lgkmcnt(2)\n\t" \
    "v_mfma_f32_16x16x32_bf16 v[184:187], v[72:75], v[24:27], 0\n\t" \
    "s_waitcnt lgkmcnt(0)\n\t" \
    "v_mfma_f32_16x16x32_bf16 v[228:231], v[124:127], v[24:27], 0\n\t" \
    "v_mfma_f32_16x16x32_bf16 v[128:131], v[72:75], v[52:55], 0\n\t" \
    "v_mfma_f32_16x16x32_bf16 v[20:23], v[124:127], v[52:55], 0\n\t" \
    "s_nop 3\n\t" \
    "v_cvt_pk_bf16_f32 v240, v184, v185\n\t" \
    "v_cvt_pk_bf16_f32 v241, v186, v187\n\t" \
    "v_cvt_pk_bf16_f32 v242, v228, v229\n\t" \
    "v_cvt_pk_bf16_f32 v243, v230, v231\n\t" \
    "buffer_store_dwordx4 v[240:243], v38, s[8:11], 0 offen offset:128 sc1\n\t" \
    "v_cvt_pk_bf16_f32 v96, v128, v129\n\t" \
    "v_cvt_pk_bf16_f32 v97, v130, v131\n\t" \
    "v_cvt_pk_bf16_f32 v98, v20, v21\n\t" \
    "v_cvt_pk_bf16_f32 v99, v22, v23\n\t" \
    "buffer_store_dwordx4 v[96:99], v38, s[8:11], 0 offen offset:3200 sc1\n\t" \
    "s_or_b32 s37, s2, s24\n\t" \
    "s_cbranch_scc1 .Lend_%=\n\t" \
    "s_mov_b32 s37, 34635776\n\t" \
    "v_mov_b32 v36, s34\n\t" \
    "s_mov_b64 exec, 1\n\t" \
    "buffer_store_dword v36, v0, s[4:7], s37 offen sc1\n\t" \
    "s_mov_b64 exec, -1\n\t" \
    ".Lend_%=:\n\t" \
    "s_endpgm\n\t" \
    ".Lq1026_%=:\n\t" \
    "s_mov_b32 s40, 0\n\t" \
    ".Lr1026_%=:\n\t" \
    "s_add_u32 s12, s55, 34603008\n\t" \
    "s_buffer_load_dword s13, s[4:7], s12 glc\n\t" \
    "s_buffer_load_dword s14, s[4:7], s12 offset:0x400 glc\n\t" \
    "s_waitcnt lgkmcnt(0)\n\t" \
    "s_xor_b32 s13, s13, s34\n\t" \
    "s_xor_b32 s14, s14, s34\n\t" \
    "s_or_b32 s13, s13, s14\n\t" \
    "s_cbranch_scc0 .Lb25_%=\n\t" \
    "s_add_u32 s40, s40, 1\n\t" \
    "s_cmp_lt_u32 s40, 1\n\t" \
    "s_cbranch_scc1 .Lr1026_%=\n\t" \
    "s_branch .Ls25_%=\n\t" \
    ".Lq1023_%=:\n\t" \
    "s_mov_b32 s40, 0\n\t" \
    ".Lr1023_%=:\n\t" \
    "s_nop 0\n\t" \
    "v_readlane_b32 s12, v3, 0\n\t" \
    "v_readlane_b32 s13, v3, 1\n\t" \
    "v_readlane_b32 s14, v3, 2\n\t" \
    "v_readlane_b32 s15, v3, 3\n\t" \
    "v_readlane_b32 s16, v3, 4\n\t" \
    "v_readlane_b32 s17, v3, 5\n\t" \
    "v_readlane_b32 s18, v3, 6\n\t" \
    "v_readlane_b32 s19, v3, 7\n\t" \
    "v_readlane_b32 s3, v3, 8\n\t" \
    "s_add_u32 s12, s12, s33\n\t" \
    "s_add_u32 s13, s13, s33\n\t" \
    "s_add_u32 s14, s14, s33\n\t" \
    "s_add_u32 s15, s15, s33\n\t" \
    "s_add_u32 s16, s16, s33\n\t" \
    "s_add_u32 s17, s17, s33\n\t" \
    "s_add_u32 s18, s18, s33\n\t" \
    "s_add_u32 s19, s19, s33\n\t" \
    "s_add_u32 s3, s3, s33\n\t" \
    "s_buffer_load_dword s12, s[4:7], s12 offset:0xc0000 glc\n\t" \
    "s_buffer_load_dword s13, s[4:7], s13 offset:0xc0000 glc\n\t" \
    "s_buffer_load_dword s14, s[4:7], s14 offset:0xc0000 glc\n\t" \
    "s_buffer_load_dword s15, s[4:7], s15 offset:0xc0000 glc\n\t" \
    "s_buffer_load_dword s16, s[4:7], s16 offset:0xc0000 glc\n\t" \
    "s_buffer_load_dword s17, s[4:7], s17 offset:0xc0000 glc\n\t" \
    "s_buffer_load_dword s18, s[4:7], s18 offset:0xc0000 glc\n\t" \
    "s_buffer_load_dword s19, s[4:7], s19 offset:0xc0000 glc\n\t" \
    "s_buffer_load_dword s3, s[4:7], s3 offset:0xc0000 glc\n\t" \
    "s_waitcnt lgkmcnt(0)\n\t" \
    "s_xor_b32 s12, s12, s34\n\t" \
    "s_xor_b32 s13, s13, s34\n\t" \
    "s_xor_b32 s14, s14, s34\n\t" \
    "s_xor_b32 s15, s15, s34\n\t" \
    "s_xor_b32 s16, s16, s34\n\t" \
    "s_xor_b32 s17, s17, s34\n\t" \
    "s_xor_b32 s18, s18, s34\n\t" \
    "s_xor_b32 s19, s19, s34\n\t" \
    "s_xor_b32 s3, s3, s34\n\t" \
    "s_or_b32 s12, s12, s13\n\t" \
    "s_or_b32 s12, s12, s14\n\t" \
    "s_or_b32 s12, s12, s15\n\t" \
    "s_or_b32 s12, s12, s16\n\t" \
    "s_or_b32 s12, s12, s17\n\t" \
    "s_or_b32 s12, s12, s18\n\t" \
    "s_or_b32 s12, s12, s19\n\t" \
    "s_or_b32 s12, s12, s3\n\t" \
    "s_cbranch_scc0 .Lb22_%=\n\t" \
    "s_add_u32 s40, s40, 1\n\t" \
    "s_cmp_lt_u32 s40, 1\n\t" \
    "s_cbranch_scc1 .Lr1023_%=\n\t" \
    "s_branch .Ls22_%=\n\t" \
    ".Lpb_%=:\n\t" \
    "s_sub_u32 s37, s24, 4\n\t" \
    "s_cbranch_scc1 .Lpe_%=\n\t" \
    "s_cmp_lg_u32 s2, 255\n\t" \
    "s_cbranch_scc1 .Lpe_%=\n\t" \
    "s_mul_i32 s38, s37, 0x2100\n\t" \
    "s_and_b32 s38, s38, 0x2300\n\t" \
    "s_add_u32 s38, s38, 34652160\n\t" \
    "s_mov_b32 s60, -1\n\t" \
    "s_mov_b32 s61, -1\n\t" \
    "s_memrealtime s[62:63]\n\t" \
    "s_mov_b64 exec, 0xff\n\t" \
    ".Lpc1_%=:\n\t" \
    "s_memtime s[56:57]\n\t" \
    "s_waitcnt lgkmcnt(0)\n\t" \
    "buffer_store_dwordx4 v[20:23], v0, s[4:7], s38 offen sc1\n\t" \
    "s_waitcnt vmcnt(0)\n\t" \
    "s_memtime s[58:59]\n\t" \
    "s_memrealtime s[22:23]\n\t" \
    "s_waitcnt lgkmcnt(0)\n\t" \
    "s_sub_u32 s56, s58, s56\n\t" \
    "s_min_u32 s56, s56, s60\n\t" \
    "s_mov_b32 s60, s61\n\t" \
    "s_mov_b32 s61, s56\n\t" \
    "s_xor_b32 s38, s38, 0x2000\n\t" \
    "s_add_u32 s38, s38, 0x400\n\t" \
    "s_andn2_b32 s38, s38, 0x1000\n\t" \
    "s_sub_u32 s22, s22, s62\n\t" \
    "s_cmp_lt_u32 s22, 60\n\t" \
    "s_cbranch_scc1 .Lpc1_%=\n\t" \
    "s_mov_b64 exec, -1\n\t" \
    "s_bitcmp1_b32 s38, 13\n\t" \
    "s_cselect_b32 s56, s61, s60\n\t" \
    "s_cselect_b32 s57, s60, s61\n\t" \
    "v_mov_b32 v20, s56\n\t" \
    "v_mov_b32 v21, s57\n\t" \
    "s_lshl3_add_u32 s37, s37, 156992\n\t" \
    "v_mov_b32 v22, s37\n\t" \
    "ds_write_b64 v22, v[20:21]\n\t" \
    "s_branch .Lpe_%=\n\t" \
    ".Lqa_%=:\n\t" \
    "s_sub_u32 s37, s24, 4\n\t" \
    "s_bfe_u32 s38, s2, 0x20002\n\t" \
    "s_lshl1_add_u32 s38, s38, s37\n\t" \
    "s_lshl_b32 s42, s38, 21\n\t" \
    "s_cmp_eq_u32 s2, 255\n\t" \
    "s_cbranch_scc1 .Lqd_%=\n\t" \
    "s_cmp_ge_u32 s37, 2\n\t" \
    "s_cbranch_scc1 .Lqd_%=\n\t" \
    "s_waitcnt lgkmcnt(0)\n\t" \
    "s_sleep 4\n\t" \
    "s_lshl_b32 s3, s38, 10\n\t" \
    "s_and_b32 s37, s8, 0x1fffff\n\t" \
    "s_add_u32 s38, s37, 16383\n\t" \
    "s_andn2_b32 s38, s38, 16383\n\t" \
    "s_add_u32 s39, s37, 9617408\n\t" \
    "s_andn2_b32 s39, s39, 16383\n\t" \
    "s_max_u32 s42, s42, s38\n\t" \
    "s_min_u32 s42, s42, s39\n\t" \
    "s_sub_u32 s42, s42, s37\n\t" \
    "s_add_u32 s16, s8, s42\n\t" \
    "s_addc_u32 s17, s9, 0\n\t" \
    "s_mov_b32 s18, 16384\n\t" \
    "s_mov_b32 s19, 0x20000\n\t" \
    "s_add_u32 s37, s3, 0x2000\n\t" \
    "buffer_store_dwordx4 v[20:23], v0, s[16:19], s3 offen sc1\n\t" \
    "buffer_store_dwordx4 v[20:23], v0, s[16:19], s37 offen sc1\n\t" \
    "s_nop 0\n\t" \
    "s_mov_b32 s39, 34635776\n\t" \
    "v_mov_b32 v20, 0\n\t" \
    "s_buffer_load_dword s0, s[16:19], s3 offset:0x380\n\t" \
    "buffer_load_dword v21, v20, s[4:7], s39 offen sc1\n\t" \
    "s_mov_b32 s60, -1\n\t" \
    "s_mov_b32 s61, -1\n\t" \
    "s_mov_b32 s40, 0\n\t" \
    "s_waitcnt vmcnt(1) lgkmcnt(0)\n\t" \
    "s_memtime s[58:59]\n\t" \
    "s_waitcnt lgkmcnt(0)\n\t" \
    ".Lqc_%=:\n\t" \
    "s_buffer_load_dword s38, s[16:19], s3\n\t" \
    "s_mov_b32 s56, s58\n\t" \
    "s_xor_b32 s3, s3, 0x2000\n\t" \
    "s_add_u32 s3, s3, 0x80\n\t" \
    "s_waitcnt lgkmcnt(0)\n\t" \
    "s_memtime s[58:59]\n\t" \
    "s_add_u32 s40, s40, 1\n\t" \
    "s_waitcnt lgkmcnt(0)\n\t" \
    "s_sub_u32 s56, s58, s56\n\t" \
    "s_min_u32 s56, s56, s60\n\t" \
    "s_mov_b32 s60, s61\n\t" \
    "s_mov_b32 s61, s56\n\t" \
    "s_cmp_lt_u32 s40, 6\n\t" \
    "s_cbranch_scc1 .Lqc_%=\n\t" \
    "s_waitcnt vmcnt(0)\n\t" \
    "v_readfirstlane_b32 s34, v21\n\t" \
    "s_cmp_gt_u32 s60, s61\n\t" \
    "s_cselect_b32 s0, 2, 0\n\t" \
    "s_mul_i32 s34, s34, 1664525\n\t" \
    "s_add_u32 s34, s34, 1013904223\n\t" \
    "s_or_b32 s34, s34, 1\n\t" \
    "s_xor_b32 s37, s0, s34\n\t" \
    "v_mov_b32 v21, s37\n\t" \
    "s_bfe_u32 s37, s2, 0x20002\n\t" \
    "s_lshl1_add_u32 s37, s37, s24\n\t" \
    "s_lshl_b32 s37, s37, 7\n\t" \
    "s_add_u32 s37, s37, 34637312\n\t" \
    "v_lshrrev_b32 v20, 2, v0\n\t" \
    "s_mov_b32 exec_hi, 0\n\t" \
    "buffer_store_dword v21, v20, s[4:7], s37 offen sc1\n\t" \
    "s_mov_b64 exec, -1\n\t" \
    "s_branch .Lqd_%=\n\t" \
    ".Lr2_%=:\n\t" \
    "s_add_u32 s40, s40, 1\n\t" \
    "s_cmp_gt_u32 s40, 32\n\t" \
    "s_cbranch_scc1 .Lr3_%=\n\t" \
    "s_sleep 2\n\t" \
    "s_and_b32 s37, s2, 7\n\t" \
    "s_lshl_b32 s37, s37, 10\n\t" \
    "s_add_u32 s37, s37, 34643968\n\t" \
    "v_and_b32 v51, 0x70, v0\n\t" \
    "v_lshlrev_b32 v51, 3, v51\n\t" \
    "buffer_load_dword v50, v51, s[4:7], s37 offen sc1\n\t" \
    "s_waitcnt vmcnt(0)\n\t" \
    "v_readlane_b32 s56, v50, 0\n\t" \
    "v_readlane_b32 s57, v50, 1\n\t" \
    "v_readlane_b32 s58, v50, 2\n\t" \
    "v_readlane_b32 s59, v50, 3\n\t" \
    "v_readlane_b32 s60, v50, 4\n\t" \
    "v_readlane_b32 s61, v50, 5\n\t" \
    "v_readlane_b32 s62, v50, 6\n\t" \
    "v_readlane_b32 s63, v50, 7\n\t" \
    "s_branch .Lr1_%=\n\t" \
    ".Lpp_%=:\n\t" \
    "ds_read_b128 v[100:103], v9 offset:64\n\t" \
    "ds_read_b128 v[104:107], v9 offset:80\n\t" \
    "s_getreg_b32 s37, hwreg(HW_REG_XCC_ID, 0, 2)\n\t" \
    "s_waitcnt lgkmcnt(0)\n\t" \
    "v_min3_u32 v100, v100, v102, v104\n\t" \
    "v_min3_u32 v101, v101, v103, v105\n\t" \
    "v_min_u32 v100, v100, v106\n\t" \
    "v_min_u32 v101, v101, v107\n\t" \
    "v_cmp_gt_u32 vcc, v100, v101\n\t" \
    "s_nop 4\n\t" \
    "s_and_b32 s38, vcc_lo, 4\n\t" \
    "s_add_u32 s37, s37, s38\n\t" \
    "s_add_u32 s37, s37, 1\n\t" \
    "s_and_b32 s37, s37, 7\n\t" \
    "s_lshl_b32 s37, s37, 1\n\t" \
    "s_xor_b32 s37, s37, s34\n\t" \
    "v_mov_b32 v100, s37\n\t" \
    "v_mov_b32 v101, s37\n\t" \
    "v_mov_b32 v102, s37\n\t" \
    "v_mov_b32 v103, s37\n\t" \
    "s_mov_b32 s39, 34636800\n\t" \
    "buffer_store_dwordx4 v[100:103], v0, s[4:7], s39 offen sc1\n\t" \
    "s_nop 1\n\t" \
    "s_branch .Lpq_%=\n\t" \
    ".Lq5_%=:\n\t" \
    "s_add_u32 s40, s40, 1\n\t" \
    "s_cmp_gt_u32 s40, 8192\n\t" \
    "s_cbranch_scc1 .Lq6_%=\n\t" \
    "s_sleep 4\n\t" \
    "v_and_b32 v46, 0x70, v0\n\t" \
    "v_lshlrev_b32 v46, 3, v46\n\t" \
    "s_mov_b32 s37, 34637824\n\t" \
    "buffer_load_dword v16, v46, s[4:7], s37 offen sc1\n\t" \
    "s_waitcnt vmcnt(0)\n\t" \
    "v_readlane_b32 s56, v16, 0\n\t" \
    "v_readlane_b32 s57, v16, 1\n\t" \
    "v_readlane_b32 s58, v16, 2\n\t" \
    "v_readlane_b32 s59, v16, 3\n\t" \
    "v_readlane_b32 s60, v16, 4\n\t" \
    "v_readlane_b32 s61, v16, 5\n\t" \
    "s_branch .Lq4_%=\n\t" \
    ".Lr5_%=:\n\t" \
    "s_add_u32 s40, s40, 1\n\t" \
    "s_cmp_gt_u32 s40, 8192\n\t" \
    "s_cbranch_scc1 .Lr6_%=\n\t" \
    "s_sleep 4\n\t" \
    "v_mov_b32 v16, 0\n\t" \
    "buffer_load_dword v8, v16, s[4:7], s52 offen sc1\n\t" \
    "s_waitcnt vmcnt(0)\n\t" \
    "v_readfirstlane_b32 s36, v8\n\t" \
    "s_branch .Lr4_%=\n\t" \
    ".Ls22_%=:\n\t" \
    "s_mov_b32 s40, 0\n\t" \
    "s_add_u32 s38, s33, 786432\n\t" \
    ".Ll22_%=:\n\t" \
    "s_sleep 2\n\t" \
    "buffer_load_dword v51, v3, s[4:7], s38 offen sc1\n\t" \
    "s_waitcnt vmcnt(0)\n\t" \
    "v_cmp_ne_u32 vcc, s34, v51\n\t" \
    "s_add_u32 s40, s40, 1\n\t" \
    "s_cmp_ge_u32 s40, 8192\n\t" \
    "s_cbranch_scc1 .Lb22_%=\n\t" \
    "s_cbranch_vccnz .Ll22_%=\n\t" \
    "s_branch .Lb22_%=\n\t" \
    ".Ls25_%=:\n\t" \
    "s_mov_b32 s40, 0\n\t" \
    "s_add_u32 s38, s55, 34603008\n\t" \
    ".Ll25_%=:\n\t" \
    "s_sleep 2\n\t" \
    "buffer_load_dword v37, v47, s[4:7], s38 offen sc1\n\t" \
    "s_waitcnt vmcnt(0)\n\t" \
    "v_cmp_ne_u32 vcc, s34, v37\n\t" \
    "s_add_u32 s40, s40, 1\n\t" \
    "s_cmp_ge_u32 s40, 8192\n\t" \
    "s_cbranch_scc1 .Lb25_%=\n\t" \
    "s_cbranch_vccnz .Ll25_%=\n\t" \
    "s_branch .Lb25_%=\n\t" \
    ".Ls39_%=:\n\t" \
    "s_mov_b32 s40, 0\n\t" \
    ".Ll39_%=:\n\t" \
    "s_add_u32 s54, s53, 0\n\t" \
    "buffer_load_dwordx4 v[112:115], v0, s[4:7], s54 offen offset:0 sc1\n\t" \
    "s_add_u32 s54, s53, 4243456\n\t" \
    "buffer_load_dwordx4 v[156:159], v0, s[4:7], s54 offen offset:0 sc1\n\t" \
    "s_waitcnt vmcnt(0)\n\t" \
    "v_and_b32 v3, v112, v114\n\t" \
    "v_bitop3_b32 v3, v3, v156, v158 bitop3:0x80\n\t" \
    "v_and_b32 v3, 1, v3\n\t" \
    "v_cmp_eq_u32 vcc, 0, v3\n\t" \
    "s_add_u32 s40, s40, 1\n\t" \
    "s_cmp_ge_u32 s40, 8192\n\t" \
    "s_cbranch_scc1 .Lb39_%=\n\t" \
    "s_cbranch_vccnz .Ll39_%=\n\t" \
    "s_branch .Lb39_%=\n\t" \
    ".Ls40_%=:\n\t" \
    "s_mov_b32 s40, 0\n\t" \
    ".Ll40_%=:\n\t" \
    "s_add_u32 s54, s53, 524288\n\t" \
    "buffer_load_dwordx4 v[220:223], v0, s[4:7], s54 offen offset:0 sc1\n\t" \
    "s_add_u32 s54, s53, 4767744\n\t" \
    "buffer_load_dwordx4 v[132:135], v0, s[4:7], s54 offen offset:0 sc1\n\t" \
    "s_waitcnt vmcnt(0)\n\t" \
    "v_and_b32 v51, v220, v222\n\t" \
    "v_bitop3_b32 v51, v51, v132, v134 bitop3:0x80\n\t" \
    "v_and_b32 v51, 1, v51\n\t" \
    "v_cmp_eq_u32 vcc, 0, v51\n\t" \
    "s_add_u32 s40, s40, 1\n\t" \
    "s_cmp_ge_u32 s40, 8192\n\t" \
    "s_cbranch_scc1 .Lb40_%=\n\t" \
    "s_cbranch_vccnz .Ll40_%=\n\t" \
    "s_branch .Lb40_%=\n\t" \
    ".Ls41_%=:\n\t" \
    "s_mov_b32 s40, 0\n\t" \
    ".Ll41_%=:\n\t" \
    "s_add_u32 s54, s53, 1048576\n\t" \
    "buffer_load_dwordx4 v[56:59], v0, s[4:7], s54 offen offset:0 sc1\n\t" \
    "s_add_u32 s54, s53, 5292032\n\t" \
    "buffer_load_dwordx4 v[104:107], v0, s[4:7], s54 offen offset:0 sc1\n\t" \
    "s_waitcnt vmcnt(0)\n\t" \
    "v_and_b32 v1, v56, v58\n\t" \
    "v_bitop3_b32 v1, v1, v104, v106 bitop3:0x80\n\t" \
    "v_and_b32 v1, 1, v1\n\t" \
    "v_cmp_eq_u32 vcc, 0, v1\n\t" \
    "s_add_u32 s40, s40, 1\n\t" \
    "s_cmp_ge_u32 s40, 8192\n\t" \
    "s_cbranch_scc1 .Lb41_%=\n\t" \
    "s_cbranch_vccnz .Ll41_%=\n\t" \
    "s_branch .Lb41_%=\n\t" \
    ".Lfin_%=:\n\t"
#define K_CLOBBERS "v1", "v2", "v3", "v4", "v5", "v6", "v7", "v8", "v9", "v10", "v11", "v12", "v13", "v14", "v15", "v16", "v17", "v18", "v19", "v20", "v21", "v22", "v23", "v24", "v25", "v26", "v27", "v28", "v29", "v30", "v31", "v32", "v33", "v34", "v35", "v36", "v37", "v38", "v39", "v40", "v41", "v42", "v43", "v44", "v45", "v46", "v47", "v48", "v49", "v50", "v51", "v52", "v53", "v54", "v55", "v56", "v57", "v58", "v59", "v60", "v61", "v62", "v63", "v64", "v65", "v66", "v67", "v68", "v69", "v70", "v71", "v72", "v73", "v74", "v75", "v76", "v77", "v78", "v79", "v80", "v81", "v82", "v83", "v84", "v85", "v86", "v87", "v88", "v89", "v90", "v91", "v92", "v93", "v94", "v95", "v96", "v97", "v98", "v99", "v100", "v101", "v102", "v103", "v104", "v105", "v106", "v107", "v108", "v109", "v110", "v111", "v112", "v113", "v114", "v115", "v116", "v117", "v118", "v119", "v120", "v121", "v122", "v123", "v124", "v125", "v126", "v127", "v128", "v129", "v130", "v131", "v132", "v133", "v134", "v135", "v136", "v137", "v138", "v139", "v140", "v141", "v142", "v143", "v144", "v145", "v146", "v147", "v148", "v149", "v150", "v151", "v152", "v153", "v154", "v155", "v156", "v157", "v158", "v159", "v160", "v161", "v162", "v163", "v164", "v165", "v166", "v167", "v168", "v169", "v170", "v171", "v172", "v173", "v174", "v175", "v176", "v177", "v178", "v179", "v180", "v181", "v182", "v183", "v184", "v185", "v186", "v187", "v188", "v189", "v190", "v191", "v192", "v193", "v194", "v195", "v196", "v197", "v198", "v199", "v200", "v201", "v202", "v203", "v204", "v205", "v206", "v207", "v208", "v209", "v210", "v211", "v212", "v213", "v214", "v215", "v216", "v217", "v218", "v219", "v220", "v221", "v222", "v223", "v224", "v225", "v226", "v227", "v228", "v229", "v230", "v231", "v232", "v233", "v234", "v235", "v236", "v237", "v238", "v239", "v240", "v241", "v242", "v243", "v244", "v245", "v246", "v247", "v248", "v249", "v250", "v251", "v252", "v253", "v254", "v255", "s3", "s4", "s5", "s6", "s7", "s8", "s9", "s10", "s11", "s12", "s13", "s14", "s15", "s16", "s17", "s18", "s19", "s20", "s21", "s22", "s23", "s24", "s25", "s26", "s27", "s28", "s29", "s30", "s31", "s32", "s33", "s34", "s35", "s36", "s37", "s38", "s39", "s40", "s41", "s42", "s43", "s44", "s45", "s46", "s47", "s48", "s49", "s50", "s51", "s52", "s53", "s54", "s55", "s56", "s57", "s58", "s59", "s60", "s61", "s62", "s63", "s64", "s65"

extern "C" __global__ __launch_bounds__(512, 2)
void k_conv(const char* __restrict__ x, const char* __restrict__ wgt, char* __restrict__ ws, char* __restrict__ out)
{
  __shared__ __attribute__((aligned(256))) char smem[157568];
  unsigned tid = threadIdx.x;
  auto kp = __builtin_amdgcn_kernarg_segment_ptr();
  asm volatile(K_BODY : "+{v0}"(tid), "+{s[0:1]}"(kp)
               : "{s2}"(blockIdx.x), "i"((__attribute__((address_space(3))) char*)smem)
               : "memory", "scc", "vcc", K_CLOBBERS);
}

extern "C" void kernel_launch(void* const* d_in, const int* in_sizes, int n_in,
                              void* d_out, int out_size, void* d_ws, size_t ws_size,
                              hipStream_t stream) {
  k_conv<<<dim3(256), dim3(512), 0, stream>>>((const char*)d_in[0], (const char*)d_in[1], (char*)d_ws + WS_OFF, (char*)d_out);
}
```

```cpp
#include <hip/hip_runtime.h>
#include <hip/hip_bf16.h>

#define WS_OFF 0
#define K_BODY \
    "s_load_dwordx2 s[16:17], s[0:1], 0x0\n\t" \
    "s_load_dwordx2 s[4:5], s[0:1], 0x10\n\t" \
    "s_load_dwordx2 s[12:13], s[0:1], 0x8\n\t" \
    "s_load_dwordx2 s[8:9], s[0:1], 0x18\n\t" \
    "s_getpc_b64 s[22:23]\n\t" \
    ".Lpc_%=:\n\t" \
    "s_cmp_ge_u32 s2, 16\n\t" \
    "s_cbranch_scc1 .Le1_%=\n\t" \
    "s_mov_b32 m0, 157312\n\t" \
    "v_lshlrev_b32 v8, 6, v0\n\t" \
    "v_min_u32 v8, .Lfin_%=-.Lpc_%=, v8\n\t" \
    "global_load_lds_dword v8, s[22:23]\n\t" \
    ".Le1_%=:\n\t" \
    "v_readfirstlane_b32 s24, v0\n\t" \
    "s_and_b32 s25, s2, 3\n\t" \
    "s_lshl_b32 s25, s25, 5\n\t" \
    "s_lshr_b32 s37, s2, 3\n\t" \
    "s_add_u32 s25, s25, s37\n\t" \
    "s_min_u32 s28, s25, 125\n\t" \
    "s_mul_i32 s26, s28, 57\n\t" \
    "s_lshr_b32 s26, s26, 9\n\t" \
    "s_mul_i32 s37, s26, 9\n\t" \
    "s_sub_u32 s27, s28, s37\n\t" \
    "s_bfe_u32 s37, s2, 0x10002\n\t" \
    "s_lshl1_add_u32 s26, s26, s37\n\t" \
    "v_and_b32 v0, 63, v0\n\t" \
    "v_lshlrev_b32 v0, 4, v0\n\t" \
    "s_lshr_b32 s24, s24, 6\n\t" \
    "s_sub_u32 s37, s24, 4\n\t" \
    "s_cbranch_scc1 .Le2_%=\n\t" \
    "s_cmp_ge_u32 s25, 126\n\t" \
    "s_cbranch_scc1 .Le2_%=\n\t" \
    "s_mul_i32 m0, s37, 12416\n\t" \
    "s_lshl2_add_u32 s37, s26, s37\n\t" \
    "s_mul_i32 s37, s37, 86016\n\t" \
    "s_mul_i32 s38, s27, 4992\n\t" \
    "s_add_u32 s37, s37, s38\n\t" \
    "s_addk_i32 s37, 896\n\t" \
    "v_add_u32 v9, s37, v0\n\t" \
    "s_cmp_eq_u32 s27, 0\n\t" \
    "s_cselect_b32 s38, 43008, 0\n\t" \
    "s_sub_u32 s39, s38, 2048\n\t" \
    "v_add_u32 v7, s39, v9\n\t" \
    "v_subrev_u32 v8, 0x400, v9\n\t" \
    "s_mov_b64 exec, 0xff\n\t" \
    "v_add_u32 v8, s38, v8\n\t" \
    "s_mov_b64 exec, -1\n\t" \
    "s_mov_b32 s18, 9633792\n\t" \
    "s_mov_b32 s19, 0x20000\n\t" \
    "s_mov_b32 s39, 43008\n\t" \
    "s_waitcnt lgkmcnt(0)\n\t" \
    "buffer_load_dwordx4 v7, s[16:19], 0 offen offset:0 sc1 nt lds\n\t" \
    "s_add_u32 m0, m0, 2080\n\t" \
    "s_nop 0\n\t" \
    "buffer_load_dwordx4 v9, s[16:19], 0 offen offset:3072 sc1 nt lds\n\t" \
    "buffer_load_dwordx4 v9, s[16:19], 0 offen offset:2048 sc1 nt lds\n\t" \
    "s_add_u32 m0, m0, 4128\n\t" \
    "s_nop 0\n\t" \
    "buffer_load_dwordx4 v7, s[16:19], s39 offen offset:0 sc1 nt lds\n\t" \
    "s_add_u32 m0, m0, 2080\n\t" \
    "s_nop 0\n\t" \
    "buffer_load_dwordx4 v9, s[16:19], s39 offen offset:3072 sc1 nt lds\n\t" \
    "buffer_load_dwordx4 v9, s[16:19], s39 offen offset:2048 sc1 nt lds\n\t" \
    "buffer_load_dwordx4 v9, s[16:19], s39 offen offset:1024 sc1 nt lds\n\t" \
    "s_add_u32 m0, m0, 4294966240\n\t" \
    "s_nop 0\n\t" \
    "buffer_load_dwordx4 v8, s[16:19], s39 offen offset:0 sc1 nt lds\n\t" \
    "s_add_u32 m0, m0, 1024\n\t" \
    "s_nop 0\n\t" \
    "buffer_load_dwordx4 v9, s[16:19], s39 offen offset:0 sc1 nt lds\n\t" \
    "s_add_u32 m0, m0, 4294960064\n\t" \
    "s_nop 0\n\t" \
    "buffer_load_dwordx4 v8, s[16:19], 0 offen offset:0 sc1 nt lds\n\t" \
    "s_add_u32 m0, m0, 1024\n\t" \
    "s_nop 0\n\t" \
    "buffer_load_dwordx4 v9, s[16:19], 0 offen offset:0 sc1 nt lds\n\t" \
    "s_add_u32 m0, m0, 32\n\t" \
    "s_nop 0\n\t" \
    "buffer_load_dwordx4 v9, s[16:19], 0 offen offset:1024 sc1 nt lds\n\t" \
    ".Le2_%=:\n\t" \
    "s_mov_b32 s6, 34668544\n\t" \
    "s_mov_b32 s7, 0x20000\n\t" \
    "v_lshrrev_b32 v8, 7, v0\n\t" \
    "v_and_b32 v9, 0x70, v0\n\t" \
    "v_lshl_or_b32 v2, v8, 15, v9\n\t" \
    "s_lshl_b32 s32, s2, 7\n\t" \
    "s_add_u32 s32, s32, 33554432\n\t" \
    "s_cmp_ge_u32 s24, 4\n\t" \
    "s_cbranch_scc1 .Le3_%=\n\t" \
    "v_mov_b32 v12, 0\n\t" \
    "v_mov_b32 v13, 0\n\t" \
    "v_mov_b32 v14, 0\n\t" \
    "v_mov_b32 v15, 0\n\t" \
    "s_lshl_b32 s37, s24, 18\n\t" \
    "s_add_u32 s37, s37, s32\n\t" \
    "s_xor_b32 s38, s24, 2\n\t" \
    "s_lshr_b32 s39, s2, 3\n\t" \
    "s_or_b32 s38, s38, s39\n\t" \
    "s_memrealtime s[22:23]\n\t" \
    "s_waitcnt lgkmcnt(0)\n\t" \
    "s_cbranch_scc1 .Le4_%=\n\t" \
    "v_lshl_or_b32 v8, v8, 10, v9\n\t" \
    "v_mov_b32 v16, s22\n\t" \
    "v_mov_b32 v17, s22\n\t" \
    "v_mov_b32 v18, s22\n\t" \
    "v_mov_b32 v19, s22\n\t" \
    "s_add_u32 s38, s32, 1089536\n\t" \
    "buffer_store_dwordx4 v[16:19], v8, s[4:7], s38 offen sc1\n\t" \
    ".Le4_%=:\n\t" \
    "\n\t" \
    ".Le3_%=:\n\t" \
    "s_bitcmp1_b32 s2, 2\n\t" \
    "s_cselect_b32 s38, 126, 0\n\t" \
    "s_add_u32 s28, s28, s38\n\t" \
    "s_mul_i32 s30, s28, 293\n\t" \
    "s_lshr_b32 s30, s30, 11\n\t" \
    "s_mul_i32 s37, s30, 7\n\t" \
    "s_sub_u32 s28, s28, s37\n\t" \
    "s_mul_i32 s29, s30, 43\n\t" \
    "s_lshr_b32 s29, s29, 7\n\t" \
    "s_mul_i32 s37, s29, 3\n\t" \
    "s_sub_u32 s30, s30, s37\n\t" \
    "s_and_b32 s37, s29, 12\n\t" \
    "s_add_u32 s37, s37, s29\n\t" \
    "s_mul_i32 s31, s37, 768\n\t" \
    "s_lshl_b32 s37, s30, 6\n\t" \
    "s_add_u32 s31, s31, s37\n\t" \
    "s_add_u32 s41, s29, s30\n\t" \
    "s_add_u32 s41, s41, s28\n\t" \
    "s_and_b32 s41, s41, 3\n\t" \
    "s_cmp_lt_u32 s25, 126\n\t" \
    "s_cselect_b32 s14, 903168, 0\n\t" \
    "s_mov_b32 s15, 0x20000\n\t" \
    "v_lshrrev_b32 v10, 6, v0\n\t" \
    "v_lshrrev_b32 v11, 9, v0\n\t" \
    "v_sub_u32 v8, v10, v11\n\t" \
    "v_mul_u32_u24 v8, 18432, v8\n\t" \
    "v_and_b32 v9, 0x30, v0\n\t" \
    "v_lshlrev_b32 v11, 5, v11\n\t" \
    "v_bitop3_b32 v1, v8, v9, v11 bitop3:0xf6\n\t" \
    "s_mov_b32 exec_lo, 0xf0000000\n\t" \
    "s_mov_b32 exec_hi, 0xf0000000\n\t" \
    "v_bfrev_b32 v1, 1\n\t" \
    "s_mov_b64 exec, -1\n\t" \
    "s_cmp_ge_u32 s24, 4\n\t" \
    "s_cbranch_scc1 .Le9_%=\n\t" \
    "v_bfe_u32 v20, v0, 4, 4\n\t" \
    "v_and_b32 v10, 12, v10\n\t" \
    "v_cvt_f32_ubyte0 v21, v10\n\t" \
    "v_and_b32 v22, 1, v20\n\t" \
    "v_cvt_f32_ubyte0 v22, v22\n\t" \
    "v_mul_f32 v22, 0x3e800000, v22\n\t" \
    "v_lshrrev_b32 v23, 1, v20\n\t" \
    "v_mov_b32 v32, 0\n\t" \
    "v_mov_b32 v33, -1\n\t" \
    "v_mov_b32 v31, 0x3e800000\n\t" \
    "s_mov_b32 s65, 0\n\t" \
    "s_mov_b32 s40, 0\n\t" \
    "s_cmp_eq_u32 s24, 1\n\t" \
    "s_cbranch_scc1 .Lm1_%=\n\t" \
    "s_cmp_eq_u32 s24, 2\n\t" \
    "s_cbranch_scc1 .Lm2_%=\n\t" \
    "s_cmp_eq_u32 s24, 3\n\t" \
    "s_cbranch_scc1 .Lm3_%=\n\t" \
    "s_add_u32 s37, s32, 1048576\n\t" \
    "s_mov_b64 exec, 0xff\n\t" \
    "\n\t" \
    "s_mov_b64 exec, -1\n\t" \
    "v_or_b32 v24, 1, v20\n\t" \
    "v_cvt_f32_ubyte0 v24, v24\n\t" \
    "v_mul_f32 v24, 0x3c800000, v24\n\t" \
    "v_lshrrev_b32 v25, 4, v0\n\t" \
    "v_and_b32 v25, 16, v25\n\t" \
    "v_lshrrev_b32 v26, 2, v11\n\t" \
    "v_or_b32 v25, v25, v26\n\t" \
    "v_cvt_f32_ubyte0 v25, v25\n\t" \
    "v_add_f32 v26, 2.0, v25\n\t" \
    "v_add_f32 v27, 4.0, v25\n\t" \
    "v_add_f32 v28, 4.0, v26\n\t" \
    "v_mov_b32 v29, v22\n\t" \
    "v_mov_b32 v30, v22\n\t" \
    "v_mov_b32 v31, v24\n\t" \
    "s_mov_b32 s64, 0x3e800000\n\t" \
    "s_mov_b32 s65, 0x3e800000\n\t" \
    "v_add_u32 v34, 154880, v0\n\t" \
    "s_branch .Ltl_%=\n\t" \
    ".Lm1_%=:\n\t" \
    "v_lshrrev_b32 v24, 5, v0\n\t" \
    "v_and_b32 v24, 12, v24\n\t" \
    "v_and_or_b32 v24, v20, 3, v24\n\t" \
    "v_cvt_f32_ubyte0 v24, v24\n\t" \
    "v_mul_f32 v24, 0x3d924925, v24\n\t" \
    "v_mul_f32 v32, 0x41000000, v24\n\t" \
    "v_lshrrev_b32 v27, 3, v11\n\t" \
    "v_cvt_f32_ubyte0 v27, v27\n\t" \
    "v_add_f32 v25, -2.0, v27\n\t" \
    "v_add_f32 v26, -1.0, v27\n\t" \
    "v_add_f32 v28, 1.0, v27\n\t" \
    "v_and_b32 v29, 4, v20\n\t" \
    "v_cvt_f32_ubyte0 v29, v29\n\t" \
    "v_mul_f32 v29, 0x3d800000, v29\n\t" \
    "v_mov_b32 v30, v29\n\t" \
    "v_mov_b32 v31, 0xbe800000\n\t" \
    "s_mov_b32 s64, 0\n\t" \
    "s_mov_b32 exec_hi, 0\n\t" \
    "v_mov_b32 v33, 0\n\t" \
    "s_mov_b64 exec, -1\n\t" \
    "v_add_u32 v34, 149760, v0\n\t" \
    "s_branch .Ltl_%=\n\t" \
    ".Lm2_%=:\n\t" \
    "v_add_u32 v24, 6, v20\n\t" \
    "v_cvt_f32_ubyte0 v24, v24\n\t" \
    "v_mul_f32 v24, 0x3d000000, v24\n\t" \
    "v_add_f32 v25, 0.5, v21\n\t" \
    "v_add_f32 v26, 1.0, v25\n\t" \
    "v_add_f32 v27, 2.0, v25\n\t" \
    "v_add_f32 v28, 2.0, v26\n\t" \
    "v_mov_b32 v29, 0\n\t" \
    "v_mov_b32 v30, 0\n\t" \
    "s_mov_b32 s64, 0.5\n\t" \
    "v_add_u32 v34, 152832, v0\n\t" \
    "s_branch .Ltl_%=\n\t" \
    ".Lm3_%=:\n\t" \
    "s_mov_b32 s37, 34635776\n\t" \
    "buffer_load_dword v3, v12, s[4:7], s37 offen sc1\n\t" \
    "v_lshrrev_b32 v8, 2, v0\n\t" \
    "v_and_b32 v8, 28, v8\n\t" \
    "v_add_u32 v8, 156928, v8\n\t" \
    "ds_write_b32 v8, v12\n\t" \
    "v_add_u32 v24, 6, v23\n\t" \
    "v_cvt_f32_ubyte0 v24, v24\n\t" \
    "v_mul_f32 v24, 0x3d924925, v24\n\t" \
    "v_mov_b32 v25, v21\n\t" \
    "v_add_f32 v26, 1.0, v21\n\t" \
    "v_add_f32 v27, 2.0, v21\n\t" \
    "v_add_f32 v28, 2.0, v26\n\t" \
    "v_sub_f32 v29, 0, v22\n\t" \
    "v_sub_f32 v30, 0, v22\n\t" \
    "s_mov_b32 exec_lo, 0\n\t" \
    "v_fma_f32 v25, v21, 0.5, 4.0\n\t" \
    "v_add_f32 v27, 1.0, v25\n\t" \
    "v_fmaak_f32 v26, v21, v31, 0x41200000\n\t" \
    "v_mov_b32 v28, v26\n\t" \
    "v_add_f32 v30, v31, v30\n\t" \
    "s_mov_b64 exec, -1\n\t" \
    "s_mov_b32 s40, 0x11\n\t" \
    "v_add_u32 v34, 151808, v0\n\t" \
    ".Ltl_%=:\n\t" \
    "v_fma_f32 v36, v24, v25, v29\n\t" \
    "v_fma_f32 v38, v24, v26, v29\n\t" \
    "v_fma_f32 v40, v24, v27, v29\n\t" \
    "v_fma_f32 v42, v24, v28, v30\n\t" \
    "v_add_f32 v37, v36, v31\n\t" \
    "v_add_f32 v39, v38, v31\n\t" \
    "v_add_f32 v41, v40, v31\n\t" \
    "v_add_f32 v43, v42, v31\n\t" \
    "v_cos_f32 v36, v36\n\t" \
    "v_cos_f32 v37, v37\n\t" \
    "v_cos_f32 v38, v38\n\t" \
    "v_cos_f32 v39, v39\n\t" \
    "v_cos_f32 v40, v40\n\t" \
    "v_cos_f32 v41, v41\n\t" \
    "v_cos_f32 v42, v42\n\t" \
    "v_cos_f32 v43, v43\n\t" \
    "s_bitcmp0_b32 s40, 4\n\t" \
    "s_cbranch_scc1 .Lt2_%=\n\t" \
    "v_mul_f32 v36, 0x3b924925, v36\n\t" \
    "v_mul_f32 v37, 0x3b924925, v37\n\t" \
    "v_mul_f32 v38, 0x3b924925, v38\n\t" \
    "v_mul_f32 v39, 0x3b924925, v39\n\t" \
    "v_mul_f32 v40, 0x3b924925, v40\n\t" \
    "v_mul_f32 v41, 0x3b924925, v41\n\t" \
    "v_mul_f32 v42, 0x3b924925, v42\n\t" \
    "v_mul_f32 v43, 0x3b924925, v43\n\t" \
    ".Lt2_%=:\n\t" \
    "v_cvt_pk_bf16_f32 v44, v36, v37\n\t" \
    "v_cvt_pk_bf16_f32 v45, v38, v39\n\t" \
    "v_cvt_pk_bf16_f32 v46, v40, v41\n\t" \
    "v_cvt_pk_bf16_f32 v47, v42, v43\n\t" \
    "v_and_b32 v44, v33, v44\n\t" \
    "v_and_b32 v45, v33, v45\n\t" \
    "v_mov_b32 v33, -1\n\t" \
    "v_add_f32 v24, s64, v24\n\t" \
    "v_add_f32 v31, s65, v31\n\t" \
    "v_add_f32 v29, v32, v29\n\t" \
    "v_add_f32 v30, v32, v30\n\t" \
    "ds_write_b128 v34, v[44:47]\n\t" \
    "s_add_u32 s40, s40, 1\n\t" \
    "s_bitcmp1_b32 s40, 0\n\t" \
    "v_add_u32 v34, 0x400, v34\n\t" \
    "s_cbranch_scc1 .Ltl_%=\n\t" \
    "s_bitcmp1_b32 s40, 2\n\t" \
    "s_cbranch_scc1 .Lt4_%=\n\t" \
    "s_mov_b32 s40, 2\n\t" \
    "s_lshl2_add_u32 s37, s28, s24\n\t" \
    "s_lshl_b32 s37, s37, 2\n\t" \
    "v_lshrrev_b32 v25, 2, v20\n\t" \
    "v_add_u32 v25, s37, v25\n\t" \
    "v_mul_u32_u24 v26, 147, v25\n\t" \
    "v_lshrrev_b32 v26, 10, v26\n\t" \
    "v_lshl_or_b32 v24, v26, 1, 1\n\t" \
    "v_cvt_f32_ubyte0 v24, v24\n\t" \
    "v_mul_f32 v24, 0x3c800000, v24\n\t" \
    "v_mul_u32_u24 v26, 7, v26\n\t" \
    "v_sub_u32 v25, v25, v26\n\t" \
    "v_xor_b32 v22, s41, v20\n\t" \
    "v_bfe_u32 v32, v22, 1, 1\n\t" \
    "v_and_b32 v22, 1, v22\n\t" \
    "v_cvt_f32_ubyte0 v22, v22\n\t" \
    "v_mul_f32 v22, 0x3e800000, v22\n\t" \
    "v_lshl_or_b32 v32, v25, 1, v32\n\t" \
    "v_cvt_f32_ubyte0 v32, v32\n\t" \
    "v_mul_f32 v32, 0x3e924925, v32\n\t" \
    "v_mul_f32 v29, 0x3d800000, v21\n\t" \
    "v_fma_f32 v29, v29, v32, v22\n\t" \
    "v_mov_b32 v30, v29\n\t" \
    "v_mov_b32 v31, v24\n\t" \
    "v_mov_b32 v25, 0\n\t" \
    "v_mov_b32 v26, 2.0\n\t" \
    "v_mov_b32 v27, 4.0\n\t" \
    "v_mov_b32 v28, 0x40c00000\n\t" \
    "s_mov_b32 s64, 0\n\t" \
    "s_mov_b32 s65, 0\n\t" \
    "s_lshl_b32 s37, s24, 11\n\t" \
    "s_add_u32 s37, s37, 49664\n\t" \
    "v_add_u32 v34, s37, v0\n\t" \
    "s_branch .Ltl_%=\n\t" \
    ".Lt4_%=:\n\t" \
    ".Lt3_%=:\n\t" \
    "s_cmp_lg_u32 s24, 3\n\t" \
    "s_cbranch_scc1 .Lx2_%=\n\t" \
    "v_lshrrev_b32 v8, 7, v0\n\t" \
    "v_mul_u32_u24 v8, 96, v8\n\t" \
    "v_and_b32 v9, 16, v0\n\t" \
    "v_bfe_u32 v10, v0, 5, 2\n\t" \
    "v_add3_u32 v8, v8, v9, v10\n\t" \
    "v_mul_u32_u24 v8, 192, v8\n\t" \
    "s_mul_i32 s37, s28, 129024\n\t" \
    "s_add_u32 s37, s37, s31\n\t" \
    "s_mov_b32 m0, 157312\n\t" \
    "s_mov_b32 exec_hi, 0xffffff\n\t" \
    "buffer_load_dword v8, s[12:15], s37 offen lds\n\t" \
    "s_mov_b64 exec, -1\n\t" \
    ".Lx2_%=:\n\t" \
    ".Le9_%=:\n\t" \
    "s_lshr_b32 s37, s24, 2\n\t" \
    "s_and_b32 s38, s24, 2\n\t" \
    "s_mul_i32 s39, s37, 24832\n\t" \
    "s_lshl4_add_u32 s39, s38, s39\n\t" \
    "v_lshrrev_b32 v4, 6, v0\n\t" \
    "v_and_b32 v5, 12, v4\n\t" \
    "v_add_u32 v6, v4, v5\n\t" \
    "v_lshrrev_b32 v7, 1, v0\n\t" \
    "v_and_b32 v7, 24, v7\n\t" \
    "v_and_b32 v4, 11, v4\n\t" \
    "v_mul_u32_u24 v4, 192, v4\n\t" \
    "v_bfe_u32 v5, v0, 8, 1\n\t" \
    "v_mul_u32_u24 v5, 3104, v5\n\t" \
    "v_add3_u32 v4, v4, v5, v7\n\t" \
    "v_add_u32 v8, s39, v4\n\t" \
    "v_mov_b32 v9, 156928\n\t" \
    "v_add_u32 v10, 149760, v0\n\t" \
    "s_lshl2_add_u32 s37, s37, s38\n\t" \
    "s_lshl_b32 s37, s37, 7\n\t" \
    "s_mul_i32 s53, s27, 28\n\t" \
    "s_add_u32 s53, s53, s26\n\t" \
    "s_andn2_b32 s39, s53, 7\n\t" \
    "s_add_u32 s53, s53, s39\n\t" \
    "s_lshl_b32 s53, s53, 10\n\t" \
    "s_add_u32 s37, s37, s53\n\t" \
    "s_and_b32 s38, s24, 1\n\t" \
    "s_lshl_b32 s39, s38, 13\n\t" \
    "s_add_u32 s37, s37, s39\n\t" \
    "s_cmp_lt_u32 s25, 126\n\t" \
    "s_cselect_b32 s43, 0, 0x80000000\n\t" \
    "s_add_u32 s37, s37, s43\n\t" \
    "v_lshrrev_b32 v11, 8, v0\n\t" \
    "v_and_b32 v12, 0xf0, v0\n\t" \
    "v_mul_u32_u24 v4, 8486912, v11\n\t" \
    "v_add3_u32 v13, v4, v12, s37\n\t" \
    "s_lshl_b32 s39, s38, 10\n\t" \
    "v_add_u32 v14, s39, v10\n\t" \
    "s_lshl_b32 s39, s38, 5\n\t" \
    "s_add_u32 s39, s39, 116736\n\t" \
    "v_lshl_add_u32 v4, v6, 6, v7\n\t" \
    "v_lshrrev_b32 v5, 3, v0\n\t" \
    "v_and_b32 v5, 32, v5\n\t" \
    "v_xor_b32 v5, s39, v5\n\t" \
    "v_add_u32 v15, v4, v5\n\t" \
    "s_and_b32 s37, s24, 6\n\t" \
    "s_lshl_b32 s37, s37, 10\n\t" \
    "v_add_u32 v16, s37, v0\n\t" \
    "s_cmp_ge_u32 s25, 126\n\t" \
    "s_cbranch_scc1 .Lqa_%=\n\t" \
    ".Lqd_%=:\n\t" \
    "s_waitcnt lgkmcnt(0)\n\t" \
    "s_barrier\n\t" \
    "ds_read_b128 v[24:27], v14 offset:5120\n\t" \
    "ds_read_b128 v[28:31], v16 offset:49664\n\t" \
    "ds_read_b128 v[32:35], v16 offset:50688\n\t" \
    "s_and_b32 s33, s2, 7\n\t" \
    "s_lshl_b32 s33, s33, 15\n\t" \
    "s_add_u32 s33, s33, 33554432\n\t" \
    "s_lshr_b32 s37, s29, 2\n\t" \
    "s_mul_i32 s39, s30, 3\n\t" \
    "s_add_u32 s37, s37, s39\n\t" \
    "s_mul_i32 s37, s37, 114688\n\t" \
    "s_and_b32 s39, s29, 3\n\t" \
    "s_lshl_b32 s39, s39, 8\n\t" \
    "s_add_u32 s37, s37, s39\n\t" \
    "s_add_u32 s37, s37, s43\n\t" \
    "s_add_u32 s37, s37, 2097152\n\t" \
    "s_and_b32 s39, s24, 6\n\t" \
    "s_lshl3_add_u32 s39, s28, s39\n\t" \
    "s_lshl_b32 s39, s39, 1\n\t" \
    "v_add_u32 v14, s39, v11\n\t" \
    "v_mul_u32_u24 v17, 147, v14\n\t" \
    "v_lshrrev_b32 v17, 10, v17\n\t" \
    "s_and_b32 s39, s24, 1\n\t" \
    "v_subrev_u32 v18, s39, v17\n\t" \
    "v_mul_i32_i24 v18, 7, v18\n\t" \
    "v_sub_u32 v14, v14, v18\n\t" \
    "v_and_b32 v18, 14, v14\n\t" \
    "v_add_lshl_u32 v14, v14, v18, 12\n\t" \
    "v_and_b32 v18, 7, v17\n\t" \
    "v_mul_u32_u24 v18, 4243456, v18\n\t" \
    "v_add3_u32 v14, v14, v18, v12\n\t" \
    "v_and_b32 v17, 8, v17\n\t" \
    "v_xor_b32 v17, 8, v17\n\t" \
    "v_lshl_add_u32 v14, v17, 10, v14\n\t" \
    "v_add_u32 v19, s37, v14\n\t" \
    "s_lshl_b32 s43, s41, 10\n\t" \
    "s_and_b32 s37, s2, 0x70\n\t" \
    "s_lshl_b32 s37, s37, 1\n\t" \
    "s_lshl2_add_u32 s57, s24, s37\n\t" \
    "s_min_u32 s58, s57, 248\n\t" \
    "s_mul_i32 s59, s58, 147\n\t" \
    "s_lshr_b32 s59, s59, 12\n\t" \
    "s_mul_i32 s37, s59, 28\n\t" \
    "s_sub_u32 s60, s58, s37\n\t" \
    "s_andn2_b32 s39, s58, 7\n\t" \
    "s_add_u32 s51, s58, s39\n\t" \
    "s_lshl_b32 s51, s51, 10\n\t" \
    "s_sub_u32 s37, s60, 1\n\t" \
    "s_cselect_b32 s52, 27, -1\n\t" \
    "s_add_u32 s52, s52, s58\n\t" \
    "s_andn2_b32 s39, s52, 7\n\t" \
    "s_add_u32 s52, s52, s39\n\t" \
    "s_lshl_b32 s52, s52, 10\n\t" \
    "v_lshrrev_b32 v16, 9, v0\n\t" \
    "v_and_b32 v18, 0x2f0, v0\n\t" \
    "v_and_b32 v17, 1, v11\n\t" \
    "v_lshl_add_u32 v14, v17, 11, v18\n\t" \
    "v_add_u32 v36, s51, v14\n\t" \
    "v_add_u32 v37, s52, v0\n\t" \
    "s_mul_i32 s44, s24, 7296\n\t" \
    "s_mul_i32 s37, s60, 344064\n\t" \
    "s_mul_i32 s39, s59, 4992\n\t" \
    "s_add_u32 s37, s37, s39\n\t" \
    "s_cmp_eq_u32 s59, 8\n\t" \
    "s_movk_i32 s39, 2496\n\t" \
    "s_cselect_b32 s39, 1536, s39\n\t" \
    "s_add_u32 s37, s37, s39\n\t" \
    "s_and_b32 s39, s8, 0x1fffff\n\t" \
    "s_add_u32 s37, s37, s39\n\t" \
    "v_bfe_u32 v38, v0, 5, 2\n\t" \
    "v_lshl_add_u32 v38, v16, 4, v38\n\t" \
    "v_mul_u32_u24 v38, 43008, v38\n\t" \
    "v_add_u32 v39, s37, v38\n\t" \
    "s_lshr_b32 s37, s60, 1\n\t" \
    "s_mul_i32 s37, s37, 9\n\t" \
    "s_add_u32 s37, s37, s59\n\t" \
    "v_mad_u32_u24 v38, v16, 9, s37\n\t" \
    "v_lshrrev_b32 v18, 5, v38\n\t" \
    "s_and_b32 s29, s51, 0x1000\n\t" \
    "s_and_b32 s30, s52, 0x1000\n\t" \
    "s_lshl_b32 s37, s24, 2\n\t" \
    "s_lshl1_add_u32 s39, s24, 8\n\t" \
    "s_min_u32 s61, s37, s39\n\t" \
    "s_add_u32 s37, s37, 2\n\t" \
    "s_add_u32 s39, s24, 20\n\t" \
    "s_cmp_lt_u32 s24, 4\n\t" \
    "s_cselect_b32 s62, s37, s39\n\t" \
    "s_bfe_u32 s63, s2, 0x10003\n\t" \
    "s_mul_i32 s47, s63, 28\n\t" \
    "s_add_u32 s45, s47, s61\n\t" \
    "s_add_u32 s46, s47, s62\n\t" \
    "s_andn2_b32 s39, s45, 7\n\t" \
    "s_add_u32 s45, s45, s39\n\t" \
    "s_lshl_b32 s45, s45, 10\n\t" \
    "s_andn2_b32 s39, s46, 7\n\t" \
    "s_add_u32 s46, s46, s39\n\t" \
    "s_lshl_b32 s46, s46, 10\n\t" \
    "v_lshlrev_b32 v17, 4, v11\n\t" \
    "v_lshrrev_b32 v14, 6, v12\n\t" \
    "v_mul_u32_u24 v14, 7296, v14\n\t" \
    "v_and_b32 v38, 16, v0\n\t" \
    "v_mul_u32_u24 v38, 226, v38\n\t" \
    "v_and_b32 v40, 32, v0\n\t" \
    "v_lshl_add_u32 v38, v40, 1, v38\n\t" \
    "v_add3_u32 v14, v14, v38, v17\n\t" \
    "s_lshl_b32 s37, s61, 7\n\t" \
    "v_add_u32 v44, s37, v14\n\t" \
    "s_and_b32 s37, s62, 30\n\t" \
    "s_lshl_b32 s37, s37, 7\n\t" \
    "v_add_u32 v45, s37, v14\n\t" \
    "s_mov_b32 s1, 0\n\t" \
    "s_mov_b32 s41, 0x18000\n\t" \
    "s_lshl_b32 s37, s63, 9\n\t" \
    "s_add_u32 s51, s51, s37\n\t" \
    "s_cmp_lt_u32 s57, 252\n\t" \
    "s_cselect_b32 s51, s51, 0x80000000\n\t" \
    "s_and_b32 s37, s2, 0x87\n\t" \
    "s_lshl4_add_u32 s37, s24, s37\n\t" \
    "s_lshl_b32 s55, s37, 7\n\t" \
    "v_and_b32 v46, 16, v0\n\t" \
    "v_lshlrev_b32 v47, 6, v46\n\t" \
    "v_mad_u32_u24 v38, v12, 12, v17\n\t" \
    "v_and_b32 v14, 0x170, v0\n\t" \
    "v_and_b32 v41, 0x80, v0\n\t" \
    "v_lshl_add_u32 v14, v41, 3, v14\n\t" \
    "v_lshl_add_u32 v14, v16, 11, v14\n\t" \
    "v_add_u32 v42, s51, v14\n\t" \
    "s_mul_i32 s37, s24, 2064\n\t" \
    "v_and_b32 v14, 0x300, v0\n\t" \
    "v_add3_u32 v43, v14, v0, s37\n\t" \
    "s_lshl_b32 s37, s37, 1\n\t" \
    "v_lshlrev_b32 v14, 7, v16\n\t" \
    "v_xor_b32 v14, v14, v0\n\t" \
    "v_add_u32 v40, s37, v14\n\t" \
    "v_lshrrev_b32 v14, 5, v12\n\t" \
    "v_mul_u32_u24 v14, 2064, v14\n\t" \
    "v_lshl_add_u32 v14, v46, 2, v14\n\t" \
    "s_lshl_b32 s37, s24, 8\n\t" \
    "v_add3_u32 v48, v14, v17, s37\n\t" \
    "v_bfe_u32 v14, v0, 7, 1\n\t" \
    "v_lshl_add_u32 v14, v11, 2, v14\n\t" \
    "v_mul_u32_u24 v14, 2064, v14\n\t" \
    "v_lshrrev_b32 v41, 3, v0\n\t" \
    "v_and_or_b32 v14, v41, 8, v14\n\t" \
    "s_and_b32 s37, s24, 3\n\t" \
    "s_lshl_b32 s37, s37, 6\n\t" \
    "s_and_b32 s39, s24, 4\n\t" \
    "s_lshl_b32 s39, s39, 8\n\t" \
    "s_or_b32 s37, s37, s39\n\t" \
    "v_and_or_b32 v41, v0, 32, s37\n\t" \
    "v_mul_u32_u24 v46, 40, v46\n\t" \
    "v_xor_b32 v41, v41, v46\n\t" \
    "v_add_u32 v49, v14, v41\n\t" \
    "ds_read_b128 v[52:55], v10 offset:0\n\t" \
    "ds_read_b128 v[56:59], v10 offset:1024\n\t" \
    "s_sleep 6\n\t" \
    "s_and_b32 s37, s24, 1\n\t" \
    "s_lshl_b32 s37, s37, 4\n\t" \
    "s_lshr_b32 s39, s24, 1\n\t" \
    "s_add_u32 s37, s37, s39\n\t" \
    "s_mul_i32 s37, s37, 192\n\t" \
    "s_add_u32 s37, s37, s31\n\t" \
    "s_lshl_b32 s39, s24, 12\n\t" \
    "s_add_u32 m0, s39, 116736\n\t" \
    "s_add_u32 s38, s37, 0\n\t" \
    "buffer_load_dwordx4 v1, s[12:15], s38 offen  lds\n\t" \
    "s_add_u32 m0, s39, 117760\n\t" \
    "s_add_u32 s38, s37, 258048\n\t" \
    "buffer_load_dwordx4 v1, s[12:15], s38 offen  lds\n\t" \
    "s_add_u32 m0, s39, 118784\n\t" \
    "s_add_u32 s38, s37, 516096\n\t" \
    "buffer_load_dwordx4 v1, s[12:15], s38 offen  lds\n\t" \
    "s_add_u32 m0, s39, 119808\n\t" \
    "s_add_u32 s38, s37, 774144\n\t" \
    "buffer_load_dwordx4 v1, s[12:15], s38 offen  lds\n\t" \
    "s_cmp_lg_u32 s24, 3\n\t" \
    "s_cbranch_scc1 .Lr9_%=\n\t" \
    "s_and_b32 s37, s2, 7\n\t" \
    "s_lshl_b32 s37, s37, 10\n\t" \
    "s_add_u32 s37, s37, 34643968\n\t" \
    "s_buffer_load_dword s56, s[4:7], s37 offset:0x0\n\t" \
    "s_buffer_load_dword s57, s[4:7], s37 offset:0x80\n\t" \
    "s_buffer_load_dword s58, s[4:7], s37 offset:0x100\n\t" \
    "s_buffer_load_dword s59, s[4:7], s37 offset:0x180\n\t" \
    "s_buffer_load_dword s60, s[4:7], s37 offset:0x200\n\t" \
    "s_buffer_load_dword s61, s[4:7], s37 offset:0x280\n\t" \
    "s_buffer_load_dword s62, s[4:7], s37 offset:0x300\n\t" \
    "s_buffer_load_dword s63, s[4:7], s37 offset:0x380\n\t" \
    "s_mov_b32 s40, 0\n\t" \
    ".Lr1_%=:\n\t" \
    "s_waitcnt lgkmcnt(0)\n\t" \
    "s_memrealtime s[22:23]\n\t" \
    "s_waitcnt lgkmcnt(0)\n\t" \
    "s_mov_b32 s37, 1000\n\t" \
    "s_mov_b32 s38, 0\n\t" \
    "s_sub_u32 s39, s22, s56\n\t" \
    "s_min_u32 s37, s37, s39\n\t" \
    "s_max_u32 s38, s38, s39\n\t" \
    "s_sub_u32 s39, s22, s57\n\t" \
    "s_min_u32 s37, s37, s39\n\t" \
    "s_max_u32 s38, s38, s39\n\t" \
    "s_sub_u32 s39, s22, s58\n\t" \
    "s_min_u32 s37, s37, s39\n\t" \
    "s_max_u32 s38, s38, s39\n\t" \
    "s_sub_u32 s39, s22, s59\n\t" \
    "s_min_u32 s37, s37, s39\n\t" \
    "s_max_u32 s38, s38, s39\n\t" \
    "s_sub_u32 s39, s22, s60\n\t" \
    "s_min_u32 s37, s37, s39\n\t" \
    "s_max_u32 s38, s38, s39\n\t" \
    "s_sub_u32 s39, s22, s61\n\t" \
    "s_min_u32 s37, s37, s39\n\t" \
    "s_max_u32 s38, s38, s39\n\t" \
    "s_sub_u32 s39, s22, s62\n\t" \
    "s_min_u32 s37, s37, s39\n\t" \
    "s_max_u32 s38, s38, s39\n\t" \
    "s_sub_u32 s39, s22, s63\n\t" \
    "s_min_u32 s37, s37, s39\n\t" \
    "s_max_u32 s38, s38, s39\n\t" \
    "s_cmp_gt_u32 s38, 1000\n\t" \
    "s_cbranch_scc1 .Lr2_%=\n\t" \
    ".Lr3_%=:\n\t" \
    "s_sub_u32 s35, s22, s37\n\t" \
    "s_waitcnt vmcnt(5)\n\t" \
    "v_readfirstlane_b32 s34, v3\n\t" \
    "s_mul_i32 s34, s34, 1664525\n\t" \
    "s_add_u32 s34, s34, 1013904223\n\t" \
    "s_or_b32 s34, s34, 1\n\t" \
    "v_mov_b32 v60, s34\n\t" \
    "v_mov_b32 v61, s35\n\t" \
    "ds_write_b64 v9, v[60:61] offset:32\n\t" \
    ".Lr9_%=:\n\t" \
    "s_cmp_ge_u32 s25, 126\n\t" \
    "s_cbranch_scc1 .Lpb_%=\n\t" \
    ".Lpe_%=:\n\t" \
    "s_cmp_lt_u32 s24, 4\n\t" \
    "s_cbranch_scc1 .Lw_%=\n\t" \
    "s_waitcnt vmcnt(4)\n\t" \
    ".Lw_%=:\n\t" \
    "s_waitcnt lgkmcnt(0)\n\t" \
    "s_barrier\n\t" \
    "ds_read_b64_tr_b16 v[64:65], v8 offset:0\n\t" \
    "ds_read_b64_tr_b16 v[66:67], v8 offset:768\n\t" \
    "ds_read_b64_tr_b16 v[68:69], v8 offset:12416\n\t" \
    "ds_read_b64_tr_b16 v[70:71], v8 offset:13184\n\t" \
    "ds_read_b64_tr_b16 v[72:73], v8 offset:6208\n\t" \
    "ds_read_b64_tr_b16 v[74:75], v8 offset:6976\n\t" \
    "ds_read_b64_tr_b16 v[76:77], v8 offset:18624\n\t" \
    "ds_read_b64_tr_b16 v[78:79], v8 offset:19392\n\t" \
    "ds_read_b128 v[80:83], v9 offset:32\n\t" \
    "s_waitcnt lgkmcnt(7)\n\t" \
    "v_mfma_f32_16x16x32_bf16 v[84:87], v[24:27], v[64:67], 0\n\t" \
    "s_waitcnt lgkmcnt(5)\n\t" \
    "v_mfma_f32_16x16x32_bf16 v[88:91], v[24:27], v[68:71], 0\n\t" \
    "s_waitcnt lgkmcnt(3)\n\t" \
    "v_mfma_f32_16x16x32_bf16 v[92:95], v[24:27], v[72:75], 0\n\t" \
    "s_waitcnt lgkmcnt(1)\n\t" \
    "v_mfma_f32_16x16x32_bf16 v[96:99], v[24:27], v[76:79], 0\n\t" \
    "s_waitcnt lgkmcnt(0)\n\t" \
    "v_readfirstlane_b32 s34, v80\n\t" \
    "v_readfirstlane_b32 s35, v81\n\t" \
    "s_lshl_b32 s37, s24, 8\n\t" \
    "s_add_u32 s37, s37, s2\n\t" \
    "s_cmp_eq_u32 s37, 255\n\t" \
    "s_cbranch_scc1 .Lpp_%=\n\t" \
    ".Lpq_%=:\n\t" \
    "v_add_u32 v84, 0x8000, v84\n\t" \
    "v_add_u32 v86, 0x8000, v86\n\t" \
    "v_add_u32 v88, 0x8000, v88\n\t" \
    "v_add_u32 v90, 0x8000, v90\n\t" \
    "v_cvt_pk_bf16_f32 v108, v84, v85\n\t" \
    "v_cvt_pk_bf16_f32 v110, v88, v89\n\t" \
    "v_cvt_pk_bf16_f32 v109, v92, v93\n\t" \
    "v_cvt_pk_bf16_f32 v111, v96, v97\n\t" \
    "v_and_b32 v108, -2, v108\n\t" \
    "v_and_b32 v110, -2, v110\n\t" \
    "v_cvt_pk_bf16_f32 v112, v86, v87\n\t" \
    "v_cvt_pk_bf16_f32 v114, v90, v91\n\t" \
    "v_cvt_pk_bf16_f32 v113, v94, v95\n\t" \
    "v_cvt_pk_bf16_f32 v115, v98, v99\n\t" \
    "v_and_b32 v112, -2, v112\n\t" \
    "v_and_b32 v114, -2, v114\n\t" \
    ".Lh27_%=:\n\t" \
    "s_memrealtime s[22:23]\n\t" \
    "s_waitcnt lgkmcnt(0)\n\t" \
    "s_sub_u32 s22, s22, s35\n\t" \
    "s_cmp_lt_u32 s22, 205\n\t" \
    "s_cbranch_scc1 .Lh27_%=\n\t" \
    "buffer_store_dwordx4 v[108:111], v13, s[4:7], 0 offen offset:0 sc1\n\t" \
    "s_mov_b32 s42, 4243456\n\t" \
    "buffer_store_dwordx4 v[112:115], v13, s[4:7], s42 offen offset:0 sc1\n\t" \
    "ds_read_b64_tr_b16 v[116:117], v8 offset:64\n\t" \
    "ds_read_b64_tr_b16 v[118:119], v8 offset:832\n\t" \
    "ds_read_b64_tr_b16 v[120:121], v8 offset:12480\n\t" \
    "ds_read_b64_tr_b16 v[122:123], v8 offset:13248\n\t" \
    "ds_read_b64_tr_b16 v[124:125], v8 offset:6272\n\t" \
    "ds_read_b64_tr_b16 v[126:127], v8 offset:7040\n\t" \
    "ds_read_b64_tr_b16 v[128:129], v8 offset:18688\n\t" \
    "ds_read_b64_tr_b16 v[130:131], v8 offset:19456\n\t" \
    "ds_read_b64_tr_b16 v[132:133], v8 offset:128\n\t" \
    "ds_read_b64_tr_b16 v[134:135], v8 offset:896\n\t" \
    "ds_read_b64_tr_b16 v[136:137], v8 offset:12544\n\t" \
    "ds_read_b64_tr_b16 v[138:139], v8 offset:13312\n\t" \
    "ds_read_b64_tr_b16 v[140:141], v8 offset:6336\n\t" \
    "ds_read_b64_tr_b16 v[142:143], v8 offset:7104\n\t" \
    "ds_read_b64_tr_b16 v[144:145], v8 offset:18752\n\t" \
    "ds_read_b64_tr_b16 v[146:147], v8 offset:19520\n\t" \
    "v_mov_b32 v148, s34\n\t" \
    "v_mov_b32 v149, s34\n\t" \
    "v_mov_b32 v150, s34\n\t" \
    "v_mov_b32 v151, s34\n\t" \
    "s_and_b32 s37, s2, 243\n\t" \
    "s_cmp_eq_u32 s37, 243\n\t" \
    "s_cbranch_scc1 .Lskip_%=\n\t" \
    "s_waitcnt lgkmcnt(14)\n\t" \
    "v_mfma_f32_16x16x32_bf16 v[152:155], v[24:27], v[116:119], 0\n\t" \
    "s_waitcnt lgkmcnt(12)\n\t" \
    "v_mfma_f32_16x16x32_bf16 v[156:159], v[24:27], v[120:123], 0\n\t" \
    "s_waitcnt lgkmcnt(10)\n\t" \
    "v_mfma_f32_16x16x32_bf16 v[160:163], v[24:27], v[124:127], 0\n\t" \
    "s_waitcnt lgkmcnt(8)\n\t" \
    "v_mfma_f32_16x16x32_bf16 v[164:167], v[24:27], v[128:131], 0\n\t" \
    "s_waitcnt lgkmcnt(6)\n\t" \
    "v_mfma_f32_16x16x32_bf16 v[168:171], v[24:27], v[132:135], 0\n\t" \
    "s_waitcnt lgkmcnt(4)\n\t" \
    "v_mfma_f32_16x16x32_bf16 v[172:175], v[24:27], v[136:139], 0\n\t" \
    "s_waitcnt lgkmcnt(2)\n\t" \
    "v_mfma_f32_16x16x32_bf16 v[176:179], v[24:27], v[140:143], 0\n\t" \
    "s_waitcnt lgkmcnt(0)\n\t" \
    "v_mfma_f32_16x16x32_bf16 v[180:183], v[24:27], v[144:147], 0\n\t" \
    "v_add_u32 v152, 0x8000, v152\n\t" \
    "v_add_u32 v154, 0x8000, v154\n\t" \
    "v_add_u32 v156, 0x8000, v156\n\t" \
    "v_add_u32 v158, 0x8000, v158\n\t" \
    "v_cvt_pk_bf16_f32 v184, v152, v153\n\t" \
    "v_cvt_pk_bf16_f32 v186, v156, v157\n\t" \
    "v_cvt_pk_bf16_f32 v185, v160, v161\n\t" \
    "v_cvt_pk_bf16_f32 v187, v164, v165\n\t" \
    "v_and_b32 v184, -2, v184\n\t" \
    "v_and_b32 v186, -2, v186\n\t" \
    "v_cvt_pk_bf16_f32 v188, v154, v155\n\t" \
    "v_cvt_pk_bf16_f32 v190, v158, v159\n\t" \
    "v_cvt_pk_bf16_f32 v189, v162, v163\n\t" \
    "v_cvt_pk_bf16_f32 v191, v166, v167\n\t" \
    "v_and_b32 v188, -2, v188\n\t" \
    "v_and_b32 v190, -2, v190\n\t" \
    "v_add_u32 v168, 0x8000, v168\n\t" \
    "v_add_u32 v170, 0x8000, v170\n\t" \
    "v_add_u32 v172, 0x8000, v172\n\t" \
    "v_add_u32 v174, 0x8000, v174\n\t" \
    "v_cvt_pk_bf16_f32 v4, v168, v169\n\t" \
    "v_cvt_pk_bf16_f32 v6, v172, v173\n\t" \
    "v_cvt_pk_bf16_f32 v5, v176, v177\n\t" \
    "v_cvt_pk_bf16_f32 v7, v180, v181\n\t" \
    "v_and_b32 v4, -2, v4\n\t" \
    "v_and_b32 v6, -2, v6\n\t" \
    "v_cvt_pk_bf16_f32 v60, v170, v171\n\t" \
    "v_cvt_pk_bf16_f32 v62, v174, v175\n\t" \
    "v_cvt_pk_bf16_f32 v61, v178, v179\n\t" \
    "v_cvt_pk_bf16_f32 v63, v182, v183\n\t" \
    "v_and_b32 v60, -2, v60\n\t" \
    "v_and_b32 v62, -2, v62\n\t" \
    ".Lh28_%=:\n\t" \
    "s_memrealtime s[22:23]\n\t" \
    "s_waitcnt lgkmcnt(0)\n\t" \
    "s_sub_u32 s22, s22, s35\n\t" \
    "s_cmp_lt_u32 s22, 255\n\t" \
    "s_cbranch_scc1 .Lh28_%=\n\t" \
    "s_mov_b32 s42, 524288\n\t" \
    "buffer_store_dwordx4 v[184:187], v13, s[4:7], s42 offen offset:0 sc1\n\t" \
    "s_mov_b32 s42, 4767744\n\t" \
    "buffer_store_dwordx4 v[188:191], v13, s[4:7], s42 offen offset:0 sc1\n\t" \
    "s_waitcnt vmcnt(4)\n\t" \
    "; wait\n\t" \
    "s_waitcnt lgkmcnt(0)\n\t" \
    "s_barrier\n\t" \
    "s_lshl_b32 s37, s24, 18\n\t" \
    "s_add_u32 s37, s37, s32\n\t" \
    "s_bitcmp1_b32 29, s24\n\t" \
    "s_cselect_b32 s39, 0, 0x80000000\n\t" \
    "v_add_u32 v16, s39, v2\n\t" \
    "v_mov_b32 v20, 0\n\t" \
    "v_mov_b32 v21, 0\n\t" \
    "v_mov_b32 v22, 0\n\t" \
    "v_mov_b32 v23, 0\n\t" \
    "s_cmp_eq_u32 s24, 4\n\t" \
    "s_cselect_b64 exec, 0xff, -1\n\t" \
    "buffer_store_dwordx4 v[20:23], v16, s[4:7], s37 offen sc1\n\t" \
    "s_mov_b64 exec, -1\n\t" \
    ".Lh29_%=:\n\t" \
    "s_memrealtime s[22:23]\n\t" \
    "s_waitcnt lgkmcnt(0)\n\t" \
    "s_sub_u32 s22, s22, s35\n\t" \
    "s_cmp_lt_u32 s22, 305\n\t" \
    "s_cbranch_scc1 .Lh29_%=\n\t" \
    "s_mov_b32 s42, 1048576\n\t" \
    "buffer_store_dwordx4 v[4:7], v13, s[4:7], s42 offen offset:0 sc1\n\t" \
    "s_mov_b32 s42, 5292032\n\t" \
    "buffer_store_dwordx4 v[60:63], v13, s[4:7], s42 offen offset:0 sc1\n\t" \
    "ds_read_b64_tr_b16 v[64:65], v15 offset:0\n\t" \
    "ds_read_b64_tr_b16 v[66:67], v15 offset:256\n\t" \
    "ds_read_b64_tr_b16 v[68:69], v15 offset:2048\n\t" \
    "ds_read_b64_tr_b16 v[70:71], v15 offset:2304\n\t" \
    "ds_read_b64_tr_b16 v[72:73], v15 offset:4096\n\t" \
    "ds_read_b64_tr_b16 v[74:75], v15 offset:4352\n\t" \
    "ds_read_b64_tr_b16 v[76:77], v15 offset:6144\n\t" \
    "ds_read_b64_tr_b16 v[78:79], v15 offset:6400\n\t" \
    "ds_read_b64_tr_b16 v[80:81], v15 offset:8192\n\t" \
    "ds_read_b64_tr_b16 v[82:83], v15 offset:8448\n\t" \
    "ds_read_b64_tr_b16 v[100:101], v15 offset:10240\n\t" \
    "ds_read_b64_tr_b16 v[102:103], v15 offset:10496\n\t" \
    "ds_read_b64_tr_b16 v[104:105], v15 offset:12288\n\t" \
    "ds_read_b64_tr_b16 v[106:107], v15 offset:12544\n\t" \
    "s_waitcnt lgkmcnt(12)\n\t" \
    "v_mfma_f32_16x16x32_bf16 v[84:87], v[28:31], v[64:67], 0\n\t" \
    "ds_read_b64_tr_b16 v[88:89], v15 offset:14336\n\t" \
    "ds_read_b64_tr_b16 v[90:91], v15 offset:14592\n\t" \
    "s_waitcnt lgkmcnt(12)\n\t" \
    "v_mfma_f32_16x16x32_bf16 v[84:87], v[32:35], v[68:71], v[84:87]\n\t" \
    "ds_read_b64_tr_b16 v[92:93], v15 offset:16384\n\t" \
    "ds_read_b64_tr_b16 v[94:95], v15 offset:16640\n\t" \
    "s_waitcnt lgkmcnt(12)\n\t" \
    "v_mfma_f32_16x16x32_bf16 v[96:99], v[28:31], v[72:75], 0\n\t" \
    "ds_read_b64_tr_b16 v[108:109], v15 offset:18432\n\t" \
    "ds_read_b64_tr_b16 v[110:111], v15 offset:18688\n\t" \
    "s_waitcnt lgkmcnt(12)\n\t" \
    "v_mfma_f32_16x16x32_bf16 v[96:99], v[32:35], v[76:79], v[96:99]\n\t" \
    "ds_read_b64_tr_b16 v[112:113], v15 offset:20480\n\t" \
    "ds_read_b64_tr_b16 v[114:115], v15 offset:20736\n\t" \
    "s_waitcnt lgkmcnt(12)\n\t" \
    "v_mfma_f32_16x16x32_bf16 v[116:119], v[28:31], v[80:83], 0\n\t" \
    "ds_read_b64_tr_b16 v[120:121], v15 offset:22528\n\t" \
    "ds_read_b64_tr_b16 v[122:123], v15 offset:22784\n\t" \
    "s_waitcnt lgkmcnt(12)\n\t" \
    "v_mfma_f32_16x16x32_bf16 v[116:119], v[32:35], v[100:103], v[116:119]\n\t" \
    "ds_read_b64_tr_b16 v[124:125], v15 offset:24576\n\t" \
    "ds_read_b64_tr_b16 v[126:127], v15 offset:24832\n\t" \
    "s_waitcnt lgkmcnt(12)\n\t" \
    "v_mfma_f32_16x16x32_bf16 v[128:131], v[28:31], v[104:107], 0\n\t" \
    "ds_read_b64_tr_b16 v[132:133], v15 offset:26624\n\t" \
    "ds_read_b64_tr_b16 v[134:135], v15 offset:26880\n\t" \
    "s_waitcnt lgkmcnt(12)\n\t" \
    "v_mfma_f32_16x16x32_bf16 v[128:131], v[32:35], v[88:91], v[128:131]\n\t" \
    "ds_read_b64_tr_b16 v[136:137], v15 offset:28672\n\t" \
    "ds_read_b64_tr_b16 v[138:139], v15 offset:28928\n\t" \
    "s_waitcnt lgkmcnt(12)\n\t" \
    "v_mfma_f32_16x16x32_bf16 v[140:143], v[28:31], v[92:95], 0\n\t" \
    "ds_read_b64_tr_b16 v[24:25], v15 offset:30720\n\t" \
    "ds_read_b64_tr_b16 v[26:27], v15 offset:30976\n\t" \
    "s_waitcnt lgkmcnt(12)\n\t" \
    "v_mfma_f32_16x16x32_bf16 v[140:143], v[32:35], v[108:111], v[140:143]\n\t" \
    "v_cvt_pk_bf16_f32 v144, v84, v96\n\t" \
    "v_cvt_pk_bf16_f32 v152, v85, v97\n\t" \
    "s_waitcnt lgkmcnt(10)\n\t" \
    "v_mfma_f32_16x16x32_bf16 v[156:159], v[28:31], v[112:115], 0\n\t" \
    "v_cvt_pk_bf16_f32 v160, v86, v98\n\t" \
    "v_cvt_pk_bf16_f32 v164, v87, v99\n\t" \
    "s_waitcnt lgkmcnt(8)\n\t" \
    "v_mfma_f32_16x16x32_bf16 v[156:159], v[32:35], v[120:123], v[156:159]\n\t" \
    "v_cvt_pk_bf16_f32 v145, v116, v128\n\t" \
    "v_cvt_pk_bf16_f32 v153, v117, v129\n\t" \
    "s_waitcnt lgkmcnt(6)\n\t" \
    "v_mfma_f32_16x16x32_bf16 v[168:171], v[28:31], v[124:127], 0\n\t" \
    "v_cvt_pk_bf16_f32 v161, v118, v130\n\t" \
    "v_cvt_pk_bf16_f32 v165, v119, v131\n\t" \
    "s_waitcnt lgkmcnt(4)\n\t" \
    "v_mfma_f32_16x16x32_bf16 v[168:171], v[32:35], v[132:135], v[168:171]\n\t" \
    "s_waitcnt lgkmcnt(2)\n\t" \
    "v_mfma_f32_16x16x32_bf16 v[172:175], v[28:31], v[136:139], 0\n\t" \
    "s_waitcnt lgkmcnt(0)\n\t" \
    "v_mfma_f32_16x16x32_bf16 v[172:175], v[32:35], v[24:27], v[172:175]\n\t" \
    "v_cvt_pk_bf16_f32 v146, v140, v156\n\t" \
    "v_cvt_pk_bf16_f32 v154, v141, v157\n\t" \
    "v_cvt_pk_bf16_f32 v162, v142, v158\n\t" \
    "v_cvt_pk_bf16_f32 v166, v143, v159\n\t" \
    "s_nop 3\n\t" \
    "v_cvt_pk_bf16_f32 v147, v168, v172\n\t" \
    "v_cvt_pk_bf16_f32 v155, v169, v173\n\t" \
    "v_cvt_pk_bf16_f32 v163, v170, v174\n\t" \
    "v_cvt_pk_bf16_f32 v167, v171, v175\n\t" \
    ".Lh30_%=:\n\t" \
    "s_memrealtime s[22:23]\n\t" \
    "s_waitcnt lgkmcnt(0)\n\t" \
    "s_sub_u32 s22, s22, s35\n\t" \
    "s_cmp_lt_u32 s22, 370\n\t" \
    "s_cbranch_scc1 .Lh30_%=\n\t" \
    "s_cmp_eq_u32 s24, 0\n\t" \
    "s_cbranch_scc1 .Lj32_%=\n\t" \
    ".Lh31_%=:\n\t" \
    "s_memrealtime s[22:23]\n\t" \
    "s_waitcnt lgkmcnt(0)\n\t" \
    "s_sub_u32 s22, s22, s35\n\t" \
    "s_cmp_lt_u32 s22, 385\n\t" \
    "s_cbranch_scc1 .Lh31_%=\n\t" \
    ".Lj32_%=:\n\t" \
    "s_waitcnt lgkmcnt(0)\n\t" \
    "s_xor_b32 s42, s43, 0\n\t" \
    "buffer_store_dwordx4 v[144:147], v19, s[4:7], s42 offen offset:0 sc1\n\t" \
    "s_waitcnt vmcnt(1)\n\t" \
    "s_mov_b64 exec, 1\n\t" \
    "ds_add_rtn_u32 v17, v9, v9 offset:8\n\t" \
    "s_add_u32 s37, s32, 524288\n\t" \
    "s_waitcnt lgkmcnt(0)\n\t" \
    "v_cmp_eq_u32 vcc, 1098496, v17\n\t" \
    "s_mov_b64 exec, -1\n\t" \
    "s_nop 0\n\t" \
    "s_cbranch_vccz .La13_%=\n\t" \
    "buffer_store_dwordx4 v[148:151], v2, s[4:7], s37 offen sc1\n\t" \
    ".La13_%=:\n\t" \
    "s_mov_b64 exec, -1\n\t" \
    "s_xor_b32 s42, s43, 1024\n\t" \
    "buffer_store_dwordx4 v[152:155], v19, s[4:7], s42 offen offset:0 sc1\n\t" \
    "s_cmp_eq_u32 s24, 0\n\t" \
    "s_cbranch_scc1 .Lj34_%=\n\t" \
    ".Lh33_%=:\n\t" \
    "s_memrealtime s[22:23]\n\t" \
    "s_waitcnt lgkmcnt(0)\n\t" \
    "s_sub_u32 s22, s22, s35\n\t" \
    "s_cmp_lt_u32 s22, 435\n\t" \
    "s_cbranch_scc1 .Lh33_%=\n\t" \
    ".Lj34_%=:\n\t" \
    "s_waitcnt lgkmcnt(0)\n\t" \
    "s_xor_b32 s42, s43, 2048\n\t" \
    "buffer_store_dwordx4 v[160:163], v19, s[4:7], s42 offen offset:0 sc1\n\t" \
    "s_xor_b32 s42, s43, 3072\n\t" \
    "buffer_store_dwordx4 v[164:167], v19, s[4:7], s42 offen offset:0 sc1\n\t" \
    ".Lskip_%=:\n\t" \
    "s_waitcnt lgkmcnt(0)\n\t" \
    "s_nop 7\n\t" \
    ".Lh35_%=:\n\t" \
    "s_memrealtime s[22:23]\n\t" \
    "s_waitcnt lgkmcnt(0)\n\t" \
    "s_sub_u32 s22, s22, s35\n\t" \
    "s_cmp_lt_u32 s22, 400\n\t" \
    "s_cbranch_scc1 .Lh35_%=\n\t" \
    "s_and_b32 s52, s2, 7\n\t" \
    "s_lshl_b32 s52, s52, 7\n\t" \
    "s_add_u32 s52, s52, 34636800\n\t" \
    "s_buffer_load_dword s36, s[4:7], s52\n\t" \
    "s_and_b32 s37, s24, 3\n\t" \
    "s_mul_i32 s37, s37, 8486912\n\t" \
    "s_add_u32 s53, s53, s37\n\t" \
    "s_and_b32 s37, s24, 4\n\t" \
    "s_lshl_b32 s39, s37, 11\n\t" \
    "s_add_u32 s53, s53, s39\n\t" \
    "s_add_u32 s37, s37, s24\n\t" \
    "s_andn2_b32 s39, s26, 1\n\t" \
    "s_lshl3_add_u32 s37, s39, s37\n\t" \
    "s_mul_i32 s37, s37, 43008\n\t" \
    "s_mul_i32 s39, s27, 4992\n\t" \
    "s_add_u32 s51, s37, s39\n\t" \
    "s_cmp_eq_u32 s27, 8\n\t" \
    "s_movk_i32 s10, 4992\n\t" \
    "s_cselect_b32 s10, 3072, s10\n\t" \
    "s_mov_b32 s11, 0x20000\n\t" \
    "s_and_b32 s37, s2, 0x70\n\t" \
    "s_lshl_b32 s37, s37, 1\n\t" \
    "v_lshrrev_b32 v11, 7, v0\n\t" \
    "v_lshl_add_u32 v11, v11, 2, s37\n\t" \
    "v_min_u32 v11, 248, v11\n\t" \
    "v_mul_u32_u24 v46, 147, v11\n\t" \
    "v_lshrrev_b32 v46, 12, v46\n\t" \
    "v_mul_u32_u24 v41, 28, v46\n\t" \
    "v_sub_u32 v11, v11, v41\n\t" \
    "v_bfe_u32 v41, v0, 4, 3\n\t" \
    "v_min_u32 v41, 4, v41\n\t" \
    "v_add3_u32 v11, v11, v41, -1\n\t" \
    "v_add_u32 v41, 28, v11\n\t" \
    "v_min_u32 v11, v11, v41\n\t" \
    "v_and_b32 v1, 1, v11\n\t" \
    "v_lshrrev_b32 v11, 1, v11\n\t" \
    "v_mad_u32_u24 v11, v11, 9, v46\n\t" \
    "v_lshl_add_u32 v11, v1, 7, v11\n\t" \
    "v_and_b32 v3, 31, v11\n\t" \
    "v_lshrrev_b32 v50, 5, v11\n\t" \
    "v_lshlrev_b32 v50, 7, v50\n\t" \
    "v_lshl_or_b32 v51, v3, 10, v50\n\t" \
    "s_mov_b32 s40, 0\n\t" \
    "s_waitcnt lgkmcnt(0)\n\t" \
    ".Lr4_%=:\n\t" \
    "s_xor_b32 s36, s36, s34\n\t" \
    "s_andn2_b32 s37, s36, 14\n\t" \
    "s_cbranch_scc1 .Lr5_%=\n\t" \
    ".Lr6_%=:\n\t" \
    "s_bfe_u32 s36, s36, 0x30001\n\t" \
    "s_add_u32 s56, s2, s36\n\t" \
    "s_and_b32 s56, s56, 7\n\t" \
    "s_lshr_b32 s37, s2, 7\n\t" \
    "s_lshl1_add_u32 s56, s56, s37\n\t" \
    "s_and_b32 s37, s56, 7\n\t" \
    "s_mul_i32 s48, s37, 4243456\n\t" \
    "s_and_b32 s37, s56, 8\n\t" \
    "s_lshl_b32 s37, s37, 10\n\t" \
    "s_add_u32 s48, s48, s37\n\t" \
    "s_add_u32 s49, s48, 524288\n\t" \
    "s_add_u32 s50, s48, 1048576\n\t" \
    "s_xor_b32 s37, s48, 0x2000\n\t" \
    "s_add_u32 s37, s37, 2097152\n\t" \
    "s_add_u32 s45, s45, s37\n\t" \
    "s_add_u32 s46, s46, s37\n\t" \
    "s_cmp_lt_u32 s24, 4\n\t" \
    "s_cselect_b32 s47, s46, 0x80000000\n\t" \
    "v_add_u32 v17, s45, v0\n\t" \
    "v_add_u32 v19, s46, v0\n\t" \
    "v_add_u32 v41, s47, v0\n\t" \
    "s_lshr_b32 s37, s24, 2\n\t" \
    "s_mul_i32 s37, s37, 6\n\t" \
    "s_mul_i32 s39, s56, 7\n\t" \
    "s_add_u32 s37, s37, s39\n\t" \
    "s_lshr_b32 s37, s37, 4\n\t" \
    "s_and_b32 s39, s24, 3\n\t" \
    "s_mul_i32 s39, s39, 63\n\t" \
    "s_add_u32 s37, s37, s39\n\t" \
    "v_lshrrev_b32 v46, 4, v0\n\t" \
    "v_min_u32 v46, 8, v46\n\t" \
    "v_mad_u32_u24 v46, v46, 7, s37\n\t" \
    "v_add_u32 v1, 2, v46\n\t" \
    "v_lshrrev_b32 v1, 7, v1\n\t" \
    "v_lshl_add_u32 v46, v1, 1, v46\n\t" \
    "v_and_b32 v1, 31, v46\n\t" \
    "v_lshrrev_b32 v11, 5, v46\n\t" \
    "v_lshlrev_b32 v11, 7, v11\n\t" \
    "v_lshl_or_b32 v3, v1, 10, v11\n\t" \
    "v_add_u32 v18, s36, v18\n\t" \
    "v_lshlrev_b32 v18, 8, v18\n\t" \
    "v_and_b32 v18, 0x400, v18\n\t" \
    "v_xor_b32 v42, v42, v18\n\t" \
    "s_sub_u32 s37, s24, s36\n\t" \
    "s_and_b32 s37, s37, 7\n\t" \
    "s_mul_i32 s39, s27, 28\n\t" \
    "s_add_u32 s39, s39, s26\n\t" \
    "s_lshr_b32 s39, s39, 5\n\t" \
    "s_lshl4_add_u32 s37, s39, s37\n\t" \
    "s_lshl_b32 s52, s37, 7\n\t" \
    "s_nop 0\n\t" \
    "v_readlane_b32 s12, v3, 0\n\t" \
    "v_readlane_b32 s13, v3, 1\n\t" \
    "v_readlane_b32 s14, v3, 2\n\t" \
    "v_readlane_b32 s15, v3, 3\n\t" \
    "v_readlane_b32 s16, v3, 4\n\t" \
    "v_readlane_b32 s17, v3, 5\n\t" \
    "v_readlane_b32 s18, v3, 6\n\t" \
    "v_readlane_b32 s19, v3, 7\n\t" \
    "v_readlane_b32 s3, v3, 8\n\t" \
    "s_add_u32 s12, s12, s33\n\t" \
    "s_add_u32 s13, s13, s33\n\t" \
    "s_add_u32 s14, s14, s33\n\t" \
    "s_add_u32 s15, s15, s33\n\t" \
    "s_add_u32 s16, s16, s33\n\t" \
    "s_add_u32 s17, s17, s33\n\t" \
    "s_add_u32 s18, s18, s33\n\t" \
    "s_add_u32 s19, s19, s33\n\t" \
    "s_add_u32 s3, s3, s33\n\t" \
    "s_waitcnt vmcnt(0)\n\t" \
    "s_mov_b64 exec, 1\n\t" \
    "ds_add_rtn_u32 v50, v9, v9 offset:12\n\t" \
    "s_add_u32 s37, s32, 786432\n\t" \
    "s_waitcnt lgkmcnt(0)\n\t" \
    "v_cmp_eq_u32 vcc, 1098496, v50\n\t" \
    "s_mov_b64 exec, -1\n\t" \
    "s_nop 0\n\t" \
    "s_cbranch_vccz .La20_%=\n\t" \
    "buffer_store_dwordx4 v[148:151], v2, s[4:7], s37 offen sc1\n\t" \
    ".La20_%=:\n\t" \
    "s_mov_b64 exec, -1\n\t" \
    ".Lh36_%=:\n\t" \
    "s_memrealtime s[22:23]\n\t" \
    "s_waitcnt lgkmcnt(0)\n\t" \
    "s_sub_u32 s22, s22, s35\n\t" \
    "s_cmp_lt_u32 s22, 490\n\t" \
    "s_cbranch_scc1 .Lh36_%=\n\t" \
    "s_cmp_lg_u32 s24, 0\n\t" \
    "s_cbranch_scc1 .Lb15_%=\n\t" \
    "s_mov_b32 s40, 0\n\t" \
    "s_add_u32 s38, s33, 524288\n\t" \
    ".Ll15_%=:\n\t" \
    "buffer_load_dword v8, v51, s[4:7], s38 offen sc1\n\t" \
    "s_waitcnt vmcnt(0)\n\t" \
    "v_cmp_ne_u32 vcc, s34, v8\n\t" \
    "s_add_u32 s40, s40, 1\n\t" \
    "s_cmp_ge_u32 s40, 8192\n\t" \
    "s_cbranch_scc1 .Lb15_%=\n\t" \
    "s_cbranch_vccz .Lb15_%=\n\t" \
    "s_sleep 2\n\t" \
    "s_branch .Ll15_%=\n\t" \
    ".Lb15_%=:\n\t" \
    "s_waitcnt lgkmcnt(0)\n\t" \
    "s_barrier\n\t" \
    "buffer_load_dwordx4 v[176:179], v36, s[4:7], s48 offen offset:0 sc1\n\t" \
    "buffer_load_dwordx4 v[180:183], v36, s[4:7], s48 offen offset:1024 sc1\n\t" \
    "buffer_load_dwordx4 v[184:187], v36, s[4:7], s48 offen offset:256 sc1\n\t" \
    "buffer_load_dwordx4 v[188:191], v36, s[4:7], s48 offen offset:1280 sc1\n\t" \
    "buffer_load_dwordx4 v[20:23], v37, s[4:7], s48 offen offset:0 sc1\n\t" \
    "s_mov_b32 s37, 34637824\n\t" \
    "s_buffer_load_dword s56, s[4:7], s37 offset:0x0\n\t" \
    "s_buffer_load_dword s57, s[4:7], s37 offset:0x80\n\t" \
    "s_buffer_load_dword s58, s[4:7], s37 offset:0x100\n\t" \
    "s_buffer_load_dword s59, s[4:7], s37 offset:0x180\n\t" \
    "s_buffer_load_dword s60, s[4:7], s37 offset:0x200\n\t" \
    "s_buffer_load_dword s61, s[4:7], s37 offset:0x280\n\t" \
    "s_cmp_lt_u32 s24, 4\n\t" \
    "s_cbranch_scc1 .Lp37_%=\n\t" \
    "s_setprio 1\n\t" \
    ".Lp37_%=:\n\t" \
    "s_waitcnt vmcnt(4)\n\t" \
    "v_mfma_f32_32x32x16_bf16 v[192:207], v[52:55], v[176:179], 0\n\t" \
    "buffer_load_dwordx4 v[4:7], v36, s[4:7], s49 offen offset:0 sc1\n\t" \
    "s_waitcnt vmcnt(4)\n\t" \
    "v_mfma_f32_32x32x16_bf16 v[192:207], v[56:59], v[180:183], v[192:207]\n\t" \
    "buffer_load_dwordx4 v[60:63], v36, s[4:7], s49 offen offset:1024 sc1\n\t" \
    "s_waitcnt vmcnt(4)\n\t" \
    "v_mfma_f32_32x32x16_bf16 v[208:223], v[52:55], v[184:187], 0\n\t" \
    "buffer_load_dwordx4 v[64:67], v36, s[4:7], s49 offen offset:256 sc1\n\t" \
    "s_waitcnt vmcnt(4)\n\t" \
    "v_mfma_f32_32x32x16_bf16 v[208:223], v[56:59], v[188:191], v[208:223]\n\t" \
    "buffer_load_dwordx4 v[68:71], v36, s[4:7], s49 offen offset:1280 sc1\n\t" \
    "v_mfma_f32_32x32x16_bf16 v[224:239], v[56:59], v[176:179], 0\n\t" \
    "s_add_u32 m0, s44, 0\n\t" \
    "s_nop 8\n\t" \
    "v_cvt_pk_bf16_f32 v16, v192, v208\n\t" \
    "v_cvt_pk_bf16_f32 v46, v193, v209\n\t" \
    "v_cvt_pk_bf16_f32 v1, v194, v210\n\t" \
    "v_cvt_pk_bf16_f32 v11, v195, v211\n\t" \
    "v_cvt_pk_bf16_f32 v18, v196, v212\n\t" \
    "s_waitcnt vmcnt(4)\n\t" \
    "v_permlane16_swap_b32 v20, v180\n\t" \
    "v_permlane16_swap_b32 v21, v181\n\t" \
    "ds_write_addtid_b32 v16 offset:3616\n\t" \
    "ds_write_addtid_b32 v46 offset:3872\n\t" \
    "ds_write_addtid_b32 v1 offset:4128\n\t" \
    "ds_write_addtid_b32 v11 offset:4384\n\t" \
    "ds_write_addtid_b32 v18 offset:4640\n\t" \
    "v_permlane16_swap_b32 v22, v182\n\t" \
    "v_permlane16_swap_b32 v23, v183\n\t" \
    "v_cvt_pk_bf16_f32 v9, v197, v213\n\t" \
    "v_cvt_pk_bf16_f32 v50, v198, v214\n\t" \
    "v_cvt_pk_bf16_f32 v51, v199, v215\n\t" \
    "v_cvt_pk_bf16_f32 v8, v200, v216\n\t" \
    "v_cvt_pk_bf16_f32 v16, v201, v217\n\t" \
    "v_mfma_f32_32x32x16_bf16 v[240:255], v[56:59], v[184:187], 0\n\t" \
    "v_permlane16_swap_b32 v180, v188\n\t" \
    "buffer_load_dwordx4 v[72:75], v37, s[4:7], s49 offen offset:0 sc1\n\t" \
    "ds_write_addtid_b32 v9 offset:4896\n\t" \
    "ds_write_addtid_b32 v50 offset:5152\n\t" \
    "ds_write_addtid_b32 v51 offset:5408\n\t" \
    "ds_write_addtid_b32 v8 offset:5664\n\t" \
    "ds_write_addtid_b32 v16 offset:5920\n\t" \
    "v_permlane16_swap_b32 v181, v189\n\t" \
    "v_permlane16_swap_b32 v182, v190\n\t" \
    "v_cvt_pk_bf16_f32 v46, v202, v218\n\t" \
    "v_cvt_pk_bf16_f32 v1, v203, v219\n\t" \
    "v_cvt_pk_bf16_f32 v11, v204, v220\n\t" \
    "v_cvt_pk_bf16_f32 v18, v205, v221\n\t" \
    "v_permlane16_swap_b32 v183, v191\n\t" \
    "v_mfma_f32_32x32x16_bf16 v[224:239], v[52:55], v[20:23], v[224:239]\n\t" \
    "ds_write_addtid_b32 v46 offset:6176\n\t" \
    "ds_write_addtid_b32 v1 offset:6432\n\t" \
    "ds_write_addtid_b32 v11 offset:6688\n\t" \
    "ds_write_addtid_b32 v18 offset:6944\n\t" \
    "v_mfma_f32_32x32x16_bf16 v[240:255], v[52:55], v[180:183], v[240:255]\n\t" \
    "s_mov_b32 s40, 0\n\t" \
    "s_waitcnt lgkmcnt(0)\n\t" \
    ".Lq4_%=:\n\t" \
    "s_mov_b32 s37, 0\n\t" \
    "s_mov_b32 s0, 0\n\t" \
    "s_xor_b32 s61, s61, s34\n\t" \
    "s_or_b32 s37, s37, s61\n\t" \
    "s_lshl1_add_u32 s0, s0, s61\n\t" \
    "s_xor_b32 s60, s60, s34\n\t" \
    "s_or_b32 s37, s37, s60\n\t" \
    "s_lshl1_add_u32 s0, s0, s60\n\t" \
    "s_xor_b32 s59, s59, s34\n\t" \
    "s_or_b32 s37, s37, s59\n\t" \
    "s_lshl1_add_u32 s0, s0, s59\n\t" \
    "s_xor_b32 s58, s58, s34\n\t" \
    "s_or_b32 s37, s37, s58\n\t" \
    "s_lshl1_add_u32 s0, s0, s58\n\t" \
    "s_xor_b32 s57, s57, s34\n\t" \
    "s_or_b32 s37, s37, s57\n\t" \
    "s_lshl1_add_u32 s0, s0, s57\n\t" \
    "s_xor_b32 s56, s56, s34\n\t" \
    "s_or_b32 s37, s37, s56\n\t" \
    "s_lshl1_add_u32 s0, s0, s56\n\t" \
    "s_andn2_b32 s37, s37, 2\n\t" \
    "s_cbranch_scc1 .Lq5_%=\n\t" \
    ".Lq6_%=:\n\t" \
    "s_lshr_b32 s0, s0, 1\n\t" \
    "s_add_u32 s37, s36, 3\n\t" \
    "s_bitcmp1_b32 s37, 2\n\t" \
    "s_cselect_b32 s37, 51, 12\n\t" \
    "s_xor_b32 s0, s0, s37\n\t" \
    "v_add_u32 v51, 344064, v39\n\t" \
    "v_lshrrev_b32 v8, 21, v51\n\t" \
    "v_bfe_u32 v8, s0, v8, 1\n\t" \
    "v_lshrrev_b32 v16, 3, v51\n\t" \
    "v_lshlrev_b32 v8, 10, v8\n\t" \
    "v_and_b32 v16, 0x400, v16\n\t" \
    "v_xor_b32 v8, v8, v16\n\t" \
    "v_xor_b32 v46, v42, v8\n\t" \
    "v_lshrrev_b32 v8, 21, v39\n\t" \
    "v_bfe_u32 v8, s0, v8, 1\n\t" \
    "v_lshrrev_b32 v16, 3, v39\n\t" \
    "v_lshlrev_b32 v8, 10, v8\n\t" \
    "v_and_b32 v16, 0x400, v16\n\t" \
    "v_xor_b32 v8, v8, v16\n\t" \
    "v_xor_b32 v42, v42, v8\n\t" \
    "s_lshr_b32 s37, s10, 1\n\t" \
    "s_add_u32 s37, s37, s51\n\t" \
    "s_and_b32 s39, s8, 0x1fffff\n\t" \
    "s_add_u32 s37, s37, s39\n\t" \
    "s_lshr_b32 s39, s37, 21\n\t" \
    "s_lshr_b32 s39, s0, s39\n\t" \
    "s_xor_b32 s37, s37, s48\n\t" \
    "s_lshr_b32 s37, s37, 13\n\t" \
    "s_xor_b32 s37, s37, s39\n\t" \
    "s_and_b32 s37, s37, 1\n\t" \
    "s_mul_i32 s37, s37, 172032\n\t" \
    "s_add_u32 s37, s37, s51\n\t" \
    "s_add_u32 s8, s8, s37\n\t" \
    "s_addc_u32 s9, s9, 0\n\t" \
    ".Lh38_%=:\n\t" \
    "s_memrealtime s[22:23]\n\t" \
    "s_waitcnt lgkmcnt(0)\n\t" \
    "s_sub_u32 s22, s22, s35\n\t" \
    "s_cmp_lt_u32 s22, 603\n\t" \
    "s_cbranch_scc1 .Lh38_%=\n\t" \
    "s_buffer_load_dword s12, s[4:7], s12 offset:0xc0000\n\t" \
    "s_buffer_load_dword s13, s[4:7], s13 offset:0xc0000\n\t" \
    "s_buffer_load_dword s14, s[4:7], s14 offset:0xc0000\n\t" \
    "s_buffer_load_dword s15, s[4:7], s15 offset:0xc0000\n\t" \
    "s_buffer_load_dword s16, s[4:7], s16 offset:0xc0000\n\t" \
    "s_buffer_load_dword s17, s[4:7], s17 offset:0xc0000\n\t" \
    "s_buffer_load_dword s18, s[4:7], s18 offset:0xc0000\n\t" \
    "s_buffer_load_dword s19, s[4:7], s19 offset:0xc0000\n\t" \
    "s_buffer_load_dword s3, s[4:7], s3 offset:0xc0000\n\t" \
    "s_waitcnt vmcnt(4)\n\t" \
    "v_mfma_f32_32x32x16_bf16 v[192:207], v[52:55], v[4:7], 0\n\t" \
    "v_cvt_pk_bf16_f32 v1, v224, v240\n\t" \
    "v_cvt_pk_bf16_f32 v11, v225, v241\n\t" \
    "v_cvt_pk_bf16_f32 v18, v226, v242\n\t" \
    "v_cvt_pk_bf16_f32 v9, v227, v243\n\t" \
    "v_cvt_pk_bf16_f32 v50, v228, v244\n\t" \
    "buffer_load_dwordx4 v[76:79], v36, s[4:7], s50 offen offset:0 sc1\n\t" \
    "ds_write_addtid_b32 v1 offset:0\n\t" \
    "ds_write_addtid_b32 v11 offset:256\n\t" \
    "ds_write_addtid_b32 v18 offset:512\n\t" \
    "ds_write_addtid_b32 v9 offset:768\n\t" \
    "ds_write_addtid_b32 v50 offset:1024\n\t" \
    "s_waitcnt vmcnt(4)\n\t" \
    "v_mfma_f32_32x32x16_bf16 v[192:207], v[56:59], v[60:63], v[192:207]\n\t" \
    "buffer_load_dwordx4 v[80:83], v36, s[4:7], s50 offen offset:1024 sc1\n\t" \
    "v_cvt_pk_bf16_f32 v51, v229, v245\n\t" \
    "v_cvt_pk_bf16_f32 v39, v230, v246\n\t" \
    "v_cvt_pk_bf16_f32 v16, v231, v247\n\t" \
    "v_cvt_pk_bf16_f32 v8, v232, v248\n\t" \
    "v_cvt_pk_bf16_f32 v1, v233, v249\n\t" \
    "s_waitcnt vmcnt(4)\n\t" \
    "v_mfma_f32_32x32x16_bf16 v[208:223], v[52:55], v[64:67], 0\n\t" \
    "buffer_load_dwordx4 v[100:103], v36, s[4:7], s50 offen offset:256 sc1\n\t" \
    "ds_write_addtid_b32 v51 offset:1280\n\t" \
    "ds_write_addtid_b32 v39 offset:1536\n\t" \
    "ds_write_addtid_b32 v16 offset:1792\n\t" \
    "ds_write_addtid_b32 v8 offset:2048\n\t" \
    "ds_write_addtid_b32 v1 offset:2304\n\t" \
    "v_cvt_pk_bf16_f32 v11, v234, v250\n\t" \
    "v_cvt_pk_bf16_f32 v18, v235, v251\n\t" \
    "v_cvt_pk_bf16_f32 v9, v236, v252\n\t" \
    "v_cvt_pk_bf16_f32 v50, v237, v253\n\t" \
    "buffer_load_dwordx4 v[104:107], v36, s[4:7], s50 offen offset:1280 sc1\n\t" \
    "s_waitcnt vmcnt(5)\n\t" \
    "v_mfma_f32_32x32x16_bf16 v[208:223], v[56:59], v[68:71], v[208:223]\n\t" \
    "ds_write_addtid_b32 v11 offset:2560\n\t" \
    "ds_write_addtid_b32 v18 offset:2816\n\t" \
    "ds_write_addtid_b32 v9 offset:3072\n\t" \
    "ds_write_addtid_b32 v50 offset:3328\n\t" \
    "buffer_load_dwordx4 v[88:91], v37, s[4:7], s50 offen offset:0 sc1\n\t" \
    "s_waitcnt lgkmcnt(0)\n\t" \
    "s_xor_b32 s12, s12, s34\n\t" \
    "s_xor_b32 s13, s13, s34\n\t" \
    "s_xor_b32 s14, s14, s34\n\t" \
    "s_xor_b32 s15, s15, s34\n\t" \
    "s_xor_b32 s16, s16, s34\n\t" \
    "s_xor_b32 s17, s17, s34\n\t" \
    "s_xor_b32 s18, s18, s34\n\t" \
    "s_xor_b32 s19, s19, s34\n\t" \
    "s_xor_b32 s3, s3, s34\n\t" \
    "s_or_b32 s12, s12, s13\n\t" \
    "s_or_b32 s12, s12, s14\n\t" \
    "s_or_b32 s12, s12, s15\n\t" \
    "s_or_b32 s12, s12, s16\n\t" \
    "s_or_b32 s12, s12, s17\n\t" \
    "s_or_b32 s12, s12, s18\n\t" \
    "s_or_b32 s12, s12, s19\n\t" \
    "s_or_b32 s12, s12, s3\n\t" \
    "s_cbranch_scc1 .Lq1023_%=\n\t" \
    ".Lb22_%=:\n\t" \
    "s_waitcnt lgkmcnt(0)\n\t" \
    "s_barrier\n\t" \
    "ds_read_b128 v[92:95], v44 offset:0\n\t" \
    "s_mov_b32 s45, 0\n\t" \
    "buffer_load_dwordx4 v[12:15], v17, s[4:7], s45 offen offset:0 sc1\n\t" \
    "ds_read_b128 v[108:111], v44 offset:128\n\t" \
    "ds_read_b128 v[112:115], v44 offset:29184\n\t" \
    "ds_read_b128 v[84:87], v44 offset:29312\n\t" \
    "ds_read_b128 v[96:99], v45 offset:0\n\t" \
    "buffer_load_dwordx4 v[120:123], v17, s[4:7], s45 offen offset:1024 sc1\n\t" \
    "ds_read_b128 v[124:127], v45 offset:128\n\t" \
    "ds_read_b128 v[116:119], v45 offset:29184\n\t" \
    "ds_read_b128 v[128:131], v45 offset:29312\n\t" \
    "v_mfma_f32_32x32x16_bf16 v[224:239], v[56:59], v[4:7], 0\n\t" \
    "v_cvt_pk_bf16_f32 v39, v192, v208\n\t" \
    "v_cvt_pk_bf16_f32 v16, v193, v209\n\t" \
    "v_cvt_pk_bf16_f32 v8, v194, v210\n\t" \
    "v_cvt_pk_bf16_f32 v1, v195, v211\n\t" \
    "v_cvt_pk_bf16_f32 v36, v196, v212\n\t" \
    "s_waitcnt vmcnt(7)\n\t" \
    "v_permlane16_swap_b32 v72, v60\n\t" \
    "v_permlane16_swap_b32 v73, v61\n\t" \
    "s_mov_b32 s46, 114688\n\t" \
    "buffer_load_dwordx4 v[132:135], v17, s[4:7], s46 offen offset:0 sc1\n\t" \
    "ds_write_addtid_b32 v39 offset:61984\n\t" \
    "ds_write_addtid_b32 v16 offset:62240\n\t" \
    "ds_write_addtid_b32 v8 offset:62496\n\t" \
    "ds_write_addtid_b32 v1 offset:62752\n\t" \
    "ds_write_addtid_b32 v36 offset:63008\n\t" \
    "v_permlane16_swap_b32 v74, v62\n\t" \
    "v_permlane16_swap_b32 v75, v63\n\t" \
    "v_cvt_pk_bf16_f32 v11, v197, v213\n\t" \
    "v_cvt_pk_bf16_f32 v18, v198, v214\n\t" \
    "v_cvt_pk_bf16_f32 v9, v199, v215\n\t" \
    "v_cvt_pk_bf16_f32 v50, v200, v216\n\t" \
    "v_cvt_pk_bf16_f32 v37, v201, v217\n\t" \
    "v_mfma_f32_32x32x16_bf16 v[240:255], v[56:59], v[64:67], 0\n\t" \
    "v_permlane16_swap_b32 v60, v68\n\t" \
    "buffer_load_dwordx4 v[28:31], v17, s[4:7], s46 offen offset:1024 sc1\n\t" \
    "ds_write_addtid_b32 v11 offset:63264\n\t" \
    "ds_write_addtid_b32 v18 offset:63520\n\t" \
    "ds_write_addtid_b32 v9 offset:63776\n\t" \
    "ds_write_addtid_b32 v50 offset:64032\n\t" \
    "ds_write_addtid_b32 v37 offset:64288\n\t" \
    "v_permlane16_swap_b32 v61, v69\n\t" \
    "v_permlane16_swap_b32 v62, v70\n\t" \
    "v_cvt_pk_bf16_f32 v3, v202, v218\n\t" \
    "v_cvt_pk_bf16_f32 v51, v203, v219\n\t" \
    "v_cvt_pk_bf16_f32 v39, v204, v220\n\t" \
    "v_cvt_pk_bf16_f32 v16, v205, v221\n\t" \
    "v_permlane16_swap_b32 v63, v71\n\t" \
    "v_mfma_f32_32x32x16_bf16 v[224:239], v[52:55], v[72:75], v[224:239]\n\t" \
    "buffer_load_dwordx4 v[136:139], v19, s[4:7], s45 offen offset:0 sc1\n\t" \
    "ds_write_addtid_b32 v3 offset:64544\n\t" \
    "ds_write_addtid_b32 v51 offset:64800\n\t" \
    "ds_write_addtid_b32 v39 offset:65056\n\t" \
    "ds_write_addtid_b32 v16 offset:65312\n\t" \
    "v_mfma_f32_32x32x16_bf16 v[240:255], v[52:55], v[60:63], v[240:255]\n\t" \
    "s_waitcnt vmcnt(9)\n\t" \
    "v_mfma_f32_32x32x16_bf16 v[192:207], v[52:55], v[76:79], 0\n\t" \
    "s_nop 9\n\t" \
    "v_cvt_pk_bf16_f32 v8, v224, v240\n\t" \
    "v_cvt_pk_bf16_f32 v1, v225, v241\n\t" \
    "v_cvt_pk_bf16_f32 v36, v226, v242\n\t" \
    "v_cvt_pk_bf16_f32 v11, v227, v243\n\t" \
    "v_cvt_pk_bf16_f32 v18, v228, v244\n\t" \
    "buffer_load_dwordx4 v[24:27], v19, s[4:7], s46 offen offset:0 sc1\n\t" \
    "ds_write_addtid_b32 v8 offset:58368\n\t" \
    "ds_write_addtid_b32 v1 offset:58624\n\t" \
    "ds_write_addtid_b32 v36 offset:58880\n\t" \
    "ds_write_addtid_b32 v11 offset:59136\n\t" \
    "ds_write_addtid_b32 v18 offset:59392\n\t" \
    "s_waitcnt vmcnt(9)\n\t" \
    "v_mfma_f32_32x32x16_bf16 v[192:207], v[56:59], v[80:83], v[192:207]\n\t" \
    "v_cvt_pk_bf16_f32 v9, v229, v245\n\t" \
    "v_cvt_pk_bf16_f32 v50, v230, v246\n\t" \
    "v_cvt_pk_bf16_f32 v37, v231, v247\n\t" \
    "v_cvt_pk_bf16_f32 v3, v232, v248\n\t" \
    "v_cvt_pk_bf16_f32 v51, v233, v249\n\t" \
    "s_mov_b32 s47, 229376\n\t" \
    "buffer_load_dwordx4 v[32:35], v17, s[4:7], s47 offen offset:0 sc1\n\t" \
    "s_waitcnt vmcnt(9)\n\t" \
    "v_mfma_f32_32x32x16_bf16 v[208:223], v[52:55], v[100:103], 0\n\t" \
    "ds_write_addtid_b32 v9 offset:59648\n\t" \
    "ds_write_addtid_b32 v50 offset:59904\n\t" \
    "ds_write_addtid_b32 v37 offset:60160\n\t" \
    "ds_write_addtid_b32 v3 offset:60416\n\t" \
    "ds_write_addtid_b32 v51 offset:60672\n\t" \
    "buffer_load_dwordx4 v[140:143], v17, s[4:7], s47 offen offset:1024 sc1\n\t" \
    "v_cvt_pk_bf16_f32 v39, v234, v250\n\t" \
    "v_cvt_pk_bf16_f32 v16, v235, v251\n\t" \
    "v_cvt_pk_bf16_f32 v8, v236, v252\n\t" \
    "v_cvt_pk_bf16_f32 v1, v237, v253\n\t" \
    "s_waitcnt vmcnt(9)\n\t" \
    "v_mfma_f32_32x32x16_bf16 v[208:223], v[56:59], v[104:107], v[208:223]\n\t" \
    "ds_write_addtid_b32 v39 offset:60928\n\t" \
    "ds_write_addtid_b32 v16 offset:61184\n\t" \
    "ds_write_addtid_b32 v8 offset:61440\n\t" \
    "ds_write_addtid_b32 v1 offset:61696\n\t" \
    "buffer_load_dwordx4 v[156:159], v19, s[4:7], s47 offen offset:0 sc1\n\t" \
    "s_waitcnt vmcnt(9)\n\t" \
    "; wait\n\t" \
    "s_waitcnt lgkmcnt(0)\n\t" \
    "s_barrier\n\t" \
    "s_memrealtime s[64:65]\n\t" \
    "s_cmp_lg_u32 s24, 7\n\t" \
    "s_cbranch_scc1 .Lar_%=\n\t" \
    "s_waitcnt lgkmcnt(0)\n\t" \
    "v_mov_b32 v149, s64\n\t" \
    "s_add_u32 s37, s32, 1048576\n\t" \
    "s_mov_b64 exec, 0xff\n\t" \
    "buffer_store_dwordx4 v[148:151], v0, s[4:7], s37 offen sc1\n\t" \
    "s_mov_b64 exec, -1\n\t" \
    ".Lar_%=:\n\t" \
    "v_add_u32 v44, 58368, v44\n\t" \
    "buffer_load_dwordx4 v[168:171], v41, s[4:7], s45 offen offset:1024 sc1\n\t" \
    "v_add_u32 v45, 58368, v45\n\t" \
    "ds_read_b128 v[172:175], v44 offset:0\n\t" \
    "ds_read_b128 v[144:147], v44 offset:128\n\t" \
    "ds_read_b128 v[152:155], v44 offset:29184\n\t" \
    "ds_read_b128 v[160:163], v44 offset:29312\n\t" \
    "buffer_load_dwordx4 v[164:167], v41, s[4:7], s46 offen offset:1024 sc1\n\t" \
    "ds_read_b128 v[176:179], v45 offset:0\n\t" \
    "ds_read_b128 v[184:187], v45 offset:128\n\t" \
    "ds_read_b128 v[188:191], v45 offset:29184\n\t" \
    "ds_read_b128 v[20:23], v45 offset:29312\n\t" \
    "v_mfma_f32_32x32x16_bf16 v[224:239], v[56:59], v[76:79], 0\n\t" \
    "v_cvt_pk_bf16_f32 v36, v192, v208\n\t" \
    "v_cvt_pk_bf16_f32 v11, v193, v209\n\t" \
    "v_cvt_pk_bf16_f32 v18, v194, v210\n\t" \
    "v_cvt_pk_bf16_f32 v9, v195, v211\n\t" \
    "v_cvt_pk_bf16_f32 v50, v196, v212\n\t" \
    "s_waitcnt vmcnt(10)\n\t" \
    "v_mfma_f32_16x16x32_bf16 v[180:183], v[12:15], v[92:95], 0\n\t" \
    "v_permlane16_swap_b32 v88, v80\n\t" \
    "buffer_load_dwordx4 v[4:7], v41, s[4:7], s47 offen offset:1024 sc1\n\t" \
    "v_permlane16_swap_b32 v89, v81\n\t" \
    "ds_write_addtid_b32 v36 offset:3616\n\t" \
    "ds_write_addtid_b32 v11 offset:3872\n\t" \
    "ds_write_addtid_b32 v18 offset:4128\n\t" \
    "ds_write_addtid_b32 v9 offset:4384\n\t" \
    "ds_write_addtid_b32 v50 offset:4640\n\t" \
    "v_mfma_f32_16x16x32_bf16 v[64:67], v[12:15], v[108:111], 0\n\t" \
    "v_permlane16_swap_b32 v90, v82\n\t" \
    "v_mfma_f32_16x16x32_bf16 v[68:71], v[12:15], v[112:115], 0\n\t" \
    "v_permlane16_swap_b32 v91, v83\n\t" \
    "v_cvt_pk_bf16_f32 v37, v197, v213\n\t" \
    "v_cvt_pk_bf16_f32 v3, v198, v214\n\t" \
    "v_cvt_pk_bf16_f32 v51, v199, v215\n\t" \
    "v_cvt_pk_bf16_f32 v39, v200, v216\n\t" \
    "v_cvt_pk_bf16_f32 v16, v201, v217\n\t" \
    "s_mov_b32 s45, 344064\n\t" \
    "buffer_load_dwordx4 v[72:75], v17, s[4:7], s45 offen offset:0 sc1\n\t" \
    "v_mfma_f32_32x32x16_bf16 v[240:255], v[56:59], v[100:103], 0\n\t" \
    "v_mfma_f32_16x16x32_bf16 v[60:63], v[12:15], v[84:87], 0\n\t" \
    "v_permlane16_swap_b32 v80, v104\n\t" \
    "ds_write_addtid_b32 v37 offset:4896\n\t" \
    "ds_write_addtid_b32 v3 offset:5152\n\t" \
    "ds_write_addtid_b32 v51 offset:5408\n\t" \
    "ds_write_addtid_b32 v39 offset:5664\n\t" \
    "ds_write_addtid_b32 v16 offset:5920\n\t" \
    "s_waitcnt vmcnt(11)\n\t" \
    "v_mfma_f32_16x16x32_bf16 v[148:151], v[120:123], v[108:111], 0\n\t" \
    "v_permlane16_swap_b32 v81, v105\n\t" \
    "buffer_load_dwordx4 v[76:79], v17, s[4:7], s45 offen offset:1024 sc1\n\t" \
    "v_permlane16_swap_b32 v82, v106\n\t" \
    "v_cvt_pk_bf16_f32 v8, v202, v218\n\t" \
    "v_cvt_pk_bf16_f32 v1, v203, v219\n\t" \
    "v_cvt_pk_bf16_f32 v36, v204, v220\n\t" \
    "v_cvt_pk_bf16_f32 v11, v205, v221\n\t" \
    "v_mfma_f32_16x16x32_bf16 v[64:67], v[120:123], v[92:95], v[64:67]\n\t" \
    "v_permlane16_swap_b32 v83, v107\n\t" \
    "v_mfma_f32_16x16x32_bf16 v[56:59], v[120:123], v[84:87], 0\n\t" \
    "v_mfma_f32_32x32x16_bf16 v[224:239], v[52:55], v[88:91], v[224:239]\n\t" \
    "ds_write_addtid_b32 v8 offset:6176\n\t" \
    "ds_write_addtid_b32 v1 offset:6432\n\t" \
    "ds_write_addtid_b32 v36 offset:6688\n\t" \
    "ds_write_addtid_b32 v11 offset:6944\n\t" \
    "s_mov_b32 s46, 458752\n\t" \
    "buffer_load_dwordx4 v[100:103], v17, s[4:7], s46 offen offset:0 sc1\n\t" \
    "v_mfma_f32_32x32x16_bf16 v[240:255], v[52:55], v[80:83], v[240:255]\n\t" \
    "v_mfma_f32_16x16x32_bf16 v[60:63], v[120:123], v[112:115], v[60:63]\n\t" \
    "s_nop 10\n\t" \
    "v_cvt_pk_bf16_f32 v18, v224, v240\n\t" \
    "v_cvt_pk_bf16_f32 v9, v225, v241\n\t" \
    "v_cvt_pk_bf16_f32 v50, v226, v242\n\t" \
    "v_cvt_pk_bf16_f32 v37, v227, v243\n\t" \
    "v_cvt_pk_bf16_f32 v3, v228, v244\n\t" \
    "s_waitcnt vmcnt(12) lgkmcnt(15)\n\t" \
    "v_mfma_f32_16x16x32_bf16 v[180:183], v[132:135], v[172:175], v[180:183]\n\t" \
    "buffer_load_dwordx4 v[12:15], v17, s[4:7], s46 offen offset:1024 sc1\n\t" \
    "v_mfma_f32_16x16x32_bf16 v[64:67], v[132:135], v[144:147], v[64:67]\n\t" \
    "ds_write_addtid_b32 v18 offset:0\n\t" \
    "ds_write_addtid_b32 v9 offset:256\n\t" \
    "ds_write_addtid_b32 v50 offset:512\n\t" \
    "ds_write_addtid_b32 v37 offset:768\n\t" \
    "ds_write_addtid_b32 v3 offset:1024\n\t" \
    "v_mfma_f32_16x16x32_bf16 v[68:71], v[132:135], v[152:155], v[68:71]\n\t" \
    "v_cvt_pk_bf16_f32 v51, v229, v245\n\t" \
    "v_cvt_pk_bf16_f32 v39, v230, v246\n\t" \
    "v_cvt_pk_bf16_f32 v16, v231, v247\n\t" \
    "v_cvt_pk_bf16_f32 v8, v232, v248\n\t" \
    "v_cvt_pk_bf16_f32 v1, v233, v249\n\t" \
    "s_mov_b32 s47, 573440\n\t" \
    "buffer_load_dwordx4 v[192:195], v17, s[4:7], s47 offen offset:0 sc1\n\t" \
    "v_mfma_f32_16x16x32_bf16 v[60:63], v[132:135], v[160:163], v[60:63]\n\t" \
    "ds_write_addtid_b32 v51 offset:1280\n\t" \
    "ds_write_addtid_b32 v39 offset:1536\n\t" \
    "ds_write_addtid_b32 v16 offset:1792\n\t" \
    "ds_write_addtid_b32 v8 offset:2048\n\t" \
    "ds_write_addtid_b32 v1 offset:2304\n\t" \
    "s_waitcnt vmcnt(13)\n\t" \
    "v_mfma_f32_16x16x32_bf16 v[148:151], v[28:31], v[144:147], v[148:151]\n\t" \
    "buffer_load_dwordx4 v[196:199], v17, s[4:7], s47 offen offset:1024 sc1\n\t" \
    "v_mfma_f32_16x16x32_bf16 v[64:67], v[28:31], v[172:175], v[64:67]\n\t" \
    "v_cvt_pk_bf16_f32 v36, v234, v250\n\t" \
    "v_cvt_pk_bf16_f32 v11, v235, v251\n\t" \
    "v_cvt_pk_bf16_f32 v18, v236, v252\n\t" \
    "v_cvt_pk_bf16_f32 v9, v237, v253\n\t" \
    "v_mfma_f32_16x16x32_bf16 v[56:59], v[28:31], v[160:163], v[56:59]\n\t" \
    "ds_write_addtid_b32 v36 offset:2560\n\t" \
    "ds_write_addtid_b32 v11 offset:2816\n\t" \
    "ds_write_addtid_b32 v18 offset:3072\n\t" \
    "ds_write_addtid_b32 v9 offset:3328\n\t" \
    "buffer_load_dwordx4 v[200:203], v19, s[4:7], s45 offen offset:0 sc1\n\t" \
    "v_mfma_f32_16x16x32_bf16 v[60:63], v[28:31], v[152:155], v[60:63]\n\t" \
    "s_waitcnt lgkmcnt(0)\n\t" \
    "s_barrier\n\t" \
    "ds_read_b128 v[204:207], v10 offset:2048\n\t" \
    "v_mov_b32 v50, 0x10000\n\t" \
    "s_waitcnt vmcnt(14)\n\t" \
    "v_mfma_f32_16x16x32_bf16 v[208:211], v[136:139], v[96:99], 0\n\t" \
    "v_subrev_u32 v44, 58368, v44\n\t" \
    "v_mfma_f32_16x16x32_bf16 v[212:215], v[136:139], v[124:127], 0\n\t" \
    "v_subrev_u32 v45, 58368, v45\n\t" \
    "v_mfma_f32_16x16x32_bf16 v[216:219], v[136:139], v[116:119], 0\n\t" \
    "ds_read_b128 v[220:223], v44 offset:0\n\t" \
    "buffer_load_dwordx4 v[104:107], v19, s[4:7], s46 offen offset:0 sc1\n\t" \
    "v_mfma_f32_16x16x32_bf16 v[88:91], v[136:139], v[128:131], 0\n\t" \
    "ds_read_b128 v[52:55], v44 offset:128\n\t" \
    "s_waitcnt vmcnt(14)\n\t" \
    "v_mfma_f32_16x16x32_bf16 v[208:211], v[24:27], v[176:179], v[208:211]\n\t" \
    "ds_read_b128 v[80:83], v44 offset:29184\n\t" \
    "v_mfma_f32_16x16x32_bf16 v[212:215], v[24:27], v[184:187], v[212:215]\n\t" \
    "ds_read_b128 v[120:123], v44 offset:29312\n\t" \
    "buffer_load_dwordx4 v[132:135], v19, s[4:7], s47 offen offset:0 sc1\n\t" \
    "v_mfma_f32_16x16x32_bf16 v[216:219], v[24:27], v[188:191], v[216:219]\n\t" \
    "ds_read_b128 v[224:227], v45 offset:0\n\t" \
    "v_mfma_f32_16x16x32_bf16 v[88:91], v[24:27], v[20:23], v[88:91]\n\t" \
    "ds_read_b128 v[228:231], v45 offset:128\n\t" \
    "s_waitcnt vmcnt(14) lgkmcnt(5)\n\t" \
    "v_mfma_f32_16x16x32_bf16 v[180:183], v[32:35], v[220:223], v[180:183]\n\t" \
    "ds_read_b128 v[232:235], v45 offset:29184\n\t" \
    "buffer_load_dwordx4 v[236:239], v41, s[4:7], s45 offen offset:1024 sc1\n\t" \
    "s_waitcnt lgkmcnt(5)\n\t" \
    "v_mfma_f32_16x16x32_bf16 v[64:67], v[32:35], v[52:55], v[64:67]\n\t" \
    "ds_read_b128 v[240:243], v45 offset:29312\n\t" \
    "s_waitcnt lgkmcnt(5)\n\t" \
    "v_mfma_f32_16x16x32_bf16 v[68:71], v[32:35], v[80:83], v[68:71]\n\t" \
    "s_waitcnt lgkmcnt(4)\n\t" \
    "v_mfma_f32_16x16x32_bf16 v[60:63], v[32:35], v[120:123], v[60:63]\n\t" \
    "buffer_load_dwordx4 v[244:247], v41, s[4:7], s46 offen offset:1024 sc1\n\t" \
    "s_waitcnt vmcnt(15)\n\t" \
    "v_mfma_f32_16x16x32_bf16 v[148:151], v[140:143], v[52:55], v[148:151]\n\t" \
    "s_waitcnt lgkmcnt(0)\n\t" \
    "s_add_u32 s12, s55, 34603008\n\t" \
    "s_buffer_load_dword s13, s[4:7], s12\n\t" \
    "s_buffer_load_dword s14, s[4:7], s12 offset:0x400\n\t" \
    "v_mfma_f32_16x16x32_bf16 v[64:67], v[140:143], v[220:223], v[64:67]\n\t" \
    "v_mfma_f32_16x16x32_bf16 v[56:59], v[140:143], v[120:123], v[56:59]\n\t" \
    "buffer_load_dwordx4 v[248:251], v41, s[4:7], s47 offen offset:1024 sc1\n\t" \
    "v_mfma_f32_16x16x32_bf16 v[60:63], v[140:143], v[80:83], v[60:63]\n\t" \
    "s_waitcnt vmcnt(15)\n\t" \
    "v_mfma_f32_16x16x32_bf16 v[208:211], v[156:159], v[224:227], v[208:211]\n\t" \
    "v_mfma_f32_16x16x32_bf16 v[212:215], v[156:159], v[228:231], v[212:215]\n\t" \
    "s_mov_b32 s45, 688128\n\t" \
    "buffer_load_dwordx4 v[252:255], v17, s[4:7], s45 offen offset:0 sc1\n\t" \
    "v_mfma_f32_16x16x32_bf16 v[216:219], v[156:159], v[232:235], v[216:219]\n\t" \
    "v_sub_f32 v180, v180, v148\n\t" \
    "v_sub_f32 v181, v181, v149\n\t" \
    "v_sub_f32 v182, v182, v150\n\t" \
    "v_mfma_f32_16x16x32_bf16 v[88:91], v[156:159], v[240:243], v[88:91]\n\t" \
    "v_sub_f32 v183, v183, v151\n\t" \
    "v_sub_f32 v68, v68, v56\n\t" \
    "v_sub_f32 v69, v69, v57\n\t" \
    "v_sub_f32 v70, v70, v58\n\t" \
    "v_sub_f32 v71, v71, v59\n\t" \
    "v_cvt_pk_bf16_f32 v30, v64, v65\n\t" \
    "v_cvt_pk_bf16_f32 v31, v66, v67\n\t" \
    "v_cvt_pk_bf16_f32 v28, v180, v181\n\t" \
    "v_cvt_pk_bf16_f32 v29, v182, v183\n\t" \
    "v_cvt_pk_bf16_f32 v138, v60, v61\n\t" \
    "v_cvt_pk_bf16_f32 v139, v62, v63\n\t" \
    "ds_write_b128 v40, v[28:31] offset:58368\n\t" \
    "v_cvt_pk_bf16_f32 v136, v68, v69\n\t" \
    "v_cvt_pk_bf16_f32 v137, v70, v71\n\t" \
    "ds_write_b128 v40, v[136:139] offset:59392\n\t" \
    "s_waitcnt vmcnt(13)\n\t" \
    "s_cmp_ge_u32 s24, 4\n\t" \
    "s_cbranch_scc1 .Lk42_%=\n\t" \
    "v_mfma_f32_16x16x32_bf16 v[24:27], v[168:171], v[124:127], 0\n\t" \
    "v_mfma_f32_16x16x32_bf16 v[24:27], v[164:167], v[184:187], v[24:27]\n\t" \
    "v_mfma_f32_16x16x32_bf16 v[24:27], v[4:7], v[228:231], v[24:27]\n\t" \
    "v_mfma_f32_16x16x32_bf16 v[32:35], v[168:171], v[128:131], 0\n\t" \
    "v_mfma_f32_16x16x32_bf16 v[32:35], v[164:167], v[20:23], v[32:35]\n\t" \
    "v_mfma_f32_16x16x32_bf16 v[32:35], v[4:7], v[240:243], v[32:35]\n\t" \
    "v_mfma_f32_16x16x32_bf16 v[212:215], v[168:171], v[96:99], v[212:215]\n\t" \
    "v_mfma_f32_16x16x32_bf16 v[212:215], v[164:167], v[176:179], v[212:215]\n\t" \
    "v_mfma_f32_16x16x32_bf16 v[212:215], v[4:7], v[224:227], v[212:215]\n\t" \
    "v_mfma_f32_16x16x32_bf16 v[88:91], v[168:171], v[116:119], v[88:91]\n\t" \
    "s_nop 0\n\t" \
    "v_sub_f32 v208, v208, v24\n\t" \
    "v_sub_f32 v209, v209, v25\n\t" \
    "v_mfma_f32_16x16x32_bf16 v[88:91], v[164:167], v[188:191], v[88:91]\n\t" \
    "v_sub_f32 v210, v210, v26\n\t" \
    "v_sub_f32 v211, v211, v27\n\t" \
    "v_mfma_f32_16x16x32_bf16 v[88:91], v[4:7], v[232:235], v[88:91]\n\t" \
    "v_sub_f32 v216, v216, v32\n\t" \
    "v_sub_f32 v217, v217, v33\n\t" \
    "v_sub_f32 v218, v218, v34\n\t" \
    "v_sub_f32 v219, v219, v35\n\t" \
    ".Lk42_%=:\n\t" \
    "s_waitcnt vmcnt(12)\n\t" \
    "v_mfma_f32_16x16x32_bf16 v[140:143], v[72:75], v[92:95], 0\n\t" \
    "buffer_load_dwordx4 v[156:159], v17, s[4:7], s45 offen offset:1024 sc1\n\t" \
    "v_mfma_f32_16x16x32_bf16 v[148:151], v[72:75], v[108:111], 0\n\t" \
    "v_cvt_pk_bf16_f32 v58, v212, v213\n\t" \
    "v_cvt_pk_bf16_f32 v59, v214, v215\n\t" \
    "v_mfma_f32_16x16x32_bf16 v[64:67], v[72:75], v[112:115], 0\n\t" \
    "v_cvt_pk_bf16_f32 v56, v208, v209\n\t" \
    "v_cvt_pk_bf16_f32 v57, v210, v211\n\t" \
    "v_mfma_f32_16x16x32_bf16 v[180:183], v[72:75], v[84:87], 0\n\t" \
    "v_cvt_pk_bf16_f32 v62, v88, v89\n\t" \
    "v_cvt_pk_bf16_f32 v63, v90, v91\n\t" \
    "s_mov_b32 s46, 802816\n\t" \
    "buffer_load_dwordx4 v[28:31], v17, s[4:7], s46 offen offset:0 sc1\n\t" \
    "s_waitcnt vmcnt(13)\n\t" \
    "v_mfma_f32_16x16x32_bf16 v[68:71], v[76:79], v[108:111], 0\n\t" \
    "ds_write_b128 v40, v[56:59] offset:60432\n\t" \
    "v_cvt_pk_bf16_f32 v60, v216, v217\n\t" \
    "v_mfma_f32_16x16x32_bf16 v[148:151], v[76:79], v[92:95], v[148:151]\n\t" \
    "v_cvt_pk_bf16_f32 v61, v218, v219\n\t" \
    "v_mfma_f32_16x16x32_bf16 v[136:139], v[76:79], v[84:87], 0\n\t" \
    "ds_write_b128 v40, v[60:63] offset:61456\n\t" \
    "buffer_load_dwordx4 v[168:171], v17, s[4:7], s46 offen offset:1024 sc1\n\t" \
    "v_mfma_f32_16x16x32_bf16 v[180:183], v[76:79], v[112:115], v[180:183]\n\t" \
    "s_waitcnt vmcnt(13)\n\t" \
    "v_mfma_f32_16x16x32_bf16 v[140:143], v[100:103], v[172:175], v[140:143]\n\t" \
    "v_mfma_f32_16x16x32_bf16 v[148:151], v[100:103], v[144:147], v[148:151]\n\t" \
    "s_waitcnt lgkmcnt(0)\n\t" \
    "s_xor_b32 s13, s13, s34\n\t" \
    "s_xor_b32 s14, s14, s34\n\t" \
    "s_or_b32 s13, s13, s14\n\t" \
    "s_cbranch_scc1 .Lq1026_%=\n\t" \
    ".Lb25_%=:\n\t" \
    "s_waitcnt lgkmcnt(0)\n\t" \
    "s_barrier\n\t" \
    "s_memrealtime s[16:17]\n\t" \
    "ds_read_b64_tr_b16 v[164:165], v49 offset:58368\n\t" \
    "ds_read_b64_tr_b16 v[166:167], v49 offset:62496\n\t" \
    "s_mov_b32 s47, 917504\n\t" \
    "buffer_load_dwordx4 v[24:27], v17, s[4:7], s47 offen offset:0 sc1\n\t" \
    "v_mfma_f32_16x16x32_bf16 v[64:67], v[100:103], v[152:155], v[64:67]\n\t" \
    "ds_read_b64_tr_b16 v[4:5], v49 offset:58624\n\t" \
    "ds_read_b64_tr_b16 v[6:7], v49 offset:62752\n\t" \
    "v_mfma_f32_16x16x32_bf16 v[180:183], v[100:103], v[160:163], v[180:183]\n\t" \
    "ds_read_b64_tr_b16 v[32:33], v49 offset:58384\n\t" \
    "ds_read_b64_tr_b16 v[34:35], v49 offset:62512\n\t" \
    "s_waitcnt vmcnt(13)\n\t" \
    "v_mfma_f32_16x16x32_bf16 v[68:71], v[12:15], v[144:147], v[68:71]\n\t" \
    "ds_read_b64_tr_b16 v[212:213], v49 offset:58640\n\t" \
    "ds_read_b64_tr_b16 v[214:215], v49 offset:62768\n\t" \
    "buffer_load_dwordx4 v[208:211], v17, s[4:7], s47 offen offset:1024 sc1\n\t" \
    "v_mfma_f32_16x16x32_bf16 v[148:151], v[12:15], v[172:175], v[148:151]\n\t" \
    "v_mfma_f32_16x16x32_bf16 v[136:139], v[12:15], v[160:163], v[136:139]\n\t" \
    "v_mfma_f32_16x16x32_bf16 v[180:183], v[12:15], v[152:155], v[180:183]\n\t" \
    "s_waitcnt lgkmcnt(6)\n\t" \
    "v_mfma_f32_16x16x32_bf16 v[72:75], v[164:167], v[204:207], 0\n\t" \
    "s_waitcnt lgkmcnt(4)\n\t" \
    "v_mfma_f32_16x16x32_bf16 v[88:91], v[4:7], v[204:207], 0\n\t" \
    "buffer_load_dwordx4 v[56:59], v19, s[4:7], s45 offen offset:0 sc1\n\t" \
    "s_waitcnt vmcnt(14)\n\t" \
    "v_mfma_f32_16x16x32_bf16 v[140:143], v[192:195], v[220:223], v[140:143]\n\t" \
    "s_waitcnt lgkmcnt(2)\n\t" \
    "v_mfma_f32_16x16x32_bf16 v[216:219], v[32:35], v[204:207], 0\n\t" \
    "s_waitcnt lgkmcnt(0)\n\t" \
    "v_mfma_f32_16x16x32_bf16 v[60:63], v[212:215], v[204:207], 0\n\t" \
    "v_mfma_f32_16x16x32_bf16 v[148:151], v[192:195], v[52:55], v[148:151]\n\t" \
    "s_cmp_lg_u32 s24, 4\n\t" \
    "s_cbranch_scc1 .Lph_%=\n\t" \
    "s_waitcnt lgkmcnt(0)\n\t" \
    "v_mov_b32 v76, s16\n\t" \
    "v_mov_b32 v77, s16\n\t" \
    "v_mov_b32 v78, s16\n\t" \
    "v_mov_b32 v79, s16\n\t" \
    "buffer_store_dwordx4 v[76:79], v2, s[4:7], s32 offen sc1\n\t" \
    "s_nop 0\n\t" \
    ".Lph_%=:\n\t" \
    "v_mfma_f32_16x16x32_bf16 v[64:67], v[192:195], v[80:83], v[64:67]\n\t" \
    "buffer_load_dwordx4 v[100:103], v19, s[4:7], s46 offen offset:0 sc1\n\t" \
    "v_mfma_f32_16x16x32_bf16 v[180:183], v[192:195], v[120:123], v[180:183]\n\t" \
    "v_bfi_b32 v72, s41, v50, v72\n\t" \
    "v_bfi_b32 v88, s41, v50, v88\n\t" \
    "s_waitcnt vmcnt(14)\n\t" \
    "v_mfma_f32_16x16x32_bf16 v[68:71], v[196:199], v[52:55], v[68:71]\n\t" \
    "v_cvt_pk_bf16_f32 v13, v74, v75\n\t" \
    "v_cvt_pk_bf16_f32 v15, v90, v91\n\t" \
    "v_mfma_f32_16x16x32_bf16 v[148:151], v[196:199], v[220:223], v[148:151]\n\t" \
    "v_cvt_pk_bf16_f32 v12, v72, v73\n\t" \
    "v_cvt_pk_bf16_f32 v14, v88, v89\n\t" \
    "v_mfma_f32_16x16x32_bf16 v[136:139], v[196:199], v[120:123], v[136:139]\n\t" \
    "v_bfi_b32 v216, s41, v50, v216\n\t" \
    "v_bfi_b32 v60, s41, v50, v60\n\t" \
    "v_mfma_f32_16x16x32_bf16 v[180:183], v[196:199], v[80:83], v[180:183]\n\t" \
    "buffer_store_dwordx4 v[12:15], v42, s[4:7], s48 offen offset:0 sc1\n\t" \
    "v_cvt_pk_bf16_f32 v165, v218, v219\n\t" \
    "s_waitcnt vmcnt(14)\n\t" \
    "v_mfma_f32_16x16x32_bf16 v[4:7], v[200:203], v[96:99], 0\n\t" \
    "v_cvt_pk_bf16_f32 v167, v62, v63\n\t" \
    "v_cvt_pk_bf16_f32 v164, v216, v217\n\t" \
    "v_mfma_f32_16x16x32_bf16 v[32:35], v[200:203], v[124:127], 0\n\t" \
    "v_cvt_pk_bf16_f32 v166, v60, v61\n\t" \
    "buffer_store_dwordx4 v[164:167], v46, s[4:7], s48 offen offset:128 sc1\n\t" \
    "v_mfma_f32_16x16x32_bf16 v[212:215], v[200:203], v[116:119], 0\n\t" \
    "v_sub_f32 v140, v140, v68\n\t" \
    "v_sub_f32 v141, v141, v69\n\t" \
    "v_mfma_f32_16x16x32_bf16 v[76:79], v[200:203], v[128:131], 0\n\t" \
    "v_sub_f32 v142, v142, v70\n\t" \
    "v_sub_f32 v143, v143, v71\n\t" \
    "s_waitcnt vmcnt(14)\n\t" \
    "v_mfma_f32_16x16x32_bf16 v[4:7], v[104:107], v[176:179], v[4:7]\n\t" \
    "v_sub_f32 v64, v64, v136\n\t" \
    "v_sub_f32 v65, v65, v137\n\t" \
    "v_mfma_f32_16x16x32_bf16 v[32:35], v[104:107], v[184:187], v[32:35]\n\t" \
    "v_sub_f32 v66, v66, v138\n\t" \
    "v_sub_f32 v67, v67, v139\n\t" \
    "v_mfma_f32_16x16x32_bf16 v[212:215], v[104:107], v[188:191], v[212:215]\n\t" \
    "v_cvt_pk_bf16_f32 v194, v148, v149\n\t" \
    "v_cvt_pk_bf16_f32 v195, v150, v151\n\t" \
    "v_mfma_f32_16x16x32_bf16 v[76:79], v[104:107], v[20:23], v[76:79]\n\t" \
    "v_cvt_pk_bf16_f32 v192, v140, v141\n\t" \
    "v_cvt_pk_bf16_f32 v193, v142, v143\n\t" \
    "s_waitcnt vmcnt(13)\n\t" \
    "v_mfma_f32_16x16x32_bf16 v[4:7], v[132:135], v[224:227], v[4:7]\n\t" \
    "v_cvt_pk_bf16_f32 v74, v180, v181\n\t" \
    "v_cvt_pk_bf16_f32 v75, v182, v183\n\t" \
    "v_mfma_f32_16x16x32_bf16 v[32:35], v[132:135], v[228:231], v[32:35]\n\t" \
    "ds_write_b128 v40, v[192:195] offset:0\n\t" \
    "v_cvt_pk_bf16_f32 v72, v64, v65\n\t" \
    "buffer_load_dwordx4 v[88:91], v19, s[4:7], s47 offen offset:0 sc1\n\t" \
    "v_mfma_f32_16x16x32_bf16 v[212:215], v[132:135], v[232:235], v[212:215]\n\t" \
    "v_cvt_pk_bf16_f32 v73, v66, v67\n\t" \
    "ds_write_b128 v40, v[72:75] offset:1024\n\t" \
    "v_mfma_f32_16x16x32_bf16 v[76:79], v[132:135], v[240:243], v[76:79]\n\t" \
    "s_waitcnt vmcnt(11)\n\t" \
    "s_cmp_ge_u32 s24, 4\n\t" \
    "s_cbranch_scc1 .Lk43_%=\n\t" \
    "v_mfma_f32_16x16x32_bf16 v[196:199], v[236:239], v[124:127], 0\n\t" \
    "v_mfma_f32_16x16x32_bf16 v[196:199], v[244:247], v[184:187], v[196:199]\n\t" \
    "v_mfma_f32_16x16x32_bf16 v[196:199], v[248:251], v[228:231], v[196:199]\n\t" \
    "v_mfma_f32_16x16x32_bf16 v[12:15], v[236:239], v[128:131], 0\n\t" \
    "v_mfma_f32_16x16x32_bf16 v[12:15], v[244:247], v[20:23], v[12:15]\n\t" \
    "v_mfma_f32_16x16x32_bf16 v[12:15], v[248:251], v[240:243], v[12:15]\n\t" \
    "v_mfma_f32_16x16x32_bf16 v[32:35], v[236:239], v[96:99], v[32:35]\n\t" \
    "v_mfma_f32_16x16x32_bf16 v[32:35], v[244:247], v[176:179], v[32:35]\n\t" \
    "v_mfma_f32_16x16x32_bf16 v[32:35], v[248:251], v[224:227], v[32:35]\n\t" \
    "v_mfma_f32_16x16x32_bf16 v[76:79], v[236:239], v[116:119], v[76:79]\n\t" \
    "s_nop 0\n\t" \
    "v_sub_f32 v4, v4, v196\n\t" \
    "v_sub_f32 v5, v5, v197\n\t" \
    "v_mfma_f32_16x16x32_bf16 v[76:79], v[244:247], v[188:191], v[76:79]\n\t" \
    "v_sub_f32 v6, v6, v198\n\t" \
    "v_sub_f32 v7, v7, v199\n\t" \
    "v_mfma_f32_16x16x32_bf16 v[76:79], v[248:251], v[232:235], v[76:79]\n\t" \
    "v_sub_f32 v212, v212, v12\n\t" \
    "v_sub_f32 v213, v213, v13\n\t" \
    "v_sub_f32 v214, v214, v14\n\t" \
    "v_sub_f32 v215, v215, v15\n\t" \
    ".Lk43_%=:\n\t" \
    "s_waitcnt vmcnt(10)\n\t" \
    "v_mfma_f32_16x16x32_bf16 v[216:219], v[252:255], v[92:95], 0\n\t" \
    "buffer_load_dwordx4 v[60:63], v41, s[4:7], s45 offen offset:1024 sc1\n\t" \
    "v_mfma_f32_16x16x32_bf16 v[164:167], v[252:255], v[108:111], 0\n\t" \
    "v_cvt_pk_bf16_f32 v202, v32, v33\n\t" \
    "v_cvt_pk_bf16_f32 v203, v34, v35\n\t" \
    "v_mfma_f32_16x16x32_bf16 v[68:71], v[252:255], v[112:115], 0\n\t" \
    "v_cvt_pk_bf16_f32 v200, v4, v5\n\t" \
    "v_cvt_pk_bf16_f32 v201, v6, v7\n\t" \
    "v_mfma_f32_16x16x32_bf16 v[136:139], v[252:255], v[84:87], 0\n\t" \
    "v_cvt_pk_bf16_f32 v150, v76, v77\n\t" \
    "v_cvt_pk_bf16_f32 v151, v78, v79\n\t" \
    "buffer_load_dwordx4 v[104:107], v41, s[4:7], s46 offen offset:1024 sc1\n\t" \
    "s_waitcnt vmcnt(11)\n\t" \
    "v_mfma_f32_16x16x32_bf16 v[140:143], v[156:159], v[108:111], 0\n\t" \
    "ds_write_b128 v40, v[200:203] offset:2064\n\t" \
    "v_cvt_pk_bf16_f32 v148, v212, v213\n\t" \
    "v_mfma_f32_16x16x32_bf16 v[164:167], v[156:159], v[92:95], v[164:167]\n\t" \
    "v_cvt_pk_bf16_f32 v149, v214, v215\n\t" \
    "v_mfma_f32_16x16x32_bf16 v[180:183], v[156:159], v[84:87], 0\n\t" \
    "ds_write_b128 v40, v[148:151] offset:3088\n\t" \
    "buffer_load_dwordx4 v[192:195], v41, s[4:7], s47 offen offset:1024 sc1\n\t" \
    "v_mfma_f32_16x16x32_bf16 v[136:139], v[156:159], v[112:115], v[136:139]\n\t" \
    "s_waitcnt vmcnt(11)\n\t" \
    "v_mfma_f32_16x16x32_bf16 v[216:219], v[28:31], v[172:175], v[216:219]\n\t" \
    "v_mfma_f32_16x16x32_bf16 v[164:167], v[28:31], v[144:147], v[164:167]\n\t" \
    "s_waitcnt lgkmcnt(0)\n\t" \
    "s_barrier\n\t" \
    "s_memrealtime s[18:19]\n\t" \
    "s_setprio 2\n\t" \
    "ds_read_b64_tr_b16 v[16:17], v49 offset:0\n\t" \
    "ds_read_b64_tr_b16 v[18:19], v49 offset:4128\n\t" \
    "v_mfma_f32_16x16x32_bf16 v[68:71], v[28:31], v[152:155], v[68:71]\n\t" \
    "ds_read_b64_tr_b16 v[64:65], v49 offset:256\n\t" \
    "ds_read_b64_tr_b16 v[66:67], v49 offset:4384\n\t" \
    "v_mfma_f32_16x16x32_bf16 v[136:139], v[28:31], v[160:163], v[136:139]\n\t" \
    "ds_read_b64_tr_b16 v[72:73], v49 offset:16\n\t" \
    "ds_read_b64_tr_b16 v[74:75], v49 offset:4144\n\t" \
    "s_waitcnt vmcnt(10)\n\t" \
    "v_mfma_f32_16x16x32_bf16 v[140:143], v[168:171], v[144:147], v[140:143]\n\t" \
    "ds_read_b64_tr_b16 v[132:133], v49 offset:272\n\t" \
    "ds_read_b64_tr_b16 v[134:135], v49 offset:4400\n\t" \
    "v_mfma_f32_16x16x32_bf16 v[164:167], v[168:171], v[172:175], v[164:167]\n\t" \
    "v_mfma_f32_16x16x32_bf16 v[180:183], v[168:171], v[160:163], v[180:183]\n\t" \
    "v_mfma_f32_16x16x32_bf16 v[136:139], v[168:171], v[152:155], v[136:139]\n\t" \
    "s_waitcnt lgkmcnt(6)\n\t" \
    "v_mfma_f32_16x16x32_bf16 v[236:239], v[16:19], v[204:207], 0\n\t" \
    "s_waitcnt lgkmcnt(4)\n\t" \
    "v_mfma_f32_16x16x32_bf16 v[244:247], v[64:67], v[204:207], 0\n\t" \
    "s_waitcnt vmcnt(9)\n\t" \
    "v_mfma_f32_16x16x32_bf16 v[216:219], v[24:27], v[220:223], v[216:219]\n\t" \
    "s_waitcnt lgkmcnt(2)\n\t" \
    "v_mfma_f32_16x16x32_bf16 v[196:199], v[72:75], v[204:207], 0\n\t" \
    "s_waitcnt lgkmcnt(0)\n\t" \
    "v_mfma_f32_16x16x32_bf16 v[248:251], v[132:135], v[204:207], 0\n\t" \
    "v_mfma_f32_16x16x32_bf16 v[164:167], v[24:27], v[52:55], v[164:167]\n\t" \
    "v_mfma_f32_16x16x32_bf16 v[68:71], v[24:27], v[80:83], v[68:71]\n\t" \
    "s_add_u32 s37, s52, s33\n\t" \
    "s_buffer_load_dword s58, s[4:7], s37 offset:0x0\n\t" \
    "s_buffer_load_dword s59, s[4:7], s37 offset:0x400\n\t" \
    "s_buffer_load_dword s60, s[4:7], s37 offset:0x4000\n\t" \
    "s_buffer_load_dword s61, s[4:7], s37 offset:0x4400\n\t" \
    "v_mfma_f32_16x16x32_bf16 v[136:139], v[24:27], v[120:123], v[136:139]\n\t" \
    "v_bfi_b32 v236, s41, v50, v236\n\t" \
    "v_bfi_b32 v244, s41, v50, v244\n\t" \
    "s_waitcnt vmcnt(8)\n\t" \
    "v_mfma_f32_16x16x32_bf16 v[140:143], v[208:211], v[52:55], v[140:143]\n\t" \
    "v_cvt_pk_bf16_f32 v13, v238, v239\n\t" \
    "v_cvt_pk_bf16_f32 v15, v246, v247\n\t" \
    "v_add_u32 v40, 91392, v40\n\t" \
    "v_mfma_f32_16x16x32_bf16 v[164:167], v[208:211], v[220:223], v[164:167]\n\t" \
    "v_cvt_pk_bf16_f32 v12, v236, v237\n\t" \
    "v_cvt_pk_bf16_f32 v14, v244, v245\n\t" \
    "v_mfma_f32_16x16x32_bf16 v[180:183], v[208:211], v[120:123], v[180:183]\n\t" \
    "v_bfi_b32 v196, s41, v50, v196\n\t" \
    "v_bfi_b32 v248, s41, v50, v248\n\t" \
    "v_mfma_f32_16x16x32_bf16 v[136:139], v[208:211], v[80:83], v[136:139]\n\t" \
    "buffer_store_dwordx4 v[12:15], v42, s[4:7], s49 offen offset:0 sc1\n\t" \
    "v_cvt_pk_bf16_f32 v33, v198, v199\n\t" \
    "s_waitcnt vmcnt(8)\n\t" \
    "v_mfma_f32_16x16x32_bf16 v[4:7], v[56:59], v[96:99], 0\n\t" \
    "v_cvt_pk_bf16_f32 v35, v250, v251\n\t" \
    "v_cvt_pk_bf16_f32 v32, v196, v197\n\t" \
    "v_mfma_f32_16x16x32_bf16 v[252:255], v[56:59], v[124:127], 0\n\t" \
    "v_cvt_pk_bf16_f32 v34, v248, v249\n\t" \
    "buffer_store_dwordx4 v[32:35], v46, s[4:7], s49 offen offset:128 sc1\n\t" \
    "v_mfma_f32_16x16x32_bf16 v[76:79], v[56:59], v[116:119], 0\n\t" \
    "s_setprio 0\n\t" \
    "s_cmp_lt_u32 s24, 4\n\t" \
    "s_cbranch_scc1 .Lp44_%=\n\t" \
    "s_setprio 1\n\t" \
    ".Lp44_%=:\n\t" \
    "v_sub_f32 v216, v216, v140\n\t" \
    "v_sub_f32 v217, v217, v141\n\t" \
    "v_mfma_f32_16x16x32_bf16 v[108:111], v[56:59], v[128:131], 0\n\t" \
    "v_sub_f32 v218, v218, v142\n\t" \
    "v_sub_f32 v219, v219, v143\n\t" \
    "s_waitcnt vmcnt(8)\n\t" \
    "v_mfma_f32_16x16x32_bf16 v[4:7], v[100:103], v[176:179], v[4:7]\n\t" \
    "v_sub_f32 v68, v68, v180\n\t" \
    "v_sub_f32 v69, v69, v181\n\t" \
    "v_mfma_f32_16x16x32_bf16 v[252:255], v[100:103], v[184:187], v[252:255]\n\t" \
    "v_sub_f32 v70, v70, v182\n\t" \
    "v_sub_f32 v71, v71, v183\n\t" \
    "v_mfma_f32_16x16x32_bf16 v[76:79], v[100:103], v[188:191], v[76:79]\n\t" \
    "v_cvt_pk_bf16_f32 v202, v164, v165\n\t" \
    "v_cvt_pk_bf16_f32 v203, v166, v167\n\t" \
    "v_mfma_f32_16x16x32_bf16 v[108:111], v[100:103], v[20:23], v[108:111]\n\t" \
    "v_cvt_pk_bf16_f32 v200, v216, v217\n\t" \
    "v_cvt_pk_bf16_f32 v201, v218, v219\n\t" \
    "s_waitcnt vmcnt(5)\n\t" \
    "v_mfma_f32_16x16x32_bf16 v[4:7], v[88:91], v[224:227], v[4:7]\n\t" \
    "v_cvt_pk_bf16_f32 v94, v136, v137\n\t" \
    "v_cvt_pk_bf16_f32 v95, v138, v139\n\t" \
    "v_mfma_f32_16x16x32_bf16 v[252:255], v[88:91], v[228:231], v[252:255]\n\t" \
    "ds_write_b128 v40, v[200:203] offset:0\n\t" \
    "v_cvt_pk_bf16_f32 v92, v68, v69\n\t" \
    "v_mfma_f32_16x16x32_bf16 v[76:79], v[88:91], v[232:235], v[76:79]\n\t" \
    "v_cvt_pk_bf16_f32 v93, v70, v71\n\t" \
    "ds_write_b128 v40, v[92:95] offset:1024\n\t" \
    "v_mfma_f32_16x16x32_bf16 v[108:111], v[88:91], v[240:243], v[108:111]\n\t" \
    "s_waitcnt vmcnt(2)\n\t" \
    "s_cmp_ge_u32 s24, 4\n\t" \
    "s_cbranch_scc1 .Lk45_%=\n\t" \
    "v_mfma_f32_16x16x32_bf16 v[212:215], v[60:63], v[124:127], 0\n\t" \
    "v_mfma_f32_16x16x32_bf16 v[212:215], v[104:107], v[184:187], v[212:215]\n\t" \
    "v_mfma_f32_16x16x32_bf16 v[212:215], v[192:195], v[228:231], v[212:215]\n\t" \
    "v_mfma_f32_16x16x32_bf16 v[84:87], v[60:63], v[128:131], 0\n\t" \
    "v_mfma_f32_16x16x32_bf16 v[84:87], v[104:107], v[20:23], v[84:87]\n\t" \
    "v_mfma_f32_16x16x32_bf16 v[84:87], v[192:195], v[240:243], v[84:87]\n\t" \
    "v_mfma_f32_16x16x32_bf16 v[252:255], v[60:63], v[96:99], v[252:255]\n\t" \
    "v_mfma_f32_16x16x32_bf16 v[252:255], v[104:107], v[176:179], v[252:255]\n\t" \
    "v_mfma_f32_16x16x32_bf16 v[252:255], v[192:195], v[224:227], v[252:255]\n\t" \
    "v_mfma_f32_16x16x32_bf16 v[108:111], v[60:63], v[116:119], v[108:111]\n\t" \
    "s_nop 0\n\t" \
    "v_sub_f32 v4, v4, v212\n\t" \
    "v_sub_f32 v5, v5, v213\n\t" \
    "v_mfma_f32_16x16x32_bf16 v[108:111], v[104:107], v[188:191], v[108:111]\n\t" \
    "v_sub_f32 v6, v6, v214\n\t" \
    "v_sub_f32 v7, v7, v215\n\t" \
    "v_mfma_f32_16x16x32_bf16 v[108:111], v[192:195], v[232:235], v[108:111]\n\t" \
    "v_sub_f32 v76, v76, v84\n\t" \
    "v_sub_f32 v77, v77, v85\n\t" \
    "v_sub_f32 v78, v78, v86\n\t" \
    "v_sub_f32 v79, v79, v87\n\t" \
    ".Lk45_%=:\n\t" \
    "v_cvt_pk_bf16_f32 v150, v252, v253\n\t" \
    "v_cvt_pk_bf16_f32 v151, v254, v255\n\t" \
    "v_cvt_pk_bf16_f32 v148, v4, v5\n\t" \
    "v_cvt_pk_bf16_f32 v149, v6, v7\n\t" \
    "s_nop 1\n\t" \
    "v_cvt_pk_bf16_f32 v114, v108, v109\n\t" \
    "v_cvt_pk_bf16_f32 v115, v110, v111\n\t" \
    "ds_write_b128 v40, v[148:151] offset:2064\n\t" \
    "v_cvt_pk_bf16_f32 v112, v76, v77\n\t" \
    "v_cvt_pk_bf16_f32 v113, v78, v79\n\t" \
    "v_add_u32 v49, 91392, v49\n\t" \
    "ds_write_b128 v40, v[112:115] offset:3088\n\t" \
    "s_waitcnt lgkmcnt(0)\n\t" \
    "s_barrier\n\t" \
    "s_memrealtime s[56:57]\n\t" \
    "s_add_u32 s54, s53, 0\n\t" \
    "buffer_load_dwordx4 v[156:159], v0, s[4:7], s54 offen offset:0 sc1\n\t" \
    "s_add_u32 s54, s53, 4243456\n\t" \
    "buffer_load_dwordx4 v[28:31], v0, s[4:7], s54 offen offset:0 sc1\n\t" \
    "ds_read_b64_tr_b16 v[144:145], v49 offset:0\n\t" \
    "ds_read_b64_tr_b16 v[146:147], v49 offset:4128\n\t" \
    "ds_read_b64_tr_b16 v[172:173], v49 offset:256\n\t" \
    "ds_read_b64_tr_b16 v[174:175], v49 offset:4384\n\t" \
    "ds_read_b64_tr_b16 v[160:161], v49 offset:16\n\t" \
    "ds_read_b64_tr_b16 v[162:163], v49 offset:4144\n\t" \
    "ds_read_b64_tr_b16 v[152:153], v49 offset:272\n\t" \
    "ds_read_b64_tr_b16 v[154:155], v49 offset:4400\n\t" \
    "s_waitcnt lgkmcnt(6)\n\t" \
    "v_mfma_f32_16x16x32_bf16 v[168:171], v[144:147], v[204:207], 0\n\t" \
    "s_waitcnt lgkmcnt(4)\n\t" \
    "v_mfma_f32_16x16x32_bf16 v[16:19], v[172:175], v[204:207], 0\n\t" \
    "s_waitcnt lgkmcnt(2)\n\t" \
    "v_mfma_f32_16x16x32_bf16 v[64:67], v[160:163], v[204:207], 0\n\t" \
    "s_waitcnt lgkmcnt(0)\n\t" \
    "v_mfma_f32_16x16x32_bf16 v[72:75], v[152:155], v[204:207], 0\n\t" \
    "s_nop 1\n\t" \
    "v_bfi_b32 v168, s41, v50, v168\n\t" \
    "s_nop 0\n\t" \
    "v_bfi_b32 v16, s41, v50, v16\n\t" \
    "v_cvt_pk_bf16_f32 v133, v170, v171\n\t" \
    "v_cvt_pk_bf16_f32 v135, v18, v19\n\t" \
    "v_cvt_pk_bf16_f32 v132, v168, v169\n\t" \
    "v_cvt_pk_bf16_f32 v134, v16, v17\n\t" \
    "v_bfi_b32 v64, s41, v50, v64\n\t" \
    "v_bfi_b32 v72, s41, v50, v72\n\t" \
    "buffer_store_dwordx4 v[132:135], v42, s[4:7], s50 offen offset:0 sc1\n\t" \
    "v_cvt_pk_bf16_f32 v25, v66, v67\n\t" \
    "v_cvt_pk_bf16_f32 v27, v74, v75\n\t" \
    "v_cvt_pk_bf16_f32 v24, v64, v65\n\t" \
    "v_cvt_pk_bf16_f32 v26, v72, v73\n\t" \
    "buffer_store_dwordx4 v[24:27], v46, s[4:7], s50 offen offset:128 sc1\n\t" \
    "s_cmp_ge_u32 s25, 126\n\t" \
    "s_cbranch_scc1 .Lend_%=\n\t" \
    "ds_read_b128 v[52:55], v10 offset:3072\n\t" \
    "ds_read_b128 v[220:223], v10 offset:4096\n\t" \
    "s_waitcnt lgkmcnt(0)\n\t" \
    "s_sub_u32 s37, s16, 300\n\t" \
    "s_mov_b32 s62, 300\n\t" \
    "s_sub_u32 s39, s58, s37\n\t" \
    "s_cmp_gt_u32 s39, 600\n\t" \
    "s_cselect_b32 s39, 400, s39\n\t" \
    "s_max_u32 s62, s62, s39\n\t" \
    "s_sub_u32 s39, s59, s37\n\t" \
    "s_cmp_gt_u32 s39, 600\n\t" \
    "s_cselect_b32 s39, 400, s39\n\t" \
    "s_max_u32 s62, s62, s39\n\t" \
    "s_sub_u32 s39, s60, s37\n\t" \
    "s_cmp_gt_u32 s39, 600\n\t" \
    "s_cselect_b32 s39, 400, s39\n\t" \
    "s_max_u32 s62, s62, s39\n\t" \
    "s_sub_u32 s39, s61, s37\n\t" \
    "s_cmp_gt_u32 s39, 600\n\t" \
    "s_cselect_b32 s39, 400, s39\n\t" \
    "s_max_u32 s62, s62, s39\n\t" \
    "s_sub_u32 s62, s62, 300\n\t" \
    "s_mul_i32 s62, s62, 5\n\t" \
    "s_lshr_b32 s62, s62, 2\n\t" \
    "s_waitcnt vmcnt(2)\n\t" \
    "v_and_b32 v3, v156, v158\n\t" \
    "v_bitop3_b32 v3, v3, v28, v30 bitop3:0x80\n\t" \
    "v_and_b32 v3, 1, v3\n\t" \
    "v_cmp_eq_u32 vcc, 0, v3\n\t" \
    "s_nop 1\n\t" \
    "s_cbranch_vccnz .Ls39_%=\n\t" \
    ".Lb39_%=:\n\t" \
    "ds_write_b128 v48, v[156:159] offset:33024\n\t" \
    "ds_write_b128 v48, v[28:31] offset:33152\n\t" \
    "s_add_u32 s37, s62, 150\n\t" \
    ".Lh46_%=:\n\t" \
    "s_memrealtime s[22:23]\n\t" \
    "s_waitcnt lgkmcnt(0)\n\t" \
    "s_sub_u32 s22, s22, s18\n\t" \
    "s_cmp_lt_u32 s22, s37\n\t" \
    "s_cbranch_scc1 .Lh46_%=\n\t" \
    "s_add_u32 s54, s53, 524288\n\t" \
    "buffer_load_dwordx4 v[236:239], v0, s[4:7], s54 offen offset:0 sc1\n\t" \
    "s_add_u32 s54, s53, 4767744\n\t" \
    "buffer_load_dwordx4 v[244:247], v0, s[4:7], s54 offen offset:0 sc1\n\t" \
    "s_waitcnt lgkmcnt(0)\n\t" \
    "s_barrier\n\t" \
    "ds_read_b64_tr_b16 v[120:121], v43 offset:33024\n\t" \
    "ds_read_b64_tr_b16 v[122:123], v43 offset:33280\n\t" \
    "ds_read_b64_tr_b16 v[80:81], v43 offset:33032\n\t" \
    "ds_read_b64_tr_b16 v[82:83], v43 offset:33288\n\t" \
    "s_waitcnt lgkmcnt(2)\n\t" \
    "v_mfma_f32_16x16x32_bf16 v[208:211], v[120:123], v[52:55], 0\n\t" \
    "s_waitcnt lgkmcnt(0)\n\t" \
    "v_mfma_f32_16x16x32_bf16 v[12:15], v[80:83], v[52:55], 0\n\t" \
    "v_mfma_f32_16x16x32_bf16 v[196:199], v[120:123], v[220:223], 0\n\t" \
    "v_mfma_f32_16x16x32_bf16 v[248:251], v[80:83], v[220:223], 0\n\t" \
    "s_nop 3\n\t" \
    "v_cvt_pk_bf16_f32 v32, v208, v209\n\t" \
    "v_cvt_pk_bf16_f32 v33, v210, v211\n\t" \
    "v_cvt_pk_bf16_f32 v34, v12, v13\n\t" \
    "v_cvt_pk_bf16_f32 v35, v14, v15\n\t" \
    "buffer_store_dwordx4 v[32:35], v38, s[8:11], 0 offen offset:0 sc1\n\t" \
    "v_cvt_pk_bf16_f32 v56, v196, v197\n\t" \
    "v_cvt_pk_bf16_f32 v57, v198, v199\n\t" \
    "v_cvt_pk_bf16_f32 v58, v248, v249\n\t" \
    "v_cvt_pk_bf16_f32 v59, v250, v251\n\t" \
    "buffer_store_dwordx4 v[56:59], v38, s[8:11], 0 offen offset:3072 sc1\n\t" \
    "s_waitcnt vmcnt(2)\n\t" \
    "v_and_b32 v51, v236, v238\n\t" \
    "v_bitop3_b32 v51, v51, v244, v246 bitop3:0x80\n\t" \
    "v_and_b32 v51, 1, v51\n\t" \
    "v_cmp_eq_u32 vcc, 0, v51\n\t" \
    "s_nop 1\n\t" \
    "s_cbranch_vccnz .Ls40_%=\n\t" \
    ".Lb40_%=:\n\t" \
    "ds_write_b128 v48, v[236:239] offset:0\n\t" \
    "ds_write_b128 v48, v[244:247] offset:128\n\t" \
    "s_add_u32 s37, s62, 120\n\t" \
    ".Lh47_%=:\n\t" \
    "s_memrealtime s[22:23]\n\t" \
    "s_waitcnt lgkmcnt(0)\n\t" \
    "s_sub_u32 s22, s22, s56\n\t" \
    "s_cmp_lt_u32 s22, s37\n\t" \
    "s_cbranch_scc1 .Lh47_%=\n\t" \
    "s_add_u32 s54, s53, 1048576\n\t" \
    "buffer_load_dwordx4 v[140:143], v0, s[4:7], s54 offen offset:0 sc1\n\t" \
    "s_add_u32 s54, s53, 5292032\n\t" \
    "buffer_load_dwordx4 v[180:183], v0, s[4:7], s54 offen offset:0 sc1\n\t" \
    "s_waitcnt lgkmcnt(0)\n\t" \
    "s_barrier\n\t" \
    "ds_read_b64_tr_b16 v[164:165], v43 offset:0\n\t" \
    "ds_read_b64_tr_b16 v[166:167], v43 offset:256\n\t" \
    "ds_read_b64_tr_b16 v[100:101], v43 offset:8\n\t" \
    "ds_read_b64_tr_b16 v[102:103], v43 offset:264\n\t" \
    "s_waitcnt lgkmcnt(2)\n\t" \
    "v_mfma_f32_16x16x32_bf16 v[216:219], v[164:167], v[52:55], 0\n\t" \
    "s_waitcnt lgkmcnt(0)\n\t" \
    "v_mfma_f32_16x16x32_bf16 v[136:139], v[100:103], v[52:55], 0\n\t" \
    "v_mfma_f32_16x16x32_bf16 v[200:203], v[164:167], v[220:223], 0\n\t" \
    "v_mfma_f32_16x16x32_bf16 v[68:71], v[100:103], v[220:223], 0\n\t" \
    "s_nop 3\n\t" \
    "v_cvt_pk_bf16_f32 v92, v216, v217\n\t" \
    "v_cvt_pk_bf16_f32 v93, v218, v219\n\t" \
    "v_cvt_pk_bf16_f32 v94, v136, v137\n\t" \
    "v_cvt_pk_bf16_f32 v95, v138, v139\n\t" \
    "buffer_store_dwordx4 v[92:95], v38, s[8:11], 0 offen offset:64 sc1\n\t" \
    "v_cvt_pk_bf16_f32 v88, v200, v201\n\t" \
    "v_cvt_pk_bf16_f32 v89, v202, v203\n\t" \
    "v_cvt_pk_bf16_f32 v90, v68, v69\n\t" \
    "v_cvt_pk_bf16_f32 v91, v70, v71\n\t" \
    "buffer_store_dwordx4 v[88:91], v38, s[8:11], 0 offen offset:3136 sc1\n\t" \
    "s_waitcnt vmcnt(2)\n\t" \
    "v_and_b32 v39, v140, v142\n\t" \
    "v_bitop3_b32 v39, v39, v180, v182 bitop3:0x80\n\t" \
    "v_and_b32 v39, 1, v39\n\t" \
    "v_cmp_eq_u32 vcc, 0, v39\n\t" \
    "s_nop 1\n\t" \
    "s_cbranch_vccnz .Ls41_%=\n\t" \
    ".Lb41_%=:\n\t" \
    "ds_write_b128 v48, v[140:143] offset:16512\n\t" \
    "ds_write_b128 v48, v[180:183] offset:16640\n\t" \
    "s_waitcnt lgkmcnt(0)\n\t" \
    "s_barrier\n\t" \
    "ds_read_b64_tr_b16 v[124:125], v43 offset:16512\n\t" \
    "ds_read_b64_tr_b16 v[126:127], v43 offset:16768\n\t" \
    "ds_read_b64_tr_b16 v[184:185], v43 offset:16520\n\t" \
    "ds_read_b64_tr_b16 v[186:187], v43 offset:16776\n\t" \
    "s_waitcnt lgkmcnt(2)\n\t" \
    "v_mfma_f32_16x16x32_bf16 v[228:231], v[124:127], v[52:55], 0\n\t" \
    "s_waitcnt lgkmcnt(0)\n\t" \
    "v_mfma_f32_16x16x32_bf16 v[128:131], v[184:187], v[52:55], 0\n\t" \
    "v_mfma_f32_16x16x32_bf16 v[20:23], v[124:127], v[220:223], 0\n\t" \
    "v_mfma_f32_16x16x32_bf16 v[240:243], v[184:187], v[220:223], 0\n\t" \
    "s_nop 3\n\t" \
    "v_cvt_pk_bf16_f32 v96, v228, v229\n\t" \
    "v_cvt_pk_bf16_f32 v97, v230, v231\n\t" \
    "v_cvt_pk_bf16_f32 v98, v128, v129\n\t" \
    "v_cvt_pk_bf16_f32 v99, v130, v131\n\t" \
    "buffer_store_dwordx4 v[96:99], v38, s[8:11], 0 offen offset:128 sc1\n\t" \
    "v_cvt_pk_bf16_f32 v176, v20, v21\n\t" \
    "v_cvt_pk_bf16_f32 v177, v22, v23\n\t" \
    "v_cvt_pk_bf16_f32 v178, v240, v241\n\t" \
    "v_cvt_pk_bf16_f32 v179, v242, v243\n\t" \
    "buffer_store_dwordx4 v[176:179], v38, s[8:11], 0 offen offset:3200 sc1\n\t" \
    "s_or_b32 s37, s2, s24\n\t" \
    "s_cbranch_scc1 .Lend_%=\n\t" \
    "s_mov_b32 s37, 34635776\n\t" \
    "v_mov_b32 v1, s34\n\t" \
    "s_mov_b64 exec, 1\n\t" \
    "buffer_store_dword v1, v0, s[4:7], s37 offen sc1\n\t" \
    "s_mov_b64 exec, -1\n\t" \
    ".Lend_%=:\n\t" \
    "s_endpgm\n\t" \
    ".Lq1026_%=:\n\t" \
    "s_mov_b32 s40, 0\n\t" \
    ".Lr1026_%=:\n\t" \
    "s_add_u32 s12, s55, 34603008\n\t" \
    "s_buffer_load_dword s13, s[4:7], s12 glc\n\t" \
    "s_buffer_load_dword s14, s[4:7], s12 offset:0x400 glc\n\t" \
    "s_waitcnt lgkmcnt(0)\n\t" \
    "s_xor_b32 s13, s13, s34\n\t" \
    "s_xor_b32 s14, s14, s34\n\t" \
    "s_or_b32 s13, s13, s14\n\t" \
    "s_cbranch_scc0 .Lb25_%=\n\t" \
    "s_add_u32 s40, s40, 1\n\t" \
    "s_cmp_lt_u32 s40, 1\n\t" \
    "s_cbranch_scc1 .Lr1026_%=\n\t" \
    "s_branch .Ls25_%=\n\t" \
    ".Lq1023_%=:\n\t" \
    "s_mov_b32 s40, 0\n\t" \
    ".Lr1023_%=:\n\t" \
    "s_nop 0\n\t" \
    "v_readlane_b32 s12, v3, 0\n\t" \
    "v_readlane_b32 s13, v3, 1\n\t" \
    "v_readlane_b32 s14, v3, 2\n\t" \
    "v_readlane_b32 s15, v3, 3\n\t" \
    "v_readlane_b32 s16, v3, 4\n\t" \
    "v_readlane_b32 s17, v3, 5\n\t" \
    "v_readlane_b32 s18, v3, 6\n\t" \
    "v_readlane_b32 s19, v3, 7\n\t" \
    "v_readlane_b32 s3, v3, 8\n\t" \
    "s_add_u32 s12, s12, s33\n\t" \
    "s_add_u32 s13, s13, s33\n\t" \
    "s_add_u32 s14, s14, s33\n\t" \
    "s_add_u32 s15, s15, s33\n\t" \
    "s_add_u32 s16, s16, s33\n\t" \
    "s_add_u32 s17, s17, s33\n\t" \
    "s_add_u32 s18, s18, s33\n\t" \
    "s_add_u32 s19, s19, s33\n\t" \
    "s_add_u32 s3, s3, s33\n\t" \
    "s_buffer_load_dword s12, s[4:7], s12 offset:0xc0000 glc\n\t" \
    "s_buffer_load_dword s13, s[4:7], s13 offset:0xc0000 glc\n\t" \
    "s_buffer_load_dword s14, s[4:7], s14 offset:0xc0000 glc\n\t" \
    "s_buffer_load_dword s15, s[4:7], s15 offset:0xc0000 glc\n\t" \
    "s_buffer_load_dword s16, s[4:7], s16 offset:0xc0000 glc\n\t" \
    "s_buffer_load_dword s17, s[4:7], s17 offset:0xc0000 glc\n\t" \
    "s_buffer_load_dword s18, s[4:7], s18 offset:0xc0000 glc\n\t" \
    "s_buffer_load_dword s19, s[4:7], s19 offset:0xc0000 glc\n\t" \
    "s_buffer_load_dword s3, s[4:7], s3 offset:0xc0000 glc\n\t" \
    "s_waitcnt lgkmcnt(0)\n\t" \
    "s_xor_b32 s12, s12, s34\n\t" \
    "s_xor_b32 s13, s13, s34\n\t" \
    "s_xor_b32 s14, s14, s34\n\t" \
    "s_xor_b32 s15, s15, s34\n\t" \
    "s_xor_b32 s16, s16, s34\n\t" \
    "s_xor_b32 s17, s17, s34\n\t" \
    "s_xor_b32 s18, s18, s34\n\t" \
    "s_xor_b32 s19, s19, s34\n\t" \
    "s_xor_b32 s3, s3, s34\n\t" \
    "s_or_b32 s12, s12, s13\n\t" \
    "s_or_b32 s12, s12, s14\n\t" \
    "s_or_b32 s12, s12, s15\n\t" \
    "s_or_b32 s12, s12, s16\n\t" \
    "s_or_b32 s12, s12, s17\n\t" \
    "s_or_b32 s12, s12, s18\n\t" \
    "s_or_b32 s12, s12, s19\n\t" \
    "s_or_b32 s12, s12, s3\n\t" \
    "s_cbranch_scc0 .Lb22_%=\n\t" \
    "s_add_u32 s40, s40, 1\n\t" \
    "s_cmp_lt_u32 s40, 1\n\t" \
    "s_cbranch_scc1 .Lr1023_%=\n\t" \
    "s_branch .Ls22_%=\n\t" \
    ".Lpb_%=:\n\t" \
    "s_sub_u32 s37, s24, 4\n\t" \
    "s_cbranch_scc1 .Lpe_%=\n\t" \
    "s_cmp_lg_u32 s2, 255\n\t" \
    "s_cbranch_scc1 .Lpe_%=\n\t" \
    "s_mul_i32 s38, s37, 0x2100\n\t" \
    "s_and_b32 s38, s38, 0x2300\n\t" \
    "s_add_u32 s38, s38, 34652160\n\t" \
    "s_mov_b32 s60, -1\n\t" \
    "s_mov_b32 s61, -1\n\t" \
    "s_memrealtime s[62:63]\n\t" \
    "s_mov_b64 exec, 0xff\n\t" \
    ".Lpc1_%=:\n\t" \
    "s_memtime s[56:57]\n\t" \
    "s_waitcnt lgkmcnt(0)\n\t" \
    "buffer_store_dwordx4 v[20:23], v0, s[4:7], s38 offen sc1\n\t" \
    "s_waitcnt vmcnt(0)\n\t" \
    "s_memtime s[58:59]\n\t" \
    "s_memrealtime s[22:23]\n\t" \
    "s_waitcnt lgkmcnt(0)\n\t" \
    "s_sub_u32 s56, s58, s56\n\t" \
    "s_min_u32 s56, s56, s60\n\t" \
    "s_mov_b32 s60, s61\n\t" \
    "s_mov_b32 s61, s56\n\t" \
    "s_xor_b32 s38, s38, 0x2000\n\t" \
    "s_add_u32 s38, s38, 0x400\n\t" \
    "s_andn2_b32 s38, s38, 0x1000\n\t" \
    "s_sub_u32 s22, s22, s62\n\t" \
    "s_cmp_lt_u32 s22, 60\n\t" \
    "s_cbranch_scc1 .Lpc1_%=\n\t" \
    "s_mov_b64 exec, -1\n\t" \
    "s_bitcmp1_b32 s38, 13\n\t" \
    "s_cselect_b32 s56, s61, s60\n\t" \
    "s_cselect_b32 s57, s60, s61\n\t" \
    "v_mov_b32 v20, s56\n\t" \
    "v_mov_b32 v21, s57\n\t" \
    "s_lshl3_add_u32 s37, s37, 156992\n\t" \
    "v_mov_b32 v22, s37\n\t" \
    "ds_write_b64 v22, v[20:21]\n\t" \
    "s_branch .Lpe_%=\n\t" \
    ".Lqa_%=:\n\t" \
    "s_sub_u32 s37, s24, 4\n\t" \
    "s_bfe_u32 s38, s2, 0x20002\n\t" \
    "s_lshl1_add_u32 s38, s38, s37\n\t" \
    "s_lshl_b32 s42, s38, 21\n\t" \
    "s_cmp_eq_u32 s2, 255\n\t" \
    "s_cbranch_scc1 .Lqd_%=\n\t" \
    "s_cmp_ge_u32 s37, 2\n\t" \
    "s_cbranch_scc1 .Lqd_%=\n\t" \
    "s_waitcnt lgkmcnt(0)\n\t" \
    "s_sleep 4\n\t" \
    "s_lshl_b32 s3, s38, 10\n\t" \
    "s_and_b32 s37, s8, 0x1fffff\n\t" \
    "s_add_u32 s38, s37, 16383\n\t" \
    "s_andn2_b32 s38, s38, 16383\n\t" \
    "s_add_u32 s39, s37, 9617408\n\t" \
    "s_andn2_b32 s39, s39, 16383\n\t" \
    "s_max_u32 s42, s42, s38\n\t" \
    "s_min_u32 s42, s42, s39\n\t" \
    "s_sub_u32 s42, s42, s37\n\t" \
    "s_add_u32 s16, s8, s42\n\t" \
    "s_addc_u32 s17, s9, 0\n\t" \
    "s_mov_b32 s18, 16384\n\t" \
    "s_mov_b32 s19, 0x20000\n\t" \
    "s_add_u32 s37, s3, 0x2000\n\t" \
    "buffer_store_dwordx4 v[20:23], v0, s[16:19], s3 offen sc1\n\t" \
    "buffer_store_dwordx4 v[20:23], v0, s[16:19], s37 offen sc1\n\t" \
    "s_nop 0\n\t" \
    "s_mov_b32 s39, 34635776\n\t" \
    "v_mov_b32 v20, 0\n\t" \
    "s_buffer_load_dword s0, s[16:19], s3 offset:0x380\n\t" \
    "buffer_load_dword v21, v20, s[4:7], s39 offen sc1\n\t" \
    "s_mov_b32 s60, -1\n\t" \
    "s_mov_b32 s61, -1\n\t" \
    "s_mov_b32 s40, 0\n\t" \
    "s_waitcnt vmcnt(1) lgkmcnt(0)\n\t" \
    "s_memtime s[58:59]\n\t" \
    "s_waitcnt lgkmcnt(0)\n\t" \
    ".Lqc_%=:\n\t" \
    "s_buffer_load_dword s38, s[16:19], s3\n\t" \
    "s_mov_b32 s56, s58\n\t" \
    "s_xor_b32 s3, s3, 0x2000\n\t" \
    "s_add_u32 s3, s3, 0x80\n\t" \
    "s_waitcnt lgkmcnt(0)\n\t" \
    "s_memtime s[58:59]\n\t" \
    "s_add_u32 s40, s40, 1\n\t" \
    "s_waitcnt lgkmcnt(0)\n\t" \
    "s_sub_u32 s56, s58, s56\n\t" \
    "s_min_u32 s56, s56, s60\n\t" \
    "s_mov_b32 s60, s61\n\t" \
    "s_mov_b32 s61, s56\n\t" \
    "s_cmp_lt_u32 s40, 6\n\t" \
    "s_cbranch_scc1 .Lqc_%=\n\t" \
    "s_waitcnt vmcnt(0)\n\t" \
    "v_readfirstlane_b32 s34, v21\n\t" \
    "s_cmp_gt_u32 s60, s61\n\t" \
    "s_cselect_b32 s0, 2, 0\n\t" \
    "s_mul_i32 s34, s34, 1664525\n\t" \
    "s_add_u32 s34, s34, 1013904223\n\t" \
    "s_or_b32 s34, s34, 1\n\t" \
    "s_xor_b32 s37, s0, s34\n\t" \
    "v_mov_b32 v21, s37\n\t" \
    "s_bfe_u32 s37, s2, 0x20002\n\t" \
    "s_lshl1_add_u32 s37, s37, s24\n\t" \
    "s_lshl_b32 s37, s37, 7\n\t" \
    "s_add_u32 s37, s37, 34637312\n\t" \
    "v_lshrrev_b32 v20, 2, v0\n\t" \
    "s_mov_b32 exec_hi, 0\n\t" \
    "buffer_store_dword v21, v20, s[4:7], s37 offen sc1\n\t" \
    "s_mov_b64 exec, -1\n\t" \
    "s_branch .Lqd_%=\n\t" \
    ".Lr2_%=:\n\t" \
    "s_add_u32 s40, s40, 1\n\t" \
    "s_cmp_gt_u32 s40, 32\n\t" \
    "s_cbranch_scc1 .Lr3_%=\n\t" \
    "s_sleep 2\n\t" \
    "s_and_b32 s37, s2, 7\n\t" \
    "s_lshl_b32 s37, s37, 10\n\t" \
    "s_add_u32 s37, s37, 34643968\n\t" \
    "v_and_b32 v51, 0x70, v0\n\t" \
    "v_lshlrev_b32 v51, 3, v51\n\t" \
    "buffer_load_dword v50, v51, s[4:7], s37 offen sc1\n\t" \
    "s_waitcnt vmcnt(0)\n\t" \
    "v_readlane_b32 s56, v50, 0\n\t" \
    "v_readlane_b32 s57, v50, 1\n\t" \
    "v_readlane_b32 s58, v50, 2\n\t" \
    "v_readlane_b32 s59, v50, 3\n\t" \
    "v_readlane_b32 s60, v50, 4\n\t" \
    "v_readlane_b32 s61, v50, 5\n\t" \
    "v_readlane_b32 s62, v50, 6\n\t" \
    "v_readlane_b32 s63, v50, 7\n\t" \
    "s_branch .Lr1_%=\n\t" \
    ".Lpp_%=:\n\t" \
    "ds_read_b128 v[100:103], v9 offset:64\n\t" \
    "ds_read_b128 v[104:107], v9 offset:80\n\t" \
    "s_getreg_b32 s37, hwreg(HW_REG_XCC_ID, 0, 2)\n\t" \
    "s_waitcnt lgkmcnt(0)\n\t" \
    "v_min3_u32 v100, v100, v102, v104\n\t" \
    "v_min3_u32 v101, v101, v103, v105\n\t" \
    "v_min_u32 v100, v100, v106\n\t" \
    "v_min_u32 v101, v101, v107\n\t" \
    "v_cmp_gt_u32 vcc, v100, v101\n\t" \
    "s_nop 4\n\t" \
    "s_and_b32 s38, vcc_lo, 4\n\t" \
    "s_add_u32 s37, s37, s38\n\t" \
    "s_add_u32 s37, s37, 1\n\t" \
    "s_and_b32 s37, s37, 7\n\t" \
    "s_lshl_b32 s37, s37, 1\n\t" \
    "s_xor_b32 s37, s37, s34\n\t" \
    "v_mov_b32 v100, s37\n\t" \
    "v_mov_b32 v101, s37\n\t" \
    "v_mov_b32 v102, s37\n\t" \
    "v_mov_b32 v103, s37\n\t" \
    "s_mov_b32 s39, 34636800\n\t" \
    "buffer_store_dwordx4 v[100:103], v0, s[4:7], s39 offen sc1\n\t" \
    "s_nop 1\n\t" \
    "s_branch .Lpq_%=\n\t" \
    ".Lq5_%=:\n\t" \
    "s_add_u32 s40, s40, 1\n\t" \
    "s_cmp_gt_u32 s40, 8192\n\t" \
    "s_cbranch_scc1 .Lq6_%=\n\t" \
    "s_sleep 4\n\t" \
    "v_and_b32 v50, 0x70, v0\n\t" \
    "v_lshlrev_b32 v50, 3, v50\n\t" \
    "s_mov_b32 s37, 34637824\n\t" \
    "buffer_load_dword v9, v50, s[4:7], s37 offen sc1\n\t" \
    "s_waitcnt vmcnt(0)\n\t" \
    "v_readlane_b32 s56, v9, 0\n\t" \
    "v_readlane_b32 s57, v9, 1\n\t" \
    "v_readlane_b32 s58, v9, 2\n\t" \
    "v_readlane_b32 s59, v9, 3\n\t" \
    "v_readlane_b32 s60, v9, 4\n\t" \
    "v_readlane_b32 s61, v9, 5\n\t" \
    "s_branch .Lq4_%=\n\t" \
    ".Lr5_%=:\n\t" \
    "s_add_u32 s40, s40, 1\n\t" \
    "s_cmp_gt_u32 s40, 8192\n\t" \
    "s_cbranch_scc1 .Lr6_%=\n\t" \
    "s_sleep 4\n\t" \
    "v_mov_b32 v16, 0\n\t" \
    "buffer_load_dword v8, v16, s[4:7], s52 offen sc1\n\t" \
    "s_waitcnt vmcnt(0)\n\t" \
    "v_readfirstlane_b32 s36, v8\n\t" \
    "s_branch .Lr4_%=\n\t" \
    ".Ls22_%=:\n\t" \
    "s_mov_b32 s40, 0\n\t" \
    "s_add_u32 s38, s33, 786432\n\t" \
    ".Ll22_%=:\n\t" \
    "s_sleep 2\n\t" \
    "buffer_load_dword v51, v3, s[4:7], s38 offen sc1\n\t" \
    "s_waitcnt vmcnt(0)\n\t" \
    "v_cmp_ne_u32 vcc, s34, v51\n\t" \
    "s_add_u32 s40, s40, 1\n\t" \
    "s_cmp_ge_u32 s40, 8192\n\t" \
    "s_cbranch_scc1 .Lb22_%=\n\t" \
    "s_cbranch_vccnz .Ll22_%=\n\t" \
    "s_branch .Lb22_%=\n\t" \
    ".Ls25_%=:\n\t" \
    "s_mov_b32 s40, 0\n\t" \
    "s_add_u32 s38, s55, 34603008\n\t" \
    ".Ll25_%=:\n\t" \
    "s_sleep 2\n\t" \
    "buffer_load_dword v37, v47, s[4:7], s38 offen sc1\n\t" \
    "s_waitcnt vmcnt(0)\n\t" \
    "v_cmp_ne_u32 vcc, s34, v37\n\t" \
    "s_add_u32 s40, s40, 1\n\t" \
    "s_cmp_ge_u32 s40, 8192\n\t" \
    "s_cbranch_scc1 .Lb25_%=\n\t" \
    "s_cbranch_vccnz .Ll25_%=\n\t" \
    "s_branch .Lb25_%=\n\t" \
    ".Ls39_%=:\n\t" \
    "s_mov_b32 s40, 0\n\t" \
    ".Ll39_%=:\n\t" \
    "s_add_u32 s54, s53, 0\n\t" \
    "buffer_load_dwordx4 v[156:159], v0, s[4:7], s54 offen offset:0 sc1\n\t" \
    "s_add_u32 s54, s53, 4243456\n\t" \
    "buffer_load_dwordx4 v[28:31], v0, s[4:7], s54 offen offset:0 sc1\n\t" \
    "s_waitcnt vmcnt(0)\n\t" \
    "v_and_b32 v3, v156, v158\n\t" \
    "v_bitop3_b32 v3, v3, v28, v30 bitop3:0x80\n\t" \
    "v_and_b32 v3, 1, v3\n\t" \
    "v_cmp_eq_u32 vcc, 0, v3\n\t" \
    "s_add_u32 s40, s40, 1\n\t" \
    "s_cmp_ge_u32 s40, 8192\n\t" \
    "s_cbranch_scc1 .Lb39_%=\n\t" \
    "s_cbranch_vccnz .Ll39_%=\n\t" \
    "s_branch .Lb39_%=\n\t" \
    ".Ls40_%=:\n\t" \
    "s_mov_b32 s40, 0\n\t" \
    ".Ll40_%=:\n\t" \
    "s_add_u32 s54, s53, 524288\n\t" \
    "buffer_load_dwordx4 v[236:239], v0, s[4:7], s54 offen offset:0 sc1\n\t" \
    "s_add_u32 s54, s53, 4767744\n\t" \
    "buffer_load_dwordx4 v[244:247], v0, s[4:7], s54 offen offset:0 sc1\n\t" \
    "s_waitcnt vmcnt(0)\n\t" \
    "v_and_b32 v51, v236, v238\n\t" \
    "v_bitop3_b32 v51, v51, v244, v246 bitop3:0x80\n\t" \
    "v_and_b32 v51, 1, v51\n\t" \
    "v_cmp_eq_u32 vcc, 0, v51\n\t" \
    "s_add_u32 s40, s40, 1\n\t" \
    "s_cmp_ge_u32 s40, 8192\n\t" \
    "s_cbranch_scc1 .Lb40_%=\n\t" \
    "s_cbranch_vccnz .Ll40_%=\n\t" \
    "s_branch .Lb40_%=\n\t" \
    ".Ls41_%=:\n\t" \
    "s_mov_b32 s40, 0\n\t" \
    ".Ll41_%=:\n\t" \
    "s_add_u32 s54, s53, 1048576\n\t" \
    "buffer_load_dwordx4 v[140:143], v0, s[4:7], s54 offen offset:0 sc1\n\t" \
    "s_add_u32 s54, s53, 5292032\n\t" \
    "buffer_load_dwordx4 v[180:183], v0, s[4:7], s54 offen offset:0 sc1\n\t" \
    "s_waitcnt vmcnt(0)\n\t" \
    "v_and_b32 v39, v140, v142\n\t" \
    "v_bitop3_b32 v39, v39, v180, v182 bitop3:0x80\n\t" \
    "v_and_b32 v39, 1, v39\n\t" \
    "v_cmp_eq_u32 vcc, 0, v39\n\t" \
    "s_add_u32 s40, s40, 1\n\t" \
    "s_cmp_ge_u32 s40, 8192\n\t" \
    "s_cbranch_scc1 .Lb41_%=\n\t" \
    "s_cbranch_vccnz .Ll41_%=\n\t" \
    "s_branch .Lb41_%=\n\t" \
    ".Lfin_%=:\n\t"
#define K_CLOBBERS "v1", "v2", "v3", "v4", "v5", "v6", "v7", "v8", "v9", "v10", "v11", "v12", "v13", "v14", "v15", "v16", "v17", "v18", "v19", "v20", "v21", "v22", "v23", "v24", "v25", "v26", "v27", "v28", "v29", "v30", "v31", "v32", "v33", "v34", "v35", "v36", "v37", "v38", "v39", "v40", "v41", "v42", "v43", "v44", "v45", "v46", "v47", "v48", "v49", "v50", "v51", "v52", "v53", "v54", "v55", "v56", "v57", "v58", "v59", "v60", "v61", "v62", "v63", "v64", "v65", "v66", "v67", "v68", "v69", "v70", "v71", "v72", "v73", "v74", "v75", "v76", "v77", "v78", "v79", "v80", "v81", "v82", "v83", "v84", "v85", "v86", "v87", "v88", "v89", "v90", "v91", "v92", "v93", "v94", "v95", "v96", "v97", "v98", "v99", "v100", "v101", "v102", "v103", "v104", "v105", "v106", "v107", "v108", "v109", "v110", "v111", "v112", "v113", "v114", "v115", "v116", "v117", "v118", "v119", "v120", "v121", "v122", "v123", "v124", "v125", "v126", "v127", "v128", "v129", "v130", "v131", "v132", "v133", "v134", "v135", "v136", "v137", "v138", "v139", "v140", "v141", "v142", "v143", "v144", "v145", "v146", "v147", "v148", "v149", "v150", "v151", "v152", "v153", "v154", "v155", "v156", "v157", "v158", "v159", "v160", "v161", "v162", "v163", "v164", "v165", "v166", "v167", "v168", "v169", "v170", "v171", "v172", "v173", "v174", "v175", "v176", "v177", "v178", "v179", "v180", "v181", "v182", "v183", "v184", "v185", "v186", "v187", "v188", "v189", "v190", "v191", "v192", "v193", "v194", "v195", "v196", "v197", "v198", "v199", "v200", "v201", "v202", "v203", "v204", "v205", "v206", "v207", "v208", "v209", "v210", "v211", "v212", "v213", "v214", "v215", "v216", "v217", "v218", "v219", "v220", "v221", "v222", "v223", "v224", "v225", "v226", "v227", "v228", "v229", "v230", "v231", "v232", "v233", "v234", "v235", "v236", "v237", "v238", "v239", "v240", "v241", "v242", "v243", "v244", "v245", "v246", "v247", "v248", "v249", "v250", "v251", "v252", "v253", "v254", "v255", "s3", "s4", "s5", "s6", "s7", "s8", "s9", "s10", "s11", "s12", "s13", "s14", "s15", "s16", "s17", "s18", "s19", "s20", "s21", "s22", "s23", "s24", "s25", "s26", "s27", "s28", "s29", "s30", "s31", "s32", "s33", "s34", "s35", "s36", "s37", "s38", "s39", "s40", "s41", "s42", "s43", "s44", "s45", "s46", "s47", "s48", "s49", "s50", "s51", "s52", "s53", "s54", "s55", "s56", "s57", "s58", "s59", "s60", "s61", "s62", "s63", "s64", "s65"

extern "C" __global__ __launch_bounds__(512, 2)
void k_conv(const char* __restrict__ x, const char* __restrict__ wgt, char* __restrict__ ws, char* __restrict__ out)
{
  __shared__ __attribute__((aligned(256))) char smem[157568];
  unsigned tid = threadIdx.x;
  auto kp = __builtin_amdgcn_kernarg_segment_ptr();
  asm volatile(K_BODY : "+{v0}"(tid), "+{s[0:1]}"(kp)
               : "{s2}"(blockIdx.x), "i"((__attribute__((address_space(3))) char*)smem)
               : "memory", "scc", "vcc", K_CLOBBERS);
}

extern "C" void kernel_launch(void* const* d_in, const int* in_sizes, int n_in,
                              void* d_out, int out_size, void* d_ws, size_t ws_size,
                              hipStream_t stream) {
  k_conv<<<dim3(256), dim3(512), 0, stream>>>((const char*)d_in[0], (const char*)d_in[1], (char*)d_ws + WS_OFF, (char*)d_out);
}
```

```cpp
#include <hip/hip_runtime.h>
#include <hip/hip_bf16.h>

#define WS_OFF 0
#define K_BODY \
    "s_load_dwordx2 s[16:17], s[0:1], 0x0\n\t" \
    "s_load_dwordx2 s[4:5], s[0:1], 0x10\n\t" \
    "s_load_dwordx2 s[12:13], s[0:1], 0x8\n\t" \
    "s_load_dwordx2 s[8:9], s[0:1], 0x18\n\t" \
    "s_getpc_b64 s[22:23]\n\t" \
    ".Lpc_%=:\n\t" \
    "s_cmp_ge_u32 s2, 16\n\t" \
    "s_cbranch_scc1 .Le1_%=\n\t" \
    "s_mov_b32 m0, 157312\n\t" \
    "v_lshlrev_b32 v8, 6, v0\n\t" \
    "v_min_u32 v8, .Lfin_%=-.Lpc_%=, v8\n\t" \
    "global_load_lds_dword v8, s[22:23]\n\t" \
    ".Le1_%=:\n\t" \
    "v_readfirstlane_b32 s24, v0\n\t" \
    "s_and_b32 s25, s2, 3\n\t" \
    "s_lshl_b32 s25, s25, 5\n\t" \
    "s_lshr_b32 s37, s2, 3\n\t" \
    "s_add_u32 s25, s25, s37\n\t" \
    "s_min_u32 s28, s25, 125\n\t" \
    "s_mul_i32 s26, s28, 57\n\t" \
    "s_lshr_b32 s26, s26, 9\n\t" \
    "s_mul_i32 s37, s26, 9\n\t" \
    "s_sub_u32 s27, s28, s37\n\t" \
    "s_bfe_u32 s37, s2, 0x10002\n\t" \
    "s_lshl1_add_u32 s26, s26, s37\n\t" \
    "v_and_b32 v0, 63, v0\n\t" \
    "v_lshlrev_b32 v0, 4, v0\n\t" \
    "s_lshr_b32 s24, s24, 6\n\t" \
    "s_sub_u32 s37, s24, 4\n\t" \
    "s_cbranch_scc1 .Le2_%=\n\t" \
    "s_cmp_ge_u32 s25, 126\n\t" \
    "s_cbranch_scc1 .Le2_%=\n\t" \
    "s_mul_i32 m0, s37, 12416\n\t" \
    "s_lshl2_add_u32 s37, s26, s37\n\t" \
    "s_mul_i32 s37, s37, 86016\n\t" \
    "s_mul_i32 s38, s27, 4992\n\t" \
    "s_add_u32 s37, s37, s38\n\t" \
    "s_addk_i32 s37, 896\n\t" \
    "v_add_u32 v9, s37, v0\n\t" \
    "s_cmp_eq_u32 s27, 0\n\t" \
    "s_cselect_b32 s38, 43008, 0\n\t" \
    "s_sub_u32 s39, s38, 2048\n\t" \
    "v_add_u32 v7, s39, v9\n\t" \
    "v_subrev_u32 v8, 0x400, v9\n\t" \
    "s_mov_b64 exec, 0xff\n\t" \
    "v_add_u32 v8, s38, v8\n\t" \
    "s_mov_b64 exec, -1\n\t" \
    "s_mov_b32 s18, 9633792\n\t" \
    "s_mov_b32 s19, 0x20000\n\t" \
    "s_mov_b32 s39, 43008\n\t" \
    "s_waitcnt lgkmcnt(0)\n\t" \
    "buffer_load_dwordx4 v7, s[16:19], 0 offen offset:0 sc1 nt lds\n\t" \
    "s_add_u32 m0, m0, 2080\n\t" \
    "s_nop 0\n\t" \
    "buffer_load_dwordx4 v9, s[16:19], 0 offen offset:3072 sc1 nt lds\n\t" \
    "buffer_load_dwordx4 v9, s[16:19], 0 offen offset:2048 sc1 nt lds\n\t" \
    "s_add_u32 m0, m0, 4128\n\t" \
    "s_nop 0\n\t" \
    "buffer_load_dwordx4 v7, s[16:19], s39 offen offset:0 sc1 nt lds\n\t" \
    "s_add_u32 m0, m0, 2080\n\t" \
    "s_nop 0\n\t" \
    "buffer_load_dwordx4 v9, s[16:19], s39 offen offset:3072 sc1 nt lds\n\t" \
    "buffer_load_dwordx4 v9, s[16:19], s39 offen offset:2048 sc1 nt lds\n\t" \
    "buffer_load_dwordx4 v9, s[16:19], s39 offen offset:1024 sc1 nt lds\n\t" \
    "s_add_u32 m0, m0, 4294966240\n\t" \
    "s_nop 0\n\t" \
    "buffer_load_dwordx4 v8, s[16:19], s39 offen offset:0 sc1 nt lds\n\t" \
    "s_add_u32 m0, m0, 1024\n\t" \
    "s_nop 0\n\t" \
    "buffer_load_dwordx4 v9, s[16:19], s39 offen offset:0 sc1 nt lds\n\t" \
    "s_add_u32 m0, m0, 4294960064\n\t" \
    "s_nop 0\n\t" \
    "buffer_load_dwordx4 v8, s[16:19], 0 offen offset:0 sc1 nt lds\n\t" \
    "s_add_u32 m0, m0, 1024\n\t" \
    "s_nop 0\n\t" \
    "buffer_load_dwordx4 v9, s[16:19], 0 offen offset:0 sc1 nt lds\n\t" \
    "s_add_u32 m0, m0, 32\n\t" \
    "s_nop 0\n\t" \
    "buffer_load_dwordx4 v9, s[16:19], 0 offen offset:1024 sc1 nt lds\n\t" \
    ".Le2_%=:\n\t" \
    "s_mov_b32 s6, 34701312\n\t" \
    "s_mov_b32 s7, 0x20000\n\t" \
    "v_lshrrev_b32 v8, 7, v0\n\t" \
    "v_and_b32 v9, 0x70, v0\n\t" \
    "v_lshrrev_b32 v2, 8, v0\n\t" \
    "v_lshl_or_b32 v2, v2, 16, v9\n\t" \
    "v_and_b32 v10, 0x80, v0\n\t" \
    "v_lshl_or_b32 v2, v10, 6, v2\n\t" \
    "s_lshl_b32 s32, s2, 7\n\t" \
    "s_andn2_b32 s37, s32, 0x1fff\n\t" \
    "s_add_u32 s32, s32, s37\n\t" \
    "s_add_u32 s32, s32, 33554432\n\t" \
    "s_cmp_ge_u32 s24, 4\n\t" \
    "s_cbranch_scc1 .Le3_%=\n\t" \
    "v_mov_b32 v12, 0\n\t" \
    "v_mov_b32 v13, 0\n\t" \
    "v_mov_b32 v14, 0\n\t" \
    "v_mov_b32 v15, 0\n\t" \
    "s_lshl_b32 s37, s24, 18\n\t" \
    "s_add_u32 s37, s37, s32\n\t" \
    "s_xor_b32 s38, s24, 2\n\t" \
    "s_lshr_b32 s39, s2, 3\n\t" \
    "s_or_b32 s38, s38, s39\n\t" \
    "s_memrealtime s[22:23]\n\t" \
    "s_waitcnt lgkmcnt(0)\n\t" \
    "s_cbranch_scc1 .Le4_%=\n\t" \
    "v_lshl_or_b32 v8, v8, 10, v9\n\t" \
    "v_mov_b32 v16, s22\n\t" \
    "v_mov_b32 v17, s22\n\t" \
    "v_mov_b32 v18, s22\n\t" \
    "v_mov_b32 v19, s22\n\t" \
    "s_add_u32 s38, s32, 1122304\n\t" \
    "buffer_store_dwordx4 v[16:19], v8, s[4:7], s38 offen sc1\n\t" \
    ".Le4_%=:\n\t" \
    "\n\t" \
    ".Le3_%=:\n\t" \
    "s_bitcmp1_b32 s2, 2\n\t" \
    "s_cselect_b32 s38, 126, 0\n\t" \
    "s_add_u32 s28, s28, s38\n\t" \
    "s_mul_i32 s30, s28, 293\n\t" \
    "s_lshr_b32 s30, s30, 11\n\t" \
    "s_mul_i32 s37, s30, 7\n\t" \
    "s_sub_u32 s28, s28, s37\n\t" \
    "s_mul_i32 s29, s30, 43\n\t" \
    "s_lshr_b32 s29, s29, 7\n\t" \
    "s_mul_i32 s37, s29, 3\n\t" \
    "s_sub_u32 s30, s30, s37\n\t" \
    "s_and_b32 s37, s29, 12\n\t" \
    "s_add_u32 s37, s37, s29\n\t" \
    "s_mul_i32 s31, s37, 768\n\t" \
    "s_lshl_b32 s37, s30, 6\n\t" \
    "s_add_u32 s31, s31, s37\n\t" \
    "s_add_u32 s41, s29, s30\n\t" \
    "s_add_u32 s41, s41, s28\n\t" \
    "s_and_b32 s41, s41, 3\n\t" \
    "s_cmp_lt_u32 s25, 126\n\t" \
    "s_cselect_b32 s14, 903168, 0\n\t" \
    "s_mov_b32 s15, 0x20000\n\t" \
    "v_lshrrev_b32 v10, 6, v0\n\t" \
    "v_lshrrev_b32 v11, 9, v0\n\t" \
    "v_sub_u32 v8, v10, v11\n\t" \
    "v_mul_u32_u24 v8, 18432, v8\n\t" \
    "v_and_b32 v9, 0x30, v0\n\t" \
    "v_lshlrev_b32 v11, 5, v11\n\t" \
    "v_bitop3_b32 v1, v8, v9, v11 bitop3:0xf6\n\t" \
    "s_mov_b32 exec_lo, 0xf0000000\n\t" \
    "s_mov_b32 exec_hi, 0xf0000000\n\t" \
    "v_bfrev_b32 v1, 1\n\t" \
    "s_mov_b64 exec, -1\n\t" \
    "s_cmp_ge_u32 s24, 4\n\t" \
    "s_cbranch_scc1 .Le9_%=\n\t" \
    "v_bfe_u32 v20, v0, 4, 4\n\t" \
    "v_and_b32 v10, 12, v10\n\t" \
    "v_cvt_f32_ubyte0 v21, v10\n\t" \
    "v_and_b32 v22, 1, v20\n\t" \
    "v_cvt_f32_ubyte0 v22, v22\n\t" \
    "v_mul_f32 v22, 0x3e800000, v22\n\t" \
    "v_lshrrev_b32 v23, 1, v20\n\t" \
    "v_mov_b32 v32, 0\n\t" \
    "v_mov_b32 v33, -1\n\t" \
    "v_mov_b32 v31, 0x3e800000\n\t" \
    "s_mov_b32 s65, 0\n\t" \
    "s_mov_b32 s40, 0\n\t" \
    "s_cmp_eq_u32 s24, 1\n\t" \
    "s_cbranch_scc1 .Lm1_%=\n\t" \
    "s_cmp_eq_u32 s24, 2\n\t" \
    "s_cbranch_scc1 .Lm2_%=\n\t" \
    "s_cmp_eq_u32 s24, 3\n\t" \
    "s_cbranch_scc1 .Lm3_%=\n\t" \
    "s_add_u32 s37, s32, 1048576\n\t" \
    "s_mov_b64 exec, 0xff\n\t" \
    "\n\t" \
    "s_mov_b64 exec, -1\n\t" \
    "v_or_b32 v24, 1, v20\n\t" \
    "v_cvt_f32_ubyte0 v24, v24\n\t" \
    "v_mul_f32 v24, 0x3c800000, v24\n\t" \
    "v_lshrrev_b32 v25, 4, v0\n\t" \
    "v_and_b32 v25, 16, v25\n\t" \
    "v_lshrrev_b32 v26, 2, v11\n\t" \
    "v_or_b32 v25, v25, v26\n\t" \
    "v_cvt_f32_ubyte0 v25, v25\n\t" \
    "v_add_f32 v26, 2.0, v25\n\t" \
    "v_add_f32 v27, 4.0, v25\n\t" \
    "v_add_f32 v28, 4.0, v26\n\t" \
    "v_mov_b32 v29, v22\n\t" \
    "v_mov_b32 v30, v22\n\t" \
    "v_mov_b32 v31, v24\n\t" \
    "s_mov_b32 s64, 0x3e800000\n\t" \
    "s_mov_b32 s65, 0x3e800000\n\t" \
    "v_add_u32 v34, 154880, v0\n\t" \
    "s_branch .Ltl_%=\n\t" \
    ".Lm1_%=:\n\t" \
    "v_lshrrev_b32 v24, 5, v0\n\t" \
    "v_and_b32 v24, 12, v24\n\t" \
    "v_and_or_b32 v24, v20, 3, v24\n\t" \
    "v_cvt_f32_ubyte0 v24, v24\n\t" \
    "v_mul_f32 v24, 0x3d924925, v24\n\t" \
    "v_mul_f32 v32, 0x41000000, v24\n\t" \
    "v_lshrrev_b32 v27, 3, v11\n\t" \
    "v_cvt_f32_ubyte0 v27, v27\n\t" \
    "v_add_f32 v25, -2.0, v27\n\t" \
    "v_add_f32 v26, -1.0, v27\n\t" \
    "v_add_f32 v28, 1.0, v27\n\t" \
    "v_and_b32 v29, 4, v20\n\t" \
    "v_cvt_f32_ubyte0 v29, v29\n\t" \
    "v_mul_f32 v29, 0x3d800000, v29\n\t" \
    "v_mov_b32 v30, v29\n\t" \
    "v_mov_b32 v31, 0xbe800000\n\t" \
    "s_mov_b32 s64, 0\n\t" \
    "s_mov_b32 exec_hi, 0\n\t" \
    "v_mov_b32 v33, 0\n\t" \
    "s_mov_b64 exec, -1\n\t" \
    "v_add_u32 v34, 149760, v0\n\t" \
    "s_branch .Ltl_%=\n\t" \
    ".Lm2_%=:\n\t" \
    "v_add_u32 v24, 6, v20\n\t" \
    "v_cvt_f32_ubyte0 v24, v24\n\t" \
    "v_mul_f32 v24, 0x3d000000, v24\n\t" \
    "v_add_f32 v25, 0.5, v21\n\t" \
    "v_add_f32 v26, 1.0, v25\n\t" \
    "v_add_f32 v27, 2.0, v25\n\t" \
    "v_add_f32 v28, 2.0, v26\n\t" \
    "v_mov_b32 v29, 0\n\t" \
    "v_mov_b32 v30, 0\n\t" \
    "s_mov_b32 s64, 0.5\n\t" \
    "v_add_u32 v34, 152832, v0\n\t" \
    "s_branch .Ltl_%=\n\t" \
    ".Lm3_%=:\n\t" \
    "s_mov_b32 s37, 34668544\n\t" \
    "buffer_load_dword v3, v12, s[4:7], s37 offen sc1\n\t" \
    "v_lshrrev_b32 v8, 2, v0\n\t" \
    "v_and_b32 v8, 28, v8\n\t" \
    "v_add_u32 v8, 156928, v8\n\t" \
    "ds_write_b32 v8, v12\n\t" \
    "v_add_u32 v24, 6, v23\n\t" \
    "v_cvt_f32_ubyte0 v24, v24\n\t" \
    "v_mul_f32 v24, 0x3d924925, v24\n\t" \
    "v_mov_b32 v25, v21\n\t" \
    "v_add_f32 v26, 1.0, v21\n\t" \
    "v_add_f32 v27, 2.0, v21\n\t" \
    "v_add_f32 v28, 2.0, v26\n\t" \
    "v_sub_f32 v29, 0, v22\n\t" \
    "v_sub_f32 v30, 0, v22\n\t" \
    "s_mov_b32 exec_lo, 0\n\t" \
    "v_fma_f32 v25, v21, 0.5, 4.0\n\t" \
    "v_add_f32 v27, 1.0, v25\n\t" \
    "v_fmaak_f32 v26, v21, v31, 0x41200000\n\t" \
    "v_mov_b32 v28, v26\n\t" \
    "v_add_f32 v30, v31, v30\n\t" \
    "s_mov_b64 exec, -1\n\t" \
    "s_mov_b32 s40, 0x11\n\t" \
    "v_add_u32 v34, 151808, v0\n\t" \
    ".Ltl_%=:\n\t" \
    "v_fma_f32 v36, v24, v25, v29\n\t" \
    "v_fma_f32 v38, v24, v26, v29\n\t" \
    "v_fma_f32 v40, v24, v27, v29\n\t" \
    "v_fma_f32 v42, v24, v28, v30\n\t" \
    "v_add_f32 v37, v36, v31\n\t" \
    "v_add_f32 v39, v38, v31\n\t" \
    "v_add_f32 v41, v40, v31\n\t" \
    "v_add_f32 v43, v42, v31\n\t" \
    "v_cos_f32 v36, v36\n\t" \
    "v_cos_f32 v37, v37\n\t" \
    "v_cos_f32 v38, v38\n\t" \
    "v_cos_f32 v39, v39\n\t" \
    "v_cos_f32 v40, v40\n\t" \
    "v_cos_f32 v41, v41\n\t" \
    "v_cos_f32 v42, v42\n\t" \
    "v_cos_f32 v43, v43\n\t" \
    "s_bitcmp0_b32 s40, 4\n\t" \
    "s_cbranch_scc1 .Lt2_%=\n\t" \
    "v_mul_f32 v36, 0x3b924925, v36\n\t" \
    "v_mul_f32 v37, 0x3b924925, v37\n\t" \
    "v_mul_f32 v38, 0x3b924925, v38\n\t" \
    "v_mul_f32 v39, 0x3b924925, v39\n\t" \
    "v_mul_f32 v40, 0x3b924925, v40\n\t" \
    "v_mul_f32 v41, 0x3b924925, v41\n\t" \
    "v_mul_f32 v42, 0x3b924925, v42\n\t" \
    "v_mul_f32 v43, 0x3b924925, v43\n\t" \
    ".Lt2_%=:\n\t" \
    "v_cvt_pk_bf16_f32 v44, v36, v37\n\t" \
    "v_cvt_pk_bf16_f32 v45, v38, v39\n\t" \
    "v_cvt_pk_bf16_f32 v46, v40, v41\n\t" \
    "v_cvt_pk_bf16_f32 v47, v42, v43\n\t" \
    "v_and_b32 v44, v33, v44\n\t" \
    "v_and_b32 v45, v33, v45\n\t" \
    "v_mov_b32 v33, -1\n\t" \
    "v_add_f32 v24, s64, v24\n\t" \
    "v_add_f32 v31, s65, v31\n\t" \
    "v_add_f32 v29, v32, v29\n\t" \
    "v_add_f32 v30, v32, v30\n\t" \
    "ds_write_b128 v34, v[44:47]\n\t" \
    "s_add_u32 s40, s40, 1\n\t" \
    "s_bitcmp1_b32 s40, 0\n\t" \
    "v_add_u32 v34, 0x400, v34\n\t" \
    "s_cbranch_scc1 .Ltl_%=\n\t" \
    "s_bitcmp1_b32 s40, 2\n\t" \
    "s_cbranch_scc1 .Lt4_%=\n\t" \
    "s_mov_b32 s40, 2\n\t" \
    "s_lshl2_add_u32 s37, s28, s24\n\t" \
    "s_lshl_b32 s37, s37, 2\n\t" \
    "v_lshrrev_b32 v25, 2, v20\n\t" \
    "v_add_u32 v25, s37, v25\n\t" \
    "v_mul_u32_u24 v26, 147, v25\n\t" \
    "v_lshrrev_b32 v26, 10, v26\n\t" \
    "v_lshl_or_b32 v24, v26, 1, 1\n\t" \
    "v_cvt_f32_ubyte0 v24, v24\n\t" \
    "v_mul_f32 v24, 0x3c800000, v24\n\t" \
    "v_mul_u32_u24 v26, 7, v26\n\t" \
    "v_sub_u32 v25, v25, v26\n\t" \
    "v_xor_b32 v22, s41, v20\n\t" \
    "v_bfe_u32 v32, v22, 1, 1\n\t" \
    "v_and_b32 v22, 1, v22\n\t" \
    "v_cvt_f32_ubyte0 v22, v22\n\t" \
    "v_mul_f32 v22, 0x3e800000, v22\n\t" \
    "v_lshl_or_b32 v32, v25, 1, v32\n\t" \
    "v_cvt_f32_ubyte0 v32, v32\n\t" \
    "v_mul_f32 v32, 0x3e924925, v32\n\t" \
    "v_mul_f32 v29, 0x3d800000, v21\n\t" \
    "v_fma_f32 v29, v29, v32, v22\n\t" \
    "v_mov_b32 v30, v29\n\t" \
    "v_mov_b32 v31, v24\n\t" \
    "v_mov_b32 v25, 0\n\t" \
    "v_mov_b32 v26, 2.0\n\t" \
    "v_mov_b32 v27, 4.0\n\t" \
    "v_mov_b32 v28, 0x40c00000\n\t" \
    "s_mov_b32 s64, 0\n\t" \
    "s_mov_b32 s65, 0\n\t" \
    "s_lshl_b32 s37, s24, 11\n\t" \
    "s_add_u32 s37, s37, 49664\n\t" \
    "v_add_u32 v34, s37, v0\n\t" \
    "s_branch .Ltl_%=\n\t" \
    ".Lt4_%=:\n\t" \
    ".Lt3_%=:\n\t" \
    "s_cmp_lg_u32 s24, 3\n\t" \
    "s_cbranch_scc1 .Lx2_%=\n\t" \
    "v_lshrrev_b32 v8, 7, v0\n\t" \
    "v_mul_u32_u24 v8, 96, v8\n\t" \
    "v_and_b32 v9, 16, v0\n\t" \
    "v_bfe_u32 v10, v0, 5, 2\n\t" \
    "v_add3_u32 v8, v8, v9, v10\n\t" \
    "v_mul_u32_u24 v8, 192, v8\n\t" \
    "s_mul_i32 s37, s28, 129024\n\t" \
    "s_add_u32 s37, s37, s31\n\t" \
    "s_mov_b32 m0, 157312\n\t" \
    "s_mov_b32 exec_hi, 0xffffff\n\t" \
    "buffer_load_dword v8, s[12:15], s37 offen lds\n\t" \
    "s_mov_b64 exec, -1\n\t" \
    ".Lx2_%=:\n\t" \
    ".Le9_%=:\n\t" \
    "s_lshr_b32 s37, s24, 2\n\t" \
    "s_and_b32 s38, s24, 2\n\t" \
    "s_mul_i32 s39, s37, 24832\n\t" \
    "s_lshl4_add_u32 s39, s38, s39\n\t" \
    "v_lshrrev_b32 v4, 6, v0\n\t" \
    "v_and_b32 v5, 12, v4\n\t" \
    "v_add_u32 v6, v4, v5\n\t" \
    "v_lshrrev_b32 v7, 1, v0\n\t" \
    "v_and_b32 v7, 24, v7\n\t" \
    "v_and_b32 v4, 11, v4\n\t" \
    "v_mul_u32_u24 v4, 192, v4\n\t" \
    "v_bfe_u32 v5, v0, 8, 1\n\t" \
    "v_mul_u32_u24 v5, 3104, v5\n\t" \
    "v_add3_u32 v4, v4, v5, v7\n\t" \
    "v_add_u32 v8, s39, v4\n\t" \
    "v_mov_b32 v9, 156928\n\t" \
    "v_add_u32 v10, 149760, v0\n\t" \
    "s_lshl2_add_u32 s37, s37, s38\n\t" \
    "s_lshl_b32 s37, s37, 7\n\t" \
    "s_mul_i32 s53, s27, 28\n\t" \
    "s_add_u32 s53, s53, s26\n\t" \
    "s_andn2_b32 s39, s53, 7\n\t" \
    "s_add_u32 s53, s53, s39\n\t" \
    "s_lshl_b32 s53, s53, 10\n\t" \
    "s_add_u32 s37, s37, s53\n\t" \
    "s_and_b32 s38, s24, 1\n\t" \
    "s_lshl_b32 s39, s38, 13\n\t" \
    "s_add_u32 s37, s37, s39\n\t" \
    "s_cmp_lt_u32 s25, 126\n\t" \
    "s_cselect_b32 s43, 0, 0x80000000\n\t" \
    "s_add_u32 s37, s37, s43\n\t" \
    "v_lshrrev_b32 v11, 8, v0\n\t" \
    "v_and_b32 v12, 0xf0, v0\n\t" \
    "v_mul_u32_u24 v4, 8486912, v11\n\t" \
    "v_add3_u32 v13, v4, v12, s37\n\t" \
    "s_lshl_b32 s39, s38, 10\n\t" \
    "v_add_u32 v14, s39, v10\n\t" \
    "s_lshl_b32 s39, s38, 5\n\t" \
    "s_add_u32 s39, s39, 116736\n\t" \
    "v_lshl_add_u32 v4, v6, 6, v7\n\t" \
    "v_lshrrev_b32 v5, 3, v0\n\t" \
    "v_and_b32 v5, 32, v5\n\t" \
    "v_xor_b32 v5, s39, v5\n\t" \
    "v_add_u32 v15, v4, v5\n\t" \
    "s_and_b32 s37, s24, 6\n\t" \
    "s_lshl_b32 s37, s37, 10\n\t" \
    "v_add_u32 v16, s37, v0\n\t" \
    "s_cmp_ge_u32 s25, 126\n\t" \
    "s_cbranch_scc1 .Lqa_%=\n\t" \
    ".Lqd_%=:\n\t" \
    "s_waitcnt lgkmcnt(0)\n\t" \
    "s_barrier\n\t" \
    "ds_read_b128 v[24:27], v14 offset:5120\n\t" \
    "ds_read_b128 v[28:31], v16 offset:49664\n\t" \
    "ds_read_b128 v[32:35], v16 offset:50688\n\t" \
    "s_and_b32 s33, s2, 7\n\t" \
    "s_lshl_b32 s33, s33, 15\n\t" \
    "s_add_u32 s33, s33, 33554432\n\t" \
    "s_lshr_b32 s37, s29, 2\n\t" \
    "s_mul_i32 s39, s30, 3\n\t" \
    "s_add_u32 s37, s37, s39\n\t" \
    "s_mul_i32 s37, s37, 114688\n\t" \
    "s_and_b32 s39, s29, 3\n\t" \
    "s_lshl_b32 s39, s39, 8\n\t" \
    "s_add_u32 s37, s37, s39\n\t" \
    "s_add_u32 s37, s37, s43\n\t" \
    "s_add_u32 s37, s37, 2097152\n\t" \
    "s_and_b32 s39, s24, 6\n\t" \
    "s_lshl3_add_u32 s39, s28, s39\n\t" \
    "s_lshl_b32 s39, s39, 1\n\t" \
    "v_add_u32 v14, s39, v11\n\t" \
    "v_mul_u32_u24 v17, 147, v14\n\t" \
    "v_lshrrev_b32 v17, 10, v17\n\t" \
    "s_and_b32 s39, s24, 1\n\t" \
    "v_subrev_u32 v18, s39, v17\n\t" \
    "v_mul_i32_i24 v18, 7, v18\n\t" \
    "v_sub_u32 v14, v14, v18\n\t" \
    "v_and_b32 v18, 14, v14\n\t" \
    "v_add_lshl_u32 v14, v14, v18, 12\n\t" \
    "v_and_b32 v18, 7, v17\n\t" \
    "v_mul_u32_u24 v18, 4243456, v18\n\t" \
    "v_add3_u32 v14, v14, v18, v12\n\t" \
    "v_and_b32 v17, 8, v17\n\t" \
    "v_xor_b32 v17, 8, v17\n\t" \
    "v_lshl_add_u32 v14, v17, 10, v14\n\t" \
    "v_add_u32 v19, s37, v14\n\t" \
    "s_lshl_b32 s43, s41, 10\n\t" \
    "s_and_b32 s37, s2, 0x70\n\t" \
    "s_lshl_b32 s37, s37, 1\n\t" \
    "s_lshl2_add_u32 s57, s24, s37\n\t" \
    "s_min_u32 s58, s57, 248\n\t" \
    "s_mul_i32 s59, s58, 147\n\t" \
    "s_lshr_b32 s59, s59, 12\n\t" \
    "s_mul_i32 s37, s59, 28\n\t" \
    "s_sub_u32 s60, s58, s37\n\t" \
    "s_andn2_b32 s39, s58, 7\n\t" \
    "s_add_u32 s51, s58, s39\n\t" \
    "s_lshl_b32 s51, s51, 10\n\t" \
    "s_sub_u32 s37, s60, 1\n\t" \
    "s_cselect_b32 s52, 27, -1\n\t" \
    "s_add_u32 s52, s52, s58\n\t" \
    "s_andn2_b32 s39, s52, 7\n\t" \
    "s_add_u32 s52, s52, s39\n\t" \
    "s_lshl_b32 s52, s52, 10\n\t" \
    "v_lshrrev_b32 v16, 9, v0\n\t" \
    "v_and_b32 v18, 0x2f0, v0\n\t" \
    "v_and_b32 v17, 1, v11\n\t" \
    "v_lshl_add_u32 v14, v17, 11, v18\n\t" \
    "v_add_u32 v36, s51, v14\n\t" \
    "v_add_u32 v37, s52, v0\n\t" \
    "s_mul_i32 s44, s24, 7296\n\t" \
    "s_mul_i32 s37, s60, 344064\n\t" \
    "s_mul_i32 s39, s59, 4992\n\t" \
    "s_add_u32 s37, s37, s39\n\t" \
    "s_cmp_eq_u32 s59, 8\n\t" \
    "s_movk_i32 s39, 2496\n\t" \
    "s_cselect_b32 s39, 1536, s39\n\t" \
    "s_add_u32 s37, s37, s39\n\t" \
    "s_and_b32 s39, s8, 0x1fffff\n\t" \
    "s_add_u32 s37, s37, s39\n\t" \
    "v_bfe_u32 v38, v0, 5, 2\n\t" \
    "v_lshl_add_u32 v38, v16, 4, v38\n\t" \
    "v_mul_u32_u24 v38, 43008, v38\n\t" \
    "v_add_u32 v39, s37, v38\n\t" \
    "s_lshr_b32 s37, s60, 1\n\t" \
    "s_mul_i32 s37, s37, 9\n\t" \
    "s_add_u32 s37, s37, s59\n\t" \
    "v_mad_u32_u24 v38, v16, 9, s37\n\t" \
    "v_lshrrev_b32 v18, 5, v38\n\t" \
    "s_and_b32 s29, s51, 0x1000\n\t" \
    "s_and_b32 s30, s52, 0x1000\n\t" \
    "s_lshl_b32 s37, s24, 2\n\t" \
    "s_lshl1_add_u32 s39, s24, 8\n\t" \
    "s_min_u32 s61, s37, s39\n\t" \
    "s_add_u32 s37, s37, 2\n\t" \
    "s_add_u32 s39, s24, 20\n\t" \
    "s_cmp_lt_u32 s24, 4\n\t" \
    "s_cselect_b32 s62, s37, s39\n\t" \
    "s_bfe_u32 s63, s2, 0x10003\n\t" \
    "s_mul_i32 s47, s63, 28\n\t" \
    "s_add_u32 s45, s47, s61\n\t" \
    "s_add_u32 s46, s47, s62\n\t" \
    "s_andn2_b32 s39, s45, 7\n\t" \
    "s_add_u32 s45, s45, s39\n\t" \
    "s_lshl_b32 s45, s45, 10\n\t" \
    "s_andn2_b32 s39, s46, 7\n\t" \
    "s_add_u32 s46, s46, s39\n\t" \
    "s_lshl_b32 s46, s46, 10\n\t" \
    "v_lshlrev_b32 v17, 4, v11\n\t" \
    "v_lshrrev_b32 v14, 6, v12\n\t" \
    "v_mul_u32_u24 v14, 7296, v14\n\t" \
    "v_and_b32 v38, 16, v0\n\t" \
    "v_mul_u32_u24 v38, 226, v38\n\t" \
    "v_and_b32 v40, 32, v0\n\t" \
    "v_lshl_add_u32 v38, v40, 1, v38\n\t" \
    "v_add3_u32 v14, v14, v38, v17\n\t" \
    "s_lshl_b32 s37, s61, 7\n\t" \
    "v_add_u32 v44, s37, v14\n\t" \
    "s_and_b32 s37, s62, 30\n\t" \
    "s_lshl_b32 s37, s37, 7\n\t" \
    "v_add_u32 v45, s37, v14\n\t" \
    "s_mov_b32 s1, 0\n\t" \
    "s_mov_b32 s41, 0x18000\n\t" \
    "s_lshl_b32 s37, s63, 9\n\t" \
    "s_add_u32 s51, s51, s37\n\t" \
    "s_cmp_lt_u32 s57, 252\n\t" \
    "s_cselect_b32 s51, s51, 0x80000000\n\t" \
    "s_and_b32 s37, s2, 0x87\n\t" \
    "s_lshl4_add_u32 s37, s24, s37\n\t" \
    "s_lshl_b32 s55, s37, 7\n\t" \
    "s_andn2_b32 s37, s55, 0x1fff\n\t" \
    "s_add_u32 s55, s55, s37\n\t" \
    "v_and_b32 v46, 16, v0\n\t" \
    "v_lshlrev_b32 v47, 6, v46\n\t" \
    "v_mad_u32_u24 v38, v12, 12, v17\n\t" \
    "v_and_b32 v14, 0x170, v0\n\t" \
    "v_and_b32 v41, 0x80, v0\n\t" \
    "v_lshl_add_u32 v14, v41, 3, v14\n\t" \
    "v_lshl_add_u32 v14, v16, 11, v14\n\t" \
    "v_add_u32 v42, s51, v14\n\t" \
    "s_mul_i32 s37, s24, 2064\n\t" \
    "v_and_b32 v14, 0x300, v0\n\t" \
    "v_add3_u32 v43, v14, v0, s37\n\t" \
    "s_lshl_b32 s37, s37, 1\n\t" \
    "v_lshlrev_b32 v14, 7, v16\n\t" \
    "v_xor_b32 v14, v14, v0\n\t" \
    "v_add_u32 v40, s37, v14\n\t" \
    "v_lshrrev_b32 v14, 5, v12\n\t" \
    "v_mul_u32_u24 v14, 2064, v14\n\t" \
    "v_lshl_add_u32 v14, v46, 2, v14\n\t" \
    "s_lshl_b32 s37, s24, 8\n\t" \
    "v_add3_u32 v48, v14, v17, s37\n\t" \
    "v_bfe_u32 v14, v0, 7, 1\n\t" \
    "v_lshl_add_u32 v14, v11, 2, v14\n\t" \
    "v_mul_u32_u24 v14, 2064, v14\n\t" \
    "v_lshrrev_b32 v41, 3, v0\n\t" \
    "v_and_or_b32 v14, v41, 8, v14\n\t" \
    "s_and_b32 s37, s24, 3\n\t" \
    "s_lshl_b32 s37, s37, 6\n\t" \
    "s_and_b32 s39, s24, 4\n\t" \
    "s_lshl_b32 s39, s39, 8\n\t" \
    "s_or_b32 s37, s37, s39\n\t" \
    "v_and_or_b32 v41, v0, 32, s37\n\t" \
    "v_mul_u32_u24 v46, 40, v46\n\t" \
    "v_xor_b32 v41, v41, v46\n\t" \
    "v_add_u32 v49, v14, v41\n\t" \
    "ds_read_b128 v[52:55], v10 offset:0\n\t" \
    "ds_read_b128 v[56:59], v10 offset:1024\n\t" \
    "s_sleep 6\n\t" \
    "s_and_b32 s37, s24, 1\n\t" \
    "s_lshl_b32 s37, s37, 4\n\t" \
    "s_lshr_b32 s39, s24, 1\n\t" \
    "s_add_u32 s37, s37, s39\n\t" \
    "s_mul_i32 s37, s37, 192\n\t" \
    "s_add_u32 s37, s37, s31\n\t" \
    "s_lshl_b32 s39, s24, 12\n\t" \
    "s_add_u32 m0, s39, 116736\n\t" \
    "s_add_u32 s38, s37, 0\n\t" \
    "buffer_load_dwordx4 v1, s[12:15], s38 offen  lds\n\t" \
    "s_add_u32 m0, s39, 117760\n\t" \
    "s_add_u32 s38, s37, 258048\n\t" \
    "buffer_load_dwordx4 v1, s[12:15], s38 offen  lds\n\t" \
    "s_add_u32 m0, s39, 118784\n\t" \
    "s_add_u32 s38, s37, 516096\n\t" \
    "buffer_load_dwordx4 v1, s[12:15], s38 offen  lds\n\t" \
    "s_add_u32 m0, s39, 119808\n\t" \
    "s_add_u32 s38, s37, 774144\n\t" \
    "buffer_load_dwordx4 v1, s[12:15], s38 offen  lds\n\t" \
    "s_cmp_lg_u32 s24, 3\n\t" \
    "s_cbranch_scc1 .Lr9_%=\n\t" \
    "s_and_b32 s37, s2, 7\n\t" \
    "s_lshl_b32 s37, s37, 10\n\t" \
    "s_add_u32 s37, s37, 34676736\n\t" \
    "s_buffer_load_dword s56, s[4:7], s37 offset:0x0\n\t" \
    "s_buffer_load_dword s57, s[4:7], s37 offset:0x80\n\t" \
    "s_buffer_load_dword s58, s[4:7], s37 offset:0x100\n\t" \
    "s_buffer_load_dword s59, s[4:7], s37 offset:0x180\n\t" \
    "s_buffer_load_dword s60, s[4:7], s37 offset:0x200\n\t" \
    "s_buffer_load_dword s61, s[4:7], s37 offset:0x280\n\t" \
    "s_buffer_load_dword s62, s[4:7], s37 offset:0x300\n\t" \
    "s_buffer_load_dword s63, s[4:7], s37 offset:0x380\n\t" \
    "s_mov_b32 s40, 0\n\t" \
    ".Lr1_%=:\n\t" \
    "s_waitcnt lgkmcnt(0)\n\t" \
    "s_memrealtime s[22:23]\n\t" \
    "s_waitcnt lgkmcnt(0)\n\t" \
    "s_mov_b32 s37, 1000\n\t" \
    "s_mov_b32 s38, 0\n\t" \
    "s_sub_u32 s39, s22, s56\n\t" \
    "s_min_u32 s37, s37, s39\n\t" \
    "s_max_u32 s38, s38, s39\n\t" \
    "s_sub_u32 s39, s22, s57\n\t" \
    "s_min_u32 s37, s37, s39\n\t" \
    "s_max_u32 s38, s38, s39\n\t" \
    "s_sub_u32 s39, s22, s58\n\t" \
    "s_min_u32 s37, s37, s39\n\t" \
    "s_max_u32 s38, s38, s39\n\t" \
    "s_sub_u32 s39, s22, s59\n\t" \
    "s_min_u32 s37, s37, s39\n\t" \
    "s_max_u32 s38, s38, s39\n\t" \
    "s_sub_u32 s39, s22, s60\n\t" \
    "s_min_u32 s37, s37, s39\n\t" \
    "s_max_u32 s38, s38, s39\n\t" \
    "s_sub_u32 s39, s22, s61\n\t" \
    "s_min_u32 s37, s37, s39\n\t" \
    "s_max_u32 s38, s38, s39\n\t" \
    "s_sub_u32 s39, s22, s62\n\t" \
    "s_min_u32 s37, s37, s39\n\t" \
    "s_max_u32 s38, s38, s39\n\t" \
    "s_sub_u32 s39, s22, s63\n\t" \
    "s_min_u32 s37, s37, s39\n\t" \
    "s_max_u32 s38, s38, s39\n\t" \
    "s_cmp_gt_u32 s38, 1000\n\t" \
    "s_cbranch_scc1 .Lr2_%=\n\t" \
    ".Lr3_%=:\n\t" \
    "s_sub_u32 s35, s22, s37\n\t" \
    "s_waitcnt vmcnt(5)\n\t" \
    "v_readfirstlane_b32 s34, v3\n\t" \
    "s_mul_i32 s34, s34, 1664525\n\t" \
    "s_add_u32 s34, s34, 1013904223\n\t" \
    "s_or_b32 s34, s34, 1\n\t" \
    "v_mov_b32 v60, s34\n\t" \
    "v_mov_b32 v61, s35\n\t" \
    "ds_write_b64 v9, v[60:61] offset:32\n\t" \
    ".Lr9_%=:\n\t" \
    "s_cmp_lt_u32 s24, 4\n\t" \
    "s_cbranch_scc1 .Lw_%=\n\t" \
    "s_waitcnt vmcnt(4)\n\t" \
    ".Lw_%=:\n\t" \
    "s_waitcnt lgkmcnt(0)\n\t" \
    "s_barrier\n\t" \
    "ds_read_b64_tr_b16 v[64:65], v8 offset:0\n\t" \
    "ds_read_b64_tr_b16 v[66:67], v8 offset:768\n\t" \
    "ds_read_b64_tr_b16 v[68:69], v8 offset:12416\n\t" \
    "ds_read_b64_tr_b16 v[70:71], v8 offset:13184\n\t" \
    "ds_read_b64_tr_b16 v[72:73], v8 offset:6208\n\t" \
    "ds_read_b64_tr_b16 v[74:75], v8 offset:6976\n\t" \
    "ds_read_b64_tr_b16 v[76:77], v8 offset:18624\n\t" \
    "ds_read_b64_tr_b16 v[78:79], v8 offset:19392\n\t" \
    "ds_read_b128 v[80:83], v9 offset:32\n\t" \
    "s_waitcnt lgkmcnt(7)\n\t" \
    "v_mfma_f32_16x16x32_bf16 v[84:87], v[24:27], v[64:67], 0\n\t" \
    "s_waitcnt lgkmcnt(5)\n\t" \
    "v_mfma_f32_16x16x32_bf16 v[88:91], v[24:27], v[68:71], 0\n\t" \
    "s_waitcnt lgkmcnt(3)\n\t" \
    "v_mfma_f32_16x16x32_bf16 v[92:95], v[24:27], v[72:75], 0\n\t" \
    "s_waitcnt lgkmcnt(1)\n\t" \
    "v_mfma_f32_16x16x32_bf16 v[96:99], v[24:27], v[76:79], 0\n\t" \
    "s_waitcnt lgkmcnt(0)\n\t" \
    "v_readfirstlane_b32 s34, v80\n\t" \
    "v_readfirstlane_b32 s35, v81\n\t" \
    "v_add_u32 v84, 0x8000, v84\n\t" \
    "v_add_u32 v86, 0x8000, v86\n\t" \
    "v_add_u32 v88, 0x8000, v88\n\t" \
    "v_add_u32 v90, 0x8000, v90\n\t" \
    "v_cvt_pk_bf16_f32 v108, v84, v85\n\t" \
    "v_cvt_pk_bf16_f32 v110, v88, v89\n\t" \
    "v_cvt_pk_bf16_f32 v109, v92, v93\n\t" \
    "v_cvt_pk_bf16_f32 v111, v96, v97\n\t" \
    "v_and_b32 v108, -2, v108\n\t" \
    "v_and_b32 v110, -2, v110\n\t" \
    "v_cvt_pk_bf16_f32 v112, v86, v87\n\t" \
    "v_cvt_pk_bf16_f32 v114, v90, v91\n\t" \
    "v_cvt_pk_bf16_f32 v113, v94, v95\n\t" \
    "v_cvt_pk_bf16_f32 v115, v98, v99\n\t" \
    "v_and_b32 v112, -2, v112\n\t" \
    "v_and_b32 v114, -2, v114\n\t" \
    ".Lh38_%=:\n\t" \
    "s_memrealtime s[22:23]\n\t" \
    "s_waitcnt lgkmcnt(0)\n\t" \
    "s_sub_u32 s22, s22, s35\n\t" \
    "s_cmp_lt_u32 s22, 205\n\t" \
    "s_cbranch_scc1 .Lh38_%=\n\t" \
    "buffer_store_dwordx4 v[108:111], v13, s[4:7], 0 offen offset:0 sc1\n\t" \
    "s_mov_b32 s42, 4243456\n\t" \
    "buffer_store_dwordx4 v[112:115], v13, s[4:7], s42 offen offset:0 sc1\n\t" \
    "ds_read_b64_tr_b16 v[116:117], v8 offset:64\n\t" \
    "ds_read_b64_tr_b16 v[118:119], v8 offset:832\n\t" \
    "ds_read_b64_tr_b16 v[120:121], v8 offset:12480\n\t" \
    "ds_read_b64_tr_b16 v[122:123], v8 offset:13248\n\t" \
    "ds_read_b64_tr_b16 v[124:125], v8 offset:6272\n\t" \
    "ds_read_b64_tr_b16 v[126:127], v8 offset:7040\n\t" \
    "ds_read_b64_tr_b16 v[128:129], v8 offset:18688\n\t" \
    "ds_read_b64_tr_b16 v[130:131], v8 offset:19456\n\t" \
    "ds_read_b64_tr_b16 v[132:133], v8 offset:128\n\t" \
    "ds_read_b64_tr_b16 v[134:135], v8 offset:896\n\t" \
    "ds_read_b64_tr_b16 v[136:137], v8 offset:12544\n\t" \
    "ds_read_b64_tr_b16 v[138:139], v8 offset:13312\n\t" \
    "ds_read_b64_tr_b16 v[140:141], v8 offset:6336\n\t" \
    "ds_read_b64_tr_b16 v[142:143], v8 offset:7104\n\t" \
    "ds_read_b64_tr_b16 v[144:145], v8 offset:18752\n\t" \
    "ds_read_b64_tr_b16 v[146:147], v8 offset:19520\n\t" \
    "v_mov_b32 v148, s34\n\t" \
    "v_mov_b32 v149, s34\n\t" \
    "v_mov_b32 v150, s34\n\t" \
    "v_mov_b32 v151, s34\n\t" \
    "s_and_b32 s37, s2, 243\n\t" \
    "s_cmp_eq_u32 s37, 243\n\t" \
    "s_cbranch_scc1 .Lskip_%=\n\t" \
    "s_waitcnt lgkmcnt(14)\n\t" \
    "v_mfma_f32_16x16x32_bf16 v[152:155], v[24:27], v[116:119], 0\n\t" \
    "s_waitcnt lgkmcnt(12)\n\t" \
    "v_mfma_f32_16x16x32_bf16 v[156:159], v[24:27], v[120:123], 0\n\t" \
    "s_waitcnt lgkmcnt(10)\n\t" \
    "v_mfma_f32_16x16x32_bf16 v[160:163], v[24:27], v[124:127], 0\n\t" \
    "s_waitcnt lgkmcnt(8)\n\t" \
    "v_mfma_f32_16x16x32_bf16 v[164:167], v[24:27], v[128:131], 0\n\t" \
    "s_waitcnt lgkmcnt(6)\n\t" \
    "v_mfma_f32_16x16x32_bf16 v[168:171], v[24:27], v[132:135], 0\n\t" \
    "s_waitcnt lgkmcnt(4)\n\t" \
    "v_mfma_f32_16x16x32_bf16 v[172:175], v[24:27], v[136:139], 0\n\t" \
    "s_waitcnt lgkmcnt(2)\n\t" \
    "v_mfma_f32_16x16x32_bf16 v[176:179], v[24:27], v[140:143], 0\n\t" \
    "s_waitcnt lgkmcnt(0)\n\t" \
    "v_mfma_f32_16x16x32_bf16 v[180:183], v[24:27], v[144:147], 0\n\t" \
    "v_add_u32 v152, 0x8000, v152\n\t" \
    "v_add_u32 v154, 0x8000, v154\n\t" \
    "v_add_u32 v156, 0x8000, v156\n\t" \
    "v_add_u32 v158, 0x8000, v158\n\t" \
    "v_cvt_pk_bf16_f32 v184, v152, v153\n\t" \
    "v_cvt_pk_bf16_f32 v186, v156, v157\n\t" \
    "v_cvt_pk_bf16_f32 v185, v160, v161\n\t" \
    "v_cvt_pk_bf16_f32 v187, v164, v165\n\t" \
    "v_and_b32 v184, -2, v184\n\t" \
    "v_and_b32 v186, -2, v186\n\t" \
    "v_cvt_pk_bf16_f32 v188, v154, v155\n\t" \
    "v_cvt_pk_bf16_f32 v190, v158, v159\n\t" \
    "v_cvt_pk_bf16_f32 v189, v162, v163\n\t" \
    "v_cvt_pk_bf16_f32 v191, v166, v167\n\t" \
    "v_and_b32 v188, -2, v188\n\t" \
    "v_and_b32 v190, -2, v190\n\t" \
    "v_add_u32 v168, 0x8000, v168\n\t" \
    "v_add_u32 v170, 0x8000, v170\n\t" \
    "v_add_u32 v172, 0x8000, v172\n\t" \
    "v_add_u32 v174, 0x8000, v174\n\t" \
    "v_cvt_pk_bf16_f32 v4, v168, v169\n\t" \
    "v_cvt_pk_bf16_f32 v6, v172, v173\n\t" \
    "v_cvt_pk_bf16_f32 v5, v176, v177\n\t" \
    "v_cvt_pk_bf16_f32 v7, v180, v181\n\t" \
    "v_and_b32 v4, -2, v4\n\t" \
    "v_and_b32 v6, -2, v6\n\t" \
    "v_cvt_pk_bf16_f32 v20, v170, v171\n\t" \
    "v_cvt_pk_bf16_f32 v22, v174, v175\n\t" \
    "v_cvt_pk_bf16_f32 v21, v178, v179\n\t" \
    "v_cvt_pk_bf16_f32 v23, v182, v183\n\t" \
    "v_and_b32 v20, -2, v20\n\t" \
    "v_and_b32 v22, -2, v22\n\t" \
    ".Lh39_%=:\n\t" \
    "s_memrealtime s[22:23]\n\t" \
    "s_waitcnt lgkmcnt(0)\n\t" \
    "s_sub_u32 s22, s22, s35\n\t" \
    "s_cmp_lt_u32 s22, 255\n\t" \
    "s_cbranch_scc1 .Lh39_%=\n\t" \
    "s_mov_b32 s42, 524288\n\t" \
    "buffer_store_dwordx4 v[184:187], v13, s[4:7], s42 offen offset:0 sc1\n\t" \
    "s_mov_b32 s42, 4767744\n\t" \
    "buffer_store_dwordx4 v[188:191], v13, s[4:7], s42 offen offset:0 sc1\n\t" \
    "s_waitcnt vmcnt(4)\n\t" \
    "; wait\n\t" \
    "s_waitcnt lgkmcnt(0)\n\t" \
    "s_barrier\n\t" \
    "s_lshl_b32 s37, s24, 18\n\t" \
    "s_add_u32 s37, s37, s32\n\t" \
    "s_bitcmp1_b32 13, s24\n\t" \
    "s_cselect_b32 s39, 0, 0x80000000\n\t" \
    "v_add_u32 v16, s39, v2\n\t" \
    "v_mov_b32 v60, 0\n\t" \
    "v_mov_b32 v61, 0\n\t" \
    "v_mov_b32 v62, 0\n\t" \
    "v_mov_b32 v63, 0\n\t" \
    "s_cmp_eq_u32 s24, 4\n\t" \
    "s_cselect_b64 exec, 0xff, -1\n\t" \
    "buffer_store_dwordx4 v[60:63], v16, s[4:7], s37 offen sc1\n\t" \
    "s_mov_b64 exec, -1\n\t" \
    ".Lh40_%=:\n\t" \
    "s_memrealtime s[22:23]\n\t" \
    "s_waitcnt lgkmcnt(0)\n\t" \
    "s_sub_u32 s22, s22, s35\n\t" \
    "s_cmp_lt_u32 s22, 305\n\t" \
    "s_cbranch_scc1 .Lh40_%=\n\t" \
    "s_mov_b32 s42, 1048576\n\t" \
    "buffer_store_dwordx4 v[4:7], v13, s[4:7], s42 offen offset:0 sc1\n\t" \
    "s_mov_b32 s42, 5292032\n\t" \
    "buffer_store_dwordx4 v[20:23], v13, s[4:7], s42 offen offset:0 sc1\n\t" \
    "ds_read_b64_tr_b16 v[64:65], v15 offset:0\n\t" \
    "ds_read_b64_tr_b16 v[66:67], v15 offset:256\n\t" \
    "ds_read_b64_tr_b16 v[68:69], v15 offset:2048\n\t" \
    "ds_read_b64_tr_b16 v[70:71], v15 offset:2304\n\t" \
    "ds_read_b64_tr_b16 v[72:73], v15 offset:4096\n\t" \
    "ds_read_b64_tr_b16 v[74:75], v15 offset:4352\n\t" \
    "ds_read_b64_tr_b16 v[76:77], v15 offset:6144\n\t" \
    "ds_read_b64_tr_b16 v[78:79], v15 offset:6400\n\t" \
    "ds_read_b64_tr_b16 v[80:81], v15 offset:8192\n\t" \
    "ds_read_b64_tr_b16 v[82:83], v15 offset:8448\n\t" \
    "ds_read_b64_tr_b16 v[100:101], v15 offset:10240\n\t" \
    "ds_read_b64_tr_b16 v[102:103], v15 offset:10496\n\t" \
    "ds_read_b64_tr_b16 v[104:105], v15 offset:12288\n\t" \
    "ds_read_b64_tr_b16 v[106:107], v15 offset:12544\n\t" \
    "s_waitcnt lgkmcnt(12)\n\t" \
    "v_mfma_f32_16x16x32_bf16 v[84:87], v[28:31], v[64:67], 0\n\t" \
    "ds_read_b64_tr_b16 v[88:89], v15 offset:14336\n\t" \
    "ds_read_b64_tr_b16 v[90:91], v15 offset:14592\n\t" \
    "s_waitcnt lgkmcnt(12)\n\t" \
    "v_mfma_f32_16x16x32_bf16 v[84:87], v[32:35], v[68:71], v[84:87]\n\t" \
    "ds_read_b64_tr_b16 v[92:93], v15 offset:16384\n\t" \
    "ds_read_b64_tr_b16 v[94:95], v15 offset:16640\n\t" \
    "s_waitcnt lgkmcnt(12)\n\t" \
    "v_mfma_f32_16x16x32_bf16 v[96:99], v[28:31], v[72:75], 0\n\t" \
    "ds_read_b64_tr_b16 v[108:109], v15 offset:18432\n\t" \
    "ds_read_b64_tr_b16 v[110:111], v15 offset:18688\n\t" \
    "s_waitcnt lgkmcnt(12)\n\t" \
    "v_mfma_f32_16x16x32_bf16 v[96:99], v[32:35], v[76:79], v[96:99]\n\t" \
    "ds_read_b64_tr_b16 v[112:113], v15 offset:20480\n\t" \
    "ds_read_b64_tr_b16 v[114:115], v15 offset:20736\n\t" \
    "s_waitcnt lgkmcnt(12)\n\t" \
    "v_mfma_f32_16x16x32_bf16 v[116:119], v[28:31], v[80:83], 0\n\t" \
    "ds_read_b64_tr_b16 v[120:121], v15 offset:22528\n\t" \
    "ds_read_b64_tr_b16 v[122:123], v15 offset:22784\n\t" \
    "s_waitcnt lgkmcnt(12)\n\t" \
    "v_mfma_f32_16x16x32_bf16 v[116:119], v[32:35], v[100:103], v[116:119]\n\t" \
    "ds_read_b64_tr_b16 v[124:125], v15 offset:24576\n\t" \
    "ds_read_b64_tr_b16 v[126:127], v15 offset:24832\n\t" \
    "s_waitcnt lgkmcnt(12)\n\t" \
    "v_mfma_f32_16x16x32_bf16 v[128:131], v[28:31], v[104:107], 0\n\t" \
    "ds_read_b64_tr_b16 v[132:133], v15 offset:26624\n\t" \
    "ds_read_b64_tr_b16 v[134:135], v15 offset:26880\n\t" \
    "s_waitcnt lgkmcnt(12)\n\t" \
    "v_mfma_f32_16x16x32_bf16 v[128:131], v[32:35], v[88:91], v[128:131]\n\t" \
    "ds_read_b64_tr_b16 v[136:137], v15 offset:28672\n\t" \
    "ds_read_b64_tr_b16 v[138:139], v15 offset:28928\n\t" \
    "s_waitcnt lgkmcnt(12)\n\t" \
    "v_mfma_f32_16x16x32_bf16 v[140:143], v[28:31], v[92:95], 0\n\t" \
    "ds_read_b64_tr_b16 v[24:25], v15 offset:30720\n\t" \
    "ds_read_b64_tr_b16 v[26:27], v15 offset:30976\n\t" \
    "s_waitcnt lgkmcnt(12)\n\t" \
    "v_mfma_f32_16x16x32_bf16 v[140:143], v[32:35], v[108:111], v[140:143]\n\t" \
    "v_cvt_pk_bf16_f32 v144, v84, v96\n\t" \
    "v_cvt_pk_bf16_f32 v152, v85, v97\n\t" \
    "s_waitcnt lgkmcnt(10)\n\t" \
    "v_mfma_f32_16x16x32_bf16 v[156:159], v[28:31], v[112:115], 0\n\t" \
    "v_cvt_pk_bf16_f32 v160, v86, v98\n\t" \
    "v_cvt_pk_bf16_f32 v164, v87, v99\n\t" \
    "s_waitcnt lgkmcnt(8)\n\t" \
    "v_mfma_f32_16x16x32_bf16 v[156:159], v[32:35], v[120:123], v[156:159]\n\t" \
    "v_cvt_pk_bf16_f32 v145, v116, v128\n\t" \
    "v_cvt_pk_bf16_f32 v153, v117, v129\n\t" \
    "s_waitcnt lgkmcnt(6)\n\t" \
    "v_mfma_f32_16x16x32_bf16 v[168:171], v[28:31], v[124:127], 0\n\t" \
    "v_cvt_pk_bf16_f32 v161, v118, v130\n\t" \
    "v_cvt_pk_bf16_f32 v165, v119, v131\n\t" \
    "s_waitcnt lgkmcnt(4)\n\t" \
    "v_mfma_f32_16x16x32_bf16 v[168:171], v[32:35], v[132:135], v[168:171]\n\t" \
    "s_waitcnt lgkmcnt(2)\n\t" \
    "v_mfma_f32_16x16x32_bf16 v[172:175], v[28:31], v[136:139], 0\n\t" \
    "s_waitcnt lgkmcnt(0)\n\t" \
    "v_mfma_f32_16x16x32_bf16 v[172:175], v[32:35], v[24:27], v[172:175]\n\t" \
    "v_cvt_pk_bf16_f32 v146, v140, v156\n\t" \
    "v_cvt_pk_bf16_f32 v154, v141, v157\n\t" \
    "v_cvt_pk_bf16_f32 v162, v142, v158\n\t" \
    "v_cvt_pk_bf16_f32 v166, v143, v159\n\t" \
    "s_nop 3\n\t" \
    "v_cvt_pk_bf16_f32 v147, v168, v172\n\t" \
    "v_cvt_pk_bf16_f32 v155, v169, v173\n\t" \
    "v_cvt_pk_bf16_f32 v163, v170, v174\n\t" \
    "v_cvt_pk_bf16_f32 v167, v171, v175\n\t" \
    ".Lh41_%=:\n\t" \
    "s_memrealtime s[22:23]\n\t" \
    "s_waitcnt lgkmcnt(0)\n\t" \
    "s_sub_u32 s22, s22, s35\n\t" \
    "s_cmp_lt_u32 s22, 370\n\t" \
    "s_cbranch_scc1 .Lh41_%=\n\t" \
    "s_cmp_eq_u32 s24, 0\n\t" \
    "s_cbranch_scc1 .Lj43_%=\n\t" \
    ".Lh42_%=:\n\t" \
    "s_memrealtime s[22:23]\n\t" \
    "s_waitcnt lgkmcnt(0)\n\t" \
    "s_sub_u32 s22, s22, s35\n\t" \
    "s_cmp_lt_u32 s22, 385\n\t" \
    "s_cbranch_scc1 .Lh42_%=\n\t" \
    ".Lj43_%=:\n\t" \
    "s_waitcnt lgkmcnt(0)\n\t" \
    "s_xor_b32 s42, s43, 0\n\t" \
    "buffer_store_dwordx4 v[144:147], v19, s[4:7], s42 offen offset:0 sc1\n\t" \
    "s_waitcnt vmcnt(1)\n\t" \
    "s_mov_b64 exec, 1\n\t" \
    "ds_add_rtn_u32 v17, v9, v9 offset:8\n\t" \
    "s_add_u32 s37, s32, 524288\n\t" \
    "s_waitcnt lgkmcnt(0)\n\t" \
    "v_cmp_eq_u32 vcc, 1098496, v17\n\t" \
    "s_mov_b64 exec, -1\n\t" \
    "s_nop 0\n\t" \
    "s_cbranch_vccz .La13_%=\n\t" \
    "buffer_store_dwordx4 v[148:151], v2, s[4:7], s37 offen sc1\n\t" \
    ".La13_%=:\n\t" \
    "s_mov_b64 exec, -1\n\t" \
    "s_xor_b32 s42, s43, 1024\n\t" \
    "buffer_store_dwordx4 v[152:155], v19, s[4:7], s42 offen offset:0 sc1\n\t" \
    "s_cmp_eq_u32 s24, 0\n\t" \
    "s_cbranch_scc1 .Lj45_%=\n\t" \
    ".Lh44_%=:\n\t" \
    "s_memrealtime s[22:23]\n\t" \
    "s_waitcnt lgkmcnt(0)\n\t" \
    "s_sub_u32 s22, s22, s35\n\t" \
    "s_cmp_lt_u32 s22, 435\n\t" \
    "s_cbranch_scc1 .Lh44_%=\n\t" \
    ".Lj45_%=:\n\t" \
    "s_waitcnt lgkmcnt(0)\n\t" \
    "s_xor_b32 s42, s43, 2048\n\t" \
    "buffer_store_dwordx4 v[160:163], v19, s[4:7], s42 offen offset:0 sc1\n\t" \
    "s_xor_b32 s42, s43, 3072\n\t" \
    "buffer_store_dwordx4 v[164:167], v19, s[4:7], s42 offen offset:0 sc1\n\t" \
    ".Lskip_%=:\n\t" \
    "s_waitcnt lgkmcnt(0)\n\t" \
    "s_nop 7\n\t" \
    ".Lh46_%=:\n\t" \
    "s_memrealtime s[22:23]\n\t" \
    "s_waitcnt lgkmcnt(0)\n\t" \
    "s_sub_u32 s22, s22, s35\n\t" \
    "s_cmp_lt_u32 s22, 410\n\t" \
    "s_cbranch_scc1 .Lh46_%=\n\t" \
    "s_and_b32 s52, s2, 7\n\t" \
    "s_lshl_b32 s52, s52, 7\n\t" \
    "s_add_u32 s52, s52, 34669568\n\t" \
    "s_buffer_load_dword s36, s[4:7], s52\n\t" \
    "s_mov_b32 s37, 34670592\n\t" \
    "s_buffer_load_dword s56, s[4:7], s37 offset:0x0\n\t" \
    "s_buffer_load_dword s57, s[4:7], s37 offset:0x80\n\t" \
    "s_buffer_load_dword s58, s[4:7], s37 offset:0x100\n\t" \
    "s_buffer_load_dword s59, s[4:7], s37 offset:0x180\n\t" \
    "s_buffer_load_dword s60, s[4:7], s37 offset:0x200\n\t" \
    "s_buffer_load_dword s61, s[4:7], s37 offset:0x280\n\t" \
    "s_and_b32 s37, s24, 3\n\t" \
    "s_mul_i32 s37, s37, 8486912\n\t" \
    "s_add_u32 s53, s53, s37\n\t" \
    "s_and_b32 s37, s24, 4\n\t" \
    "s_lshl_b32 s39, s37, 11\n\t" \
    "s_add_u32 s53, s53, s39\n\t" \
    "s_add_u32 s37, s37, s24\n\t" \
    "s_andn2_b32 s39, s26, 1\n\t" \
    "s_lshl3_add_u32 s37, s39, s37\n\t" \
    "s_mul_i32 s37, s37, 43008\n\t" \
    "s_mul_i32 s39, s27, 4992\n\t" \
    "s_add_u32 s51, s37, s39\n\t" \
    "s_cmp_eq_u32 s27, 8\n\t" \
    "s_movk_i32 s10, 4992\n\t" \
    "s_cselect_b32 s10, 3072, s10\n\t" \
    "s_mov_b32 s11, 0x20000\n\t" \
    "s_and_b32 s37, s2, 0x70\n\t" \
    "s_lshl_b32 s37, s37, 1\n\t" \
    "v_lshrrev_b32 v11, 7, v0\n\t" \
    "v_lshl_add_u32 v11, v11, 2, s37\n\t" \
    "v_min_u32 v11, 248, v11\n\t" \
    "v_mul_u32_u24 v46, 147, v11\n\t" \
    "v_lshrrev_b32 v46, 12, v46\n\t" \
    "v_mul_u32_u24 v41, 28, v46\n\t" \
    "v_sub_u32 v11, v11, v41\n\t" \
    "v_bfe_u32 v41, v0, 4, 3\n\t" \
    "v_min_u32 v41, 4, v41\n\t" \
    "v_add3_u32 v11, v11, v41, -1\n\t" \
    "v_add_u32 v41, 28, v11\n\t" \
    "v_min_u32 v11, v11, v41\n\t" \
    "v_and_b32 v1, 1, v11\n\t" \
    "v_lshrrev_b32 v11, 1, v11\n\t" \
    "v_mad_u32_u24 v11, v11, 9, v46\n\t" \
    "v_lshl_add_u32 v11, v1, 7, v11\n\t" \
    "v_and_b32 v3, 31, v11\n\t" \
    "v_and_b32 v50, 24, v3\n\t" \
    "v_add_u32 v3, v3, v50\n\t" \
    "v_lshrrev_b32 v50, 5, v11\n\t" \
    "v_lshlrev_b32 v50, 7, v50\n\t" \
    "v_lshl_or_b32 v51, v3, 10, v50\n\t" \
    "s_mov_b32 s40, 0\n\t" \
    "s_waitcnt lgkmcnt(0)\n\t" \
    ".Lr4_%=:\n\t" \
    "s_xor_b32 s36, s36, s34\n\t" \
    "s_andn2_b32 s37, s36, 14\n\t" \
    "s_cbranch_scc1 .Lr5_%=\n\t" \
    ".Lr6_%=:\n\t" \
    "s_bfe_u32 s36, s36, 0x30001\n\t" \
    "s_mov_b32 s40, 0\n\t" \
    "s_waitcnt lgkmcnt(0)\n\t" \
    ".Lq4_%=:\n\t" \
    "s_mov_b32 s37, 0\n\t" \
    "s_mov_b32 s0, 0\n\t" \
    "s_xor_b32 s61, s61, s34\n\t" \
    "s_or_b32 s37, s37, s61\n\t" \
    "s_lshl1_add_u32 s0, s0, s61\n\t" \
    "s_xor_b32 s60, s60, s34\n\t" \
    "s_or_b32 s37, s37, s60\n\t" \
    "s_lshl1_add_u32 s0, s0, s60\n\t" \
    "s_xor_b32 s59, s59, s34\n\t" \
    "s_or_b32 s37, s37, s59\n\t" \
    "s_lshl1_add_u32 s0, s0, s59\n\t" \
    "s_xor_b32 s58, s58, s34\n\t" \
    "s_or_b32 s37, s37, s58\n\t" \
    "s_lshl1_add_u32 s0, s0, s58\n\t" \
    "s_xor_b32 s57, s57, s34\n\t" \
    "s_or_b32 s37, s37, s57\n\t" \
    "s_lshl1_add_u32 s0, s0, s57\n\t" \
    "s_xor_b32 s56, s56, s34\n\t" \
    "s_or_b32 s37, s37, s56\n\t" \
    "s_lshl1_add_u32 s0, s0, s56\n\t" \
    "s_andn2_b32 s37, s37, 2\n\t" \
    "s_cbranch_scc1 .Lq5_%=\n\t" \
    ".Lq6_%=:\n\t" \
    "s_lshr_b32 s0, s0, 1\n\t" \
    "s_add_u32 s37, s36, 3\n\t" \
    "s_bitcmp1_b32 s37, 2\n\t" \
    "s_cselect_b32 s37, 51, 12\n\t" \
    "s_xor_b32 s0, s0, s37\n\t" \
    "s_add_u32 s56, s2, s36\n\t" \
    "s_and_b32 s56, s56, 7\n\t" \
    "s_lshr_b32 s37, s2, 7\n\t" \
    "s_lshl1_add_u32 s56, s56, s37\n\t" \
    "s_and_b32 s37, s56, 7\n\t" \
    "s_mul_i32 s48, s37, 4243456\n\t" \
    "s_and_b32 s37, s56, 8\n\t" \
    "s_lshl_b32 s37, s37, 10\n\t" \
    "s_add_u32 s48, s48, s37\n\t" \
    "s_add_u32 s49, s48, 524288\n\t" \
    "s_add_u32 s50, s48, 1048576\n\t" \
    "s_xor_b32 s37, s48, 0x2000\n\t" \
    "s_add_u32 s37, s37, 2097152\n\t" \
    "s_add_u32 s45, s45, s37\n\t" \
    "s_add_u32 s46, s46, s37\n\t" \
    "s_cmp_lt_u32 s24, 4\n\t" \
    "s_cselect_b32 s47, s46, 0x80000000\n\t" \
    "v_add_u32 v41, s45, v0\n\t" \
    "v_add_u32 v46, s46, v0\n\t" \
    "s_lshr_b32 s37, s24, 2\n\t" \
    "s_mul_i32 s37, s37, 6\n\t" \
    "s_mul_i32 s39, s56, 7\n\t" \
    "s_add_u32 s37, s37, s39\n\t" \
    "s_lshr_b32 s37, s37, 4\n\t" \
    "s_and_b32 s39, s24, 3\n\t" \
    "s_mul_i32 s39, s39, 63\n\t" \
    "s_add_u32 s37, s37, s39\n\t" \
    "v_lshrrev_b32 v1, 4, v0\n\t" \
    "v_min_u32 v1, 8, v1\n\t" \
    "v_mad_u32_u24 v1, v1, 7, s37\n\t" \
    "v_add_u32 v11, 2, v1\n\t" \
    "v_lshrrev_b32 v11, 7, v11\n\t" \
    "v_lshl_add_u32 v1, v11, 1, v1\n\t" \
    "v_and_b32 v11, 31, v1\n\t" \
    "v_and_b32 v3, 24, v11\n\t" \
    "v_add_u32 v11, v11, v3\n\t" \
    "v_lshrrev_b32 v3, 5, v1\n\t" \
    "v_lshlrev_b32 v3, 7, v3\n\t" \
    "v_lshl_or_b32 v50, v11, 10, v3\n\t" \
    "v_add_u32 v18, s36, v18\n\t" \
    "v_lshlrev_b32 v18, 8, v18\n\t" \
    "v_and_b32 v18, 0x400, v18\n\t" \
    "v_xor_b32 v42, v42, v18\n\t" \
    "s_sub_u32 s37, s24, s36\n\t" \
    "s_and_b32 s37, s37, 7\n\t" \
    "s_mul_i32 s39, s27, 28\n\t" \
    "s_add_u32 s39, s39, s26\n\t" \
    "s_lshr_b32 s39, s39, 5\n\t" \
    "s_lshl4_add_u32 s37, s39, s37\n\t" \
    "s_lshl_b32 s52, s37, 7\n\t" \
    "s_andn2_b32 s37, s52, 0x1fff\n\t" \
    "s_add_u32 s52, s52, s37\n\t" \
    "s_and_b32 s37, s56, 6\n\t" \
    "s_lshl_b32 s37, s37, 15\n\t" \
    "s_and_b32 s39, s48, 0x2000\n\t" \
    "s_add_u32 s37, s37, s39\n\t" \
    "s_add_u32 s33, s37, 33554432\n\t" \
    "s_add_u32 s55, s55, s39\n\t" \
    "s_waitcnt vmcnt(0)\n\t" \
    "s_mov_b64 exec, 1\n\t" \
    "ds_add_rtn_u32 v8, v9, v9 offset:12\n\t" \
    "s_add_u32 s37, s32, 786432\n\t" \
    "s_waitcnt lgkmcnt(0)\n\t" \
    "v_cmp_eq_u32 vcc, 1098496, v8\n\t" \
    "s_mov_b64 exec, -1\n\t" \
    "s_nop 0\n\t" \
    "s_cbranch_vccz .La22_%=\n\t" \
    "buffer_store_dwordx4 v[148:151], v2, s[4:7], s37 offen sc1\n\t" \
    ".La22_%=:\n\t" \
    "s_mov_b64 exec, -1\n\t" \
    ".Lh47_%=:\n\t" \
    "s_memrealtime s[22:23]\n\t" \
    "s_waitcnt lgkmcnt(0)\n\t" \
    "s_sub_u32 s22, s22, s35\n\t" \
    "s_cmp_lt_u32 s22, 485\n\t" \
    "s_cbranch_scc1 .Lh47_%=\n\t" \
    "s_cmp_lg_u32 s24, 0\n\t" \
    "s_cbranch_scc1 .Lb15_%=\n\t" \
    "s_mov_b32 s40, 0\n\t" \
    "s_add_u32 s38, s33, 524288\n\t" \
    ".Ll15_%=:\n\t" \
    "buffer_load_dword v1, v51, s[4:7], s38 offen sc1\n\t" \
    "s_waitcnt vmcnt(0)\n\t" \
    "v_cmp_ne_u32 vcc, s34, v1\n\t" \
    "s_add_u32 s40, s40, 1\n\t" \
    "s_cmp_ge_u32 s40, 8192\n\t" \
    "s_cbranch_scc1 .Lb15_%=\n\t" \
    "s_cbranch_vccz .Lb15_%=\n\t" \
    "s_sleep 2\n\t" \
    "s_branch .Ll15_%=\n\t" \
    ".Lb15_%=:\n\t" \
    "s_waitcnt lgkmcnt(0)\n\t" \
    "s_barrier\n\t" \
    "buffer_load_dwordx4 v[176:179], v36, s[4:7], s48 offen offset:0 sc1\n\t" \
    "buffer_load_dwordx4 v[180:183], v36, s[4:7], s48 offen offset:1024 sc1\n\t" \
    "buffer_load_dwordx4 v[184:187], v36, s[4:7], s48 offen offset:256 sc1\n\t" \
    "buffer_load_dwordx4 v[188:191], v36, s[4:7], s48 offen offset:1280 sc1\n\t" \
    "buffer_load_dwordx4 v[60:63], v37, s[4:7], s48 offen offset:0 sc1\n\t" \
    "s_cmp_lt_u32 s24, 4\n\t" \
    "s_cbranch_scc1 .Lp48_%=\n\t" \
    "s_setprio 1\n\t" \
    ".Lp48_%=:\n\t" \
    "v_add_u32 v11, 344064, v39\n\t" \
    "v_lshrrev_b32 v3, 21, v11\n\t" \
    "v_bfe_u32 v3, s0, v3, 1\n\t" \
    "v_lshrrev_b32 v9, 3, v11\n\t" \
    "v_lshlrev_b32 v3, 10, v3\n\t" \
    "v_and_b32 v9, 0x400, v9\n\t" \
    "v_xor_b32 v3, v3, v9\n\t" \
    "v_xor_b32 v8, v42, v3\n\t" \
    "v_lshrrev_b32 v3, 21, v39\n\t" \
    "v_bfe_u32 v3, s0, v3, 1\n\t" \
    "v_lshrrev_b32 v9, 3, v39\n\t" \
    "v_lshlrev_b32 v3, 10, v3\n\t" \
    "v_and_b32 v9, 0x400, v9\n\t" \
    "v_xor_b32 v3, v3, v9\n\t" \
    "v_xor_b32 v42, v42, v3\n\t" \
    "s_lshr_b32 s37, s10, 1\n\t" \
    "s_add_u32 s37, s37, s51\n\t" \
    "s_and_b32 s39, s8, 0x1fffff\n\t" \
    "s_add_u32 s37, s37, s39\n\t" \
    "s_lshr_b32 s39, s37, 21\n\t" \
    "s_lshr_b32 s39, s0, s39\n\t" \
    "s_xor_b32 s37, s37, s48\n\t" \
    "s_lshr_b32 s37, s37, 13\n\t" \
    "s_xor_b32 s37, s37, s39\n\t" \
    "s_and_b32 s37, s37, 1\n\t" \
    "s_mul_i32 s37, s37, 172032\n\t" \
    "s_add_u32 s37, s37, s51\n\t" \
    "s_add_u32 s8, s8, s37\n\t" \
    "s_addc_u32 s9, s9, 0\n\t" \
    "s_nop 0\n\t" \
    "v_readlane_b32 s12, v50, 0\n\t" \
    "v_readlane_b32 s13, v50, 1\n\t" \
    "v_readlane_b32 s14, v50, 2\n\t" \
    "v_readlane_b32 s15, v50, 3\n\t" \
    "v_readlane_b32 s16, v50, 4\n\t" \
    "v_readlane_b32 s17, v50, 5\n\t" \
    "v_readlane_b32 s18, v50, 6\n\t" \
    "v_readlane_b32 s19, v50, 7\n\t" \
    "v_readlane_b32 s3, v50, 8\n\t" \
    "s_add_u32 s12, s12, s33\n\t" \
    "s_add_u32 s13, s13, s33\n\t" \
    "s_add_u32 s14, s14, s33\n\t" \
    "s_add_u32 s15, s15, s33\n\t" \
    "s_add_u32 s16, s16, s33\n\t" \
    "s_add_u32 s17, s17, s33\n\t" \
    "s_add_u32 s18, s18, s33\n\t" \
    "s_add_u32 s19, s19, s33\n\t" \
    "s_add_u32 s3, s3, s33\n\t" \
    "s_waitcnt vmcnt(4)\n\t" \
    "v_mfma_f32_32x32x16_bf16 v[192:207], v[52:55], v[176:179], 0\n\t" \
    "buffer_load_dwordx4 v[4:7], v36, s[4:7], s49 offen offset:0 sc1\n\t" \
    "s_waitcnt vmcnt(4)\n\t" \
    "v_mfma_f32_32x32x16_bf16 v[192:207], v[56:59], v[180:183], v[192:207]\n\t" \
    "buffer_load_dwordx4 v[20:23], v36, s[4:7], s49 offen offset:1024 sc1\n\t" \
    "s_waitcnt vmcnt(4)\n\t" \
    "v_mfma_f32_32x32x16_bf16 v[208:223], v[52:55], v[184:187], 0\n\t" \
    "buffer_load_dwordx4 v[64:67], v36, s[4:7], s49 offen offset:256 sc1\n\t" \
    "s_waitcnt vmcnt(4)\n\t" \
    "v_mfma_f32_32x32x16_bf16 v[208:223], v[56:59], v[188:191], v[208:223]\n\t" \
    "buffer_load_dwordx4 v[68:71], v36, s[4:7], s49 offen offset:1280 sc1\n\t" \
    "v_mfma_f32_32x32x16_bf16 v[224:239], v[56:59], v[176:179], 0\n\t" \
    "s_add_u32 m0, s44, 0\n\t" \
    "s_nop 8\n\t" \
    "v_cvt_pk_bf16_f32 v51, v192, v208\n\t" \
    "v_cvt_pk_bf16_f32 v1, v193, v209\n\t" \
    "v_cvt_pk_bf16_f32 v11, v194, v210\n\t" \
    "v_cvt_pk_bf16_f32 v39, v195, v211\n\t" \
    "v_cvt_pk_bf16_f32 v9, v196, v212\n\t" \
    "s_waitcnt vmcnt(4)\n\t" \
    "v_permlane16_swap_b32 v60, v180\n\t" \
    "v_permlane16_swap_b32 v61, v181\n\t" \
    "ds_write_addtid_b32 v51 offset:3616\n\t" \
    "ds_write_addtid_b32 v1 offset:3872\n\t" \
    "ds_write_addtid_b32 v11 offset:4128\n\t" \
    "ds_write_addtid_b32 v39 offset:4384\n\t" \
    "ds_write_addtid_b32 v9 offset:4640\n\t" \
    "v_permlane16_swap_b32 v62, v182\n\t" \
    "v_permlane16_swap_b32 v63, v183\n\t" \
    "v_cvt_pk_bf16_f32 v3, v197, v213\n\t" \
    "v_cvt_pk_bf16_f32 v51, v198, v214\n\t" \
    "v_cvt_pk_bf16_f32 v1, v199, v215\n\t" \
    "v_cvt_pk_bf16_f32 v11, v200, v216\n\t" \
    "v_cvt_pk_bf16_f32 v39, v201, v217\n\t" \
    "v_mfma_f32_32x32x16_bf16 v[240:255], v[56:59], v[184:187], 0\n\t" \
    "v_permlane16_swap_b32 v180, v188\n\t" \
    "buffer_load_dwordx4 v[72:75], v37, s[4:7], s49 offen offset:0 sc1\n\t" \
    "ds_write_addtid_b32 v3 offset:4896\n\t" \
    "ds_write_addtid_b32 v51 offset:5152\n\t" \
    "ds_write_addtid_b32 v1 offset:5408\n\t" \
    "ds_write_addtid_b32 v11 offset:5664\n\t" \
    "ds_write_addtid_b32 v39 offset:5920\n\t" \
    "v_permlane16_swap_b32 v181, v189\n\t" \
    "v_permlane16_swap_b32 v182, v190\n\t" \
    "v_cvt_pk_bf16_f32 v9, v202, v218\n\t" \
    "v_cvt_pk_bf16_f32 v3, v203, v219\n\t" \
    "v_cvt_pk_bf16_f32 v51, v204, v220\n\t" \
    "v_cvt_pk_bf16_f32 v1, v205, v221\n\t" \
    "v_permlane16_swap_b32 v183, v191\n\t" \
    "v_mfma_f32_32x32x16_bf16 v[224:239], v[52:55], v[60:63], v[224:239]\n\t" \
    "ds_write_addtid_b32 v9 offset:6176\n\t" \
    "ds_write_addtid_b32 v3 offset:6432\n\t" \
    "ds_write_addtid_b32 v51 offset:6688\n\t" \
    "ds_write_addtid_b32 v1 offset:6944\n\t" \
    "v_mfma_f32_32x32x16_bf16 v[240:255], v[52:55], v[180:183], v[240:255]\n\t" \
    ".Lh49_%=:\n\t" \
    "s_memrealtime s[22:23]\n\t" \
    "s_waitcnt lgkmcnt(0)\n\t" \
    "s_sub_u32 s22, s22, s35\n\t" \
    "s_cmp_lt_u32 s22, 597\n\t" \
    "s_cbranch_scc1 .Lh49_%=\n\t" \
    "s_buffer_load_dword s12, s[4:7], s12 offset:0xc0000\n\t" \
    "s_buffer_load_dword s13, s[4:7], s13 offset:0xc0000\n\t" \
    "s_buffer_load_dword s14, s[4:7], s14 offset:0xc0000\n\t" \
    "s_buffer_load_dword s15, s[4:7], s15 offset:0xc0000\n\t" \
    "s_buffer_load_dword s16, s[4:7], s16 offset:0xc0000\n\t" \
    "s_buffer_load_dword s17, s[4:7], s17 offset:0xc0000\n\t" \
    "s_buffer_load_dword s18, s[4:7], s18 offset:0xc0000\n\t" \
    "s_buffer_load_dword s19, s[4:7], s19 offset:0xc0000\n\t" \
    "s_buffer_load_dword s3, s[4:7], s3 offset:0xc0000\n\t" \
    "s_waitcnt vmcnt(4)\n\t" \
    "v_mfma_f32_32x32x16_bf16 v[192:207], v[52:55], v[4:7], 0\n\t" \
    "v_cvt_pk_bf16_f32 v11, v224, v240\n\t" \
    "v_cvt_pk_bf16_f32 v39, v225, v241\n\t" \
    "v_cvt_pk_bf16_f32 v9, v226, v242\n\t" \
    "v_cvt_pk_bf16_f32 v3, v227, v243\n\t" \
    "v_cvt_pk_bf16_f32 v51, v228, v244\n\t" \
    "buffer_load_dwordx4 v[76:79], v36, s[4:7], s50 offen offset:0 sc1\n\t" \
    "ds_write_addtid_b32 v11 offset:0\n\t" \
    "ds_write_addtid_b32 v39 offset:256\n\t" \
    "ds_write_addtid_b32 v9 offset:512\n\t" \
    "ds_write_addtid_b32 v3 offset:768\n\t" \
    "ds_write_addtid_b32 v51 offset:1024\n\t" \
    "s_waitcnt vmcnt(4)\n\t" \
    "v_mfma_f32_32x32x16_bf16 v[192:207], v[56:59], v[20:23], v[192:207]\n\t" \
    "buffer_load_dwordx4 v[80:83], v36, s[4:7], s50 offen offset:1024 sc1\n\t" \
    "v_cvt_pk_bf16_f32 v1, v229, v245\n\t" \
    "v_cvt_pk_bf16_f32 v11, v230, v246\n\t" \
    "v_cvt_pk_bf16_f32 v39, v231, v247\n\t" \
    "v_cvt_pk_bf16_f32 v9, v232, v248\n\t" \
    "v_cvt_pk_bf16_f32 v3, v233, v249\n\t" \
    "s_waitcnt vmcnt(4)\n\t" \
    "v_mfma_f32_32x32x16_bf16 v[208:223], v[52:55], v[64:67], 0\n\t" \
    "buffer_load_dwordx4 v[100:103], v36, s[4:7], s50 offen offset:256 sc1\n\t" \
    "ds_write_addtid_b32 v1 offset:1280\n\t" \
    "ds_write_addtid_b32 v11 offset:1536\n\t" \
    "ds_write_addtid_b32 v39 offset:1792\n\t" \
    "ds_write_addtid_b32 v9 offset:2048\n\t" \
    "ds_write_addtid_b32 v3 offset:2304\n\t" \
    "v_cvt_pk_bf16_f32 v51, v234, v250\n\t" \
    "v_cvt_pk_bf16_f32 v12, v235, v251\n\t" \
    "v_cvt_pk_bf16_f32 v14, v236, v252\n\t" \
    "v_cvt_pk_bf16_f32 v13, v237, v253\n\t" \
    "buffer_load_dwordx4 v[104:107], v36, s[4:7], s50 offen offset:1280 sc1\n\t" \
    "s_waitcnt vmcnt(5)\n\t" \
    "v_mfma_f32_32x32x16_bf16 v[208:223], v[56:59], v[68:71], v[208:223]\n\t" \
    "ds_write_addtid_b32 v51 offset:2560\n\t" \
    "ds_write_addtid_b32 v12 offset:2816\n\t" \
    "ds_write_addtid_b32 v14 offset:3072\n\t" \
    "ds_write_addtid_b32 v13 offset:3328\n\t" \
    "buffer_load_dwordx4 v[88:91], v37, s[4:7], s50 offen offset:0 sc1\n\t" \
    "s_waitcnt lgkmcnt(0)\n\t" \
    "s_xor_b32 s12, s12, s34\n\t" \
    "s_xor_b32 s13, s13, s34\n\t" \
    "s_xor_b32 s14, s14, s34\n\t" \
    "s_xor_b32 s15, s15, s34\n\t" \
    "s_xor_b32 s16, s16, s34\n\t" \
    "s_xor_b32 s17, s17, s34\n\t" \
    "s_xor_b32 s18, s18, s34\n\t" \
    "s_xor_b32 s19, s19, s34\n\t" \
    "s_xor_b32 s3, s3, s34\n\t" \
    "s_or_b32 s12, s12, s13\n\t" \
    "s_or_b32 s12, s12, s14\n\t" \
    "s_or_b32 s12, s12, s15\n\t" \
    "s_or_b32 s12, s12, s16\n\t" \
    "s_or_b32 s12, s12, s17\n\t" \
    "s_or_b32 s12, s12, s18\n\t" \
    "s_or_b32 s12, s12, s19\n\t" \
    "s_or_b32 s12, s12, s3\n\t" \
    "s_cbranch_scc1 .Lq1025_%=\n\t" \
    ".Lb24_%=:\n\t" \
    "s_waitcnt lgkmcnt(0)\n\t" \
    "s_barrier\n\t" \
    "ds_read_b128 v[92:95], v44 offset:0\n\t" \
    "s_mov_b32 s45, 0\n\t" \
    "buffer_load_dwordx4 v[108:111], v41, s[4:7], s45 offen offset:0 sc1\n\t" \
    "ds_read_b128 v[112:115], v44 offset:128\n\t" \
    "ds_read_b128 v[84:87], v44 offset:29184\n\t" \
    "ds_read_b128 v[96:99], v44 offset:29312\n\t" \
    "ds_read_b128 v[120:123], v45 offset:0\n\t" \
    "buffer_load_dwordx4 v[124:127], v41, s[4:7], s45 offen offset:1024 sc1\n\t" \
    "ds_read_b128 v[116:119], v45 offset:128\n\t" \
    "ds_read_b128 v[128:131], v45 offset:29184\n\t" \
    "ds_read_b128 v[132:135], v45 offset:29312\n\t" \
    "v_mfma_f32_32x32x16_bf16 v[224:239], v[56:59], v[4:7], 0\n\t" \
    "v_cvt_pk_bf16_f32 v11, v192, v208\n\t" \
    "v_cvt_pk_bf16_f32 v39, v193, v209\n\t" \
    "v_cvt_pk_bf16_f32 v9, v194, v210\n\t" \
    "v_cvt_pk_bf16_f32 v3, v195, v211\n\t" \
    "v_cvt_pk_bf16_f32 v36, v196, v212\n\t" \
    "s_waitcnt vmcnt(7)\n\t" \
    "v_permlane16_swap_b32 v72, v20\n\t" \
    "v_permlane16_swap_b32 v73, v21\n\t" \
    "s_mov_b32 s46, 114688\n\t" \
    "buffer_load_dwordx4 v[28:31], v41, s[4:7], s46 offen offset:0 sc1\n\t" \
    "ds_write_addtid_b32 v11 offset:61984\n\t" \
    "ds_write_addtid_b32 v39 offset:62240\n\t" \
    "ds_write_addtid_b32 v9 offset:62496\n\t" \
    "ds_write_addtid_b32 v3 offset:62752\n\t" \
    "ds_write_addtid_b32 v36 offset:63008\n\t" \
    "v_permlane16_swap_b32 v74, v22\n\t" \
    "v_permlane16_swap_b32 v75, v23\n\t" \
    "v_cvt_pk_bf16_f32 v51, v197, v213\n\t" \
    "v_cvt_pk_bf16_f32 v37, v198, v214\n\t" \
    "v_cvt_pk_bf16_f32 v50, v199, v215\n\t" \
    "v_cvt_pk_bf16_f32 v1, v200, v216\n\t" \
    "v_cvt_pk_bf16_f32 v11, v201, v217\n\t" \
    "v_mfma_f32_32x32x16_bf16 v[240:255], v[56:59], v[64:67], 0\n\t" \
    "v_permlane16_swap_b32 v20, v68\n\t" \
    "buffer_load_dwordx4 v[136:139], v41, s[4:7], s46 offen offset:1024 sc1\n\t" \
    "ds_write_addtid_b32 v51 offset:63264\n\t" \
    "ds_write_addtid_b32 v37 offset:63520\n\t" \
    "ds_write_addtid_b32 v50 offset:63776\n\t" \
    "ds_write_addtid_b32 v1 offset:64032\n\t" \
    "ds_write_addtid_b32 v11 offset:64288\n\t" \
    "v_permlane16_swap_b32 v21, v69\n\t" \
    "v_permlane16_swap_b32 v22, v70\n\t" \
    "v_cvt_pk_bf16_f32 v39, v202, v218\n\t" \
    "v_cvt_pk_bf16_f32 v9, v203, v219\n\t" \
    "v_cvt_pk_bf16_f32 v3, v204, v220\n\t" \
    "v_cvt_pk_bf16_f32 v36, v205, v221\n\t" \
    "v_permlane16_swap_b32 v23, v71\n\t" \
    "v_mfma_f32_32x32x16_bf16 v[224:239], v[52:55], v[72:75], v[224:239]\n\t" \
    "buffer_load_dwordx4 v[24:27], v46, s[4:7], s45 offen offset:0 sc1\n\t" \
    "ds_write_addtid_b32 v39 offset:64544\n\t" \
    "ds_write_addtid_b32 v9 offset:64800\n\t" \
    "ds_write_addtid_b32 v3 offset:65056\n\t" \
    "ds_write_addtid_b32 v36 offset:65312\n\t" \
    "v_mfma_f32_32x32x16_bf16 v[240:255], v[52:55], v[20:23], v[240:255]\n\t" \
    "s_waitcnt vmcnt(9)\n\t" \
    "v_mfma_f32_32x32x16_bf16 v[192:207], v[52:55], v[76:79], 0\n\t" \
    "s_nop 9\n\t" \
    "v_cvt_pk_bf16_f32 v51, v224, v240\n\t" \
    "v_cvt_pk_bf16_f32 v37, v225, v241\n\t" \
    "v_cvt_pk_bf16_f32 v50, v226, v242\n\t" \
    "v_cvt_pk_bf16_f32 v1, v227, v243\n\t" \
    "v_cvt_pk_bf16_f32 v11, v228, v244\n\t" \
    "buffer_load_dwordx4 v[32:35], v46, s[4:7], s46 offen offset:0 sc1\n\t" \
    "ds_write_addtid_b32 v51 offset:58368\n\t" \
    "ds_write_addtid_b32 v37 offset:58624\n\t" \
    "ds_write_addtid_b32 v50 offset:58880\n\t" \
    "ds_write_addtid_b32 v1 offset:59136\n\t" \
    "ds_write_addtid_b32 v11 offset:59392\n\t" \
    "s_waitcnt vmcnt(9)\n\t" \
    "v_mfma_f32_32x32x16_bf16 v[192:207], v[56:59], v[80:83], v[192:207]\n\t" \
    "v_cvt_pk_bf16_f32 v39, v229, v245\n\t" \
    "v_cvt_pk_bf16_f32 v9, v230, v246\n\t" \
    "v_cvt_pk_bf16_f32 v3, v231, v247\n\t" \
    "v_cvt_pk_bf16_f32 v36, v232, v248\n\t" \
    "v_cvt_pk_bf16_f32 v51, v233, v249\n\t" \
    "s_mov_b32 s47, 229376\n\t" \
    "buffer_load_dwordx4 v[140:143], v41, s[4:7], s47 offen offset:0 sc1\n\t" \
    "s_waitcnt vmcnt(9)\n\t" \
    "v_mfma_f32_32x32x16_bf16 v[208:223], v[52:55], v[100:103], 0\n\t" \
    "ds_write_addtid_b32 v39 offset:59648\n\t" \
    "ds_write_addtid_b32 v9 offset:59904\n\t" \
    "ds_write_addtid_b32 v3 offset:60160\n\t" \
    "ds_write_addtid_b32 v36 offset:60416\n\t" \
    "ds_write_addtid_b32 v51 offset:60672\n\t" \
    "buffer_load_dwordx4 v[156:159], v41, s[4:7], s47 offen offset:1024 sc1\n\t" \
    "v_cvt_pk_bf16_f32 v37, v234, v250\n\t" \
    "v_cvt_pk_bf16_f32 v50, v235, v251\n\t" \
    "v_cvt_pk_bf16_f32 v1, v236, v252\n\t" \
    "v_cvt_pk_bf16_f32 v11, v237, v253\n\t" \
    "s_waitcnt vmcnt(9)\n\t" \
    "v_mfma_f32_32x32x16_bf16 v[208:223], v[56:59], v[104:107], v[208:223]\n\t" \
    "ds_write_addtid_b32 v37 offset:60928\n\t" \
    "ds_write_addtid_b32 v50 offset:61184\n\t" \
    "ds_write_addtid_b32 v1 offset:61440\n\t" \
    "ds_write_addtid_b32 v11 offset:61696\n\t" \
    "buffer_load_dwordx4 v[168:171], v46, s[4:7], s47 offen offset:0 sc1\n\t" \
    "s_waitcnt vmcnt(9)\n\t" \
    "; wait\n\t" \
    "s_waitcnt lgkmcnt(0)\n\t" \
    "s_barrier\n\t" \
    "s_memrealtime s[64:65]\n\t" \
    "s_cmp_lg_u32 s24, 7\n\t" \
    "s_cbranch_scc1 .Lar_%=\n\t" \
    "s_waitcnt lgkmcnt(0)\n\t" \
    "v_mov_b32 v149, s64\n\t" \
    "s_and_b32 s37, s48, 8192\n\t" \
    "s_add_u32 s37, s37, s32\n\t" \
    "s_add_u32 s37, s37, 1048576\n\t" \
    "s_mov_b64 exec, 0xff\n\t" \
    "buffer_store_dwordx4 v[148:151], v0, s[4:7], s37 offen sc1\n\t" \
    "s_mov_b64 exec, -1\n\t" \
    ".Lar_%=:\n\t" \
    "v_add_u32 v44, 58368, v44\n\t" \
    "s_bitcmp1_b32 s24, 2\n\t" \
    "s_cbranch_scc1 .Ly29_%=\n\t" \
    "buffer_load_dwordx4 v[172:175], v46, s[4:7], s45 offen offset:1024 sc1\n\t" \
    ".Ly29_%=:\n\t" \
    "v_add_u32 v45, 58368, v45\n\t" \
    "ds_read_b128 v[144:147], v44 offset:0\n\t" \
    "ds_read_b128 v[152:155], v44 offset:128\n\t" \
    "ds_read_b128 v[160:163], v44 offset:29184\n\t" \
    "ds_read_b128 v[164:167], v44 offset:29312\n\t" \
    "s_bitcmp1_b32 s24, 2\n\t" \
    "s_cbranch_scc1 .Ly30_%=\n\t" \
    "buffer_load_dwordx4 v[16:19], v46, s[4:7], s46 offen offset:1024 sc1\n\t" \
    ".Ly30_%=:\n\t" \
    "ds_read_b128 v[176:179], v45 offset:0\n\t" \
    "ds_read_b128 v[184:187], v45 offset:128\n\t" \
    "ds_read_b128 v[188:191], v45 offset:29184\n\t" \
    "ds_read_b128 v[60:63], v45 offset:29312\n\t" \
    "v_mfma_f32_32x32x16_bf16 v[224:239], v[56:59], v[76:79], 0\n\t" \
    "v_cvt_pk_bf16_f32 v39, v192, v208\n\t" \
    "v_cvt_pk_bf16_f32 v9, v193, v209\n\t" \
    "v_cvt_pk_bf16_f32 v3, v194, v210\n\t" \
    "v_cvt_pk_bf16_f32 v36, v195, v211\n\t" \
    "v_cvt_pk_bf16_f32 v51, v196, v212\n\t" \
    "s_waitcnt vmcnt(8)\n\t" \
    "v_mfma_f32_16x16x32_bf16 v[180:183], v[108:111], v[92:95], 0\n\t" \
    "v_permlane16_swap_b32 v88, v80\n\t" \
    "s_bitcmp1_b32 s24, 2\n\t" \
    "s_cbranch_scc1 .Ly31_%=\n\t" \
    "buffer_load_dwordx4 v[12:15], v46, s[4:7], s47 offen offset:1024 sc1\n\t" \
    ".Ly31_%=:\n\t" \
    "v_permlane16_swap_b32 v89, v81\n\t" \
    "ds_write_addtid_b32 v39 offset:3616\n\t" \
    "ds_write_addtid_b32 v9 offset:3872\n\t" \
    "ds_write_addtid_b32 v3 offset:4128\n\t" \
    "ds_write_addtid_b32 v36 offset:4384\n\t" \
    "ds_write_addtid_b32 v51 offset:4640\n\t" \
    "v_mfma_f32_16x16x32_bf16 v[4:7], v[108:111], v[112:115], 0\n\t" \
    "v_permlane16_swap_b32 v90, v82\n\t" \
    "v_mfma_f32_16x16x32_bf16 v[64:67], v[108:111], v[84:87], 0\n\t" \
    "v_permlane16_swap_b32 v91, v83\n\t" \
    "v_cvt_pk_bf16_f32 v37, v197, v213\n\t" \
    "v_cvt_pk_bf16_f32 v50, v198, v214\n\t" \
    "v_cvt_pk_bf16_f32 v1, v199, v215\n\t" \
    "v_cvt_pk_bf16_f32 v11, v200, v216\n\t" \
    "v_cvt_pk_bf16_f32 v39, v201, v217\n\t" \
    "s_mov_b32 s45, 344064\n\t" \
    "buffer_load_dwordx4 v[68:71], v41, s[4:7], s45 offen offset:0 sc1\n\t" \
    "v_mfma_f32_32x32x16_bf16 v[240:255], v[56:59], v[100:103], 0\n\t" \
    "v_mfma_f32_16x16x32_bf16 v[72:75], v[108:111], v[96:99], 0\n\t" \
    "v_permlane16_swap_b32 v80, v104\n\t" \
    "ds_write_addtid_b32 v37 offset:4896\n\t" \
    "ds_write_addtid_b32 v50 offset:5152\n\t" \
    "ds_write_addtid_b32 v1 offset:5408\n\t" \
    "ds_write_addtid_b32 v11 offset:5664\n\t" \
    "ds_write_addtid_b32 v39 offset:5920\n\t" \
    "s_waitcnt vmcnt(8)\n\t" \
    "v_mfma_f32_16x16x32_bf16 v[20:23], v[124:127], v[112:115], 0\n\t" \
    "v_permlane16_swap_b32 v81, v105\n\t" \
    "buffer_load_dwordx4 v[148:151], v41, s[4:7], s45 offen offset:1024 sc1\n\t" \
    "v_permlane16_swap_b32 v82, v106\n\t" \
    "v_cvt_pk_bf16_f32 v9, v202, v218\n\t" \
    "v_cvt_pk_bf16_f32 v3, v203, v219\n\t" \
    "v_cvt_pk_bf16_f32 v36, v204, v220\n\t" \
    "v_cvt_pk_bf16_f32 v51, v205, v221\n\t" \
    "v_mfma_f32_16x16x32_bf16 v[4:7], v[124:127], v[92:95], v[4:7]\n\t" \
    "v_permlane16_swap_b32 v83, v107\n\t" \
    "v_mfma_f32_16x16x32_bf16 v[76:79], v[124:127], v[96:99], 0\n\t" \
    "v_mfma_f32_32x32x16_bf16 v[224:239], v[52:55], v[88:91], v[224:239]\n\t" \
    "ds_write_addtid_b32 v9 offset:6176\n\t" \
    "ds_write_addtid_b32 v3 offset:6432\n\t" \
    "ds_write_addtid_b32 v36 offset:6688\n\t" \
    "ds_write_addtid_b32 v51 offset:6944\n\t" \
    "s_mov_b32 s46, 458752\n\t" \
    "buffer_load_dwordx4 v[56:59], v41, s[4:7], s46 offen offset:0 sc1\n\t" \
    "v_mfma_f32_32x32x16_bf16 v[240:255], v[52:55], v[80:83], v[240:255]\n\t" \
    "v_mfma_f32_16x16x32_bf16 v[72:75], v[124:127], v[84:87], v[72:75]\n\t" \
    "s_nop 10\n\t" \
    "v_cvt_pk_bf16_f32 v37, v224, v240\n\t" \
    "v_cvt_pk_bf16_f32 v50, v225, v241\n\t" \
    "v_cvt_pk_bf16_f32 v1, v226, v242\n\t" \
    "v_cvt_pk_bf16_f32 v11, v227, v243\n\t" \
    "v_cvt_pk_bf16_f32 v39, v228, v244\n\t" \
    "s_waitcnt vmcnt(9) lgkmcnt(15)\n\t" \
    "v_mfma_f32_16x16x32_bf16 v[180:183], v[28:31], v[144:147], v[180:183]\n\t" \
    "buffer_load_dwordx4 v[100:103], v41, s[4:7], s46 offen offset:1024 sc1\n\t" \
    "v_mfma_f32_16x16x32_bf16 v[4:7], v[28:31], v[152:155], v[4:7]\n\t" \
    "ds_write_addtid_b32 v37 offset:0\n\t" \
    "ds_write_addtid_b32 v50 offset:256\n\t" \
    "ds_write_addtid_b32 v1 offset:512\n\t" \
    "ds_write_addtid_b32 v11 offset:768\n\t" \
    "ds_write_addtid_b32 v39 offset:1024\n\t" \
    "v_mfma_f32_16x16x32_bf16 v[64:67], v[28:31], v[160:163], v[64:67]\n\t" \
    "v_cvt_pk_bf16_f32 v9, v229, v245\n\t" \
    "v_cvt_pk_bf16_f32 v3, v230, v246\n\t" \
    "v_cvt_pk_bf16_f32 v36, v231, v247\n\t" \
    "v_cvt_pk_bf16_f32 v51, v232, v248\n\t" \
    "v_cvt_pk_bf16_f32 v37, v233, v249\n\t" \
    "s_mov_b32 s47, 573440\n\t" \
    "buffer_load_dwordx4 v[108:111], v41, s[4:7], s47 offen offset:0 sc1\n\t" \
    "v_mfma_f32_16x16x32_bf16 v[72:75], v[28:31], v[164:167], v[72:75]\n\t" \
    "ds_write_addtid_b32 v9 offset:1280\n\t" \
    "ds_write_addtid_b32 v3 offset:1536\n\t" \
    "ds_write_addtid_b32 v36 offset:1792\n\t" \
    "ds_write_addtid_b32 v51 offset:2048\n\t" \
    "ds_write_addtid_b32 v37 offset:2304\n\t" \
    "s_waitcnt vmcnt(10)\n\t" \
    "v_mfma_f32_16x16x32_bf16 v[20:23], v[136:139], v[152:155], v[20:23]\n\t" \
    "buffer_load_dwordx4 v[192:195], v41, s[4:7], s47 offen offset:1024 sc1\n\t" \
    "v_mfma_f32_16x16x32_bf16 v[4:7], v[136:139], v[144:147], v[4:7]\n\t" \
    "v_cvt_pk_bf16_f32 v50, v234, v250\n\t" \
    "v_cvt_pk_bf16_f32 v1, v235, v251\n\t" \
    "v_cvt_pk_bf16_f32 v11, v236, v252\n\t" \
    "v_cvt_pk_bf16_f32 v39, v237, v253\n\t" \
    "v_mfma_f32_16x16x32_bf16 v[76:79], v[136:139], v[164:167], v[76:79]\n\t" \
    "ds_write_addtid_b32 v50 offset:2560\n\t" \
    "ds_write_addtid_b32 v1 offset:2816\n\t" \
    "ds_write_addtid_b32 v11 offset:3072\n\t" \
    "ds_write_addtid_b32 v39 offset:3328\n\t" \
    "buffer_load_dwordx4 v[196:199], v46, s[4:7], s45 offen offset:0 sc1\n\t" \
    "v_mfma_f32_16x16x32_bf16 v[72:75], v[136:139], v[160:163], v[72:75]\n\t" \
    "s_waitcnt lgkmcnt(0)\n\t" \
    "s_barrier\n\t" \
    "ds_read_b128 v[200:203], v10 offset:2048\n\t" \
    "v_mov_b32 v9, 0x10000\n\t" \
    "s_waitcnt vmcnt(11)\n\t" \
    "v_mfma_f32_16x16x32_bf16 v[204:207], v[24:27], v[120:123], 0\n\t" \
    "v_subrev_u32 v44, 58368, v44\n\t" \
    "v_mfma_f32_16x16x32_bf16 v[208:211], v[24:27], v[116:119], 0\n\t" \
    "v_subrev_u32 v45, 58368, v45\n\t" \
    "v_mfma_f32_16x16x32_bf16 v[212:215], v[24:27], v[128:131], 0\n\t" \
    "ds_read_b128 v[216:219], v44 offset:0\n\t" \
    "buffer_load_dwordx4 v[220:223], v46, s[4:7], s46 offen offset:0 sc1\n\t" \
    "v_mfma_f32_16x16x32_bf16 v[104:107], v[24:27], v[132:135], 0\n\t" \
    "ds_read_b128 v[88:91], v44 offset:128\n\t" \
    "s_waitcnt vmcnt(11)\n\t" \
    "v_mfma_f32_16x16x32_bf16 v[204:207], v[32:35], v[176:179], v[204:207]\n\t" \
    "ds_read_b128 v[52:55], v44 offset:29184\n\t" \
    "v_mfma_f32_16x16x32_bf16 v[208:211], v[32:35], v[184:187], v[208:211]\n\t" \
    "ds_read_b128 v[80:83], v44 offset:29312\n\t" \
    "buffer_load_dwordx4 v[124:127], v46, s[4:7], s47 offen offset:0 sc1\n\t" \
    "v_mfma_f32_16x16x32_bf16 v[212:215], v[32:35], v[188:191], v[212:215]\n\t" \
    "ds_read_b128 v[28:31], v45 offset:0\n\t" \
    "v_mfma_f32_16x16x32_bf16 v[104:107], v[32:35], v[60:63], v[104:107]\n\t" \
    "ds_read_b128 v[224:227], v45 offset:128\n\t" \
    "s_waitcnt vmcnt(11) lgkmcnt(5)\n\t" \
    "v_mfma_f32_16x16x32_bf16 v[180:183], v[140:143], v[216:219], v[180:183]\n\t" \
    "ds_read_b128 v[228:231], v45 offset:29184\n\t" \
    "s_bitcmp1_b32 s24, 2\n\t" \
    "s_cbranch_scc1 .Ly32_%=\n\t" \
    "buffer_load_dwordx4 v[232:235], v46, s[4:7], s45 offen offset:1024 sc1\n\t" \
    ".Ly32_%=:\n\t" \
    "s_waitcnt lgkmcnt(5)\n\t" \
    "v_mfma_f32_16x16x32_bf16 v[4:7], v[140:143], v[88:91], v[4:7]\n\t" \
    "ds_read_b128 v[236:239], v45 offset:29312\n\t" \
    "s_waitcnt lgkmcnt(5)\n\t" \
    "v_mfma_f32_16x16x32_bf16 v[64:67], v[140:143], v[52:55], v[64:67]\n\t" \
    "s_waitcnt lgkmcnt(4)\n\t" \
    "v_mfma_f32_16x16x32_bf16 v[72:75], v[140:143], v[80:83], v[72:75]\n\t" \
    "s_bitcmp1_b32 s24, 2\n\t" \
    "s_cbranch_scc1 .Ly33_%=\n\t" \
    "buffer_load_dwordx4 v[240:243], v46, s[4:7], s46 offen offset:1024 sc1\n\t" \
    ".Ly33_%=:\n\t" \
    "s_waitcnt vmcnt(10)\n\t" \
    "v_mfma_f32_16x16x32_bf16 v[20:23], v[156:159], v[88:91], v[20:23]\n\t" \
    "s_waitcnt lgkmcnt(0)\n\t" \
    "s_add_u32 s12, s55, 34603008\n\t" \
    "s_buffer_load_dword s13, s[4:7], s12\n\t" \
    "s_buffer_load_dword s14, s[4:7], s12 offset:0x400\n\t" \
    "v_mfma_f32_16x16x32_bf16 v[4:7], v[156:159], v[216:219], v[4:7]\n\t" \
    "v_mfma_f32_16x16x32_bf16 v[76:79], v[156:159], v[80:83], v[76:79]\n\t" \
    "s_bitcmp1_b32 s24, 2\n\t" \
    "s_cbranch_scc1 .Ly34_%=\n\t" \
    "buffer_load_dwordx4 v[244:247], v46, s[4:7], s47 offen offset:1024 sc1\n\t" \
    ".Ly34_%=:\n\t" \
    "v_mfma_f32_16x16x32_bf16 v[72:75], v[156:159], v[52:55], v[72:75]\n\t" \
    "s_waitcnt vmcnt(9)\n\t" \
    "v_mfma_f32_16x16x32_bf16 v[204:207], v[168:171], v[28:31], v[204:207]\n\t" \
    "v_mfma_f32_16x16x32_bf16 v[208:211], v[168:171], v[224:227], v[208:211]\n\t" \
    "s_mov_b32 s45, 688128\n\t" \
    "buffer_load_dwordx4 v[248:251], v41, s[4:7], s45 offen offset:0 sc1\n\t" \
    "v_mfma_f32_16x16x32_bf16 v[212:215], v[168:171], v[228:231], v[212:215]\n\t" \
    "v_sub_f32 v180, v180, v20\n\t" \
    "v_sub_f32 v181, v181, v21\n\t" \
    "v_sub_f32 v182, v182, v22\n\t" \
    "v_mfma_f32_16x16x32_bf16 v[104:107], v[168:171], v[236:239], v[104:107]\n\t" \
    "v_sub_f32 v183, v183, v23\n\t" \
    "v_sub_f32 v64, v64, v76\n\t" \
    "v_sub_f32 v65, v65, v77\n\t" \
    "v_sub_f32 v66, v66, v78\n\t" \
    "v_sub_f32 v67, v67, v79\n\t" \
    "v_cvt_pk_bf16_f32 v254, v4, v5\n\t" \
    "v_cvt_pk_bf16_f32 v255, v6, v7\n\t" \
    "v_cvt_pk_bf16_f32 v252, v180, v181\n\t" \
    "v_cvt_pk_bf16_f32 v253, v182, v183\n\t" \
    "v_cvt_pk_bf16_f32 v138, v72, v73\n\t" \
    "v_cvt_pk_bf16_f32 v139, v74, v75\n\t" \
    "ds_write_b128 v40, v[252:255] offset:58368\n\t" \
    "v_cvt_pk_bf16_f32 v136, v64, v65\n\t" \
    "v_cvt_pk_bf16_f32 v137, v66, v67\n\t" \
    "ds_write_b128 v40, v[136:139] offset:59392\n\t" \
    "s_waitcnt vmcnt(10)\n\t" \
    "s_cmp_ge_u32 s24, 4\n\t" \
    "s_cbranch_scc1 .Lk53_%=\n\t" \
    "v_mfma_f32_16x16x32_bf16 v[24:27], v[172:175], v[116:119], 0\n\t" \
    "v_mfma_f32_16x16x32_bf16 v[24:27], v[16:19], v[184:187], v[24:27]\n\t" \
    "v_mfma_f32_16x16x32_bf16 v[24:27], v[12:15], v[224:227], v[24:27]\n\t" \
    "v_mfma_f32_16x16x32_bf16 v[32:35], v[172:175], v[132:135], 0\n\t" \
    "v_mfma_f32_16x16x32_bf16 v[32:35], v[16:19], v[60:63], v[32:35]\n\t" \
    "v_mfma_f32_16x16x32_bf16 v[32:35], v[12:15], v[236:239], v[32:35]\n\t" \
    "v_mfma_f32_16x16x32_bf16 v[208:211], v[172:175], v[120:123], v[208:211]\n\t" \
    "v_mfma_f32_16x16x32_bf16 v[208:211], v[16:19], v[176:179], v[208:211]\n\t" \
    "v_mfma_f32_16x16x32_bf16 v[208:211], v[12:15], v[28:31], v[208:211]\n\t" \
    "v_mfma_f32_16x16x32_bf16 v[104:107], v[172:175], v[128:131], v[104:107]\n\t" \
    "s_nop 0\n\t" \
    "v_sub_f32 v204, v204, v24\n\t" \
    "v_sub_f32 v205, v205, v25\n\t" \
    "v_mfma_f32_16x16x32_bf16 v[104:107], v[16:19], v[188:191], v[104:107]\n\t" \
    "v_sub_f32 v206, v206, v26\n\t" \
    "v_sub_f32 v207, v207, v27\n\t" \
    "v_mfma_f32_16x16x32_bf16 v[104:107], v[12:15], v[228:231], v[104:107]\n\t" \
    "v_sub_f32 v212, v212, v32\n\t" \
    "v_sub_f32 v213, v213, v33\n\t" \
    "v_sub_f32 v214, v214, v34\n\t" \
    "v_sub_f32 v215, v215, v35\n\t" \
    ".Lk53_%=:\n\t" \
    "s_waitcnt vmcnt(9)\n\t" \
    "v_mfma_f32_16x16x32_bf16 v[140:143], v[68:71], v[92:95], 0\n\t" \
    "buffer_load_dwordx4 v[156:159], v41, s[4:7], s45 offen offset:1024 sc1\n\t" \
    "v_mfma_f32_16x16x32_bf16 v[168:171], v[68:71], v[112:115], 0\n\t" \
    "v_cvt_pk_bf16_f32 v22, v208, v209\n\t" \
    "v_cvt_pk_bf16_f32 v23, v210, v211\n\t" \
    "v_mfma_f32_16x16x32_bf16 v[76:79], v[68:71], v[84:87], 0\n\t" \
    "v_cvt_pk_bf16_f32 v20, v204, v205\n\t" \
    "v_cvt_pk_bf16_f32 v21, v206, v207\n\t" \
    "v_mfma_f32_16x16x32_bf16 v[4:7], v[68:71], v[96:99], 0\n\t" \
    "v_cvt_pk_bf16_f32 v182, v104, v105\n\t" \
    "v_cvt_pk_bf16_f32 v183, v106, v107\n\t" \
    "s_mov_b32 s46, 802816\n\t" \
    "buffer_load_dwordx4 v[72:75], v41, s[4:7], s46 offen offset:0 sc1\n\t" \
    "s_waitcnt vmcnt(10)\n\t" \
    "v_mfma_f32_16x16x32_bf16 v[252:255], v[148:151], v[112:115], 0\n\t" \
    "ds_write_b128 v40, v[20:23] offset:60432\n\t" \
    "v_cvt_pk_bf16_f32 v180, v212, v213\n\t" \
    "v_mfma_f32_16x16x32_bf16 v[168:171], v[148:151], v[92:95], v[168:171]\n\t" \
    "v_cvt_pk_bf16_f32 v181, v214, v215\n\t" \
    "v_mfma_f32_16x16x32_bf16 v[64:67], v[148:151], v[96:99], 0\n\t" \
    "ds_write_b128 v40, v[180:183] offset:61456\n\t" \
    "buffer_load_dwordx4 v[136:139], v41, s[4:7], s46 offen offset:1024 sc1\n\t" \
    "v_mfma_f32_16x16x32_bf16 v[4:7], v[148:151], v[84:87], v[4:7]\n\t" \
    "s_waitcnt vmcnt(10)\n\t" \
    "v_mfma_f32_16x16x32_bf16 v[140:143], v[56:59], v[144:147], v[140:143]\n\t" \
    "v_mfma_f32_16x16x32_bf16 v[168:171], v[56:59], v[152:155], v[168:171]\n\t" \
    "s_waitcnt lgkmcnt(0)\n\t" \
    "s_xor_b32 s13, s13, s34\n\t" \
    "s_xor_b32 s14, s14, s34\n\t" \
    "s_or_b32 s13, s13, s14\n\t" \
    "s_cbranch_scc1 .Lq1028_%=\n\t" \
    ".Lb27_%=:\n\t" \
    "s_waitcnt lgkmcnt(0)\n\t" \
    "s_barrier\n\t" \
    "s_memrealtime s[16:17]\n\t" \
    "ds_read_b64_tr_b16 v[172:173], v49 offset:58368\n\t" \
    "ds_read_b64_tr_b16 v[174:175], v49 offset:62496\n\t" \
    "ds_read_b64_tr_b16 v[16:17], v49 offset:58624\n\t" \
    "ds_read_b64_tr_b16 v[18:19], v49 offset:62752\n\t" \
    "s_mov_b32 s47, 917504\n\t" \
    "buffer_load_dwordx4 v[24:27], v41, s[4:7], s47 offen offset:0 sc1\n\t" \
    "v_mfma_f32_16x16x32_bf16 v[76:79], v[56:59], v[160:163], v[76:79]\n\t" \
    "ds_read_b64_tr_b16 v[12:13], v49 offset:58384\n\t" \
    "ds_read_b64_tr_b16 v[14:15], v49 offset:62512\n\t" \
    "ds_read_b64_tr_b16 v[32:33], v49 offset:58640\n\t" \
    "ds_read_b64_tr_b16 v[34:35], v49 offset:62768\n\t" \
    "v_mfma_f32_16x16x32_bf16 v[4:7], v[56:59], v[164:167], v[4:7]\n\t" \
    "s_waitcnt vmcnt(10)\n\t" \
    "v_mfma_f32_16x16x32_bf16 v[252:255], v[100:103], v[152:155], v[252:255]\n\t" \
    "buffer_load_dwordx4 v[208:211], v41, s[4:7], s47 offen offset:1024 sc1\n\t" \
    "v_mfma_f32_16x16x32_bf16 v[168:171], v[100:103], v[144:147], v[168:171]\n\t" \
    "s_waitcnt lgkmcnt(6)\n\t" \
    "v_mfma_f32_16x16x32_bf16 v[204:207], v[172:175], v[200:203], 0\n\t" \
    "s_waitcnt lgkmcnt(4)\n\t" \
    "v_mfma_f32_16x16x32_bf16 v[68:71], v[16:19], v[200:203], 0\n\t" \
    "v_mfma_f32_16x16x32_bf16 v[64:67], v[100:103], v[164:167], v[64:67]\n\t" \
    "s_waitcnt lgkmcnt(2)\n\t" \
    "v_mfma_f32_16x16x32_bf16 v[104:107], v[12:15], v[200:203], 0\n\t" \
    "s_waitcnt lgkmcnt(0)\n\t" \
    "v_mfma_f32_16x16x32_bf16 v[20:23], v[32:35], v[200:203], 0\n\t" \
    "v_mfma_f32_16x16x32_bf16 v[4:7], v[100:103], v[160:163], v[4:7]\n\t" \
    "s_cmp_lg_u32 s24, 4\n\t" \
    "s_cbranch_scc1 .Lph_%=\n\t" \
    "s_waitcnt lgkmcnt(0)\n\t" \
    "v_mov_b32 v212, s16\n\t" \
    "v_mov_b32 v213, s16\n\t" \
    "v_mov_b32 v214, s16\n\t" \
    "v_mov_b32 v215, s16\n\t" \
    "buffer_store_dwordx4 v[212:215], v2, s[4:7], s32 offen sc1\n\t" \
    "s_nop 0\n\t" \
    ".Lph_%=:\n\t" \
    "buffer_load_dwordx4 v[180:183], v46, s[4:7], s45 offen offset:0 sc1\n\t" \
    "s_waitcnt vmcnt(11)\n\t" \
    "v_mfma_f32_16x16x32_bf16 v[140:143], v[108:111], v[216:219], v[140:143]\n\t" \
    "v_bfi_b32 v204, s41, v9, v204\n\t" \
    "v_bfi_b32 v68, s41, v9, v68\n\t" \
    "v_cvt_pk_bf16_f32 v149, v206, v207\n\t" \
    "v_mfma_f32_16x16x32_bf16 v[168:171], v[108:111], v[88:91], v[168:171]\n\t" \
    "v_cvt_pk_bf16_f32 v151, v70, v71\n\t" \
    "v_cvt_pk_bf16_f32 v148, v204, v205\n\t" \
    "v_cvt_pk_bf16_f32 v150, v68, v69\n\t" \
    "v_mfma_f32_16x16x32_bf16 v[76:79], v[108:111], v[52:55], v[76:79]\n\t" \
    "v_bfi_b32 v104, s41, v9, v104\n\t" \
    "v_bfi_b32 v20, s41, v9, v20\n\t" \
    "buffer_store_dwordx4 v[148:151], v42, s[4:7], s48 offen offset:0 sc1\n\t" \
    "v_mfma_f32_16x16x32_bf16 v[4:7], v[108:111], v[80:83], v[4:7]\n\t" \
    "v_cvt_pk_bf16_f32 v57, v106, v107\n\t" \
    "v_cvt_pk_bf16_f32 v59, v22, v23\n\t" \
    "v_cvt_pk_bf16_f32 v56, v104, v105\n\t" \
    "s_waitcnt vmcnt(11)\n\t" \
    "v_mfma_f32_16x16x32_bf16 v[252:255], v[192:195], v[88:91], v[252:255]\n\t" \
    "v_cvt_pk_bf16_f32 v58, v20, v21\n\t" \
    "buffer_store_dwordx4 v[56:59], v8, s[4:7], s48 offen offset:128 sc1\n\t" \
    "v_mfma_f32_16x16x32_bf16 v[168:171], v[192:195], v[216:219], v[168:171]\n\t" \
    "v_mfma_f32_16x16x32_bf16 v[64:67], v[192:195], v[80:83], v[64:67]\n\t" \
    "v_mfma_f32_16x16x32_bf16 v[4:7], v[192:195], v[52:55], v[4:7]\n\t" \
    "s_waitcnt vmcnt(11)\n\t" \
    "v_mfma_f32_16x16x32_bf16 v[172:175], v[196:199], v[120:123], 0\n\t" \
    "v_mfma_f32_16x16x32_bf16 v[16:19], v[196:199], v[116:119], 0\n\t" \
    "v_mfma_f32_16x16x32_bf16 v[12:15], v[196:199], v[128:131], 0\n\t" \
    "v_sub_f32 v140, v140, v252\n\t" \
    "v_sub_f32 v141, v141, v253\n\t" \
    "v_mfma_f32_16x16x32_bf16 v[32:35], v[196:199], v[132:135], 0\n\t" \
    "v_sub_f32 v142, v142, v254\n\t" \
    "v_sub_f32 v143, v143, v255\n\t" \
    "s_waitcnt vmcnt(10)\n\t" \
    "v_mfma_f32_16x16x32_bf16 v[172:175], v[220:223], v[176:179], v[172:175]\n\t" \
    "v_sub_f32 v76, v76, v64\n\t" \
    "v_sub_f32 v77, v77, v65\n\t" \
    "v_mfma_f32_16x16x32_bf16 v[16:19], v[220:223], v[184:187], v[16:19]\n\t" \
    "v_sub_f32 v78, v78, v66\n\t" \
    "v_sub_f32 v79, v79, v67\n\t" \
    "v_mfma_f32_16x16x32_bf16 v[12:15], v[220:223], v[188:191], v[12:15]\n\t" \
    "v_cvt_pk_bf16_f32 v102, v168, v169\n\t" \
    "v_cvt_pk_bf16_f32 v103, v170, v171\n\t" \
    "buffer_load_dwordx4 v[212:215], v46, s[4:7], s46 offen offset:0 sc1\n\t" \
    "v_mfma_f32_16x16x32_bf16 v[32:35], v[220:223], v[60:63], v[32:35]\n\t" \
    "v_cvt_pk_bf16_f32 v100, v140, v141\n\t" \
    "v_cvt_pk_bf16_f32 v101, v142, v143\n\t" \
    "s_waitcnt vmcnt(10)\n\t" \
    "v_mfma_f32_16x16x32_bf16 v[172:175], v[124:127], v[28:31], v[172:175]\n\t" \
    "v_cvt_pk_bf16_f32 v206, v4, v5\n\t" \
    "v_cvt_pk_bf16_f32 v207, v6, v7\n\t" \
    "v_mfma_f32_16x16x32_bf16 v[16:19], v[124:127], v[224:227], v[16:19]\n\t" \
    "ds_write_b128 v40, v[100:103] offset:0\n\t" \
    "v_cvt_pk_bf16_f32 v204, v76, v77\n\t" \
    "buffer_load_dwordx4 v[68:71], v46, s[4:7], s47 offen offset:0 sc1\n\t" \
    "v_mfma_f32_16x16x32_bf16 v[12:15], v[124:127], v[228:231], v[12:15]\n\t" \
    "v_cvt_pk_bf16_f32 v205, v78, v79\n\t" \
    "ds_write_b128 v40, v[204:207] offset:1024\n\t" \
    "v_mfma_f32_16x16x32_bf16 v[32:35], v[124:127], v[236:239], v[32:35]\n\t" \
    "s_waitcnt vmcnt(11)\n\t" \
    "s_cmp_ge_u32 s24, 4\n\t" \
    "s_cbranch_scc1 .Lk54_%=\n\t" \
    "v_mfma_f32_16x16x32_bf16 v[148:151], v[232:235], v[116:119], 0\n\t" \
    "v_mfma_f32_16x16x32_bf16 v[148:151], v[240:243], v[184:187], v[148:151]\n\t" \
    "v_mfma_f32_16x16x32_bf16 v[148:151], v[244:247], v[224:227], v[148:151]\n\t" \
    "v_mfma_f32_16x16x32_bf16 v[108:111], v[232:235], v[132:135], 0\n\t" \
    "v_mfma_f32_16x16x32_bf16 v[108:111], v[240:243], v[60:63], v[108:111]\n\t" \
    "v_mfma_f32_16x16x32_bf16 v[108:111], v[244:247], v[236:239], v[108:111]\n\t" \
    "v_mfma_f32_16x16x32_bf16 v[16:19], v[232:235], v[120:123], v[16:19]\n\t" \
    "v_mfma_f32_16x16x32_bf16 v[16:19], v[240:243], v[176:179], v[16:19]\n\t" \
    "v_mfma_f32_16x16x32_bf16 v[16:19], v[244:247], v[28:31], v[16:19]\n\t" \
    "v_mfma_f32_16x16x32_bf16 v[32:35], v[232:235], v[128:131], v[32:35]\n\t" \
    "s_nop 0\n\t" \
    "v_sub_f32 v172, v172, v148\n\t" \
    "v_sub_f32 v173, v173, v149\n\t" \
    "v_mfma_f32_16x16x32_bf16 v[32:35], v[240:243], v[188:191], v[32:35]\n\t" \
    "v_sub_f32 v174, v174, v150\n\t" \
    "v_sub_f32 v175, v175, v151\n\t" \
    "v_mfma_f32_16x16x32_bf16 v[32:35], v[244:247], v[228:231], v[32:35]\n\t" \
    "v_sub_f32 v12, v12, v108\n\t" \
    "v_sub_f32 v13, v13, v109\n\t" \
    "v_sub_f32 v14, v14, v110\n\t" \
    "v_sub_f32 v15, v15, v111\n\t" \
    ".Lk54_%=:\n\t" \
    "s_waitcnt vmcnt(10)\n\t" \
    "v_mfma_f32_16x16x32_bf16 v[104:107], v[248:251], v[92:95], 0\n\t" \
    "s_bitcmp1_b32 s24, 2\n\t" \
    "s_cbranch_scc1 .Ly35_%=\n\t" \
    "buffer_load_dwordx4 v[20:23], v46, s[4:7], s45 offen offset:1024 sc1\n\t" \
    ".Ly35_%=:\n\t" \
    "v_mfma_f32_16x16x32_bf16 v[56:59], v[248:251], v[112:115], 0\n\t" \
    "v_cvt_pk_bf16_f32 v194, v16, v17\n\t" \
    "v_cvt_pk_bf16_f32 v195, v18, v19\n\t" \
    "v_mfma_f32_16x16x32_bf16 v[196:199], v[248:251], v[84:87], 0\n\t" \
    "v_cvt_pk_bf16_f32 v192, v172, v173\n\t" \
    "v_cvt_pk_bf16_f32 v193, v174, v175\n\t" \
    "v_mfma_f32_16x16x32_bf16 v[252:255], v[248:251], v[96:99], 0\n\t" \
    "v_cvt_pk_bf16_f32 v66, v32, v33\n\t" \
    "v_cvt_pk_bf16_f32 v67, v34, v35\n\t" \
    "s_bitcmp1_b32 s24, 2\n\t" \
    "s_cbranch_scc1 .Ly36_%=\n\t" \
    "buffer_load_dwordx4 v[168:171], v46, s[4:7], s46 offen offset:1024 sc1\n\t" \
    ".Ly36_%=:\n\t" \
    "s_waitcnt vmcnt(9)\n\t" \
    "v_mfma_f32_16x16x32_bf16 v[220:223], v[156:159], v[112:115], 0\n\t" \
    "ds_write_b128 v40, v[192:195] offset:2064\n\t" \
    "v_cvt_pk_bf16_f32 v64, v12, v13\n\t" \
    "v_mfma_f32_16x16x32_bf16 v[56:59], v[156:159], v[92:95], v[56:59]\n\t" \
    "v_cvt_pk_bf16_f32 v65, v14, v15\n\t" \
    "v_mfma_f32_16x16x32_bf16 v[140:143], v[156:159], v[96:99], 0\n\t" \
    "ds_write_b128 v40, v[64:67] offset:3088\n\t" \
    "s_bitcmp1_b32 s24, 2\n\t" \
    "s_cbranch_scc1 .Ly37_%=\n\t" \
    "buffer_load_dwordx4 v[4:7], v46, s[4:7], s47 offen offset:1024 sc1\n\t" \
    ".Ly37_%=:\n\t" \
    "v_mfma_f32_16x16x32_bf16 v[252:255], v[156:159], v[84:87], v[252:255]\n\t" \
    "s_waitcnt vmcnt(8)\n\t" \
    "v_mfma_f32_16x16x32_bf16 v[104:107], v[72:75], v[144:147], v[104:107]\n\t" \
    "v_mfma_f32_16x16x32_bf16 v[56:59], v[72:75], v[152:155], v[56:59]\n\t" \
    "s_waitcnt lgkmcnt(0)\n\t" \
    "s_barrier\n\t" \
    "s_memrealtime s[18:19]\n\t" \
    "s_setprio 2\n\t" \
    "ds_read_b64_tr_b16 v[100:101], v49 offset:0\n\t" \
    "ds_read_b64_tr_b16 v[102:103], v49 offset:4128\n\t" \
    "ds_read_b64_tr_b16 v[76:77], v49 offset:256\n\t" \
    "ds_read_b64_tr_b16 v[78:79], v49 offset:4384\n\t" \
    "v_mfma_f32_16x16x32_bf16 v[196:199], v[72:75], v[160:163], v[196:199]\n\t" \
    "ds_read_b64_tr_b16 v[204:205], v49 offset:16\n\t" \
    "ds_read_b64_tr_b16 v[206:207], v49 offset:4144\n\t" \
    "ds_read_b64_tr_b16 v[124:125], v49 offset:272\n\t" \
    "ds_read_b64_tr_b16 v[126:127], v49 offset:4400\n\t" \
    "v_mfma_f32_16x16x32_bf16 v[252:255], v[72:75], v[164:167], v[252:255]\n\t" \
    "s_waitcnt vmcnt(7)\n\t" \
    "v_mfma_f32_16x16x32_bf16 v[220:223], v[136:139], v[152:155], v[220:223]\n\t" \
    "v_mfma_f32_16x16x32_bf16 v[56:59], v[136:139], v[144:147], v[56:59]\n\t" \
    "s_waitcnt lgkmcnt(6)\n\t" \
    "v_mfma_f32_16x16x32_bf16 v[232:235], v[100:103], v[200:203], 0\n\t" \
    "s_waitcnt lgkmcnt(4)\n\t" \
    "v_mfma_f32_16x16x32_bf16 v[240:243], v[76:79], v[200:203], 0\n\t" \
    "v_mfma_f32_16x16x32_bf16 v[140:143], v[136:139], v[164:167], v[140:143]\n\t" \
    "s_waitcnt lgkmcnt(2)\n\t" \
    "v_mfma_f32_16x16x32_bf16 v[148:151], v[204:207], v[200:203], 0\n\t" \
    "s_waitcnt lgkmcnt(0)\n\t" \
    "v_mfma_f32_16x16x32_bf16 v[244:247], v[124:127], v[200:203], 0\n\t" \
    "v_mfma_f32_16x16x32_bf16 v[252:255], v[136:139], v[160:163], v[252:255]\n\t" \
    "s_waitcnt vmcnt(6)\n\t" \
    "v_mfma_f32_16x16x32_bf16 v[104:107], v[24:27], v[216:219], v[104:107]\n\t" \
    "v_bfi_b32 v232, s41, v9, v232\n\t" \
    "v_bfi_b32 v240, s41, v9, v240\n\t" \
    "v_cvt_pk_bf16_f32 v109, v234, v235\n\t" \
    "v_mfma_f32_16x16x32_bf16 v[56:59], v[24:27], v[88:91], v[56:59]\n\t" \
    "v_cvt_pk_bf16_f32 v111, v242, v243\n\t" \
    "v_cvt_pk_bf16_f32 v108, v232, v233\n\t" \
    "v_cvt_pk_bf16_f32 v110, v240, v241\n\t" \
    "v_mfma_f32_16x16x32_bf16 v[196:199], v[24:27], v[52:55], v[196:199]\n\t" \
    "v_bfi_b32 v148, s41, v9, v148\n\t" \
    "v_bfi_b32 v244, s41, v9, v244\n\t" \
    "buffer_store_dwordx4 v[108:111], v42, s[4:7], s49 offen offset:0 sc1\n\t" \
    "s_add_u32 s37, s52, s33\n\t" \
    "s_buffer_load_dword s58, s[4:7], s37 offset:0x0\n\t" \
    "s_buffer_load_dword s59, s[4:7], s37 offset:0x400\n\t" \
    "s_buffer_load_dword s60, s[4:7], s37 offset:0x8000\n\t" \
    "s_buffer_load_dword s61, s[4:7], s37 offset:0x8400\n\t" \
    "v_mfma_f32_16x16x32_bf16 v[252:255], v[24:27], v[80:83], v[252:255]\n\t" \
    "v_cvt_pk_bf16_f32 v17, v150, v151\n\t" \
    "v_cvt_pk_bf16_f32 v19, v246, v247\n\t" \
    "v_cvt_pk_bf16_f32 v16, v148, v149\n\t" \
    "s_waitcnt vmcnt(6)\n\t" \
    "v_mfma_f32_16x16x32_bf16 v[220:223], v[208:211], v[88:91], v[220:223]\n\t" \
    "v_cvt_pk_bf16_f32 v18, v244, v245\n\t" \
    "buffer_store_dwordx4 v[16:19], v8, s[4:7], s49 offen offset:128 sc1\n\t" \
    "s_setprio 0\n\t" \
    "v_add_u32 v40, 91392, v40\n\t" \
    "ds_read_b128 v[172:175], v10 offset:3072\n\t" \
    "ds_read_b128 v[248:251], v10 offset:4096\n\t" \
    "v_mfma_f32_16x16x32_bf16 v[56:59], v[208:211], v[216:219], v[56:59]\n\t" \
    "s_cmp_lt_u32 s24, 4\n\t" \
    "s_cbranch_scc1 .Lp55_%=\n\t" \
    "s_setprio 1\n\t" \
    ".Lp55_%=:\n\t" \
    "v_mfma_f32_16x16x32_bf16 v[140:143], v[208:211], v[80:83], v[140:143]\n\t" \
    "v_mfma_f32_16x16x32_bf16 v[252:255], v[208:211], v[52:55], v[252:255]\n\t" \
    "s_waitcnt vmcnt(6)\n\t" \
    "v_mfma_f32_16x16x32_bf16 v[32:35], v[180:183], v[120:123], 0\n\t" \
    "v_mfma_f32_16x16x32_bf16 v[112:115], v[180:183], v[116:119], 0\n\t" \
    "v_mfma_f32_16x16x32_bf16 v[192:195], v[180:183], v[128:131], 0\n\t" \
    "v_sub_f32 v104, v104, v220\n\t" \
    "v_sub_f32 v105, v105, v221\n\t" \
    "v_mfma_f32_16x16x32_bf16 v[92:95], v[180:183], v[132:135], 0\n\t" \
    "v_sub_f32 v106, v106, v222\n\t" \
    "v_sub_f32 v107, v107, v223\n\t" \
    "s_waitcnt vmcnt(3)\n\t" \
    "v_mfma_f32_16x16x32_bf16 v[32:35], v[212:215], v[176:179], v[32:35]\n\t" \
    "v_sub_f32 v196, v196, v140\n\t" \
    "v_sub_f32 v197, v197, v141\n\t" \
    "v_mfma_f32_16x16x32_bf16 v[112:115], v[212:215], v[184:187], v[112:115]\n\t" \
    "v_sub_f32 v198, v198, v142\n\t" \
    "v_sub_f32 v199, v199, v143\n\t" \
    "v_mfma_f32_16x16x32_bf16 v[192:195], v[212:215], v[188:191], v[192:195]\n\t" \
    "v_cvt_pk_bf16_f32 v14, v56, v57\n\t" \
    "v_cvt_pk_bf16_f32 v15, v58, v59\n\t" \
    "v_mfma_f32_16x16x32_bf16 v[92:95], v[212:215], v[60:63], v[92:95]\n\t" \
    "v_cvt_pk_bf16_f32 v12, v104, v105\n\t" \
    "v_cvt_pk_bf16_f32 v13, v106, v107\n\t" \
    "s_waitcnt vmcnt(2)\n\t" \
    "v_mfma_f32_16x16x32_bf16 v[32:35], v[68:71], v[28:31], v[32:35]\n\t" \
    "v_cvt_pk_bf16_f32 v98, v252, v253\n\t" \
    "v_cvt_pk_bf16_f32 v99, v254, v255\n\t" \
    "v_mfma_f32_16x16x32_bf16 v[112:115], v[68:71], v[224:227], v[112:115]\n\t" \
    "ds_write_b128 v40, v[12:15] offset:0\n\t" \
    "v_cvt_pk_bf16_f32 v96, v196, v197\n\t" \
    "s_add_u32 s54, s53, 0\n\t" \
    "buffer_load_dwordx4 v[64:67], v0, s[4:7], s54 offen offset:0 sc1\n\t" \
    "s_add_u32 s54, s53, 4243456\n\t" \
    "buffer_load_dwordx4 v[44:47], v0, s[4:7], s54 offen offset:0 sc1\n\t" \
    "v_mfma_f32_16x16x32_bf16 v[192:195], v[68:71], v[228:231], v[192:195]\n\t" \
    "v_cvt_pk_bf16_f32 v97, v198, v199\n\t" \
    "ds_write_b128 v40, v[96:99] offset:1024\n\t" \
    "v_mfma_f32_16x16x32_bf16 v[92:95], v[68:71], v[236:239], v[92:95]\n\t" \
    "s_waitcnt vmcnt(4)\n\t" \
    "s_cmp_ge_u32 s24, 4\n\t" \
    "s_cbranch_scc1 .Lk56_%=\n\t" \
    "v_mfma_f32_16x16x32_bf16 v[84:87], v[20:23], v[116:119], 0\n\t" \
    "v_mfma_f32_16x16x32_bf16 v[84:87], v[168:171], v[184:187], v[84:87]\n\t" \
    "v_mfma_f32_16x16x32_bf16 v[84:87], v[4:7], v[224:227], v[84:87]\n\t" \
    "v_mfma_f32_16x16x32_bf16 v[156:159], v[20:23], v[132:135], 0\n\t" \
    "v_mfma_f32_16x16x32_bf16 v[156:159], v[168:171], v[60:63], v[156:159]\n\t" \
    "v_mfma_f32_16x16x32_bf16 v[156:159], v[4:7], v[236:239], v[156:159]\n\t" \
    "v_mfma_f32_16x16x32_bf16 v[112:115], v[20:23], v[120:123], v[112:115]\n\t" \
    "v_mfma_f32_16x16x32_bf16 v[112:115], v[168:171], v[176:179], v[112:115]\n\t" \
    "v_mfma_f32_16x16x32_bf16 v[112:115], v[4:7], v[28:31], v[112:115]\n\t" \
    "v_mfma_f32_16x16x32_bf16 v[92:95], v[20:23], v[128:131], v[92:95]\n\t" \
    "s_nop 0\n\t" \
    "v_sub_f32 v32, v32, v84\n\t" \
    "v_sub_f32 v33, v33, v85\n\t" \
    "v_mfma_f32_16x16x32_bf16 v[92:95], v[168:171], v[188:191], v[92:95]\n\t" \
    "v_sub_f32 v34, v34, v86\n\t" \
    "v_sub_f32 v35, v35, v87\n\t" \
    "v_mfma_f32_16x16x32_bf16 v[92:95], v[4:7], v[228:231], v[92:95]\n\t" \
    "v_sub_f32 v192, v192, v156\n\t" \
    "v_sub_f32 v193, v193, v157\n\t" \
    "v_sub_f32 v194, v194, v158\n\t" \
    "v_sub_f32 v195, v195, v159\n\t" \
    ".Lk56_%=:\n\t" \
    "v_cvt_pk_bf16_f32 v74, v112, v113\n\t" \
    "v_cvt_pk_bf16_f32 v75, v114, v115\n\t" \
    "v_cvt_pk_bf16_f32 v72, v32, v33\n\t" \
    "v_cvt_pk_bf16_f32 v73, v34, v35\n\t" \
    "s_nop 1\n\t" \
    "v_cvt_pk_bf16_f32 v154, v92, v93\n\t" \
    "v_cvt_pk_bf16_f32 v155, v94, v95\n\t" \
    "ds_write_b128 v40, v[72:75] offset:2064\n\t" \
    "v_cvt_pk_bf16_f32 v152, v192, v193\n\t" \
    "v_cvt_pk_bf16_f32 v153, v194, v195\n\t" \
    "v_add_u32 v49, 91392, v49\n\t" \
    "ds_write_b128 v40, v[152:155] offset:3088\n\t" \
    "s_waitcnt lgkmcnt(0)\n\t" \
    "s_barrier\n\t" \
    "ds_read_b64_tr_b16 v[144:145], v49 offset:0\n\t" \
    "ds_read_b64_tr_b16 v[146:147], v49 offset:4128\n\t" \
    "ds_read_b64_tr_b16 v[100:101], v49 offset:256\n\t" \
    "ds_read_b64_tr_b16 v[102:103], v49 offset:4384\n\t" \
    "ds_read_b64_tr_b16 v[76:77], v49 offset:16\n\t" \
    "ds_read_b64_tr_b16 v[78:79], v49 offset:4144\n\t" \
    "ds_read_b64_tr_b16 v[164:165], v49 offset:272\n\t" \
    "ds_read_b64_tr_b16 v[166:167], v49 offset:4400\n\t" \
    "s_memrealtime s[56:57]\n\t" \
    "s_waitcnt lgkmcnt(6)\n\t" \
    "v_mfma_f32_16x16x32_bf16 v[204:207], v[144:147], v[200:203], 0\n\t" \
    "s_waitcnt lgkmcnt(4)\n\t" \
    "v_mfma_f32_16x16x32_bf16 v[124:127], v[100:103], v[200:203], 0\n\t" \
    "s_waitcnt lgkmcnt(2)\n\t" \
    "v_mfma_f32_16x16x32_bf16 v[136:139], v[76:79], v[200:203], 0\n\t" \
    "s_waitcnt lgkmcnt(0)\n\t" \
    "v_mfma_f32_16x16x32_bf16 v[160:163], v[164:167], v[200:203], 0\n\t" \
    "s_nop 1\n\t" \
    "v_bfi_b32 v204, s41, v9, v204\n\t" \
    "s_nop 0\n\t" \
    "v_bfi_b32 v124, s41, v9, v124\n\t" \
    "v_cvt_pk_bf16_f32 v233, v206, v207\n\t" \
    "v_cvt_pk_bf16_f32 v235, v126, v127\n\t" \
    "v_cvt_pk_bf16_f32 v232, v204, v205\n\t" \
    "v_cvt_pk_bf16_f32 v234, v124, v125\n\t" \
    "v_bfi_b32 v136, s41, v9, v136\n\t" \
    "v_bfi_b32 v160, s41, v9, v160\n\t" \
    "buffer_store_dwordx4 v[232:235], v42, s[4:7], s50 offen offset:0 sc1\n\t" \
    "v_cvt_pk_bf16_f32 v241, v138, v139\n\t" \
    "v_cvt_pk_bf16_f32 v243, v162, v163\n\t" \
    "v_cvt_pk_bf16_f32 v240, v136, v137\n\t" \
    "v_cvt_pk_bf16_f32 v242, v160, v161\n\t" \
    "buffer_store_dwordx4 v[240:243], v8, s[4:7], s50 offen offset:128 sc1\n\t" \
    "s_cmp_ge_u32 s25, 126\n\t" \
    "s_cbranch_scc1 .Lend_%=\n\t" \
    "s_waitcnt lgkmcnt(0)\n\t" \
    "s_sub_u32 s37, s16, 300\n\t" \
    "s_mov_b32 s62, 300\n\t" \
    "s_sub_u32 s39, s58, s37\n\t" \
    "s_cmp_gt_u32 s39, 600\n\t" \
    "s_cselect_b32 s39, 400, s39\n\t" \
    "s_max_u32 s62, s62, s39\n\t" \
    "s_sub_u32 s39, s59, s37\n\t" \
    "s_cmp_gt_u32 s39, 600\n\t" \
    "s_cselect_b32 s39, 400, s39\n\t" \
    "s_max_u32 s62, s62, s39\n\t" \
    "s_sub_u32 s39, s60, s37\n\t" \
    "s_cmp_gt_u32 s39, 600\n\t" \
    "s_cselect_b32 s39, 400, s39\n\t" \
    "s_max_u32 s62, s62, s39\n\t" \
    "s_sub_u32 s39, s61, s37\n\t" \
    "s_cmp_gt_u32 s39, 600\n\t" \
    "s_cselect_b32 s39, 400, s39\n\t" \
    "s_max_u32 s62, s62, s39\n\t" \
    "s_sub_u32 s62, s62, 300\n\t" \
    "s_mul_i32 s62, s62, 5\n\t" \
    "s_lshr_b32 s62, s62, 2\n\t" \
    "s_waitcnt vmcnt(2)\n\t" \
    "v_and_b32 v36, v64, v66\n\t" \
    "v_bitop3_b32 v36, v36, v44, v46 bitop3:0x80\n\t" \
    "v_and_b32 v36, 1, v36\n\t" \
    "v_cmp_eq_u32 vcc, 0, v36\n\t" \
    "s_nop 1\n\t" \
    "s_cbranch_vccnz .Ls50_%=\n\t" \
    ".Lb50_%=:\n\t" \
    "ds_write_b128 v48, v[64:67] offset:33024\n\t" \
    "ds_write_b128 v48, v[44:47] offset:33152\n\t" \
    "s_add_u32 s37, s62, 125\n\t" \
    ".Lh57_%=:\n\t" \
    "s_memrealtime s[22:23]\n\t" \
    "s_waitcnt lgkmcnt(0)\n\t" \
    "s_sub_u32 s22, s22, s18\n\t" \
    "s_cmp_lt_u32 s22, s37\n\t" \
    "s_cbranch_scc1 .Lh57_%=\n\t" \
    "s_add_u32 s54, s53, 524288\n\t" \
    "buffer_load_dwordx4 v[108:111], v0, s[4:7], s54 offen offset:0 sc1\n\t" \
    "s_add_u32 s54, s53, 4767744\n\t" \
    "buffer_load_dwordx4 v[24:27], v0, s[4:7], s54 offen offset:0 sc1\n\t" \
    "s_waitcnt lgkmcnt(0)\n\t" \
    "s_barrier\n\t" \
    "ds_read_b64_tr_b16 v[148:149], v43 offset:33024\n\t" \
    "ds_read_b64_tr_b16 v[150:151], v43 offset:33280\n\t" \
    "ds_read_b64_tr_b16 v[88:89], v43 offset:33032\n\t" \
    "ds_read_b64_tr_b16 v[90:91], v43 offset:33288\n\t" \
    "s_waitcnt lgkmcnt(2)\n\t" \
    "v_mfma_f32_16x16x32_bf16 v[244:247], v[148:151], v[172:175], 0\n\t" \
    "s_waitcnt lgkmcnt(0)\n\t" \
    "v_mfma_f32_16x16x32_bf16 v[16:19], v[88:91], v[172:175], 0\n\t" \
    "v_mfma_f32_16x16x32_bf16 v[216:219], v[148:151], v[248:251], 0\n\t" \
    "v_mfma_f32_16x16x32_bf16 v[80:83], v[88:91], v[248:251], 0\n\t" \
    "s_nop 3\n\t" \
    "v_cvt_pk_bf16_f32 v52, v244, v245\n\t" \
    "v_cvt_pk_bf16_f32 v53, v246, v247\n\t" \
    "v_cvt_pk_bf16_f32 v54, v16, v17\n\t" \
    "v_cvt_pk_bf16_f32 v55, v18, v19\n\t" \
    "buffer_store_dwordx4 v[52:55], v38, s[8:11], 0 offen offset:0 sc1\n\t" \
    "v_cvt_pk_bf16_f32 v208, v216, v217\n\t" \
    "v_cvt_pk_bf16_f32 v209, v218, v219\n\t" \
    "v_cvt_pk_bf16_f32 v210, v80, v81\n\t" \
    "v_cvt_pk_bf16_f32 v211, v82, v83\n\t" \
    "buffer_store_dwordx4 v[208:211], v38, s[8:11], 0 offen offset:3072 sc1\n\t" \
    "s_waitcnt vmcnt(2)\n\t" \
    "v_and_b32 v51, v108, v110\n\t" \
    "v_bitop3_b32 v51, v51, v24, v26 bitop3:0x80\n\t" \
    "v_and_b32 v51, 1, v51\n\t" \
    "v_cmp_eq_u32 vcc, 0, v51\n\t" \
    "s_nop 1\n\t" \
    "s_cbranch_vccnz .Ls51_%=\n\t" \
    ".Lb51_%=:\n\t" \
    "ds_write_b128 v48, v[108:111] offset:0\n\t" \
    "ds_write_b128 v48, v[24:27] offset:128\n\t" \
    "s_add_u32 s37, s62, 100\n\t" \
    ".Lh58_%=:\n\t" \
    "s_memrealtime s[22:23]\n\t" \
    "s_waitcnt lgkmcnt(0)\n\t" \
    "s_sub_u32 s22, s22, s56\n\t" \
    "s_cmp_lt_u32 s22, s37\n\t" \
    "s_cbranch_scc1 .Lh58_%=\n\t" \
    "s_add_u32 s54, s53, 1048576\n\t" \
    "buffer_load_dwordx4 v[180:183], v0, s[4:7], s54 offen offset:0 sc1\n\t" \
    "s_add_u32 s54, s53, 5292032\n\t" \
    "buffer_load_dwordx4 v[220:223], v0, s[4:7], s54 offen offset:0 sc1\n\t" \
    "s_waitcnt lgkmcnt(0)\n\t" \
    "s_barrier\n\t" \
    "ds_read_b64_tr_b16 v[140:141], v43 offset:0\n\t" \
    "ds_read_b64_tr_b16 v[142:143], v43 offset:256\n\t" \
    "ds_read_b64_tr_b16 v[56:57], v43 offset:8\n\t" \
    "ds_read_b64_tr_b16 v[58:59], v43 offset:264\n\t" \
    "s_waitcnt lgkmcnt(2)\n\t" \
    "v_mfma_f32_16x16x32_bf16 v[212:215], v[140:143], v[172:175], 0\n\t" \
    "s_waitcnt lgkmcnt(0)\n\t" \
    "v_mfma_f32_16x16x32_bf16 v[104:107], v[56:59], v[172:175], 0\n\t" \
    "v_mfma_f32_16x16x32_bf16 v[252:255], v[140:143], v[248:251], 0\n\t" \
    "v_mfma_f32_16x16x32_bf16 v[12:15], v[56:59], v[248:251], 0\n\t" \
    "s_nop 3\n\t" \
    "v_cvt_pk_bf16_f32 v196, v212, v213\n\t" \
    "v_cvt_pk_bf16_f32 v197, v214, v215\n\t" \
    "v_cvt_pk_bf16_f32 v198, v104, v105\n\t" \
    "v_cvt_pk_bf16_f32 v199, v106, v107\n\t" \
    "buffer_store_dwordx4 v[196:199], v38, s[8:11], 0 offen offset:64 sc1\n\t" \
    "v_cvt_pk_bf16_f32 v96, v252, v253\n\t" \
    "v_cvt_pk_bf16_f32 v97, v254, v255\n\t" \
    "v_cvt_pk_bf16_f32 v98, v12, v13\n\t" \
    "v_cvt_pk_bf16_f32 v99, v14, v15\n\t" \
    "buffer_store_dwordx4 v[96:99], v38, s[8:11], 0 offen offset:3136 sc1\n\t" \
    "s_waitcnt vmcnt(2)\n\t" \
    "v_and_b32 v37, v180, v182\n\t" \
    "v_bitop3_b32 v37, v37, v220, v222 bitop3:0x80\n\t" \
    "v_and_b32 v37, 1, v37\n\t" \
    "v_cmp_eq_u32 vcc, 0, v37\n\t" \
    "s_nop 1\n\t" \
    "s_cbranch_vccnz .Ls52_%=\n\t" \
    ".Lb52_%=:\n\t" \
    "ds_write_b128 v48, v[180:183] offset:16512\n\t" \
    "ds_write_b128 v48, v[220:223] offset:16640\n\t" \
    "s_waitcnt lgkmcnt(0)\n\t" \
    "s_barrier\n\t" \
    "ds_read_b64_tr_b16 v[68:69], v43 offset:16512\n\t" \
    "ds_read_b64_tr_b16 v[70:71], v43 offset:16768\n\t" \
    "ds_read_b64_tr_b16 v[116:117], v43 offset:16520\n\t" \
    "ds_read_b64_tr_b16 v[118:119], v43 offset:16776\n\t" \
    "s_waitcnt lgkmcnt(2)\n\t" \
    "v_mfma_f32_16x16x32_bf16 v[184:187], v[68:71], v[172:175], 0\n\t" \
    "s_waitcnt lgkmcnt(0)\n\t" \
    "v_mfma_f32_16x16x32_bf16 v[224:227], v[116:119], v[172:175], 0\n\t" \
    "v_mfma_f32_16x16x32_bf16 v[132:135], v[68:71], v[248:251], 0\n\t" \
    "v_mfma_f32_16x16x32_bf16 v[60:63], v[116:119], v[248:251], 0\n\t" \
    "s_nop 3\n\t" \
    "v_cvt_pk_bf16_f32 v236, v184, v185\n\t" \
    "v_cvt_pk_bf16_f32 v237, v186, v187\n\t" \
    "v_cvt_pk_bf16_f32 v238, v224, v225\n\t" \
    "v_cvt_pk_bf16_f32 v239, v226, v227\n\t" \
    "buffer_store_dwordx4 v[236:239], v38, s[8:11], 0 offen offset:128 sc1\n\t" \
    "v_cvt_pk_bf16_f32 v120, v132, v133\n\t" \
    "v_cvt_pk_bf16_f32 v121, v134, v135\n\t" \
    "v_cvt_pk_bf16_f32 v122, v60, v61\n\t" \
    "v_cvt_pk_bf16_f32 v123, v62, v63\n\t" \
    "buffer_store_dwordx4 v[120:123], v38, s[8:11], 0 offen offset:3200 sc1\n\t" \
    "s_or_b32 s37, s2, s24\n\t" \
    "s_cbranch_scc1 .Lend_%=\n\t" \
    "s_mov_b32 s37, 34668544\n\t" \
    "v_mov_b32 v1, s34\n\t" \
    "s_mov_b64 exec, 1\n\t" \
    "buffer_store_dword v1, v0, s[4:7], s37 offen sc1\n\t" \
    "s_mov_b64 exec, -1\n\t" \
    ".Lend_%=:\n\t" \
    "s_endpgm\n\t" \
    ".Lq1028_%=:\n\t" \
    "s_mov_b32 s40, 0\n\t" \
    ".Lr1028_%=:\n\t" \
    "s_add_u32 s12, s55, 34603008\n\t" \
    "s_buffer_load_dword s13, s[4:7], s12 glc\n\t" \
    "s_buffer_load_dword s14, s[4:7], s12 offset:0x400 glc\n\t" \
    "s_waitcnt lgkmcnt(0)\n\t" \
    "s_xor_b32 s13, s13, s34\n\t" \
    "s_xor_b32 s14, s14, s34\n\t" \
    "s_or_b32 s13, s13, s14\n\t" \
    "s_cbranch_scc0 .Lb27_%=\n\t" \
    "s_add_u32 s40, s40, 1\n\t" \
    "s_cmp_lt_u32 s40, 1\n\t" \
    "s_cbranch_scc1 .Lr1028_%=\n\t" \
    "s_branch .Ls27_%=\n\t" \
    ".Lq1025_%=:\n\t" \
    "s_mov_b32 s40, 0\n\t" \
    ".Lr1025_%=:\n\t" \
    "s_nop 0\n\t" \
    "v_readlane_b32 s12, v50, 0\n\t" \
    "v_readlane_b32 s13, v50, 1\n\t" \
    "v_readlane_b32 s14, v50, 2\n\t" \
    "v_readlane_b32 s15, v50, 3\n\t" \
    "v_readlane_b32 s16, v50, 4\n\t" \
    "v_readlane_b32 s17, v50, 5\n\t" \
    "v_readlane_b32 s18, v50, 6\n\t" \
    "v_readlane_b32 s19, v50, 7\n\t" \
    "v_readlane_b32 s3, v50, 8\n\t" \
    "s_add_u32 s12, s12, s33\n\t" \
    "s_add_u32 s13, s13, s33\n\t" \
    "s_add_u32 s14, s14, s33\n\t" \
    "s_add_u32 s15, s15, s33\n\t" \
    "s_add_u32 s16, s16, s33\n\t" \
    "s_add_u32 s17, s17, s33\n\t" \
    "s_add_u32 s18, s18, s33\n\t" \
    "s_add_u32 s19, s19, s33\n\t" \
    "s_add_u32 s3, s3, s33\n\t" \
    "s_buffer_load_dword s12, s[4:7], s12 offset:0xc0000 glc\n\t" \
    "s_buffer_load_dword s13, s[4:7], s13 offset:0xc0000 glc\n\t" \
    "s_buffer_load_dword s14, s[4:7], s14 offset:0xc0000 glc\n\t" \
    "s_buffer_load_dword s15, s[4:7], s15 offset:0xc0000 glc\n\t" \
    "s_buffer_load_dword s16, s[4:7], s16 offset:0xc0000 glc\n\t" \
    "s_buffer_load_dword s17, s[4:7], s17 offset:0xc0000 glc\n\t" \
    "s_buffer_load_dword s18, s[4:7], s18 offset:0xc0000 glc\n\t" \
    "s_buffer_load_dword s19, s[4:7], s19 offset:0xc0000 glc\n\t" \
    "s_buffer_load_dword s3, s[4:7], s3 offset:0xc0000 glc\n\t" \
    "s_waitcnt lgkmcnt(0)\n\t" \
    "s_xor_b32 s12, s12, s34\n\t" \
    "s_xor_b32 s13, s13, s34\n\t" \
    "s_xor_b32 s14, s14, s34\n\t" \
    "s_xor_b32 s15, s15, s34\n\t" \
    "s_xor_b32 s16, s16, s34\n\t" \
    "s_xor_b32 s17, s17, s34\n\t" \
    "s_xor_b32 s18, s18, s34\n\t" \
    "s_xor_b32 s19, s19, s34\n\t" \
    "s_xor_b32 s3, s3, s34\n\t" \
    "s_or_b32 s12, s12, s13\n\t" \
    "s_or_b32 s12, s12, s14\n\t" \
    "s_or_b32 s12, s12, s15\n\t" \
    "s_or_b32 s12, s12, s16\n\t" \
    "s_or_b32 s12, s12, s17\n\t" \
    "s_or_b32 s12, s12, s18\n\t" \
    "s_or_b32 s12, s12, s19\n\t" \
    "s_or_b32 s12, s12, s3\n\t" \
    "s_cbranch_scc0 .Lb24_%=\n\t" \
    "s_add_u32 s40, s40, 1\n\t" \
    "s_cmp_lt_u32 s40, 1\n\t" \
    "s_cbranch_scc1 .Lr1025_%=\n\t" \
    "s_branch .Ls24_%=\n\t" \
    ".Lqa_%=:\n\t" \
    "s_sub_u32 s37, s24, 4\n\t" \
    "s_bfe_u32 s38, s2, 0x20002\n\t" \
    "s_lshl1_add_u32 s38, s38, s37\n\t" \
    "s_lshl_b32 s42, s38, 21\n\t" \
    "s_cmp_ge_u32 s38, 7\n\t" \
    "s_cbranch_scc1 .Lqd_%=\n\t" \
    "s_cmp_ge_u32 s37, 2\n\t" \
    "s_cbranch_scc1 .Lqd_%=\n\t" \
    "s_memtime s[62:63]\n\t" \
    "s_waitcnt lgkmcnt(0)\n\t" \
    "s_sleep 4\n\t" \
    "s_lshl_b32 s3, s38, 10\n\t" \
    "s_and_b32 s37, s8, 0x1fffff\n\t" \
    "s_add_u32 s38, s37, 16383\n\t" \
    "s_andn2_b32 s38, s38, 16383\n\t" \
    "s_add_u32 s39, s37, 9617408\n\t" \
    "s_andn2_b32 s39, s39, 16383\n\t" \
    "s_max_u32 s42, s42, s38\n\t" \
    "s_min_u32 s42, s42, s39\n\t" \
    "s_sub_u32 s42, s42, s37\n\t" \
    "s_cmp_eq_u32 s2, 255\n\t" \
    "s_cselect_b64 s[16:17], s[4:5], s[8:9]\n\t" \
    "s_cselect_b32 s42, 34684928, s42\n\t" \
    "s_add_u32 s16, s16, s42\n\t" \
    "s_addc_u32 s17, s17, 0\n\t" \
    "s_mov_b32 s18, 16384\n\t" \
    "s_mov_b32 s19, 0x20000\n\t" \
    "s_add_u32 s37, s3, 0x2000\n\t" \
    "buffer_store_dwordx4 v[20:23], v0, s[16:19], s3 offen sc1\n\t" \
    "buffer_store_dwordx4 v[20:23], v0, s[16:19], s37 offen sc1\n\t" \
    "s_nop 0\n\t" \
    "s_mov_b32 s39, 34668544\n\t" \
    "v_mov_b32 v20, 0\n\t" \
    "buffer_load_dword v21, v20, s[4:7], s39 offen sc1\n\t" \
    "s_mov_b32 s60, -1\n\t" \
    "s_mov_b32 s61, -1\n\t" \
    "s_mov_b32 s40, 0\n\t" \
    "s_waitcnt vmcnt(1) lgkmcnt(0)\n\t" \
    "s_memtime s[58:59]\n\t" \
    "s_waitcnt lgkmcnt(0)\n\t" \
    ".Lqc_%=:\n\t" \
    "s_buffer_load_dword s38, s[16:19], s3\n\t" \
    "s_mov_b32 s56, s58\n\t" \
    "s_xor_b32 s3, s3, 0x2000\n\t" \
    "s_waitcnt lgkmcnt(0)\n\t" \
    "s_memtime s[58:59]\n\t" \
    "s_add_u32 s40, s40, 1\n\t" \
    "s_waitcnt lgkmcnt(0)\n\t" \
    "s_sub_u32 s56, s58, s56\n\t" \
    "s_min_u32 s56, s56, s60\n\t" \
    "s_mov_b32 s60, s61\n\t" \
    "s_mov_b32 s61, s56\n\t" \
    "s_bitcmp1_b32 s40, 0\n\t" \
    "s_cbranch_scc1 .Lqc_%=\n\t" \
    "s_add_u32 s3, s3, 0x80\n\t" \
    "s_sub_u32 s37, s58, s62\n\t" \
    "s_cmp_gt_u32 s37, 5200\n\t" \
    "s_cselect_b32 s40, 98, s40\n\t" \
    "s_absdiff_i32 s37, s60, s61\n\t" \
    "s_min_u32 s38, s60, s61\n\t" \
    "s_cmp_gt_u32 s38, 500\n\t" \
    "s_cselect_b32 s37, 0, s37\n\t" \
    "s_cmp_lt_u32 s37, 80\n\t" \
    "s_cselect_b32 s37, 16, 4\n\t" \
    "s_cmp_lt_u32 s40, s37\n\t" \
    "s_cbranch_scc1 .Lqc_%=\n\t" \
    "s_waitcnt vmcnt(0)\n\t" \
    "v_readfirstlane_b32 s34, v21\n\t" \
    "s_cmp_gt_u32 s60, s61\n\t" \
    "s_cselect_b32 s0, 2, 0\n\t" \
    "s_mul_i32 s34, s34, 1664525\n\t" \
    "s_add_u32 s34, s34, 1013904223\n\t" \
    "s_or_b32 s34, s34, 1\n\t" \
    "s_cmp_eq_u32 s2, 255\n\t" \
    "s_cbranch_scc1 .Lqf_%=\n\t" \
    "s_xor_b32 s37, s0, s34\n\t" \
    "v_mov_b32 v21, s37\n\t" \
    "s_bfe_u32 s37, s2, 0x20002\n\t" \
    "s_lshl1_add_u32 s37, s37, s24\n\t" \
    "s_lshl_b32 s37, s37, 7\n\t" \
    "s_add_u32 s37, s37, 34670080\n\t" \
    "v_lshrrev_b32 v20, 2, v0\n\t" \
    "s_mov_b32 exec_hi, 0\n\t" \
    "buffer_store_dword v21, v20, s[4:7], s37 offen sc1\n\t" \
    "s_mov_b64 exec, -1\n\t" \
    ".Lqg_%=:\n\t" \
    "s_branch .Lqd_%=\n\t" \
    ".Lqf_%=:\n\t" \
    "s_getreg_b32 s37, hwreg(HW_REG_XCC_ID, 0, 2)\n\t" \
    "s_lshl1_add_u32 s37, s0, s37\n\t" \
    "s_add_u32 s37, s37, 1\n\t" \
    "s_and_b32 s37, s37, 7\n\t" \
    "s_lshl_b32 s37, s37, 1\n\t" \
    "s_xor_b32 s37, s37, s34\n\t" \
    "v_mov_b32 v20, s37\n\t" \
    "v_mov_b32 v21, s37\n\t" \
    "v_mov_b32 v22, s37\n\t" \
    "v_mov_b32 v23, s37\n\t" \
    "s_mov_b32 s39, 34669568\n\t" \
    "buffer_store_dwordx4 v[20:23], v0, s[4:7], s39 offen sc1\n\t" \
    "s_nop 0\n\t" \
    "s_branch .Lqg_%=\n\t" \
    ".Lr2_%=:\n\t" \
    "s_add_u32 s40, s40, 1\n\t" \
    "s_cmp_gt_u32 s40, 32\n\t" \
    "s_cbranch_scc1 .Lr3_%=\n\t" \
    "s_sleep 2\n\t" \
    "s_and_b32 s37, s2, 7\n\t" \
    "s_lshl_b32 s37, s37, 10\n\t" \
    "s_add_u32 s37, s37, 34676736\n\t" \
    "v_and_b32 v51, 0x70, v0\n\t" \
    "v_lshlrev_b32 v51, 3, v51\n\t" \
    "buffer_load_dword v50, v51, s[4:7], s37 offen sc1\n\t" \
    "s_waitcnt vmcnt(0)\n\t" \
    "v_readlane_b32 s56, v50, 0\n\t" \
    "v_readlane_b32 s57, v50, 1\n\t" \
    "v_readlane_b32 s58, v50, 2\n\t" \
    "v_readlane_b32 s59, v50, 3\n\t" \
    "v_readlane_b32 s60, v50, 4\n\t" \
    "v_readlane_b32 s61, v50, 5\n\t" \
    "v_readlane_b32 s62, v50, 6\n\t" \
    "v_readlane_b32 s63, v50, 7\n\t" \
    "s_branch .Lr1_%=\n\t" \
    ".Lq5_%=:\n\t" \
    "s_add_u32 s40, s40, 1\n\t" \
    "s_cmp_gt_u32 s40, 8192\n\t" \
    "s_cbranch_scc1 .Lq6_%=\n\t" \
    "s_sleep 4\n\t" \
    "v_and_b32 v19, 0x70, v0\n\t" \
    "v_lshlrev_b32 v19, 3, v19\n\t" \
    "s_mov_b32 s37, 34670592\n\t" \
    "buffer_load_dword v17, v19, s[4:7], s37 offen sc1\n\t" \
    "s_waitcnt vmcnt(0)\n\t" \
    "v_readlane_b32 s56, v17, 0\n\t" \
    "v_readlane_b32 s57, v17, 1\n\t" \
    "v_readlane_b32 s58, v17, 2\n\t" \
    "v_readlane_b32 s59, v17, 3\n\t" \
    "v_readlane_b32 s60, v17, 4\n\t" \
    "v_readlane_b32 s61, v17, 5\n\t" \
    "s_branch .Lq4_%=\n\t" \
    ".Lr5_%=:\n\t" \
    "s_add_u32 s40, s40, 1\n\t" \
    "s_cmp_gt_u32 s40, 8192\n\t" \
    "s_cbranch_scc1 .Lr6_%=\n\t" \
    "s_sleep 4\n\t" \
    "v_mov_b32 v16, 0\n\t" \
    "buffer_load_dword v8, v16, s[4:7], s52 offen sc1\n\t" \
    "s_waitcnt vmcnt(0)\n\t" \
    "v_readfirstlane_b32 s36, v8\n\t" \
    "s_branch .Lr4_%=\n\t" \
    ".Ls24_%=:\n\t" \
    "s_mov_b32 s40, 0\n\t" \
    "s_add_u32 s38, s33, 786432\n\t" \
    ".Ll24_%=:\n\t" \
    "s_sleep 2\n\t" \
    "buffer_load_dword v1, v50, s[4:7], s38 offen sc1\n\t" \
    "s_waitcnt vmcnt(0)\n\t" \
    "v_cmp_ne_u32 vcc, s34, v1\n\t" \
    "s_add_u32 s40, s40, 1\n\t" \
    "s_cmp_ge_u32 s40, 8192\n\t" \
    "s_cbranch_scc1 .Lb24_%=\n\t" \
    "s_cbranch_vccnz .Ll24_%=\n\t" \
    "s_branch .Lb24_%=\n\t" \
    ".Ls27_%=:\n\t" \
    "s_mov_b32 s40, 0\n\t" \
    "s_add_u32 s38, s55, 34603008\n\t" \
    ".Ll27_%=:\n\t" \
    "s_sleep 2\n\t" \
    "buffer_load_dword v3, v47, s[4:7], s38 offen sc1\n\t" \
    "s_waitcnt vmcnt(0)\n\t" \
    "v_cmp_ne_u32 vcc, s34, v3\n\t" \
    "s_add_u32 s40, s40, 1\n\t" \
    "s_cmp_ge_u32 s40, 8192\n\t" \
    "s_cbranch_scc1 .Lb27_%=\n\t" \
    "s_cbranch_vccnz .Ll27_%=\n\t" \
    "s_branch .Lb27_%=\n\t" \
    ".Ls50_%=:\n\t" \
    "s_mov_b32 s40, 0\n\t" \
    ".Ll50_%=:\n\t" \
    "s_add_u32 s54, s53, 0\n\t" \
    "buffer_load_dwordx4 v[64:67], v0, s[4:7], s54 offen offset:0 sc1\n\t" \
    "s_add_u32 s54, s53, 4243456\n\t" \
    "buffer_load_dwordx4 v[44:47], v0, s[4:7], s54 offen offset:0 sc1\n\t" \
    "s_waitcnt vmcnt(0)\n\t" \
    "v_and_b32 v36, v64, v66\n\t" \
    "v_bitop3_b32 v36, v36, v44, v46 bitop3:0x80\n\t" \
    "v_and_b32 v36, 1, v36\n\t" \
    "v_cmp_eq_u32 vcc, 0, v36\n\t" \
    "s_add_u32 s40, s40, 1\n\t" \
    "s_cmp_ge_u32 s40, 8192\n\t" \
    "s_cbranch_scc1 .Lb50_%=\n\t" \
    "s_cbranch_vccnz .Ll50_%=\n\t" \
    "s_branch .Lb50_%=\n\t" \
    ".Ls51_%=:\n\t" \
    "s_mov_b32 s40, 0\n\t" \
    ".Ll51_%=:\n\t" \
    "s_add_u32 s54, s53, 524288\n\t" \
    "buffer_load_dwordx4 v[108:111], v0, s[4:7], s54 offen offset:0 sc1\n\t" \
    "s_add_u32 s54, s53, 4767744\n\t" \
    "buffer_load_dwordx4 v[24:27], v0, s[4:7], s54 offen offset:0 sc1\n\t" \
    "s_waitcnt vmcnt(0)\n\t" \
    "v_and_b32 v51, v108, v110\n\t" \
    "v_bitop3_b32 v51, v51, v24, v26 bitop3:0x80\n\t" \
    "v_and_b32 v51, 1, v51\n\t" \
    "v_cmp_eq_u32 vcc, 0, v51\n\t" \
    "s_add_u32 s40, s40, 1\n\t" \
    "s_cmp_ge_u32 s40, 8192\n\t" \
    "s_cbranch_scc1 .Lb51_%=\n\t" \
    "s_cbranch_vccnz .Ll51_%=\n\t" \
    "s_branch .Lb51_%=\n\t" \
    ".Ls52_%=:\n\t" \
    "s_mov_b32 s40, 0\n\t" \
    ".Ll52_%=:\n\t" \
    "s_add_u32 s54, s53, 1048576\n\t" \
    "buffer_load_dwordx4 v[180:183], v0, s[4:7], s54 offen offset:0 sc1\n\t" \
    "s_add_u32 s54, s53, 5292032\n\t" \
    "buffer_load_dwordx4 v[220:223], v0, s[4:7], s54 offen offset:0 sc1\n\t" \
    "s_waitcnt vmcnt(0)\n\t" \
    "v_and_b32 v37, v180, v182\n\t" \
    "v_bitop3_b32 v37, v37, v220, v222 bitop3:0x80\n\t" \
    "v_and_b32 v37, 1, v37\n\t" \
    "v_cmp_eq_u32 vcc, 0, v37\n\t" \
    "s_add_u32 s40, s40, 1\n\t" \
    "s_cmp_ge_u32 s40, 8192\n\t" \
    "s_cbranch_scc1 .Lb52_%=\n\t" \
    "s_cbranch_vccnz .Ll52_%=\n\t" \
    "s_branch .Lb52_%=\n\t" \
    ".Lfin_%=:\n\t"
#define K_CLOBBERS "v1", "v2", "v3", "v4", "v5", "v6", "v7", "v8", "v9", "v10", "v11", "v12", "v13", "v14", "v15", "v16", "v17", "v18", "v19", "v20", "v21", "v22", "v23", "v24", "v25", "v26", "v27", "v28", "v29", "v30", "v31", "v32", "v33", "v34", "v35", "v36", "v37", "v38", "v39", "v40", "v41", "v42", "v43", "v44", "v45", "v46", "v47", "v48", "v49", "v50", "v51", "v52", "v53", "v54", "v55", "v56", "v57", "v58", "v59", "v60", "v61", "v62", "v63", "v64", "v65", "v66", "v67", "v68", "v69", "v70", "v71", "v72", "v73", "v74", "v75", "v76", "v77", "v78", "v79", "v80", "v81", "v82", "v83", "v84", "v85", "v86", "v87", "v88", "v89", "v90", "v91", "v92", "v93", "v94", "v95", "v96", "v97", "v98", "v99", "v100", "v101", "v102", "v103", "v104", "v105", "v106", "v107", "v108", "v109", "v110", "v111", "v112", "v113", "v114", "v115", "v116", "v117", "v118", "v119", "v120", "v121", "v122", "v123", "v124", "v125", "v126", "v127", "v128", "v129", "v130", "v131", "v132", "v133", "v134", "v135", "v136", "v137", "v138", "v139", "v140", "v141", "v142", "v143", "v144", "v145", "v146", "v147", "v148", "v149", "v150", "v151", "v152", "v153", "v154", "v155", "v156", "v157", "v158", "v159", "v160", "v161", "v162", "v163", "v164", "v165", "v166", "v167", "v168", "v169", "v170", "v171", "v172", "v173", "v174", "v175", "v176", "v177", "v178", "v179", "v180", "v181", "v182", "v183", "v184", "v185", "v186", "v187", "v188", "v189", "v190", "v191", "v192", "v193", "v194", "v195", "v196", "v197", "v198", "v199", "v200", "v201", "v202", "v203", "v204", "v205", "v206", "v207", "v208", "v209", "v210", "v211", "v212", "v213", "v214", "v215", "v216", "v217", "v218", "v219", "v220", "v221", "v222", "v223", "v224", "v225", "v226", "v227", "v228", "v229", "v230", "v231", "v232", "v233", "v234", "v235", "v236", "v237", "v238", "v239", "v240", "v241", "v242", "v243", "v244", "v245", "v246", "v247", "v248", "v249", "v250", "v251", "v252", "v253", "v254", "v255", "s3", "s4", "s5", "s6", "s7", "s8", "s9", "s10", "s11", "s12", "s13", "s14", "s15", "s16", "s17", "s18", "s19", "s20", "s21", "s22", "s23", "s24", "s25", "s26", "s27", "s28", "s29", "s30", "s31", "s32", "s33", "s34", "s35", "s36", "s37", "s38", "s39", "s40", "s41", "s42", "s43", "s44", "s45", "s46", "s47", "s48", "s49", "s50", "s51", "s52", "s53", "s54", "s55", "s56", "s57", "s58", "s59", "s60", "s61", "s62", "s63", "s64", "s65"

extern "C" __global__ __launch_bounds__(512, 2)
void k_conv(const char* __restrict__ x, const char* __restrict__ wgt, char* __restrict__ ws, char* __restrict__ out)
{
  __shared__ __attribute__((aligned(256))) char smem[157568];
  unsigned tid = threadIdx.x;
  auto kp = __builtin_amdgcn_kernarg_segment_ptr();
  asm volatile(K_BODY : "+{v0}"(tid), "+{s[0:1]}"(kp)
               : "{s2}"(blockIdx.x), "i"((__attribute__((address_space(3))) char*)smem)
               : "memory", "scc", "vcc", K_CLOBBERS);
}

extern "C" void kernel_launch(void* const* d_in, const int* in_sizes, int n_in,
                              void* d_out, int out_size, void* d_ws, size_t ws_size,
                              hipStream_t stream) {
  k_conv<<<dim3(256), dim3(512), 0, stream>>>((const char*)d_in[0], (const char*)d_in[1], (char*)d_ws + WS_OFF, (char*)d_out);
}
```

```cpp
#include <hip/hip_runtime.h>
#include <hip/hip_bf16.h>

#define K_BODY \
    "s_load_dwordx2 s[16:17], s[0:1], 0x0\n\t" \
    "s_load_dwordx2 s[4:5], s[0:1], 0x10\n\t" \
    "s_load_dwordx2 s[12:13], s[0:1], 0x8\n\t" \
    "s_load_dwordx2 s[8:9], s[0:1], 0x18\n\t" \
    "s_getpc_b64 s[22:23]\n\t" \
    ".Lpc_%=:\n\t" \
    "s_cmp_ge_u32 s2, 16\n\t" \
    "s_cbranch_scc1 .Le1_%=\n\t" \
    "s_mov_b32 m0, 157312\n\t" \
    "v_lshlrev_b32 v8, 6, v0\n\t" \
    "v_min_u32 v8, .Lfin_%=-.Lpc_%=, v8\n\t" \
    "global_load_lds_dword v8, s[22:23]\n\t" \
    ".Le1_%=:\n\t" \
    "v_readfirstlane_b32 s24, v0\n\t" \
    "s_and_b32 s25, s2, 3\n\t" \
    "s_lshl_b32 s25, s25, 5\n\t" \
    "s_lshr_b32 s37, s2, 3\n\t" \
    "s_add_u32 s25, s25, s37\n\t" \
    "s_min_u32 s28, s25, 125\n\t" \
    "s_mul_i32 s26, s28, 57\n\t" \
    "s_lshr_b32 s26, s26, 9\n\t" \
    "s_mul_i32 s37, s26, 9\n\t" \
    "s_sub_u32 s27, s28, s37\n\t" \
    "s_bfe_u32 s37, s2, 0x10002\n\t" \
    "s_lshl1_add_u32 s26, s26, s37\n\t" \
    "v_and_b32 v0, 63, v0\n\t" \
    "v_lshlrev_b32 v0, 4, v0\n\t" \
    "s_lshr_b32 s24, s24, 6\n\t" \
    "s_sub_u32 s37, s24, 4\n\t" \
    "s_cbranch_scc1 .Le2_%=\n\t" \
    "s_cmp_ge_u32 s25, 126\n\t" \
    "s_cbranch_scc1 .Le2_%=\n\t" \
    "s_mul_i32 m0, s37, 12416\n\t" \
    "s_lshl2_add_u32 s37, s26, s37\n\t" \
    "s_mul_i32 s37, s37, 86016\n\t" \
    "s_mul_i32 s38, s27, 4992\n\t" \
    "s_add_u32 s37, s37, s38\n\t" \
    "s_addk_i32 s37, 896\n\t" \
    "v_add_u32 v9, s37, v0\n\t" \
    "s_cmp_eq_u32 s27, 0\n\t" \
    "s_cselect_b32 s38, 43008, 0\n\t" \
    "s_sub_u32 s39, s38, 2048\n\t" \
    "v_add_u32 v7, s39, v9\n\t" \
    "v_subrev_u32 v8, 0x400, v9\n\t" \
    "s_mov_b64 exec, 0xff\n\t" \
    "v_add_u32 v8, s38, v8\n\t" \
    "s_mov_b64 exec, -1\n\t" \
    "s_mov_b32 s18, 9633792\n\t" \
    "s_mov_b32 s19, 0x20000\n\t" \
    "s_mov_b32 s39, 43008\n\t" \
    "s_waitcnt lgkmcnt(0)\n\t" \
    "buffer_load_dwordx4 v7, s[16:19], 0 offen offset:0 sc1 nt lds\n\t" \
    "s_add_u32 m0, m0, 2080\n\t" \
    "s_nop 0\n\t" \
    "buffer_load_dwordx4 v9, s[16:19], 0 offen offset:3072 sc1 nt lds\n\t" \
    "buffer_load_dwordx4 v9, s[16:19], 0 offen offset:2048 sc1 nt lds\n\t" \
    "s_add_u32 m0, m0, 4128\n\t" \
    "s_nop 0\n\t" \
    "buffer_load_dwordx4 v7, s[16:19], s39 offen offset:0 sc1 nt lds\n\t" \
    "s_add_u32 m0, m0, 2080\n\t" \
    "s_nop 0\n\t" \
    "buffer_load_dwordx4 v9, s[16:19], s39 offen offset:3072 sc1 nt lds\n\t" \
    "buffer_load_dwordx4 v9, s[16:19], s39 offen offset:2048 sc1 nt lds\n\t" \
    "buffer_load_dwordx4 v9, s[16:19], s39 offen offset:1024 sc1 nt lds\n\t" \
    "s_add_u32 m0, m0, 4294966240\n\t" \
    "s_nop 0\n\t" \
    "buffer_load_dwordx4 v8, s[16:19], s39 offen offset:0 sc1 nt lds\n\t" \
    "s_add_u32 m0, m0, 1024\n\t" \
    "s_nop 0\n\t" \
    "buffer_load_dwordx4 v9, s[16:19], s39 offen offset:0 sc1 nt lds\n\t" \
    "s_add_u32 m0, m0, 4294960064\n\t" \
    "s_nop 0\n\t" \
    "buffer_load_dwordx4 v8, s[16:19], 0 offen offset:0 sc1 nt lds\n\t" \
    "s_add_u32 m0, m0, 1024\n\t" \
    "s_nop 0\n\t" \
    "buffer_load_dwordx4 v9, s[16:19], 0 offen offset:0 sc1 nt lds\n\t" \
    "s_add_u32 m0, m0, 32\n\t" \
    "s_nop 0\n\t" \
    "buffer_load_dwordx4 v9, s[16:19], 0 offen offset:1024 sc1 nt lds\n\t" \
    ".Le2_%=:\n\t" \
    "s_mov_b32 s6, 34701312\n\t" \
    "s_mov_b32 s7, 0x20000\n\t" \
    "v_lshrrev_b32 v8, 7, v0\n\t" \
    "v_and_b32 v9, 0x70, v0\n\t" \
    "v_lshrrev_b32 v2, 8, v0\n\t" \
    "v_lshl_or_b32 v2, v2, 16, v9\n\t" \
    "v_and_b32 v10, 0x80, v0\n\t" \
    "v_lshl_or_b32 v2, v10, 6, v2\n\t" \
    "s_lshl_b32 s32, s2, 7\n\t" \
    "s_andn2_b32 s37, s32, 0x1fff\n\t" \
    "s_add_u32 s32, s32, s37\n\t" \
    "s_add_u32 s32, s32, 33554432\n\t" \
    "s_cmp_ge_u32 s24, 4\n\t" \
    "s_cbranch_scc1 .Le3_%=\n\t" \
    "v_mov_b32 v12, 0\n\t" \
    "v_mov_b32 v13, 0\n\t" \
    "v_mov_b32 v14, 0\n\t" \
    "v_mov_b32 v15, 0\n\t" \
    "s_lshl_b32 s37, s24, 18\n\t" \
    "s_add_u32 s37, s37, s32\n\t" \
    "s_xor_b32 s38, s24, 2\n\t" \
    "s_lshr_b32 s39, s2, 3\n\t" \
    "s_or_b32 s38, s38, s39\n\t" \
    "s_memrealtime s[22:23]\n\t" \
    "s_waitcnt lgkmcnt(0)\n\t" \
    "s_cbranch_scc1 .Le4_%=\n\t" \
    "v_lshl_or_b32 v8, v8, 10, v9\n\t" \
    "v_mov_b32 v16, s22\n\t" \
    "v_mov_b32 v17, s22\n\t" \
    "v_mov_b32 v18, s22\n\t" \
    "v_mov_b32 v19, s22\n\t" \
    "s_add_u32 s38, s32, 1122304\n\t" \
    "buffer_store_dwordx4 v[16:19], v8, s[4:7], s38 offen sc1\n\t" \
    ".Le4_%=:\n\t" \
    ".Le3_%=:\n\t" \
    "s_bitcmp1_b32 s2, 2\n\t" \
    "s_cselect_b32 s38, 126, 0\n\t" \
    "s_add_u32 s28, s28, s38\n\t" \
    "s_mul_i32 s30, s28, 293\n\t" \
    "s_lshr_b32 s30, s30, 11\n\t" \
    "s_mul_i32 s37, s30, 7\n\t" \
    "s_sub_u32 s28, s28, s37\n\t" \
    "s_mul_i32 s29, s30, 43\n\t" \
    "s_lshr_b32 s29, s29, 7\n\t" \
    "s_mul_i32 s37, s29, 3\n\t" \
    "s_sub_u32 s30, s30, s37\n\t" \
    "s_and_b32 s37, s29, 12\n\t" \
    "s_add_u32 s37, s37, s29\n\t" \
    "s_mul_i32 s31, s37, 768\n\t" \
    "s_lshl_b32 s37, s30, 6\n\t" \
    "s_add_u32 s31, s31, s37\n\t" \
    "s_add_u32 s41, s29, s30\n\t" \
    "s_add_u32 s41, s41, s28\n\t" \
    "s_and_b32 s41, s41, 3\n\t" \
    "s_cmp_lt_u32 s25, 126\n\t" \
    "s_cselect_b32 s14, 903168, 0\n\t" \
    "s_mov_b32 s15, 0x20000\n\t" \
    "v_lshrrev_b32 v10, 6, v0\n\t" \
    "v_lshrrev_b32 v11, 9, v0\n\t" \
    "v_sub_u32 v8, v10, v11\n\t" \
    "v_mul_u32_u24 v8, 18432, v8\n\t" \
    "v_and_b32 v9, 0x30, v0\n\t" \
    "v_lshlrev_b32 v11, 5, v11\n\t" \
    "v_bitop3_b32 v1, v8, v9, v11 bitop3:0xf6\n\t" \
    "s_mov_b32 exec_lo, 0xf0000000\n\t" \
    "s_mov_b32 exec_hi, 0xf0000000\n\t" \
    "v_bfrev_b32 v1, 1\n\t" \
    "s_mov_b64 exec, -1\n\t" \
    "s_cmp_ge_u32 s24, 4\n\t" \
    "s_cbranch_scc1 .Le9_%=\n\t" \
    "v_bfe_u32 v20, v0, 4, 4\n\t" \
    "v_and_b32 v10, 12, v10\n\t" \
    "v_cvt_f32_ubyte0 v21, v10\n\t" \
    "v_and_b32 v22, 1, v20\n\t" \
    "v_cvt_f32_ubyte0 v22, v22\n\t" \
    "v_mul_f32 v22, 0x3e800000, v22\n\t" \
    "v_lshrrev_b32 v23, 1, v20\n\t" \
    "v_mov_b32 v32, 0\n\t" \
    "v_mov_b32 v33, -1\n\t" \
    "v_mov_b32 v31, 0x3e800000\n\t" \
    "s_mov_b32 s65, 0\n\t" \
    "s_mov_b32 s40, 0\n\t" \
    "s_cmp_eq_u32 s24, 1\n\t" \
    "s_cbranch_scc1 .Lm1_%=\n\t" \
    "s_cmp_eq_u32 s24, 2\n\t" \
    "s_cbranch_scc1 .Lm2_%=\n\t" \
    "s_cmp_eq_u32 s24, 3\n\t" \
    "s_cbranch_scc1 .Lm3_%=\n\t" \
    "s_add_u32 s37, s32, 1048576\n\t" \
    "s_mov_b64 exec, 0xff\n\t" \
    "s_mov_b64 exec, -1\n\t" \
    "v_or_b32 v24, 1, v20\n\t" \
    "v_cvt_f32_ubyte0 v24, v24\n\t" \
    "v_mul_f32 v24, 0x3c800000, v24\n\t" \
    "v_lshrrev_b32 v25, 4, v0\n\t" \
    "v_and_b32 v25, 16, v25\n\t" \
    "v_lshrrev_b32 v26, 2, v11\n\t" \
    "v_or_b32 v25, v25, v26\n\t" \
    "v_cvt_f32_ubyte0 v25, v25\n\t" \
    "v_add_f32 v26, 2.0, v25\n\t" \
    "v_add_f32 v27, 4.0, v25\n\t" \
    "v_add_f32 v28, 4.0, v26\n\t" \
    "v_mov_b32 v29, v22\n\t" \
    "v_mov_b32 v30, v22\n\t" \
    "v_mov_b32 v31, v24\n\t" \
    "s_mov_b32 s64, 0x3e800000\n\t" \
    "s_mov_b32 s65, 0x3e800000\n\t" \
    "v_add_u32 v34, 154880, v0\n\t" \
    "s_branch .Ltl_%=\n\t" \
    ".Lm1_%=:\n\t" \
    "v_lshrrev_b32 v24, 5, v0\n\t" \
    "v_and_b32 v24, 12, v24\n\t" \
    "v_and_or_b32 v24, v20, 3, v24\n\t" \
    "v_cvt_f32_ubyte0 v24, v24\n\t" \
    "v_mul_f32 v24, 0x3d924925, v24\n\t" \
    "v_mul_f32 v32, 0x41000000, v24\n\t" \
    "v_lshrrev_b32 v27, 3, v11\n\t" \
    "v_cvt_f32_ubyte0 v27, v27\n\t" \
    "v_add_f32 v25, -2.0, v27\n\t" \
    "v_add_f32 v26, -1.0, v27\n\t" \
    "v_add_f32 v28, 1.0, v27\n\t" \
    "v_and_b32 v29, 4, v20\n\t" \
    "v_cvt_f32_ubyte0 v29, v29\n\t" \
    "v_mul_f32 v29, 0x3d800000, v29\n\t" \
    "v_mov_b32 v30, v29\n\t" \
    "v_mov_b32 v31, 0xbe800000\n\t" \
    "s_mov_b32 s64, 0\n\t" \
    "s_mov_b32 exec_hi, 0\n\t" \
    "v_mov_b32 v33, 0\n\t" \
    "s_mov_b64 exec, -1\n\t" \
    "v_add_u32 v34, 149760, v0\n\t" \
    "s_branch .Ltl_%=\n\t" \
    ".Lm2_%=:\n\t" \
    "v_add_u32 v24, 6, v20\n\t" \
    "v_cvt_f32_ubyte0 v24, v24\n\t" \
    "v_mul_f32 v24, 0x3d000000, v24\n\t" \
    "v_add_f32 v25, 0.5, v21\n\t" \
    "v_add_f32 v26, 1.0, v25\n\t" \
    "v_add_f32 v27, 2.0, v25\n\t" \
    "v_add_f32 v28, 2.0, v26\n\t" \
    "v_mov_b32 v29, 0\n\t" \
    "v_mov_b32 v30, 0\n\t" \
    "s_mov_b32 s64, 0.5\n\t" \
    "v_add_u32 v34, 152832, v0\n\t" \
    "s_branch .Ltl_%=\n\t" \
    ".Lm3_%=:\n\t" \
    "s_mov_b32 s37, 34668544\n\t" \
    "buffer_load_dword v3, v12, s[4:7], s37 offen sc1\n\t" \
    "v_lshrrev_b32 v8, 2, v0\n\t" \
    "v_and_b32 v8, 28, v8\n\t" \
    "v_add_u32 v8, 156928, v8\n\t" \
    "ds_write_b32 v8, v12\n\t" \
    "v_add_u32 v24, 6, v23\n\t" \
    "v_cvt_f32_ubyte0 v24, v24\n\t" \
    "v_mul_f32 v24, 0x3d924925, v24\n\t" \
    "v_mov_b32 v25, v21\n\t" \
    "v_add_f32 v26, 1.0, v21\n\t" \
    "v_add_f32 v27, 2.0, v21\n\t" \
    "v_add_f32 v28, 2.0, v26\n\t" \
    "v_sub_f32 v29, 0, v22\n\t" \
    "v_sub_f32 v30, 0, v22\n\t" \
    "s_mov_b32 exec_lo, 0\n\t" \
    "v_fma_f32 v25, v21, 0.5, 4.0\n\t" \
    "v_add_f32 v27, 1.0, v25\n\t" \
    "v_fmaak_f32 v26, v21, v31, 0x41200000\n\t" \
    "v_mov_b32 v28, v26\n\t" \
    "v_add_f32 v30, v31, v30\n\t" \
    "s_mov_b64 exec, -1\n\t" \
    "s_mov_b32 s40, 0x11\n\t" \
    "v_add_u32 v34, 151808, v0\n\t" \
    ".Ltl_%=:\n\t" \
    "v_fma_f32 v36, v24, v25, v29\n\t" \
    "v_fma_f32 v38, v24, v26, v29\n\t" \
    "v_fma_f32 v40, v24, v27, v29\n\t" \
    "v_fma_f32 v42, v24, v28, v30\n\t" \
    "v_add_f32 v37, v36, v31\n\t" \
    "v_add_f32 v39, v38, v31\n\t" \
    "v_add_f32 v41, v40, v31\n\t" \
    "v_add_f32 v43, v42, v31\n\t" \
    "v_cos_f32 v36, v36\n\t" \
    "v_cos_f32 v37, v37\n\t" \
    "v_cos_f32 v38, v38\n\t" \
    "v_cos_f32 v39, v39\n\t" \
    "v_cos_f32 v40, v40\n\t" \
    "v_cos_f32 v41, v41\n\t" \
    "v_cos_f32 v42, v42\n\t" \
    "v_cos_f32 v43, v43\n\t" \
    "s_bitcmp0_b32 s40, 4\n\t" \
    "s_cbranch_scc1 .Lt2_%=\n\t" \
    "v_mul_f32 v36, 0x3b924925, v36\n\t" \
    "v_mul_f32 v37, 0x3b924925, v37\n\t" \
    "v_mul_f32 v38, 0x3b924925, v38\n\t" \
    "v_mul_f32 v39, 0x3b924925, v39\n\t" \
    "v_mul_f32 v40, 0x3b924925, v40\n\t" \
    "v_mul_f32 v41, 0x3b924925, v41\n\t" \
    "v_mul_f32 v42, 0x3b924925, v42\n\t" \
    "v_mul_f32 v43, 0x3b924925, v43\n\t" \
    ".Lt2_%=:\n\t" \
    "v_cvt_pk_bf16_f32 v44, v36, v37\n\t" \
    "v_cvt_pk_bf16_f32 v45, v38, v39\n\t" \
    "v_cvt_pk_bf16_f32 v46, v40, v41\n\t" \
    "v_cvt_pk_bf16_f32 v47, v42, v43\n\t" \
    "v_and_b32 v44, v33, v44\n\t" \
    "v_and_b32 v45, v33, v45\n\t" \
    "v_mov_b32 v33, -1\n\t" \
    "v_add_f32 v24, s64, v24\n\t" \
    "v_add_f32 v31, s65, v31\n\t" \
    "v_add_f32 v29, v32, v29\n\t" \
    "v_add_f32 v30, v32, v30\n\t" \
    "ds_write_b128 v34, v[44:47]\n\t" \
    "s_add_u32 s40, s40, 1\n\t" \
    "s_bitcmp1_b32 s40, 0\n\t" \
    "v_add_u32 v34, 0x400, v34\n\t" \
    "s_cbranch_scc1 .Ltl_%=\n\t" \
    "s_bitcmp1_b32 s40, 2\n\t" \
    "s_cbranch_scc1 .Lt4_%=\n\t" \
    "s_mov_b32 s40, 2\n\t" \
    "s_lshl2_add_u32 s37, s28, s24\n\t" \
    "s_lshl_b32 s37, s37, 2\n\t" \
    "v_lshrrev_b32 v25, 2, v20\n\t" \
    "v_add_u32 v25, s37, v25\n\t" \
    "v_mul_u32_u24 v26, 147, v25\n\t" \
    "v_lshrrev_b32 v26, 10, v26\n\t" \
    "v_lshl_or_b32 v24, v26, 1, 1\n\t" \
    "v_cvt_f32_ubyte0 v24, v24\n\t" \
    "v_mul_f32 v24, 0x3c800000, v24\n\t" \
    "v_mul_u32_u24 v26, 7, v26\n\t" \
    "v_sub_u32 v25, v25, v26\n\t" \
    "v_xor_b32 v22, s41, v20\n\t" \
    "v_bfe_u32 v32, v22, 1, 1\n\t" \
    "v_and_b32 v22, 1, v22\n\t" \
    "v_cvt_f32_ubyte0 v22, v22\n\t" \
    "v_mul_f32 v22, 0x3e800000, v22\n\t" \
    "v_lshl_or_b32 v32, v25, 1, v32\n\t" \
    "v_cvt_f32_ubyte0 v32, v32\n\t" \
    "v_mul_f32 v32, 0x3e924925, v32\n\t" \
    "v_mul_f32 v29, 0x3d800000, v21\n\t" \
    "v_fma_f32 v29, v29, v32, v22\n\t" \
    "v_mov_b32 v30, v29\n\t" \
    "v_mov_b32 v31, v24\n\t" \
    "v_mov_b32 v25, 0\n\t" \
    "v_mov_b32 v26, 2.0\n\t" \
    "v_mov_b32 v27, 4.0\n\t" \
    "v_mov_b32 v28, 0x40c00000\n\t" \
    "s_mov_b32 s64, 0\n\t" \
    "s_mov_b32 s65, 0\n\t" \
    "s_lshl_b32 s37, s24, 11\n\t" \
    "s_add_u32 s37, s37, 49664\n\t" \
    "v_add_u32 v34, s37, v0\n\t" \
    "s_branch .Ltl_%=\n\t" \
    ".Lt4_%=:\n\t" \
    ".Lt3_%=:\n\t" \
    "s_cmp_lg_u32 s24, 3\n\t" \
    "s_cbranch_scc1 .Lx2_%=\n\t" \
    "v_lshrrev_b32 v8, 7, v0\n\t" \
    "v_mul_u32_u24 v8, 96, v8\n\t" \
    "v_and_b32 v9, 16, v0\n\t" \
    "v_bfe_u32 v10, v0, 5, 2\n\t" \
    "v_add3_u32 v8, v8, v9, v10\n\t" \
    "v_mul_u32_u24 v8, 192, v8\n\t" \
    "s_mul_i32 s37, s28, 129024\n\t" \
    "s_add_u32 s37, s37, s31\n\t" \
    "s_mov_b32 m0, 157312\n\t" \
    "s_mov_b32 exec_hi, 0xffffff\n\t" \
    "buffer_load_dword v8, s[12:15], s37 offen lds\n\t" \
    "s_mov_b64 exec, -1\n\t" \
    ".Lx2_%=:\n\t" \
    ".Le9_%=:\n\t" \
    "s_lshr_b32 s37, s24, 2\n\t" \
    "s_and_b32 s38, s24, 2\n\t" \
    "s_mul_i32 s39, s37, 24832\n\t" \
    "s_lshl4_add_u32 s39, s38, s39\n\t" \
    "v_lshrrev_b32 v4, 6, v0\n\t" \
    "v_and_b32 v5, 12, v4\n\t" \
    "v_add_u32 v6, v4, v5\n\t" \
    "v_lshrrev_b32 v7, 1, v0\n\t" \
    "v_and_b32 v7, 24, v7\n\t" \
    "v_and_b32 v4, 11, v4\n\t" \
    "v_mul_u32_u24 v4, 192, v4\n\t" \
    "v_bfe_u32 v5, v0, 8, 1\n\t" \
    "v_mul_u32_u24 v5, 3104, v5\n\t" \
    "v_add3_u32 v4, v4, v5, v7\n\t" \
    "v_add_u32 v8, s39, v4\n\t" \
    "v_mov_b32 v9, 156928\n\t" \
    "v_add_u32 v10, 149760, v0\n\t" \
    "s_lshl2_add_u32 s37, s37, s38\n\t" \
    "s_lshl_b32 s37, s37, 7\n\t" \
    "s_mul_i32 s53, s27, 28\n\t" \
    "s_add_u32 s53, s53, s26\n\t" \
    "s_andn2_b32 s39, s53, 7\n\t" \
    "s_add_u32 s53, s53, s39\n\t" \
    "s_lshl_b32 s53, s53, 10\n\t" \
    "s_add_u32 s37, s37, s53\n\t" \
    "s_and_b32 s38, s24, 1\n\t" \
    "s_lshl_b32 s39, s38, 13\n\t" \
    "s_add_u32 s37, s37, s39\n\t" \
    "s_cmp_lt_u32 s25, 126\n\t" \
    "s_cselect_b32 s43, 0, 0x80000000\n\t" \
    "s_add_u32 s37, s37, s43\n\t" \
    "v_lshrrev_b32 v11, 8, v0\n\t" \
    "v_and_b32 v12, 0xf0, v0\n\t" \
    "v_mul_u32_u24 v4, 8486912, v11\n\t" \
    "v_add3_u32 v13, v4, v12, s37\n\t" \
    "s_lshl_b32 s39, s38, 10\n\t" \
    "v_add_u32 v14, s39, v10\n\t" \
    "s_lshl_b32 s39, s38, 5\n\t" \
    "s_add_u32 s39, s39, 116736\n\t" \
    "v_lshl_add_u32 v4, v6, 6, v7\n\t" \
    "v_lshrrev_b32 v5, 3, v0\n\t" \
    "v_and_b32 v5, 32, v5\n\t" \
    "v_xor_b32 v5, s39, v5\n\t" \
    "v_add_u32 v15, v4, v5\n\t" \
    "s_and_b32 s37, s24, 6\n\t" \
    "s_lshl_b32 s37, s37, 10\n\t" \
    "v_add_u32 v16, s37, v0\n\t" \
    "s_cmp_ge_u32 s25, 126\n\t" \
    "s_cbranch_scc1 .Lqa_%=\n\t" \
    ".Lqd_%=:\n\t" \
    "s_waitcnt lgkmcnt(0)\n\t" \
    "s_barrier\n\t" \
    "ds_read_b128 v[24:27], v14 offset:5120\n\t" \
    "ds_read_b128 v[28:31], v16 offset:49664\n\t" \
    "ds_read_b128 v[32:35], v16 offset:50688\n\t" \
    "s_and_b32 s33, s2, 7\n\t" \
    "s_lshl_b32 s33, s33, 15\n\t" \
    "s_add_u32 s33, s33, 33554432\n\t" \
    "s_lshr_b32 s37, s29, 2\n\t" \
    "s_mul_i32 s39, s30, 3\n\t" \
    "s_add_u32 s37, s37, s39\n\t" \
    "s_mul_i32 s37, s37, 114688\n\t" \
    "s_and_b32 s39, s29, 3\n\t" \
    "s_lshl_b32 s39, s39, 8\n\t" \
    "s_add_u32 s37, s37, s39\n\t" \
    "s_add_u32 s37, s37, s43\n\t" \
    "s_add_u32 s37, s37, 2097152\n\t" \
    "s_and_b32 s39, s24, 6\n\t" \
    "s_lshl3_add_u32 s39, s28, s39\n\t" \
    "s_lshl_b32 s39, s39, 1\n\t" \
    "v_add_u32 v14, s39, v11\n\t" \
    "v_mul_u32_u24 v17, 147, v14\n\t" \
    "v_lshrrev_b32 v17, 10, v17\n\t" \
    "s_and_b32 s39, s24, 1\n\t" \
    "v_subrev_u32 v18, s39, v17\n\t" \
    "v_mul_i32_i24 v18, 7, v18\n\t" \
    "v_sub_u32 v14, v14, v18\n\t" \
    "v_and_b32 v18, 14, v14\n\t" \
    "v_add_lshl_u32 v14, v14, v18, 12\n\t" \
    "v_and_b32 v18, 7, v17\n\t" \
    "v_mul_u32_u24 v18, 4243456, v18\n\t" \
    "v_add3_u32 v14, v14, v18, v12\n\t" \
    "v_and_b32 v17, 8, v17\n\t" \
    "v_xor_b32 v17, 8, v17\n\t" \
    "v_lshl_add_u32 v14, v17, 10, v14\n\t" \
    "v_add_u32 v19, s37, v14\n\t" \
    "s_lshl_b32 s43, s41, 10\n\t" \
    "s_and_b32 s37, s2, 0x70\n\t" \
    "s_lshl_b32 s37, s37, 1\n\t" \
    "s_lshl2_add_u32 s57, s24, s37\n\t" \
    "s_min_u32 s58, s57, 248\n\t" \
    "s_mul_i32 s59, s58, 147\n\t" \
    "s_lshr_b32 s59, s59, 12\n\t" \
    "s_mul_i32 s37, s59, 28\n\t" \
    "s_sub_u32 s60, s58, s37\n\t" \
    "s_andn2_b32 s39, s58, 7\n\t" \
    "s_add_u32 s51, s58, s39\n\t" \
    "s_lshl_b32 s51, s51, 10\n\t" \
    "s_sub_u32 s37, s60, 1\n\t" \
    "s_cselect_b32 s52, 27, -1\n\t" \
    "s_add_u32 s52, s52, s58\n\t" \
    "s_andn2_b32 s39, s52, 7\n\t" \
    "s_add_u32 s52, s52, s39\n\t" \
    "s_lshl_b32 s52, s52, 10\n\t" \
    "v_lshrrev_b32 v16, 9, v0\n\t" \
    "v_and_b32 v18, 0x2f0, v0\n\t" \
    "v_and_b32 v17, 1, v11\n\t" \
    "v_lshl_add_u32 v14, v17, 11, v18\n\t" \
    "v_add_u32 v36, s51, v14\n\t" \
    "v_add_u32 v37, s52, v0\n\t" \
    "s_mul_i32 s44, s24, 7296\n\t" \
    "s_mul_i32 s37, s60, 344064\n\t" \
    "s_mul_i32 s39, s59, 4992\n\t" \
    "s_add_u32 s37, s37, s39\n\t" \
    "s_cmp_eq_u32 s59, 8\n\t" \
    "s_movk_i32 s39, 2496\n\t" \
    "s_cselect_b32 s39, 1536, s39\n\t" \
    "s_add_u32 s37, s37, s39\n\t" \
    "s_and_b32 s39, s8, 0x1fffff\n\t" \
    "s_add_u32 s37, s37, s39\n\t" \
    "v_bfe_u32 v38, v0, 5, 2\n\t" \
    "v_lshl_add_u32 v38, v16, 4, v38\n\t" \
    "v_mul_u32_u24 v38, 43008, v38\n\t" \
    "v_add_u32 v39, s37, v38\n\t" \
    "s_lshr_b32 s37, s60, 1\n\t" \
    "s_mul_i32 s37, s37, 9\n\t" \
    "s_add_u32 s37, s37, s59\n\t" \
    "v_mad_u32_u24 v38, v16, 9, s37\n\t" \
    "v_lshrrev_b32 v18, 5, v38\n\t" \
    "s_and_b32 s29, s51, 0x1000\n\t" \
    "s_and_b32 s30, s52, 0x1000\n\t" \
    "s_lshl_b32 s37, s24, 2\n\t" \
    "s_lshl1_add_u32 s39, s24, 8\n\t" \
    "s_min_u32 s61, s37, s39\n\t" \
    "s_add_u32 s37, s37, 2\n\t" \
    "s_add_u32 s39, s24, 20\n\t" \
    "s_cmp_lt_u32 s24, 4\n\t" \
    "s_cselect_b32 s62, s37, s39\n\t" \
    "s_bfe_u32 s63, s2, 0x10003\n\t" \
    "s_mul_i32 s47, s63, 28\n\t" \
    "s_add_u32 s45, s47, s61\n\t" \
    "s_add_u32 s46, s47, s62\n\t" \
    "s_andn2_b32 s39, s45, 7\n\t" \
    "s_add_u32 s45, s45, s39\n\t" \
    "s_lshl_b32 s45, s45, 10\n\t" \
    "s_andn2_b32 s39, s46, 7\n\t" \
    "s_add_u32 s46, s46, s39\n\t" \
    "s_lshl_b32 s46, s46, 10\n\t" \
    "v_lshlrev_b32 v17, 4, v11\n\t" \
    "v_lshrrev_b32 v14, 6, v12\n\t" \
    "v_mul_u32_u24 v14, 7296, v14\n\t" \
    "v_and_b32 v38, 16, v0\n\t" \
    "v_mul_u32_u24 v38, 226, v38\n\t" \
    "v_and_b32 v40, 32, v0\n\t" \
    "v_lshl_add_u32 v38, v40, 1, v38\n\t" \
    "v_add3_u32 v14, v14, v38, v17\n\t" \
    "s_lshl_b32 s37, s61, 7\n\t" \
    "v_add_u32 v44, s37, v14\n\t" \
    "s_and_b32 s37, s62, 30\n\t" \
    "s_lshl_b32 s37, s37, 7\n\t" \
    "v_add_u32 v45, s37, v14\n\t" \
    "s_mov_b32 s1, 0\n\t" \
    "s_mov_b32 s41, 0x18000\n\t" \
    "s_lshl_b32 s37, s63, 9\n\t" \
    "s_add_u32 s51, s51, s37\n\t" \
    "s_cmp_lt_u32 s57, 252\n\t" \
    "s_cselect_b32 s51, s51, 0x80000000\n\t" \
    "s_and_b32 s37, s2, 0x87\n\t" \
    "s_lshl4_add_u32 s37, s24, s37\n\t" \
    "s_lshl_b32 s55, s37, 7\n\t" \
    "s_andn2_b32 s37, s55, 0x1fff\n\t" \
    "s_add_u32 s55, s55, s37\n\t" \
    "v_and_b32 v46, 16, v0\n\t" \
    "v_lshlrev_b32 v47, 6, v46\n\t" \
    "v_mad_u32_u24 v38, v12, 12, v17\n\t" \
    "v_and_b32 v14, 0x170, v0\n\t" \
    "v_and_b32 v41, 0x80, v0\n\t" \
    "v_lshl_add_u32 v14, v41, 3, v14\n\t" \
    "v_lshl_add_u32 v14, v16, 11, v14\n\t" \
    "v_add_u32 v42, s51, v14\n\t" \
    "s_mul_i32 s37, s24, 2064\n\t" \
    "v_and_b32 v14, 0x300, v0\n\t" \
    "v_add3_u32 v43, v14, v0, s37\n\t" \
    "s_lshl_b32 s37, s37, 1\n\t" \
    "v_lshlrev_b32 v14, 7, v16\n\t" \
    "v_xor_b32 v14, v14, v0\n\t" \
    "v_add_u32 v40, s37, v14\n\t" \
    "v_lshrrev_b32 v14, 5, v12\n\t" \
    "v_mul_u32_u24 v14, 2064, v14\n\t" \
    "v_lshl_add_u32 v14, v46, 2, v14\n\t" \
    "s_lshl_b32 s37, s24, 8\n\t" \
    "v_add3_u32 v48, v14, v17, s37\n\t" \
    "v_bfe_u32 v14, v0, 7, 1\n\t" \
    "v_lshl_add_u32 v14, v11, 2, v14\n\t" \
    "v_mul_u32_u24 v14, 2064, v14\n\t" \
    "v_lshrrev_b32 v41, 3, v0\n\t" \
    "v_and_or_b32 v14, v41, 8, v14\n\t" \
    "s_and_b32 s37, s24, 3\n\t" \
    "s_lshl_b32 s37, s37, 6\n\t" \
    "s_and_b32 s39, s24, 4\n\t" \
    "s_lshl_b32 s39, s39, 8\n\t" \
    "s_or_b32 s37, s37, s39\n\t" \
    "v_and_or_b32 v41, v0, 32, s37\n\t" \
    "v_mul_u32_u24 v46, 40, v46\n\t" \
    "v_xor_b32 v41, v41, v46\n\t" \
    "v_add_u32 v49, v14, v41\n\t" \
    "ds_read_b128 v[52:55], v10 offset:0\n\t" \
    "ds_read_b128 v[56:59], v10 offset:1024\n\t" \
    "s_sleep 6\n\t" \
    "s_and_b32 s37, s24, 1\n\t" \
    "s_lshl_b32 s37, s37, 4\n\t" \
    "s_lshr_b32 s39, s24, 1\n\t" \
    "s_add_u32 s37, s37, s39\n\t" \
    "s_mul_i32 s37, s37, 192\n\t" \
    "s_add_u32 s37, s37, s31\n\t" \
    "s_lshl_b32 s39, s24, 12\n\t" \
    "s_add_u32 m0, s39, 116736\n\t" \
    "s_add_u32 s38, s37, 0\n\t" \
    "buffer_load_dwordx4 v1, s[12:15], s38 offen  lds\n\t" \
    "s_add_u32 m0, s39, 117760\n\t" \
    "s_add_u32 s38, s37, 258048\n\t" \
    "buffer_load_dwordx4 v1, s[12:15], s38 offen  lds\n\t" \
    "s_add_u32 m0, s39, 118784\n\t" \
    "s_add_u32 s38, s37, 516096\n\t" \
    "buffer_load_dwordx4 v1, s[12:15], s38 offen  lds\n\t" \
    "s_add_u32 m0, s39, 119808\n\t" \
    "s_add_u32 s38, s37, 774144\n\t" \
    "buffer_load_dwordx4 v1, s[12:15], s38 offen  lds\n\t" \
    "s_cmp_lg_u32 s24, 3\n\t" \
    "s_cbranch_scc1 .Lr9_%=\n\t" \
    "s_and_b32 s37, s2, 7\n\t" \
    "s_lshl_b32 s37, s37, 10\n\t" \
    "s_add_u32 s37, s37, 34676736\n\t" \
    "s_buffer_load_dword s56, s[4:7], s37 offset:0x0\n\t" \
    "s_buffer_load_dword s57, s[4:7], s37 offset:0x80\n\t" \
    "s_buffer_load_dword s58, s[4:7], s37 offset:0x100\n\t" \
    "s_buffer_load_dword s59, s[4:7], s37 offset:0x180\n\t" \
    "s_buffer_load_dword s60, s[4:7], s37 offset:0x200\n\t" \
    "s_buffer_load_dword s61, s[4:7], s37 offset:0x280\n\t" \
    "s_buffer_load_dword s62, s[4:7], s37 offset:0x300\n\t" \
    "s_buffer_load_dword s63, s[4:7], s37 offset:0x380\n\t" \
    "s_mov_b32 s40, 0\n\t" \
    ".Lr1_%=:\n\t" \
    "s_waitcnt lgkmcnt(0)\n\t" \
    "s_memrealtime s[22:23]\n\t" \
    "s_waitcnt lgkmcnt(0)\n\t" \
    "s_mov_b32 s37, 1000\n\t" \
    "s_mov_b32 s38, 0\n\t" \
    "s_sub_u32 s39, s22, s56\n\t" \
    "s_min_u32 s37, s37, s39\n\t" \
    "s_max_u32 s38, s38, s39\n\t" \
    "s_sub_u32 s39, s22, s57\n\t" \
    "s_min_u32 s37, s37, s39\n\t" \
    "s_max_u32 s38, s38, s39\n\t" \
    "s_sub_u32 s39, s22, s58\n\t" \
    "s_min_u32 s37, s37, s39\n\t" \
    "s_max_u32 s38, s38, s39\n\t" \
    "s_sub_u32 s39, s22, s59\n\t" \
    "s_min_u32 s37, s37, s39\n\t" \
    "s_max_u32 s38, s38, s39\n\t" \
    "s_sub_u32 s39, s22, s60\n\t" \
    "s_min_u32 s37, s37, s39\n\t" \
    "s_max_u32 s38, s38, s39\n\t" \
    "s_sub_u32 s39, s22, s61\n\t" \
    "s_min_u32 s37, s37, s39\n\t" \
    "s_max_u32 s38, s38, s39\n\t" \
    "s_sub_u32 s39, s22, s62\n\t" \
    "s_min_u32 s37, s37, s39\n\t" \
    "s_max_u32 s38, s38, s39\n\t" \
    "s_sub_u32 s39, s22, s63\n\t" \
    "s_min_u32 s37, s37, s39\n\t" \
    "s_max_u32 s38, s38, s39\n\t" \
    "s_cmp_gt_u32 s38, 1000\n\t" \
    "s_cbranch_scc1 .Lr2_%=\n\t" \
    ".Lr3_%=:\n\t" \
    "s_sub_u32 s35, s22, s37\n\t" \
    "s_waitcnt vmcnt(5)\n\t" \
    "v_readfirstlane_b32 s34, v3\n\t" \
    "s_mul_i32 s34, s34, 1664525\n\t" \
    "s_add_u32 s34, s34, 1013904223\n\t" \
    "s_or_b32 s34, s34, 1\n\t" \
    "v_mov_b32 v60, s34\n\t" \
    "v_mov_b32 v61, s35\n\t" \
    "ds_write_b64 v9, v[60:61] offset:32\n\t" \
    ".Lr9_%=:\n\t" \
    "s_cmp_lt_u32 s24, 4\n\t" \
    "s_cbranch_scc1 .Lw_%=\n\t" \
    "s_waitcnt vmcnt(4)\n\t" \
    ".Lw_%=:\n\t" \
    "s_waitcnt lgkmcnt(0)\n\t" \
    "s_barrier\n\t" \
    "ds_read_b64_tr_b16 v[64:65], v8 offset:0\n\t" \
    "ds_read_b64_tr_b16 v[66:67], v8 offset:768\n\t" \
    "ds_read_b64_tr_b16 v[68:69], v8 offset:12416\n\t" \
    "ds_read_b64_tr_b16 v[70:71], v8 offset:13184\n\t" \
    "ds_read_b64_tr_b16 v[72:73], v8 offset:6208\n\t" \
    "ds_read_b64_tr_b16 v[74:75], v8 offset:6976\n\t" \
    "ds_read_b64_tr_b16 v[76:77], v8 offset:18624\n\t" \
    "ds_read_b64_tr_b16 v[78:79], v8 offset:19392\n\t" \
    "ds_read_b128 v[80:83], v9 offset:32\n\t" \
    "s_waitcnt lgkmcnt(7)\n\t" \
    "v_mfma_f32_16x16x32_bf16 v[84:87], v[24:27], v[64:67], 0\n\t" \
    "s_waitcnt lgkmcnt(5)\n\t" \
    "v_mfma_f32_16x16x32_bf16 v[88:91], v[24:27], v[68:71], 0\n\t" \
    "s_waitcnt lgkmcnt(3)\n\t" \
    "v_mfma_f32_16x16x32_bf16 v[92:95], v[24:27], v[72:75], 0\n\t" \
    "s_waitcnt lgkmcnt(1)\n\t" \
    "v_mfma_f32_16x16x32_bf16 v[96:99], v[24:27], v[76:79], 0\n\t" \
    "s_waitcnt lgkmcnt(0)\n\t" \
    "v_readfirstlane_b32 s34, v80\n\t" \
    "v_readfirstlane_b32 s35, v81\n\t" \
    "v_add_u32 v84, 0x8000, v84\n\t" \
    "v_add_u32 v86, 0x8000, v86\n\t" \
    "v_add_u32 v88, 0x8000, v88\n\t" \
    "v_add_u32 v90, 0x8000, v90\n\t" \
    "v_cvt_pk_bf16_f32 v108, v84, v85\n\t" \
    "v_cvt_pk_bf16_f32 v110, v88, v89\n\t" \
    "v_cvt_pk_bf16_f32 v109, v92, v93\n\t" \
    "v_cvt_pk_bf16_f32 v111, v96, v97\n\t" \
    "v_and_b32 v108, -2, v108\n\t" \
    "v_and_b32 v110, -2, v110\n\t" \
    "v_cvt_pk_bf16_f32 v112, v86, v87\n\t" \
    "v_cvt_pk_bf16_f32 v114, v90, v91\n\t" \
    "v_cvt_pk_bf16_f32 v113, v94, v95\n\t" \
    "v_cvt_pk_bf16_f32 v115, v98, v99\n\t" \
    "v_and_b32 v112, -2, v112\n\t" \
    "v_and_b32 v114, -2, v114\n\t" \
    ".Lh38_%=:\n\t" \
    "s_memrealtime s[22:23]\n\t" \
    "s_waitcnt lgkmcnt(0)\n\t" \
    "s_sub_u32 s22, s22, s35\n\t" \
    "s_cmp_lt_u32 s22, 205\n\t" \
    "s_cbranch_scc1 .Lh38_%=\n\t" \
    "buffer_store_dwordx4 v[108:111], v13, s[4:7], 0 offen offset:0 sc1\n\t" \
    "s_mov_b32 s42, 4243456\n\t" \
    "buffer_store_dwordx4 v[112:115], v13, s[4:7], s42 offen offset:0 sc1\n\t" \
    "ds_read_b64_tr_b16 v[116:117], v8 offset:64\n\t" \
    "ds_read_b64_tr_b16 v[118:119], v8 offset:832\n\t" \
    "ds_read_b64_tr_b16 v[120:121], v8 offset:12480\n\t" \
    "ds_read_b64_tr_b16 v[122:123], v8 offset:13248\n\t" \
    "ds_read_b64_tr_b16 v[124:125], v8 offset:6272\n\t" \
    "ds_read_b64_tr_b16 v[126:127], v8 offset:7040\n\t" \
    "ds_read_b64_tr_b16 v[128:129], v8 offset:18688\n\t" \
    "ds_read_b64_tr_b16 v[130:131], v8 offset:19456\n\t" \
    "ds_read_b64_tr_b16 v[132:133], v8 offset:128\n\t" \
    "ds_read_b64_tr_b16 v[134:135], v8 offset:896\n\t" \
    "ds_read_b64_tr_b16 v[136:137], v8 offset:12544\n\t" \
    "ds_read_b64_tr_b16 v[138:139], v8 offset:13312\n\t" \
    "ds_read_b64_tr_b16 v[140:141], v8 offset:6336\n\t" \
    "ds_read_b64_tr_b16 v[142:143], v8 offset:7104\n\t" \
    "ds_read_b64_tr_b16 v[144:145], v8 offset:18752\n\t" \
    "ds_read_b64_tr_b16 v[146:147], v8 offset:19520\n\t" \
    "v_mov_b32 v148, s34\n\t" \
    "v_mov_b32 v149, s34\n\t" \
    "v_mov_b32 v150, s34\n\t" \
    "v_mov_b32 v151, s34\n\t" \
    "s_and_b32 s37, s2, 243\n\t" \
    "s_cmp_eq_u32 s37, 243\n\t" \
    "s_cbranch_scc1 .Lskip_%=\n\t" \
    "s_waitcnt lgkmcnt(14)\n\t" \
    "v_mfma_f32_16x16x32_bf16 v[152:155], v[24:27], v[116:119], 0\n\t" \
    "s_waitcnt lgkmcnt(12)\n\t" \
    "v_mfma_f32_16x16x32_bf16 v[156:159], v[24:27], v[120:123], 0\n\t" \
    "s_waitcnt lgkmcnt(10)\n\t" \
    "v_mfma_f32_16x16x32_bf16 v[160:163], v[24:27], v[124:127], 0\n\t" \
    "s_waitcnt lgkmcnt(8)\n\t" \
    "v_mfma_f32_16x16x32_bf16 v[164:167], v[24:27], v[128:131], 0\n\t" \
    "s_waitcnt lgkmcnt(6)\n\t" \
    "v_mfma_f32_16x16x32_bf16 v[168:171], v[24:27], v[132:135], 0\n\t" \
    "s_waitcnt lgkmcnt(4)\n\t" \
    "v_mfma_f32_16x16x32_bf16 v[172:175], v[24:27], v[136:139], 0\n\t" \
    "s_waitcnt lgkmcnt(2)\n\t" \
    "v_mfma_f32_16x16x32_bf16 v[176:179], v[24:27], v[140:143], 0\n\t" \
    "s_waitcnt lgkmcnt(0)\n\t" \
    "v_mfma_f32_16x16x32_bf16 v[180:183], v[24:27], v[144:147], 0\n\t" \
    "v_add_u32 v152, 0x8000, v152\n\t" \
    "v_add_u32 v154, 0x8000, v154\n\t" \
    "v_add_u32 v156, 0x8000, v156\n\t" \
    "v_add_u32 v158, 0x8000, v158\n\t" \
    "v_cvt_pk_bf16_f32 v184, v152, v153\n\t" \
    "v_cvt_pk_bf16_f32 v186, v156, v157\n\t" \
    "v_cvt_pk_bf16_f32 v185, v160, v161\n\t" \
    "v_cvt_pk_bf16_f32 v187, v164, v165\n\t" \
    "v_and_b32 v184, -2, v184\n\t" \
    "v_and_b32 v186, -2, v186\n\t" \
    "v_cvt_pk_bf16_f32 v188, v154, v155\n\t" \
    "v_cvt_pk_bf16_f32 v190, v158, v159\n\t" \
    "v_cvt_pk_bf16_f32 v189, v162, v163\n\t" \
    "v_cvt_pk_bf16_f32 v191, v166, v167\n\t" \
    "v_and_b32 v188, -2, v188\n\t" \
    "v_and_b32 v190, -2, v190\n\t" \
    "v_add_u32 v168, 0x8000, v168\n\t" \
    "v_add_u32 v170, 0x8000, v170\n\t" \
    "v_add_u32 v172, 0x8000, v172\n\t" \
    "v_add_u32 v174, 0x8000, v174\n\t" \
    "v_cvt_pk_bf16_f32 v4, v168, v169\n\t" \
    "v_cvt_pk_bf16_f32 v6, v172, v173\n\t" \
    "v_cvt_pk_bf16_f32 v5, v176, v177\n\t" \
    "v_cvt_pk_bf16_f32 v7, v180, v181\n\t" \
    "v_and_b32 v4, -2, v4\n\t" \
    "v_and_b32 v6, -2, v6\n\t" \
    "v_cvt_pk_bf16_f32 v20, v170, v171\n\t" \
    "v_cvt_pk_bf16_f32 v22, v174, v175\n\t" \
    "v_cvt_pk_bf16_f32 v21, v178, v179\n\t" \
    "v_cvt_pk_bf16_f32 v23, v182, v183\n\t" \
    "v_and_b32 v20, -2, v20\n\t" \
    "v_and_b32 v22, -2, v22\n\t" \
    ".Lh39_%=:\n\t" \
    "s_memrealtime s[22:23]\n\t" \
    "s_waitcnt lgkmcnt(0)\n\t" \
    "s_sub_u32 s22, s22, s35\n\t" \
    "s_cmp_lt_u32 s22, 255\n\t" \
    "s_cbranch_scc1 .Lh39_%=\n\t" \
    "s_mov_b32 s42, 524288\n\t" \
    "buffer_store_dwordx4 v[184:187], v13, s[4:7], s42 offen offset:0 sc1\n\t" \
    "s_mov_b32 s42, 4767744\n\t" \
    "buffer_store_dwordx4 v[188:191], v13, s[4:7], s42 offen offset:0 sc1\n\t" \
    "s_waitcnt vmcnt(4)\n\t" \
    "s_waitcnt lgkmcnt(0)\n\t" \
    "s_barrier\n\t" \
    "s_lshl_b32 s37, s24, 18\n\t" \
    "s_add_u32 s37, s37, s32\n\t" \
    "s_bitcmp1_b32 13, s24\n\t" \
    "s_cselect_b32 s39, 0, 0x80000000\n\t" \
    "v_add_u32 v16, s39, v2\n\t" \
    "v_mov_b32 v60, 0\n\t" \
    "v_mov_b32 v61, 0\n\t" \
    "v_mov_b32 v62, 0\n\t" \
    "v_mov_b32 v63, 0\n\t" \
    "s_cmp_eq_u32 s24, 4\n\t" \
    "s_cselect_b64 exec, 0xff, -1\n\t" \
    "buffer_store_dwordx4 v[60:63], v16, s[4:7], s37 offen sc1\n\t" \
    "s_mov_b64 exec, -1\n\t" \
    ".Lh40_%=:\n\t" \
    "s_memrealtime s[22:23]\n\t" \
    "s_waitcnt lgkmcnt(0)\n\t" \
    "s_sub_u32 s22, s22, s35\n\t" \
    "s_cmp_lt_u32 s22, 305\n\t" \
    "s_cbranch_scc1 .Lh40_%=\n\t" \
    "s_mov_b32 s42, 1048576\n\t" \
    "buffer_store_dwordx4 v[4:7], v13, s[4:7], s42 offen offset:0 sc1\n\t" \
    "s_mov_b32 s42, 5292032\n\t" \
    "buffer_store_dwordx4 v[20:23], v13, s[4:7], s42 offen offset:0 sc1\n\t" \
    "ds_read_b64_tr_b16 v[64:65], v15 offset:0\n\t" \
    "ds_read_b64_tr_b16 v[66:67], v15 offset:256\n\t" \
    "ds_read_b64_tr_b16 v[68:69], v15 offset:2048\n\t" \
    "ds_read_b64_tr_b16 v[70:71], v15 offset:2304\n\t" \
    "ds_read_b64_tr_b16 v[72:73], v15 offset:4096\n\t" \
    "ds_read_b64_tr_b16 v[74:75], v15 offset:4352\n\t" \
    "ds_read_b64_tr_b16 v[76:77], v15 offset:6144\n\t" \
    "ds_read_b64_tr_b16 v[78:79], v15 offset:6400\n\t" \
    "ds_read_b64_tr_b16 v[80:81], v15 offset:8192\n\t" \
    "ds_read_b64_tr_b16 v[82:83], v15 offset:8448\n\t" \
    "ds_read_b64_tr_b16 v[100:101], v15 offset:10240\n\t" \
    "ds_read_b64_tr_b16 v[102:103], v15 offset:10496\n\t" \
    "ds_read_b64_tr_b16 v[104:105], v15 offset:12288\n\t" \
    "ds_read_b64_tr_b16 v[106:107], v15 offset:12544\n\t" \
    "s_waitcnt lgkmcnt(12)\n\t" \
    "v_mfma_f32_16x16x32_bf16 v[84:87], v[28:31], v[64:67], 0\n\t" \
    "ds_read_b64_tr_b16 v[88:89], v15 offset:14336\n\t" \
    "ds_read_b64_tr_b16 v[90:91], v15 offset:14592\n\t" \
    "s_waitcnt lgkmcnt(12)\n\t" \
    "v_mfma_f32_16x16x32_bf16 v[84:87], v[32:35], v[68:71], v[84:87]\n\t" \
    "ds_read_b64_tr_b16 v[92:93], v15 offset:16384\n\t" \
    "ds_read_b64_tr_b16 v[94:95], v15 offset:16640\n\t" \
    "s_waitcnt lgkmcnt(12)\n\t" \
    "v_mfma_f32_16x16x32_bf16 v[96:99], v[28:31], v[72:75], 0\n\t" \
    "ds_read_b64_tr_b16 v[108:109], v15 offset:18432\n\t" \
    "ds_read_b64_tr_b16 v[110:111], v15 offset:18688\n\t" \
    "s_waitcnt lgkmcnt(12)\n\t" \
    "v_mfma_f32_16x16x32_bf16 v[96:99], v[32:35], v[76:79], v[96:99]\n\t" \
    "ds_read_b64_tr_b16 v[112:113], v15 offset:20480\n\t" \
    "ds_read_b64_tr_b16 v[114:115], v15 offset:20736\n\t" \
    "s_waitcnt lgkmcnt(12)\n\t" \
    "v_mfma_f32_16x16x32_bf16 v[116:119], v[28:31], v[80:83], 0\n\t" \
    "ds_read_b64_tr_b16 v[120:121], v15 offset:22528\n\t" \
    "ds_read_b64_tr_b16 v[122:123], v15 offset:22784\n\t" \
    "s_waitcnt lgkmcnt(12)\n\t" \
    "v_mfma_f32_16x16x32_bf16 v[116:119], v[32:35], v[100:103], v[116:119]\n\t" \
    "ds_read_b64_tr_b16 v[124:125], v15 offset:24576\n\t" \
    "ds_read_b64_tr_b16 v[126:127], v15 offset:24832\n\t" \
    "s_waitcnt lgkmcnt(12)\n\t" \
    "v_mfma_f32_16x16x32_bf16 v[128:131], v[28:31], v[104:107], 0\n\t" \
    "ds_read_b64_tr_b16 v[132:133], v15 offset:26624\n\t" \
    "ds_read_b64_tr_b16 v[134:135], v15 offset:26880\n\t" \
    "s_waitcnt lgkmcnt(12)\n\t" \
    "v_mfma_f32_16x16x32_bf16 v[128:131], v[32:35], v[88:91], v[128:131]\n\t" \
    "ds_read_b64_tr_b16 v[136:137], v15 offset:28672\n\t" \
    "ds_read_b64_tr_b16 v[138:139], v15 offset:28928\n\t" \
    "s_waitcnt lgkmcnt(12)\n\t" \
    "v_mfma_f32_16x16x32_bf16 v[140:143], v[28:31], v[92:95], 0\n\t" \
    "ds_read_b64_tr_b16 v[24:25], v15 offset:30720\n\t" \
    "ds_read_b64_tr_b16 v[26:27], v15 offset:30976\n\t" \
    "s_waitcnt lgkmcnt(12)\n\t" \
    "v_mfma_f32_16x16x32_bf16 v[140:143], v[32:35], v[108:111], v[140:143]\n\t" \
    "v_cvt_pk_bf16_f32 v144, v84, v96\n\t" \
    "v_cvt_pk_bf16_f32 v152, v85, v97\n\t" \
    "s_waitcnt lgkmcnt(10)\n\t" \
    "v_mfma_f32_16x16x32_bf16 v[156:159], v[28:31], v[112:115], 0\n\t" \
    "v_cvt_pk_bf16_f32 v160, v86, v98\n\t" \
    "v_cvt_pk_bf16_f32 v164, v87, v99\n\t" \
    "s_waitcnt lgkmcnt(8)\n\t" \
    "v_mfma_f32_16x16x32_bf16 v[156:159], v[32:35], v[120:123], v[156:159]\n\t" \
    "v_cvt_pk_bf16_f32 v145, v116, v128\n\t" \
    "v_cvt_pk_bf16_f32 v153, v117, v129\n\t" \
    "s_waitcnt lgkmcnt(6)\n\t" \
    "v_mfma_f32_16x16x32_bf16 v[168:171], v[28:31], v[124:127], 0\n\t" \
    "v_cvt_pk_bf16_f32 v161, v118, v130\n\t" \
    "v_cvt_pk_bf16_f32 v165, v119, v131\n\t" \
    "s_waitcnt lgkmcnt(4)\n\t" \
    "v_mfma_f32_16x16x32_bf16 v[168:171], v[32:35], v[132:135], v[168:171]\n\t" \
    "s_waitcnt lgkmcnt(2)\n\t" \
    "v_mfma_f32_16x16x32_bf16 v[172:175], v[28:31], v[136:139], 0\n\t" \
    "s_waitcnt lgkmcnt(0)\n\t" \
    "v_mfma_f32_16x16x32_bf16 v[172:175], v[32:35], v[24:27], v[172:175]\n\t" \
    "v_cvt_pk_bf16_f32 v146, v140, v156\n\t" \
    "v_cvt_pk_bf16_f32 v154, v141, v157\n\t" \
    "v_cvt_pk_bf16_f32 v162, v142, v158\n\t" \
    "v_cvt_pk_bf16_f32 v166, v143, v159\n\t" \
    "s_nop 3\n\t" \
    "v_cvt_pk_bf16_f32 v147, v168, v172\n\t" \
    "v_cvt_pk_bf16_f32 v155, v169, v173\n\t" \
    "v_cvt_pk_bf16_f32 v163, v170, v174\n\t" \
    "v_cvt_pk_bf16_f32 v167, v171, v175\n\t" \
    ".Lh41_%=:\n\t" \
    "s_memrealtime s[22:23]\n\t" \
    "s_waitcnt lgkmcnt(0)\n\t" \
    "s_sub_u32 s22, s22, s35\n\t" \
    "s_cmp_lt_u32 s22, 370\n\t" \
    "s_cbranch_scc1 .Lh41_%=\n\t" \
    "s_cmp_eq_u32 s24, 0\n\t" \
    "s_cbranch_scc1 .Lj43_%=\n\t" \
    ".Lh42_%=:\n\t" \
    "s_memrealtime s[22:23]\n\t" \
    "s_waitcnt lgkmcnt(0)\n\t" \
    "s_sub_u32 s22, s22, s35\n\t" \
    "s_cmp_lt_u32 s22, 385\n\t" \
    "s_cbranch_scc1 .Lh42_%=\n\t" \
    ".Lj43_%=:\n\t" \
    "s_waitcnt lgkmcnt(0)\n\t" \
    "s_xor_b32 s42, s43, 0\n\t" \
    "buffer_store_dwordx4 v[144:147], v19, s[4:7], s42 offen offset:0 sc1\n\t" \
    "s_waitcnt vmcnt(1)\n\t" \
    "s_mov_b64 exec, 1\n\t" \
    "ds_add_rtn_u32 v17, v9, v9 offset:8\n\t" \
    "s_add_u32 s37, s32, 524288\n\t" \
    "s_waitcnt lgkmcnt(0)\n\t" \
    "v_cmp_eq_u32 vcc, 1098496, v17\n\t" \
    "s_mov_b64 exec, -1\n\t" \
    "s_nop 0\n\t" \
    "s_cbranch_vccz .La13_%=\n\t" \
    "buffer_store_dwordx4 v[148:151], v2, s[4:7], s37 offen sc1\n\t" \
    ".La13_%=:\n\t" \
    "s_mov_b64 exec, -1\n\t" \
    "s_xor_b32 s42, s43, 1024\n\t" \
    "buffer_store_dwordx4 v[152:155], v19, s[4:7], s42 offen offset:0 sc1\n\t" \
    "s_cmp_eq_u32 s24, 0\n\t" \
    "s_cbranch_scc1 .Lj45_%=\n\t" \
    ".Lh44_%=:\n\t" \
    "s_memrealtime s[22:23]\n\t" \
    "s_waitcnt lgkmcnt(0)\n\t" \
    "s_sub_u32 s22, s22, s35\n\t" \
    "s_cmp_lt_u32 s22, 435\n\t" \
    "s_cbranch_scc1 .Lh44_%=\n\t" \
    ".Lj45_%=:\n\t" \
    "s_waitcnt lgkmcnt(0)\n\t" \
    "s_xor_b32 s42, s43, 2048\n\t" \
    "buffer_store_dwordx4 v[160:163], v19, s[4:7], s42 offen offset:0 sc1\n\t" \
    "s_xor_b32 s42, s43, 3072\n\t" \
    "buffer_store_dwordx4 v[164:167], v19, s[4:7], s42 offen offset:0 sc1\n\t" \
    ".Lskip_%=:\n\t" \
    "s_waitcnt lgkmcnt(0)\n\t" \
    "s_nop 7\n\t" \
    ".Lh46_%=:\n\t" \
    "s_memrealtime s[22:23]\n\t" \
    "s_waitcnt lgkmcnt(0)\n\t" \
    "s_sub_u32 s22, s22, s35\n\t" \
    "s_cmp_lt_u32 s22, 410\n\t" \
    "s_cbranch_scc1 .Lh46_%=\n\t" \
    "s_and_b32 s52, s2, 7\n\t" \
    "s_lshl_b32 s52, s52, 7\n\t" \
    "s_add_u32 s52, s52, 34669568\n\t" \
    "s_buffer_load_dword s36, s[4:7], s52\n\t" \
    "s_mov_b32 s37, 34670592\n\t" \
    "s_buffer_load_dword s56, s[4:7], s37 offset:0x0\n\t" \
    "s_buffer_load_dword s57, s[4:7], s37 offset:0x80\n\t" \
    "s_buffer_load_dword s58, s[4:7], s37 offset:0x100\n\t" \
    "s_buffer_load_dword s59, s[4:7], s37 offset:0x180\n\t" \
    "s_buffer_load_dword s60, s[4:7], s37 offset:0x200\n\t" \
    "s_buffer_load_dword s61, s[4:7], s37 offset:0x280\n\t" \
    "s_and_b32 s37, s24, 3\n\t" \
    "s_mul_i32 s37, s37, 8486912\n\t" \
    "s_add_u32 s53, s53, s37\n\t" \
    "s_and_b32 s37, s24, 4\n\t" \
    "s_lshl_b32 s39, s37, 11\n\t" \
    "s_add_u32 s53, s53, s39\n\t" \
    "s_add_u32 s37, s37, s24\n\t" \
    "s_andn2_b32 s39, s26, 1\n\t" \
    "s_lshl3_add_u32 s37, s39, s37\n\t" \
    "s_mul_i32 s37, s37, 43008\n\t" \
    "s_mul_i32 s39, s27, 4992\n\t" \
    "s_add_u32 s51, s37, s39\n\t" \
    "s_cmp_eq_u32 s27, 8\n\t" \
    "s_movk_i32 s10, 4992\n\t" \
    "s_cselect_b32 s10, 3072, s10\n\t" \
    "s_mov_b32 s11, 0x20000\n\t" \
    "s_and_b32 s37, s2, 0x70\n\t" \
    "s_lshl_b32 s37, s37, 1\n\t" \
    "v_lshrrev_b32 v11, 7, v0\n\t" \
    "v_lshl_add_u32 v11, v11, 2, s37\n\t" \
    "v_min_u32 v11, 248, v11\n\t" \
    "v_mul_u32_u24 v46, 147, v11\n\t" \
    "v_lshrrev_b32 v46, 12, v46\n\t" \
    "v_mul_u32_u24 v41, 28, v46\n\t" \
    "v_sub_u32 v11, v11, v41\n\t" \
    "v_bfe_u32 v41, v0, 4, 3\n\t" \
    "v_min_u32 v41, 4, v41\n\t" \
    "v_add3_u32 v11, v11, v41, -1\n\t" \
    "v_add_u32 v41, 28, v11\n\t" \
    "v_min_u32 v11, v11, v41\n\t" \
    "v_and_b32 v1, 1, v11\n\t" \
    "v_lshrrev_b32 v11, 1, v11\n\t" \
    "v_mad_u32_u24 v11, v11, 9, v46\n\t" \
    "v_lshl_add_u32 v11, v1, 7, v11\n\t" \
    "v_and_b32 v3, 31, v11\n\t" \
    "v_and_b32 v50, 24, v3\n\t" \
    "v_add_u32 v3, v3, v50\n\t" \
    "v_lshrrev_b32 v50, 5, v11\n\t" \
    "v_lshlrev_b32 v50, 7, v50\n\t" \
    "v_lshl_or_b32 v51, v3, 10, v50\n\t" \
    "s_mov_b32 s40, 0\n\t" \
    "s_waitcnt lgkmcnt(0)\n\t" \
    ".Lr4_%=:\n\t" \
    "s_xor_b32 s36, s36, s34\n\t" \
    "s_andn2_b32 s37, s36, 14\n\t" \
    "s_cbranch_scc1 .Lr5_%=\n\t" \
    ".Lr6_%=:\n\t" \
    "s_bfe_u32 s36, s36, 0x30001\n\t" \
    "s_mov_b32 s40, 0\n\t" \
    "s_waitcnt lgkmcnt(0)\n\t" \
    ".Lq4_%=:\n\t" \
    "s_mov_b32 s37, 0\n\t" \
    "s_mov_b32 s0, 0\n\t" \
    "s_xor_b32 s61, s61, s34\n\t" \
    "s_or_b32 s37, s37, s61\n\t" \
    "s_lshl1_add_u32 s0, s0, s61\n\t" \
    "s_xor_b32 s60, s60, s34\n\t" \
    "s_or_b32 s37, s37, s60\n\t" \
    "s_lshl1_add_u32 s0, s0, s60\n\t" \
    "s_xor_b32 s59, s59, s34\n\t" \
    "s_or_b32 s37, s37, s59\n\t" \
    "s_lshl1_add_u32 s0, s0, s59\n\t" \
    "s_xor_b32 s58, s58, s34\n\t" \
    "s_or_b32 s37, s37, s58\n\t" \
    "s_lshl1_add_u32 s0, s0, s58\n\t" \
    "s_xor_b32 s57, s57, s34\n\t" \
    "s_or_b32 s37, s37, s57\n\t" \
    "s_lshl1_add_u32 s0, s0, s57\n\t" \
    "s_xor_b32 s56, s56, s34\n\t" \
    "s_or_b32 s37, s37, s56\n\t" \
    "s_lshl1_add_u32 s0, s0, s56\n\t" \
    "s_andn2_b32 s37, s37, 2\n\t" \
    "s_cbranch_scc1 .Lq5_%=\n\t" \
    ".Lq6_%=:\n\t" \
    "s_lshr_b32 s0, s0, 1\n\t" \
    "s_add_u32 s37, s36, 3\n\t" \
    "s_bitcmp1_b32 s37, 2\n\t" \
    "s_cselect_b32 s37, 51, 12\n\t" \
    "s_xor_b32 s0, s0, s37\n\t" \
    "s_add_u32 s56, s2, s36\n\t" \
    "s_and_b32 s56, s56, 7\n\t" \
    "s_lshr_b32 s37, s2, 7\n\t" \
    "s_lshl1_add_u32 s56, s56, s37\n\t" \
    "s_and_b32 s37, s56, 7\n\t" \
    "s_mul_i32 s48, s37, 4243456\n\t" \
    "s_and_b32 s37, s56, 8\n\t" \
    "s_lshl_b32 s37, s37, 10\n\t" \
    "s_add_u32 s48, s48, s37\n\t" \
    "s_add_u32 s49, s48, 524288\n\t" \
    "s_add_u32 s50, s48, 1048576\n\t" \
    "s_xor_b32 s37, s48, 0x2000\n\t" \
    "s_add_u32 s37, s37, 2097152\n\t" \
    "s_add_u32 s45, s45, s37\n\t" \
    "s_add_u32 s46, s46, s37\n\t" \
    "s_cmp_lt_u32 s24, 4\n\t" \
    "s_cselect_b32 s47, s46, 0x80000000\n\t" \
    "v_add_u32 v41, s45, v0\n\t" \
    "v_add_u32 v46, s46, v0\n\t" \
    "s_lshr_b32 s37, s24, 2\n\t" \
    "s_mul_i32 s37, s37, 6\n\t" \
    "s_mul_i32 s39, s56, 7\n\t" \
    "s_add_u32 s37, s37, s39\n\t" \
    "s_lshr_b32 s37, s37, 4\n\t" \
    "s_and_b32 s39, s24, 3\n\t" \
    "s_mul_i32 s39, s39, 63\n\t" \
    "s_add_u32 s37, s37, s39\n\t" \
    "v_lshrrev_b32 v1, 4, v0\n\t" \
    "v_min_u32 v1, 8, v1\n\t" \
    "v_mad_u32_u24 v1, v1, 7, s37\n\t" \
    "v_add_u32 v11, 2, v1\n\t" \
    "v_lshrrev_b32 v11, 7, v11\n\t" \
    "v_lshl_add_u32 v1, v11, 1, v1\n\t" \
    "v_and_b32 v11, 31, v1\n\t" \
    "v_and_b32 v3, 24, v11\n\t" \
    "v_add_u32 v11, v11, v3\n\t" \
    "v_lshrrev_b32 v3, 5, v1\n\t" \
    "v_lshlrev_b32 v3, 7, v3\n\t" \
    "v_lshl_or_b32 v50, v11, 10, v3\n\t" \
    "v_add_u32 v18, s36, v18\n\t" \
    "v_lshlrev_b32 v18, 8, v18\n\t" \
    "v_and_b32 v18, 0x400, v18\n\t" \
    "v_xor_b32 v42, v42, v18\n\t" \
    "s_sub_u32 s37, s24, s36\n\t" \
    "s_and_b32 s37, s37, 7\n\t" \
    "s_mul_i32 s39, s27, 28\n\t" \
    "s_add_u32 s39, s39, s26\n\t" \
    "s_lshr_b32 s39, s39, 5\n\t" \
    "s_lshl4_add_u32 s37, s39, s37\n\t" \
    "s_lshl_b32 s52, s37, 7\n\t" \
    "s_andn2_b32 s37, s52, 0x1fff\n\t" \
    "s_add_u32 s52, s52, s37\n\t" \
    "s_and_b32 s37, s56, 6\n\t" \
    "s_lshl_b32 s37, s37, 15\n\t" \
    "s_and_b32 s39, s48, 0x2000\n\t" \
    "s_add_u32 s37, s37, s39\n\t" \
    "s_add_u32 s33, s37, 33554432\n\t" \
    "s_add_u32 s55, s55, s39\n\t" \
    "s_waitcnt vmcnt(0)\n\t" \
    "s_mov_b64 exec, 1\n\t" \
    "ds_add_rtn_u32 v8, v9, v9 offset:12\n\t" \
    "s_add_u32 s37, s32, 786432\n\t" \
    "s_waitcnt lgkmcnt(0)\n\t" \
    "v_cmp_eq_u32 vcc, 1098496, v8\n\t" \
    "s_mov_b64 exec, -1\n\t" \
    "s_nop 0\n\t" \
    "s_cbranch_vccz .La22_%=\n\t" \
    "buffer_store_dwordx4 v[148:151], v2, s[4:7], s37 offen sc1\n\t" \
    ".La22_%=:\n\t" \
    "s_mov_b64 exec, -1\n\t" \
    ".Lh47_%=:\n\t" \
    "s_memrealtime s[22:23]\n\t" \
    "s_waitcnt lgkmcnt(0)\n\t" \
    "s_sub_u32 s22, s22, s35\n\t" \
    "s_cmp_lt_u32 s22, 485\n\t" \
    "s_cbranch_scc1 .Lh47_%=\n\t" \
    "s_cmp_lg_u32 s24, 0\n\t" \
    "s_cbranch_scc1 .Lb15_%=\n\t" \
    "s_mov_b32 s40, 0\n\t" \
    "s_add_u32 s38, s33, 524288\n\t" \
    ".Ll15_%=:\n\t" \
    "buffer_load_dword v1, v51, s[4:7], s38 offen sc1\n\t" \
    "s_waitcnt vmcnt(0)\n\t" \
    "v_cmp_ne_u32 vcc, s34, v1\n\t" \
    "s_add_u32 s40, s40, 1\n\t" \
    "s_cmp_ge_u32 s40, 65536\n\t" \
    "s_cbranch_scc1 .Lb15_%=\n\t" \
    "s_cbranch_vccz .Lb15_%=\n\t" \
    "s_sleep 2\n\t" \
    "s_branch .Ll15_%=\n\t" \
    ".Lb15_%=:\n\t" \
    "s_waitcnt lgkmcnt(0)\n\t" \
    "s_barrier\n\t" \
    "buffer_load_dwordx4 v[176:179], v36, s[4:7], s48 offen offset:0 sc1\n\t" \
    "buffer_load_dwordx4 v[180:183], v36, s[4:7], s48 offen offset:1024 sc1\n\t" \
    "buffer_load_dwordx4 v[184:187], v36, s[4:7], s48 offen offset:256 sc1\n\t" \
    "buffer_load_dwordx4 v[188:191], v36, s[4:7], s48 offen offset:1280 sc1\n\t" \
    "buffer_load_dwordx4 v[60:63], v37, s[4:7], s48 offen offset:0 sc1\n\t" \
    "s_cmp_lt_u32 s24, 4\n\t" \
    "s_cbranch_scc1 .Lp48_%=\n\t" \
    "s_setprio 1\n\t" \
    ".Lp48_%=:\n\t" \
    "v_add_u32 v11, 344064, v39\n\t" \
    "v_lshrrev_b32 v3, 21, v11\n\t" \
    "v_bfe_u32 v3, s0, v3, 1\n\t" \
    "v_lshrrev_b32 v9, 3, v11\n\t" \
    "v_lshlrev_b32 v3, 10, v3\n\t" \
    "v_and_b32 v9, 0x400, v9\n\t" \
    "v_xor_b32 v3, v3, v9\n\t" \
    "v_xor_b32 v8, v42, v3\n\t" \
    "v_lshrrev_b32 v3, 21, v39\n\t" \
    "v_bfe_u32 v3, s0, v3, 1\n\t" \
    "v_lshrrev_b32 v9, 3, v39\n\t" \
    "v_lshlrev_b32 v3, 10, v3\n\t" \
    "v_and_b32 v9, 0x400, v9\n\t" \
    "v_xor_b32 v3, v3, v9\n\t" \
    "v_xor_b32 v42, v42, v3\n\t" \
    "s_lshr_b32 s37, s10, 1\n\t" \
    "s_add_u32 s37, s37, s51\n\t" \
    "s_and_b32 s39, s8, 0x1fffff\n\t" \
    "s_add_u32 s37, s37, s39\n\t" \
    "s_lshr_b32 s39, s37, 21\n\t" \
    "s_lshr_b32 s39, s0, s39\n\t" \
    "s_xor_b32 s37, s37, s48\n\t" \
    "s_lshr_b32 s37, s37, 13\n\t" \
    "s_xor_b32 s37, s37, s39\n\t" \
    "s_and_b32 s37, s37, 1\n\t" \
    "s_mul_i32 s37, s37, 172032\n\t" \
    "s_add_u32 s37, s37, s51\n\t" \
    "s_add_u32 s8, s8, s37\n\t" \
    "s_addc_u32 s9, s9, 0\n\t" \
    "s_nop 0\n\t" \
    "v_readlane_b32 s12, v50, 0\n\t" \
    "v_readlane_b32 s13, v50, 1\n\t" \
    "v_readlane_b32 s14, v50, 2\n\t" \
    "v_readlane_b32 s15, v50, 3\n\t" \
    "v_readlane_b32 s16, v50, 4\n\t" \
    "v_readlane_b32 s17, v50, 5\n\t" \
    "v_readlane_b32 s18, v50, 6\n\t" \
    "v_readlane_b32 s19, v50, 7\n\t" \
    "v_readlane_b32 s3, v50, 8\n\t" \
    "s_add_u32 s12, s12, s33\n\t" \
    "s_add_u32 s13, s13, s33\n\t" \
    "s_add_u32 s14, s14, s33\n\t" \
    "s_add_u32 s15, s15, s33\n\t" \
    "s_add_u32 s16, s16, s33\n\t" \
    "s_add_u32 s17, s17, s33\n\t" \
    "s_add_u32 s18, s18, s33\n\t" \
    "s_add_u32 s19, s19, s33\n\t" \
    "s_add_u32 s3, s3, s33\n\t" \
    "s_waitcnt vmcnt(4)\n\t" \
    "v_mfma_f32_32x32x16_bf16 v[192:207], v[52:55], v[176:179], 0\n\t" \
    "buffer_load_dwordx4 v[4:7], v36, s[4:7], s49 offen offset:0 sc1\n\t" \
    "s_waitcnt vmcnt(4)\n\t" \
    "v_mfma_f32_32x32x16_bf16 v[192:207], v[56:59], v[180:183], v[192:207]\n\t" \
    "buffer_load_dwordx4 v[20:23], v36, s[4:7], s49 offen offset:1024 sc1\n\t" \
    "s_waitcnt vmcnt(4)\n\t" \
    "v_mfma_f32_32x32x16_bf16 v[208:223], v[52:55], v[184:187], 0\n\t" \
    "buffer_load_dwordx4 v[64:67], v36, s[4:7], s49 offen offset:256 sc1\n\t" \
    "s_waitcnt vmcnt(4)\n\t" \
    "v_mfma_f32_32x32x16_bf16 v[208:223], v[56:59], v[188:191], v[208:223]\n\t" \
    "buffer_load_dwordx4 v[68:71], v36, s[4:7], s49 offen offset:1280 sc1\n\t" \
    "v_mfma_f32_32x32x16_bf16 v[224:239], v[56:59], v[176:179], 0\n\t" \
    "s_add_u32 m0, s44, 0\n\t" \
    "s_nop 8\n\t" \
    "v_cvt_pk_bf16_f32 v51, v192, v208\n\t" \
    "v_cvt_pk_bf16_f32 v1, v193, v209\n\t" \
    "v_cvt_pk_bf16_f32 v11, v194, v210\n\t" \
    "v_cvt_pk_bf16_f32 v39, v195, v211\n\t" \
    "v_cvt_pk_bf16_f32 v9, v196, v212\n\t" \
    "s_waitcnt vmcnt(4)\n\t" \
    "v_permlane16_swap_b32 v60, v180\n\t" \
    "v_permlane16_swap_b32 v61, v181\n\t" \
    "ds_write_addtid_b32 v51 offset:3616\n\t" \
    "ds_write_addtid_b32 v1 offset:3872\n\t" \
    "ds_write_addtid_b32 v11 offset:4128\n\t" \
    "ds_write_addtid_b32 v39 offset:4384\n\t" \
    "ds_write_addtid_b32 v9 offset:4640\n\t" \
    "v_permlane16_swap_b32 v62, v182\n\t" \
    "v_permlane16_swap_b32 v63, v183\n\t" \
    "v_cvt_pk_bf16_f32 v3, v197, v213\n\t" \
    "v_cvt_pk_bf16_f32 v51, v198, v214\n\t" \
    "v_cvt_pk_bf16_f32 v1, v199, v215\n\t" \
    "v_cvt_pk_bf16_f32 v11, v200, v216\n\t" \
    "v_cvt_pk_bf16_f32 v39, v201, v217\n\t" \
    "v_mfma_f32_32x32x16_bf16 v[240:255], v[56:59], v[184:187], 0\n\t" \
    "v_permlane16_swap_b32 v180, v188\n\t" \
    "buffer_load_dwordx4 v[72:75], v37, s[4:7], s49 offen offset:0 sc1\n\t" \
    "ds_write_addtid_b32 v3 offset:4896\n\t" \
    "ds_write_addtid_b32 v51 offset:5152\n\t" \
    "ds_write_addtid_b32 v1 offset:5408\n\t" \
    "ds_write_addtid_b32 v11 offset:5664\n\t" \
    "ds_write_addtid_b32 v39 offset:5920\n\t" \
    "v_permlane16_swap_b32 v181, v189\n\t" \
    "v_permlane16_swap_b32 v182, v190\n\t" \
    "v_cvt_pk_bf16_f32 v9, v202, v218\n\t" \
    "v_cvt_pk_bf16_f32 v3, v203, v219\n\t" \
    "v_cvt_pk_bf16_f32 v51, v204, v220\n\t" \
    "v_cvt_pk_bf16_f32 v1, v205, v221\n\t" \
    "v_permlane16_swap_b32 v183, v191\n\t" \
    "v_mfma_f32_32x32x16_bf16 v[224:239], v[52:55], v[60:63], v[224:239]\n\t" \
    "ds_write_addtid_b32 v9 offset:6176\n\t" \
    "ds_write_addtid_b32 v3 offset:6432\n\t" \
    "ds_write_addtid_b32 v51 offset:6688\n\t" \
    "ds_write_addtid_b32 v1 offset:6944\n\t" \
    "v_mfma_f32_32x32x16_bf16 v[240:255], v[52:55], v[180:183], v[240:255]\n\t" \
    ".Lh49_%=:\n\t" \
    "s_memrealtime s[22:23]\n\t" \
    "s_waitcnt lgkmcnt(0)\n\t" \
    "s_sub_u32 s22, s22, s35\n\t" \
    "s_cmp_lt_u32 s22, 597\n\t" \
    "s_cbranch_scc1 .Lh49_%=\n\t" \
    "s_buffer_load_dword s12, s[4:7], s12 offset:0xc0000\n\t" \
    "s_buffer_load_dword s13, s[4:7], s13 offset:0xc0000\n\t" \
    "s_buffer_load_dword s14, s[4:7], s14 offset:0xc0000\n\t" \
    "s_buffer_load_dword s15, s[4:7], s15 offset:0xc0000\n\t" \
    "s_buffer_load_dword s16, s[4:7], s16 offset:0xc0000\n\t" \
    "s_buffer_load_dword s17, s[4:7], s17 offset:0xc0000\n\t" \
    "s_buffer_load_dword s18, s[4:7], s18 offset:0xc0000\n\t" \
    "s_buffer_load_dword s19, s[4:7], s19 offset:0xc0000\n\t" \
    "s_buffer_load_dword s3, s[4:7], s3 offset:0xc0000\n\t" \
    "s_waitcnt vmcnt(4)\n\t" \
    "v_mfma_f32_32x32x16_bf16 v[192:207], v[52:55], v[4:7], 0\n\t" \
    "v_cvt_pk_bf16_f32 v11, v224, v240\n\t" \
    "v_cvt_pk_bf16_f32 v39, v225, v241\n\t" \
    "v_cvt_pk_bf16_f32 v9, v226, v242\n\t" \
    "v_cvt_pk_bf16_f32 v3, v227, v243\n\t" \
    "v_cvt_pk_bf16_f32 v51, v228, v244\n\t" \
    "buffer_load_dwordx4 v[76:79], v36, s[4:7], s50 offen offset:0 sc1\n\t" \
    "ds_write_addtid_b32 v11 offset:0\n\t" \
    "ds_write_addtid_b32 v39 offset:256\n\t" \
    "ds_write_addtid_b32 v9 offset:512\n\t" \
    "ds_write_addtid_b32 v3 offset:768\n\t" \
    "ds_write_addtid_b32 v51 offset:1024\n\t" \
    "s_waitcnt vmcnt(4)\n\t" \
    "v_mfma_f32_32x32x16_bf16 v[192:207], v[56:59], v[20:23], v[192:207]\n\t" \
    "buffer_load_dwordx4 v[80:83], v36, s[4:7], s50 offen offset:1024 sc1\n\t" \
    "v_cvt_pk_bf16_f32 v1, v229, v245\n\t" \
    "v_cvt_pk_bf16_f32 v11, v230, v246\n\t" \
    "v_cvt_pk_bf16_f32 v39, v231, v247\n\t" \
    "v_cvt_pk_bf16_f32 v9, v232, v248\n\t" \
    "v_cvt_pk_bf16_f32 v3, v233, v249\n\t" \
    "s_waitcnt vmcnt(4)\n\t" \
    "v_mfma_f32_32x32x16_bf16 v[208:223], v[52:55], v[64:67], 0\n\t" \
    "buffer_load_dwordx4 v[100:103], v36, s[4:7], s50 offen offset:256 sc1\n\t" \
    "ds_write_addtid_b32 v1 offset:1280\n\t" \
    "ds_write_addtid_b32 v11 offset:1536\n\t" \
    "ds_write_addtid_b32 v39 offset:1792\n\t" \
    "ds_write_addtid_b32 v9 offset:2048\n\t" \
    "ds_write_addtid_b32 v3 offset:2304\n\t" \
    "v_cvt_pk_bf16_f32 v51, v234, v250\n\t" \
    "v_cvt_pk_bf16_f32 v12, v235, v251\n\t" \
    "v_cvt_pk_bf16_f32 v14, v236, v252\n\t" \
    "v_cvt_pk_bf16_f32 v13, v237, v253\n\t" \
    "buffer_load_dwordx4 v[104:107], v36, s[4:7], s50 offen offset:1280 sc1\n\t" \
    "s_waitcnt vmcnt(5)\n\t" \
    "v_mfma_f32_32x32x16_bf16 v[208:223], v[56:59], v[68:71], v[208:223]\n\t" \
    "ds_write_addtid_b32 v51 offset:2560\n\t" \
    "ds_write_addtid_b32 v12 offset:2816\n\t" \
    "ds_write_addtid_b32 v14 offset:3072\n\t" \
    "ds_write_addtid_b32 v13 offset:3328\n\t" \
    "buffer_load_dwordx4 v[88:91], v37, s[4:7], s50 offen offset:0 sc1\n\t" \
    "s_waitcnt lgkmcnt(0)\n\t" \
    "s_xor_b32 s12, s12, s34\n\t" \
    "s_xor_b32 s13, s13, s34\n\t" \
    "s_xor_b32 s14, s14, s34\n\t" \
    "s_xor_b32 s15, s15, s34\n\t" \
    "s_xor_b32 s16, s16, s34\n\t" \
    "s_xor_b32 s17, s17, s34\n\t" \
    "s_xor_b32 s18, s18, s34\n\t" \
    "s_xor_b32 s19, s19, s34\n\t" \
    "s_xor_b32 s3, s3, s34\n\t" \
    "s_or_b32 s12, s12, s13\n\t" \
    "s_or_b32 s12, s12, s14\n\t" \
    "s_or_b32 s12, s12, s15\n\t" \
    "s_or_b32 s12, s12, s16\n\t" \
    "s_or_b32 s12, s12, s17\n\t" \
    "s_or_b32 s12, s12, s18\n\t" \
    "s_or_b32 s12, s12, s19\n\t" \
    "s_or_b32 s12, s12, s3\n\t" \
    "s_cbranch_scc1 .Lq1025_%=\n\t" \
    ".Lb24_%=:\n\t" \
    "s_waitcnt lgkmcnt(0)\n\t" \
    "s_barrier\n\t" \
    "ds_read_b128 v[92:95], v44 offset:0\n\t" \
    "s_mov_b32 s45, 0\n\t" \
    "buffer_load_dwordx4 v[108:111], v41, s[4:7], s45 offen offset:0 sc1\n\t" \
    "ds_read_b128 v[112:115], v44 offset:128\n\t" \
    "ds_read_b128 v[84:87], v44 offset:29184\n\t" \
    "ds_read_b128 v[96:99], v44 offset:29312\n\t" \
    "ds_read_b128 v[120:123], v45 offset:0\n\t" \
    "buffer_load_dwordx4 v[124:127], v41, s[4:7], s45 offen offset:1024 sc1\n\t" \
    "ds_read_b128 v[116:119], v45 offset:128\n\t" \
    "ds_read_b128 v[128:131], v45 offset:29184\n\t" \
    "ds_read_b128 v[132:135], v45 offset:29312\n\t" \
    "v_mfma_f32_32x32x16_bf16 v[224:239], v[56:59], v[4:7], 0\n\t" \
    "v_cvt_pk_bf16_f32 v11, v192, v208\n\t" \
    "v_cvt_pk_bf16_f32 v39, v193, v209\n\t" \
    "v_cvt_pk_bf16_f32 v9, v194, v210\n\t" \
    "v_cvt_pk_bf16_f32 v3, v195, v211\n\t" \
    "v_cvt_pk_bf16_f32 v36, v196, v212\n\t" \
    "s_waitcnt vmcnt(7)\n\t" \
    "v_permlane16_swap_b32 v72, v20\n\t" \
    "v_permlane16_swap_b32 v73, v21\n\t" \
    "s_mov_b32 s46, 114688\n\t" \
    "buffer_load_dwordx4 v[28:31], v41, s[4:7], s46 offen offset:0 sc1\n\t" \
    "ds_write_addtid_b32 v11 offset:61984\n\t" \
    "ds_write_addtid_b32 v39 offset:62240\n\t" \
    "ds_write_addtid_b32 v9 offset:62496\n\t" \
    "ds_write_addtid_b32 v3 offset:62752\n\t" \
    "ds_write_addtid_b32 v36 offset:63008\n\t" \
    "v_permlane16_swap_b32 v74, v22\n\t" \
    "v_permlane16_swap_b32 v75, v23\n\t" \
    "v_cvt_pk_bf16_f32 v51, v197, v213\n\t" \
    "v_cvt_pk_bf16_f32 v37, v198, v214\n\t" \
    "v_cvt_pk_bf16_f32 v50, v199, v215\n\t" \
    "v_cvt_pk_bf16_f32 v1, v200, v216\n\t" \
    "v_cvt_pk_bf16_f32 v11, v201, v217\n\t" \
    "v_mfma_f32_32x32x16_bf16 v[240:255], v[56:59], v[64:67], 0\n\t" \
    "v_permlane16_swap_b32 v20, v68\n\t" \
    "buffer_load_dwordx4 v[136:139], v41, s[4:7], s46 offen offset:1024 sc1\n\t" \
    "ds_write_addtid_b32 v51 offset:63264\n\t" \
    "ds_write_addtid_b32 v37 offset:63520\n\t" \
    "ds_write_addtid_b32 v50 offset:63776\n\t" \
    "ds_write_addtid_b32 v1 offset:64032\n\t" \
    "ds_write_addtid_b32 v11 offset:64288\n\t" \
    "v_permlane16_swap_b32 v21, v69\n\t" \
    "v_permlane16_swap_b32 v22, v70\n\t" \
    "v_cvt_pk_bf16_f32 v39, v202, v218\n\t" \
    "v_cvt_pk_bf16_f32 v9, v203, v219\n\t" \
    "v_cvt_pk_bf16_f32 v3, v204, v220\n\t" \
    "v_cvt_pk_bf16_f32 v36, v205, v221\n\t" \
    "v_permlane16_swap_b32 v23, v71\n\t" \
    "v_mfma_f32_32x32x16_bf16 v[224:239], v[52:55], v[72:75], v[224:239]\n\t" \
    "buffer_load_dwordx4 v[24:27], v46, s[4:7], s45 offen offset:0 sc1\n\t" \
    "ds_write_addtid_b32 v39 offset:64544\n\t" \
    "ds_write_addtid_b32 v9 offset:64800\n\t" \
    "ds_write_addtid_b32 v3 offset:65056\n\t" \
    "ds_write_addtid_b32 v36 offset:65312\n\t" \
    "v_mfma_f32_32x32x16_bf16 v[240:255], v[52:55], v[20:23], v[240:255]\n\t" \
    "s_waitcnt vmcnt(9)\n\t" \
    "v_mfma_f32_32x32x16_bf16 v[192:207], v[52:55], v[76:79], 0\n\t" \
    "s_nop 9\n\t" \
    "v_cvt_pk_bf16_f32 v51, v224, v240\n\t" \
    "v_cvt_pk_bf16_f32 v37, v225, v241\n\t" \
    "v_cvt_pk_bf16_f32 v50, v226, v242\n\t" \
    "v_cvt_pk_bf16_f32 v1, v227, v243\n\t" \
    "v_cvt_pk_bf16_f32 v11, v228, v244\n\t" \
    "buffer_load_dwordx4 v[32:35], v46, s[4:7], s46 offen offset:0 sc1\n\t" \
    "ds_write_addtid_b32 v51 offset:58368\n\t" \
    "ds_write_addtid_b32 v37 offset:58624\n\t" \
    "ds_write_addtid_b32 v50 offset:58880\n\t" \
    "ds_write_addtid_b32 v1 offset:59136\n\t" \
    "ds_write_addtid_b32 v11 offset:59392\n\t" \
    "s_waitcnt vmcnt(9)\n\t" \
    "v_mfma_f32_32x32x16_bf16 v[192:207], v[56:59], v[80:83], v[192:207]\n\t" \
    "v_cvt_pk_bf16_f32 v39, v229, v245\n\t" \
    "v_cvt_pk_bf16_f32 v9, v230, v246\n\t" \
    "v_cvt_pk_bf16_f32 v3, v231, v247\n\t" \
    "v_cvt_pk_bf16_f32 v36, v232, v248\n\t" \
    "v_cvt_pk_bf16_f32 v51, v233, v249\n\t" \
    "s_mov_b32 s47, 229376\n\t" \
    "buffer_load_dwordx4 v[140:143], v41, s[4:7], s47 offen offset:0 sc1\n\t" \
    "s_waitcnt vmcnt(9)\n\t" \
    "v_mfma_f32_32x32x16_bf16 v[208:223], v[52:55], v[100:103], 0\n\t" \
    "ds_write_addtid_b32 v39 offset:59648\n\t" \
    "ds_write_addtid_b32 v9 offset:59904\n\t" \
    "ds_write_addtid_b32 v3 offset:60160\n\t" \
    "ds_write_addtid_b32 v36 offset:60416\n\t" \
    "ds_write_addtid_b32 v51 offset:60672\n\t" \
    "buffer_load_dwordx4 v[156:159], v41, s[4:7], s47 offen offset:1024 sc1\n\t" \
    "v_cvt_pk_bf16_f32 v37, v234, v250\n\t" \
    "v_cvt_pk_bf16_f32 v50, v235, v251\n\t" \
    "v_cvt_pk_bf16_f32 v1, v236, v252\n\t" \
    "v_cvt_pk_bf16_f32 v11, v237, v253\n\t" \
    "s_waitcnt vmcnt(9)\n\t" \
    "v_mfma_f32_32x32x16_bf16 v[208:223], v[56:59], v[104:107], v[208:223]\n\t" \
    "ds_write_addtid_b32 v37 offset:60928\n\t" \
    "ds_write_addtid_b32 v50 offset:61184\n\t" \
    "ds_write_addtid_b32 v1 offset:61440\n\t" \
    "ds_write_addtid_b32 v11 offset:61696\n\t" \
    "buffer_load_dwordx4 v[168:171], v46, s[4:7], s47 offen offset:0 sc1\n\t" \
    "s_waitcnt vmcnt(9)\n\t" \
    "s_waitcnt lgkmcnt(0)\n\t" \
    "s_barrier\n\t" \
    "s_memrealtime s[64:65]\n\t" \
    "s_cmp_lg_u32 s24, 7\n\t" \
    "s_cbranch_scc1 .Lar_%=\n\t" \
    "s_waitcnt lgkmcnt(0)\n\t" \
    "v_mov_b32 v149, s64\n\t" \
    "s_and_b32 s37, s48, 8192\n\t" \
    "s_add_u32 s37, s37, s32\n\t" \
    "s_add_u32 s37, s37, 1048576\n\t" \
    "s_mov_b64 exec, 0xff\n\t" \
    "buffer_store_dwordx4 v[148:151], v0, s[4:7], s37 offen sc1\n\t" \
    "s_mov_b64 exec, -1\n\t" \
    ".Lar_%=:\n\t" \
    "v_add_u32 v44, 58368, v44\n\t" \
    "s_bitcmp1_b32 s24, 2\n\t" \
    "s_cbranch_scc1 .Ly29_%=\n\t" \
    "buffer_load_dwordx4 v[172:175], v46, s[4:7], s45 offen offset:1024 sc1\n\t" \
    ".Ly29_%=:\n\t" \
    "v_add_u32 v45, 58368, v45\n\t" \
    "ds_read_b128 v[144:147], v44 offset:0\n\t" \
    "ds_read_b128 v[152:155], v44 offset:128\n\t" \
    "ds_read_b128 v[160:163], v44 offset:29184\n\t" \
    "ds_read_b128 v[164:167], v44 offset:29312\n\t" \
    "s_bitcmp1_b32 s24, 2\n\t" \
    "s_cbranch_scc1 .Ly30_%=\n\t" \
    "buffer_load_dwordx4 v[16:19], v46, s[4:7], s46 offen offset:1024 sc1\n\t" \
    ".Ly30_%=:\n\t" \
    "ds_read_b128 v[176:179], v45 offset:0\n\t" \
    "ds_read_b128 v[184:187], v45 offset:128\n\t" \
    "ds_read_b128 v[188:191], v45 offset:29184\n\t" \
    "ds_read_b128 v[60:63], v45 offset:29312\n\t" \
    "v_mfma_f32_32x32x16_bf16 v[224:239], v[56:59], v[76:79], 0\n\t" \
    "v_cvt_pk_bf16_f32 v39, v192, v208\n\t" \
    "v_cvt_pk_bf16_f32 v9, v193, v209\n\t" \
    "v_cvt_pk_bf16_f32 v3, v194, v210\n\t" \
    "v_cvt_pk_bf16_f32 v36, v195, v211\n\t" \
    "v_cvt_pk_bf16_f32 v51, v196, v212\n\t" \
    "s_waitcnt vmcnt(8)\n\t" \
    "v_mfma_f32_16x16x32_bf16 v[180:183], v[108:111], v[92:95], 0\n\t" \
    "v_permlane16_swap_b32 v88, v80\n\t" \
    "s_bitcmp1_b32 s24, 2\n\t" \
    "s_cbranch_scc1 .Ly31_%=\n\t" \
    "buffer_load_dwordx4 v[12:15], v46, s[4:7], s47 offen offset:1024 sc1\n\t" \
    ".Ly31_%=:\n\t" \
    "v_permlane16_swap_b32 v89, v81\n\t" \
    "ds_write_addtid_b32 v39 offset:3616\n\t" \
    "ds_write_addtid_b32 v9 offset:3872\n\t" \
    "ds_write_addtid_b32 v3 offset:4128\n\t" \
    "ds_write_addtid_b32 v36 offset:4384\n\t" \
    "ds_write_addtid_b32 v51 offset:4640\n\t" \
    "v_mfma_f32_16x16x32_bf16 v[4:7], v[108:111], v[112:115], 0\n\t" \
    "v_permlane16_swap_b32 v90, v82\n\t" \
    "v_mfma_f32_16x16x32_bf16 v[64:67], v[108:111], v[84:87], 0\n\t" \
    "v_permlane16_swap_b32 v91, v83\n\t" \
    "v_cvt_pk_bf16_f32 v37, v197, v213\n\t" \
    "v_cvt_pk_bf16_f32 v50, v198, v214\n\t" \
    "v_cvt_pk_bf16_f32 v1, v199, v215\n\t" \
    "v_cvt_pk_bf16_f32 v11, v200, v216\n\t" \
    "v_cvt_pk_bf16_f32 v39, v201, v217\n\t" \
    "s_mov_b32 s45, 344064\n\t" \
    "buffer_load_dwordx4 v[68:71], v41, s[4:7], s45 offen offset:0 sc1\n\t" \
    "v_mfma_f32_32x32x16_bf16 v[240:255], v[56:59], v[100:103], 0\n\t" \
    "v_mfma_f32_16x16x32_bf16 v[72:75], v[108:111], v[96:99], 0\n\t" \
    "v_permlane16_swap_b32 v80, v104\n\t" \
    "ds_write_addtid_b32 v37 offset:4896\n\t" \
    "ds_write_addtid_b32 v50 offset:5152\n\t" \
    "ds_write_addtid_b32 v1 offset:5408\n\t" \
    "ds_write_addtid_b32 v11 offset:5664\n\t" \
    "ds_write_addtid_b32 v39 offset:5920\n\t" \
    "s_waitcnt vmcnt(8)\n\t" \
    "v_mfma_f32_16x16x32_bf16 v[20:23], v[124:127], v[112:115], 0\n\t" \
    "v_permlane16_swap_b32 v81, v105\n\t" \
    "buffer_load_dwordx4 v[148:151], v41, s[4:7], s45 offen offset:1024 sc1\n\t" \
    "v_permlane16_swap_b32 v82, v106\n\t" \
    "v_cvt_pk_bf16_f32 v9, v202, v218\n\t" \
    "v_cvt_pk_bf16_f32 v3, v203, v219\n\t" \
    "v_cvt_pk_bf16_f32 v36, v204, v220\n\t" \
    "v_cvt_pk_bf16_f32 v51, v205, v221\n\t" \
    "v_mfma_f32_16x16x32_bf16 v[4:7], v[124:127], v[92:95], v[4:7]\n\t" \
    "v_permlane16_swap_b32 v83, v107\n\t" \
    "v_mfma_f32_16x16x32_bf16 v[76:79], v[124:127], v[96:99], 0\n\t" \
    "v_mfma_f32_32x32x16_bf16 v[224:239], v[52:55], v[88:91], v[224:239]\n\t" \
    "ds_write_addtid_b32 v9 offset:6176\n\t" \
    "ds_write_addtid_b32 v3 offset:6432\n\t" \
    "ds_write_addtid_b32 v36 offset:6688\n\t" \
    "ds_write_addtid_b32 v51 offset:6944\n\t" \
    "s_mov_b32 s46, 458752\n\t" \
    "buffer_load_dwordx4 v[56:59], v41, s[4:7], s46 offen offset:0 sc1\n\t" \
    "v_mfma_f32_32x32x16_bf16 v[240:255], v[52:55], v[80:83], v[240:255]\n\t" \
    "v_mfma_f32_16x16x32_bf16 v[72:75], v[124:127], v[84:87], v[72:75]\n\t" \
    "s_nop 10\n\t" \
    "v_cvt_pk_bf16_f32 v37, v224, v240\n\t" \
    "v_cvt_pk_bf16_f32 v50, v225, v241\n\t" \
    "v_cvt_pk_bf16_f32 v1, v226, v242\n\t" \
    "v_cvt_pk_bf16_f32 v11, v227, v243\n\t" \
    "v_cvt_pk_bf16_f32 v39, v228, v244\n\t" \
    "s_waitcnt vmcnt(9) lgkmcnt(15)\n\t" \
    "v_mfma_f32_16x16x32_bf16 v[180:183], v[28:31], v[144:147], v[180:183]\n\t" \
    "buffer_load_dwordx4 v[100:103], v41, s[4:7], s46 offen offset:1024 sc1\n\t" \
    "v_mfma_f32_16x16x32_bf16 v[4:7], v[28:31], v[152:155], v[4:7]\n\t" \
    "ds_write_addtid_b32 v37 offset:0\n\t" \
    "ds_write_addtid_b32 v50 offset:256\n\t" \
    "ds_write_addtid_b32 v1 offset:512\n\t" \
    "ds_write_addtid_b32 v11 offset:768\n\t" \
    "ds_write_addtid_b32 v39 offset:1024\n\t" \
    "v_mfma_f32_16x16x32_bf16 v[64:67], v[28:31], v[160:163], v[64:67]\n\t" \
    "v_cvt_pk_bf16_f32 v9, v229, v245\n\t" \
    "v_cvt_pk_bf16_f32 v3, v230, v246\n\t" \
    "v_cvt_pk_bf16_f32 v36, v231, v247\n\t" \
    "v_cvt_pk_bf16_f32 v51, v232, v248\n\t" \
    "v_cvt_pk_bf16_f32 v37, v233, v249\n\t" \
    "s_mov_b32 s47, 573440\n\t" \
    "buffer_load_dwordx4 v[108:111], v41, s[4:7], s47 offen offset:0 sc1\n\t" \
    "v_mfma_f32_16x16x32_bf16 v[72:75], v[28:31], v[164:167], v[72:75]\n\t" \
    "ds_write_addtid_b32 v9 offset:1280\n\t" \
    "ds_write_addtid_b32 v3 offset:1536\n\t" \
    "ds_write_addtid_b32 v36 offset:1792\n\t" \
    "ds_write_addtid_b32 v51 offset:2048\n\t" \
    "ds_write_addtid_b32 v37 offset:2304\n\t" \
    "s_waitcnt vmcnt(10)\n\t" \
    "v_mfma_f32_16x16x32_bf16 v[20:23], v[136:139], v[152:155], v[20:23]\n\t" \
    "buffer_load_dwordx4 v[192:195], v41, s[4:7], s47 offen offset:1024 sc1\n\t" \
    "v_mfma_f32_16x16x32_bf16 v[4:7], v[136:139], v[144:147], v[4:7]\n\t" \
    "v_cvt_pk_bf16_f32 v50, v234, v250\n\t" \
    "v_cvt_pk_bf16_f32 v1, v235, v251\n\t" \
    "v_cvt_pk_bf16_f32 v11, v236, v252\n\t" \
    "v_cvt_pk_bf16_f32 v39, v237, v253\n\t" \
    "v_mfma_f32_16x16x32_bf16 v[76:79], v[136:139], v[164:167], v[76:79]\n\t" \
    "ds_write_addtid_b32 v50 offset:2560\n\t" \
    "ds_write_addtid_b32 v1 offset:2816\n\t" \
    "ds_write_addtid_b32 v11 offset:3072\n\t" \
    "ds_write_addtid_b32 v39 offset:3328\n\t" \
    "buffer_load_dwordx4 v[196:199], v46, s[4:7], s45 offen offset:0 sc1\n\t" \
    "v_mfma_f32_16x16x32_bf16 v[72:75], v[136:139], v[160:163], v[72:75]\n\t" \
    "s_waitcnt lgkmcnt(0)\n\t" \
    "s_barrier\n\t" \
    "ds_read_b128 v[200:203], v10 offset:2048\n\t" \
    "v_mov_b32 v9, 0x10000\n\t" \
    "s_waitcnt vmcnt(11)\n\t" \
    "v_mfma_f32_16x16x32_bf16 v[204:207], v[24:27], v[120:123], 0\n\t" \
    "v_subrev_u32 v44, 58368, v44\n\t" \
    "v_mfma_f32_16x16x32_bf16 v[208:211], v[24:27], v[116:119], 0\n\t" \
    "v_subrev_u32 v45, 58368, v45\n\t" \
    "v_mfma_f32_16x16x32_bf16 v[212:215], v[24:27], v[128:131], 0\n\t" \
    "ds_read_b128 v[216:219], v44 offset:0\n\t" \
    "buffer_load_dwordx4 v[220:223], v46, s[4:7], s46 offen offset:0 sc1\n\t" \
    "v_mfma_f32_16x16x32_bf16 v[104:107], v[24:27], v[132:135], 0\n\t" \
    "ds_read_b128 v[88:91], v44 offset:128\n\t" \
    "s_waitcnt vmcnt(11)\n\t" \
    "v_mfma_f32_16x16x32_bf16 v[204:207], v[32:35], v[176:179], v[204:207]\n\t" \
    "ds_read_b128 v[52:55], v44 offset:29184\n\t" \
    "v_mfma_f32_16x16x32_bf16 v[208:211], v[32:35], v[184:187], v[208:211]\n\t" \
    "ds_read_b128 v[80:83], v44 offset:29312\n\t" \
    "buffer_load_dwordx4 v[124:127], v46, s[4:7], s47 offen offset:0 sc1\n\t" \
    "v_mfma_f32_16x16x32_bf16 v[212:215], v[32:35], v[188:191], v[212:215]\n\t" \
    "ds_read_b128 v[28:31], v45 offset:0\n\t" \
    "v_mfma_f32_16x16x32_bf16 v[104:107], v[32:35], v[60:63], v[104:107]\n\t" \
    "ds_read_b128 v[224:227], v45 offset:128\n\t" \
    "s_waitcnt vmcnt(11) lgkmcnt(5)\n\t" \
    "v_mfma_f32_16x16x32_bf16 v[180:183], v[140:143], v[216:219], v[180:183]\n\t" \
    "ds_read_b128 v[228:231], v45 offset:29184\n\t" \
    "s_bitcmp1_b32 s24, 2\n\t" \
    "s_cbranch_scc1 .Ly32_%=\n\t" \
    "buffer_load_dwordx4 v[232:235], v46, s[4:7], s45 offen offset:1024 sc1\n\t" \
    ".Ly32_%=:\n\t" \
    "s_waitcnt lgkmcnt(5)\n\t" \
    "v_mfma_f32_16x16x32_bf16 v[4:7], v[140:143], v[88:91], v[4:7]\n\t" \
    "ds_read_b128 v[236:239], v45 offset:29312\n\t" \
    "s_waitcnt lgkmcnt(5)\n\t" \
    "v_mfma_f32_16x16x32_bf16 v[64:67], v[140:143], v[52:55], v[64:67]\n\t" \
    "s_waitcnt lgkmcnt(4)\n\t" \
    "v_mfma_f32_16x16x32_bf16 v[72:75], v[140:143], v[80:83], v[72:75]\n\t" \
    "s_bitcmp1_b32 s24, 2\n\t" \
    "s_cbranch_scc1 .Ly33_%=\n\t" \
    "buffer_load_dwordx4 v[240:243], v46, s[4:7], s46 offen offset:1024 sc1\n\t" \
    ".Ly33_%=:\n\t" \
    "s_waitcnt vmcnt(10)\n\t" \
    "v_mfma_f32_16x16x32_bf16 v[20:23], v[156:159], v[88:91], v[20:23]\n\t" \
    "s_waitcnt lgkmcnt(0)\n\t" \
    "s_add_u32 s12, s55, 34603008\n\t" \
    "s_buffer_load_dword s13, s[4:7], s12\n\t" \
    "s_buffer_load_dword s14, s[4:7], s12 offset:0x400\n\t" \
    "v_mfma_f32_16x16x32_bf16 v[4:7], v[156:159], v[216:219], v[4:7]\n\t" \
    "v_mfma_f32_16x16x32_bf16 v[76:79], v[156:159], v[80:83], v[76:79]\n\t" \
    "s_bitcmp1_b32 s24, 2\n\t" \
    "s_cbranch_scc1 .Ly34_%=\n\t" \
    "buffer_load_dwordx4 v[244:247], v46, s[4:7], s47 offen offset:1024 sc1\n\t" \
    ".Ly34_%=:\n\t" \
    "v_mfma_f32_16x16x32_bf16 v[72:75], v[156:159], v[52:55], v[72:75]\n\t" \
    "s_waitcnt vmcnt(9)\n\t" \
    "v_mfma_f32_16x16x32_bf16 v[204:207], v[168:171], v[28:31], v[204:207]\n\t" \
    "v_mfma_f32_16x16x32_bf16 v[208:211], v[168:171], v[224:227], v[208:211]\n\t" \
    "s_mov_b32 s45, 688128\n\t" \
    "buffer_load_dwordx4 v[248:251], v41, s[4:7], s45 offen offset:0 sc1\n\t" \
    "v_mfma_f32_16x16x32_bf16 v[212:215], v[168:171], v[228:231], v[212:215]\n\t" \
    "v_sub_f32 v180, v180, v20\n\t" \
    "v_sub_f32 v181, v181, v21\n\t" \
    "v_sub_f32 v182, v182, v22\n\t" \
    "v_mfma_f32_16x16x32_bf16 v[104:107], v[168:171], v[236:239], v[104:107]\n\t" \
    "v_sub_f32 v183, v183, v23\n\t" \
    "v_sub_f32 v64, v64, v76\n\t" \
    "v_sub_f32 v65, v65, v77\n\t" \
    "v_sub_f32 v66, v66, v78\n\t" \
    "v_sub_f32 v67, v67, v79\n\t" \
    "v_cvt_pk_bf16_f32 v254, v4, v5\n\t" \
    "v_cvt_pk_bf16_f32 v255, v6, v7\n\t" \
    "v_cvt_pk_bf16_f32 v252, v180, v181\n\t" \
    "v_cvt_pk_bf16_f32 v253, v182, v183\n\t" \
    "v_cvt_pk_bf16_f32 v138, v72, v73\n\t" \
    "v_cvt_pk_bf16_f32 v139, v74, v75\n\t" \
    "ds_write_b128 v40, v[252:255] offset:58368\n\t" \
    "v_cvt_pk_bf16_f32 v136, v64, v65\n\t" \
    "v_cvt_pk_bf16_f32 v137, v66, v67\n\t" \
    "ds_write_b128 v40, v[136:139] offset:59392\n\t" \
    "s_waitcnt vmcnt(10)\n\t" \
    "s_cmp_ge_u32 s24, 4\n\t" \
    "s_cbranch_scc1 .Lk53_%=\n\t" \
    "v_mfma_f32_16x16x32_bf16 v[24:27], v[172:175], v[116:119], 0\n\t" \
    "v_mfma_f32_16x16x32_bf16 v[24:27], v[16:19], v[184:187], v[24:27]\n\t" \
    "v_mfma_f32_16x16x32_bf16 v[24:27], v[12:15], v[224:227], v[24:27]\n\t" \
    "v_mfma_f32_16x16x32_bf16 v[32:35], v[172:175], v[132:135], 0\n\t" \
    "v_mfma_f32_16x16x32_bf16 v[32:35], v[16:19], v[60:63], v[32:35]\n\t" \
    "v_mfma_f32_16x16x32_bf16 v[32:35], v[12:15], v[236:239], v[32:35]\n\t" \
    "v_mfma_f32_16x16x32_bf16 v[208:211], v[172:175], v[120:123], v[208:211]\n\t" \
    "v_mfma_f32_16x16x32_bf16 v[208:211], v[16:19], v[176:179], v[208:211]\n\t" \
    "v_mfma_f32_16x16x32_bf16 v[208:211], v[12:15], v[28:31], v[208:211]\n\t" \
    "v_mfma_f32_16x16x32_bf16 v[104:107], v[172:175], v[128:131], v[104:107]\n\t" \
    "s_nop 0\n\t" \
    "v_sub_f32 v204, v204, v24\n\t" \
    "v_sub_f32 v205, v205, v25\n\t" \
    "v_mfma_f32_16x16x32_bf16 v[104:107], v[16:19], v[188:191], v[104:107]\n\t" \
    "v_sub_f32 v206, v206, v26\n\t" \
    "v_sub_f32 v207, v207, v27\n\t" \
    "v_mfma_f32_16x16x32_bf16 v[104:107], v[12:15], v[228:231], v[104:107]\n\t" \
    "v_sub_f32 v212, v212, v32\n\t" \
    "v_sub_f32 v213, v213, v33\n\t" \
    "v_sub_f32 v214, v214, v34\n\t" \
    "v_sub_f32 v215, v215, v35\n\t" \
    ".Lk53_%=:\n\t" \
    "s_waitcnt vmcnt(9)\n\t" \
    "v_mfma_f32_16x16x32_bf16 v[140:143], v[68:71], v[92:95], 0\n\t" \
    "buffer_load_dwordx4 v[156:159], v41, s[4:7], s45 offen offset:1024 sc1\n\t" \
    "v_mfma_f32_16x16x32_bf16 v[168:171], v[68:71], v[112:115], 0\n\t" \
    "v_cvt_pk_bf16_f32 v22, v208, v209\n\t" \
    "v_cvt_pk_bf16_f32 v23, v210, v211\n\t" \
    "v_mfma_f32_16x16x32_bf16 v[76:79], v[68:71], v[84:87], 0\n\t" \
    "v_cvt_pk_bf16_f32 v20, v204, v205\n\t" \
    "v_cvt_pk_bf16_f32 v21, v206, v207\n\t" \
    "v_mfma_f32_16x16x32_bf16 v[4:7], v[68:71], v[96:99], 0\n\t" \
    "v_cvt_pk_bf16_f32 v182, v104, v105\n\t" \
    "v_cvt_pk_bf16_f32 v183, v106, v107\n\t" \
    "s_mov_b32 s46, 802816\n\t" \
    "buffer_load_dwordx4 v[72:75], v41, s[4:7], s46 offen offset:0 sc1\n\t" \
    "s_waitcnt vmcnt(10)\n\t" \
    "v_mfma_f32_16x16x32_bf16 v[252:255], v[148:151], v[112:115], 0\n\t" \
    "ds_write_b128 v40, v[20:23] offset:60432\n\t" \
    "v_cvt_pk_bf16_f32 v180, v212, v213\n\t" \
    "v_mfma_f32_16x16x32_bf16 v[168:171], v[148:151], v[92:95], v[168:171]\n\t" \
    "v_cvt_pk_bf16_f32 v181, v214, v215\n\t" \
    "v_mfma_f32_16x16x32_bf16 v[64:67], v[148:151], v[96:99], 0\n\t" \
    "ds_write_b128 v40, v[180:183] offset:61456\n\t" \
    "buffer_load_dwordx4 v[136:139], v41, s[4:7], s46 offen offset:1024 sc1\n\t" \
    "v_mfma_f32_16x16x32_bf16 v[4:7], v[148:151], v[84:87], v[4:7]\n\t" \
    "s_waitcnt vmcnt(10)\n\t" \
    "v_mfma_f32_16x16x32_bf16 v[140:143], v[56:59], v[144:147], v[140:143]\n\t" \
    "v_mfma_f32_16x16x32_bf16 v[168:171], v[56:59], v[152:155], v[168:171]\n\t" \
    "s_waitcnt lgkmcnt(0)\n\t" \
    "s_xor_b32 s13, s13, s34\n\t" \
    "s_xor_b32 s14, s14, s34\n\t" \
    "s_or_b32 s13, s13, s14\n\t" \
    "s_cbranch_scc1 .Lq1028_%=\n\t" \
    ".Lb27_%=:\n\t" \
    "s_waitcnt lgkmcnt(0)\n\t" \
    "s_barrier\n\t" \
    "s_memrealtime s[16:17]\n\t" \
    "ds_read_b64_tr_b16 v[172:173], v49 offset:58368\n\t" \
    "ds_read_b64_tr_b16 v[174:175], v49 offset:62496\n\t" \
    "ds_read_b64_tr_b16 v[16:17], v49 offset:58624\n\t" \
    "ds_read_b64_tr_b16 v[18:19], v49 offset:62752\n\t" \
    "s_mov_b32 s47, 917504\n\t" \
    "buffer_load_dwordx4 v[24:27], v41, s[4:7], s47 offen offset:0 sc1\n\t" \
    "v_mfma_f32_16x16x32_bf16 v[76:79], v[56:59], v[160:163], v[76:79]\n\t" \
    "ds_read_b64_tr_b16 v[12:13], v49 offset:58384\n\t" \
    "ds_read_b64_tr_b16 v[14:15], v49 offset:62512\n\t" \
    "ds_read_b64_tr_b16 v[32:33], v49 offset:58640\n\t" \
    "ds_read_b64_tr_b16 v[34:35], v49 offset:62768\n\t" \
    "v_mfma_f32_16x16x32_bf16 v[4:7], v[56:59], v[164:167], v[4:7]\n\t" \
    "s_waitcnt vmcnt(10)\n\t" \
    "v_mfma_f32_16x16x32_bf16 v[252:255], v[100:103], v[152:155], v[252:255]\n\t" \
    "buffer_load_dwordx4 v[208:211], v41, s[4:7], s47 offen offset:1024 sc1\n\t" \
    "v_mfma_f32_16x16x32_bf16 v[168:171], v[100:103], v[144:147], v[168:171]\n\t" \
    "s_waitcnt lgkmcnt(6)\n\t" \
    "v_mfma_f32_16x16x32_bf16 v[204:207], v[172:175], v[200:203], 0\n\t" \
    "s_waitcnt lgkmcnt(4)\n\t" \
    "v_mfma_f32_16x16x32_bf16 v[68:71], v[16:19], v[200:203], 0\n\t" \
    "v_mfma_f32_16x16x32_bf16 v[64:67], v[100:103], v[164:167], v[64:67]\n\t" \
    "s_waitcnt lgkmcnt(2)\n\t" \
    "v_mfma_f32_16x16x32_bf16 v[104:107], v[12:15], v[200:203], 0\n\t" \
    "s_waitcnt lgkmcnt(0)\n\t" \
    "v_mfma_f32_16x16x32_bf16 v[20:23], v[32:35], v[200:203], 0\n\t" \
    "v_mfma_f32_16x16x32_bf16 v[4:7], v[100:103], v[160:163], v[4:7]\n\t" \
    "s_cmp_lg_u32 s24, 4\n\t" \
    "s_cbranch_scc1 .Lph_%=\n\t" \
    "s_waitcnt lgkmcnt(0)\n\t" \
    "v_mov_b32 v212, s16\n\t" \
    "v_mov_b32 v213, s16\n\t" \
    "v_mov_b32 v214, s16\n\t" \
    "v_mov_b32 v215, s16\n\t" \
    "buffer_store_dwordx4 v[212:215], v2, s[4:7], s32 offen sc1\n\t" \
    "s_nop 0\n\t" \
    ".Lph_%=:\n\t" \
    "buffer_load_dwordx4 v[180:183], v46, s[4:7], s45 offen offset:0 sc1\n\t" \
    "s_waitcnt vmcnt(11)\n\t" \
    "v_mfma_f32_16x16x32_bf16 v[140:143], v[108:111], v[216:219], v[140:143]\n\t" \
    "v_bfi_b32 v204, s41, v9, v204\n\t" \
    "v_bfi_b32 v68, s41, v9, v68\n\t" \
    "v_cvt_pk_bf16_f32 v149, v206, v207\n\t" \
    "v_mfma_f32_16x16x32_bf16 v[168:171], v[108:111], v[88:91], v[168:171]\n\t" \
    "v_cvt_pk_bf16_f32 v151, v70, v71\n\t" \
    "v_cvt_pk_bf16_f32 v148, v204, v205\n\t" \
    "v_cvt_pk_bf16_f32 v150, v68, v69\n\t" \
    "v_mfma_f32_16x16x32_bf16 v[76:79], v[108:111], v[52:55], v[76:79]\n\t" \
    "v_bfi_b32 v104, s41, v9, v104\n\t" \
    "v_bfi_b32 v20, s41, v9, v20\n\t" \
    "buffer_store_dwordx4 v[148:151], v42, s[4:7], s48 offen offset:0 sc1\n\t" \
    "v_mfma_f32_16x16x32_bf16 v[4:7], v[108:111], v[80:83], v[4:7]\n\t" \
    "v_cvt_pk_bf16_f32 v57, v106, v107\n\t" \
    "v_cvt_pk_bf16_f32 v59, v22, v23\n\t" \
    "v_cvt_pk_bf16_f32 v56, v104, v105\n\t" \
    "s_waitcnt vmcnt(11)\n\t" \
    "v_mfma_f32_16x16x32_bf16 v[252:255], v[192:195], v[88:91], v[252:255]\n\t" \
    "v_cvt_pk_bf16_f32 v58, v20, v21\n\t" \
    "buffer_store_dwordx4 v[56:59], v8, s[4:7], s48 offen offset:128 sc1\n\t" \
    "v_mfma_f32_16x16x32_bf16 v[168:171], v[192:195], v[216:219], v[168:171]\n\t" \
    "v_mfma_f32_16x16x32_bf16 v[64:67], v[192:195], v[80:83], v[64:67]\n\t" \
    "v_mfma_f32_16x16x32_bf16 v[4:7], v[192:195], v[52:55], v[4:7]\n\t" \
    "s_waitcnt vmcnt(11)\n\t" \
    "v_mfma_f32_16x16x32_bf16 v[172:175], v[196:199], v[120:123], 0\n\t" \
    "v_mfma_f32_16x16x32_bf16 v[16:19], v[196:199], v[116:119], 0\n\t" \
    "v_mfma_f32_16x16x32_bf16 v[12:15], v[196:199], v[128:131], 0\n\t" \
    "v_sub_f32 v140, v140, v252\n\t" \
    "v_sub_f32 v141, v141, v253\n\t" \
    "v_mfma_f32_16x16x32_bf16 v[32:35], v[196:199], v[132:135], 0\n\t" \
    "v_sub_f32 v142, v142, v254\n\t" \
    "v_sub_f32 v143, v143, v255\n\t" \
    "s_waitcnt vmcnt(10)\n\t" \
    "v_mfma_f32_16x16x32_bf16 v[172:175], v[220:223], v[176:179], v[172:175]\n\t" \
    "v_sub_f32 v76, v76, v64\n\t" \
    "v_sub_f32 v77, v77, v65\n\t" \
    "v_mfma_f32_16x16x32_bf16 v[16:19], v[220:223], v[184:187], v[16:19]\n\t" \
    "v_sub_f32 v78, v78, v66\n\t" \
    "v_sub_f32 v79, v79, v67\n\t" \
    "v_mfma_f32_16x16x32_bf16 v[12:15], v[220:223], v[188:191], v[12:15]\n\t" \
    "v_cvt_pk_bf16_f32 v102, v168, v169\n\t" \
    "v_cvt_pk_bf16_f32 v103, v170, v171\n\t" \
    "buffer_load_dwordx4 v[212:215], v46, s[4:7], s46 offen offset:0 sc1\n\t" \
    "v_mfma_f32_16x16x32_bf16 v[32:35], v[220:223], v[60:63], v[32:35]\n\t" \
    "v_cvt_pk_bf16_f32 v100, v140, v141\n\t" \
    "v_cvt_pk_bf16_f32 v101, v142, v143\n\t" \
    "s_waitcnt vmcnt(10)\n\t" \
    "v_mfma_f32_16x16x32_bf16 v[172:175], v[124:127], v[28:31], v[172:175]\n\t" \
    "v_cvt_pk_bf16_f32 v206, v4, v5\n\t" \
    "v_cvt_pk_bf16_f32 v207, v6, v7\n\t" \
    "v_mfma_f32_16x16x32_bf16 v[16:19], v[124:127], v[224:227], v[16:19]\n\t" \
    "ds_write_b128 v40, v[100:103] offset:0\n\t" \
    "v_cvt_pk_bf16_f32 v204, v76, v77\n\t" \
    "buffer_load_dwordx4 v[68:71], v46, s[4:7], s47 offen offset:0 sc1\n\t" \
    "v_mfma_f32_16x16x32_bf16 v[12:15], v[124:127], v[228:231], v[12:15]\n\t" \
    "v_cvt_pk_bf16_f32 v205, v78, v79\n\t" \
    "ds_write_b128 v40, v[204:207] offset:1024\n\t" \
    "v_mfma_f32_16x16x32_bf16 v[32:35], v[124:127], v[236:239], v[32:35]\n\t" \
    "s_waitcnt vmcnt(11)\n\t" \
    "s_cmp_ge_u32 s24, 4\n\t" \
    "s_cbranch_scc1 .Lk54_%=\n\t" \
    "v_mfma_f32_16x16x32_bf16 v[148:151], v[232:235], v[116:119], 0\n\t" \
    "v_mfma_f32_16x16x32_bf16 v[148:151], v[240:243], v[184:187], v[148:151]\n\t" \
    "v_mfma_f32_16x16x32_bf16 v[148:151], v[244:247], v[224:227], v[148:151]\n\t" \
    "v_mfma_f32_16x16x32_bf16 v[108:111], v[232:235], v[132:135], 0\n\t" \
    "v_mfma_f32_16x16x32_bf16 v[108:111], v[240:243], v[60:63], v[108:111]\n\t" \
    "v_mfma_f32_16x16x32_bf16 v[108:111], v[244:247], v[236:239], v[108:111]\n\t" \
    "v_mfma_f32_16x16x32_bf16 v[16:19], v[232:235], v[120:123], v[16:19]\n\t" \
    "v_mfma_f32_16x16x32_bf16 v[16:19], v[240:243], v[176:179], v[16:19]\n\t" \
    "v_mfma_f32_16x16x32_bf16 v[16:19], v[244:247], v[28:31], v[16:19]\n\t" \
    "v_mfma_f32_16x16x32_bf16 v[32:35], v[232:235], v[128:131], v[32:35]\n\t" \
    "s_nop 0\n\t" \
    "v_sub_f32 v172, v172, v148\n\t" \
    "v_sub_f32 v173, v173, v149\n\t" \
    "v_mfma_f32_16x16x32_bf16 v[32:35], v[240:243], v[188:191], v[32:35]\n\t" \
    "v_sub_f32 v174, v174, v150\n\t" \
    "v_sub_f32 v175, v175, v151\n\t" \
    "v_mfma_f32_16x16x32_bf16 v[32:35], v[244:247], v[228:231], v[32:35]\n\t" \
    "v_sub_f32 v12, v12, v108\n\t" \
    "v_sub_f32 v13, v13, v109\n\t" \
    "v_sub_f32 v14, v14, v110\n\t" \
    "v_sub_f32 v15, v15, v111\n\t" \
    ".Lk54_%=:\n\t" \
    "s_waitcnt vmcnt(10)\n\t" \
    "v_mfma_f32_16x16x32_bf16 v[104:107], v[248:251], v[92:95], 0\n\t" \
    "s_bitcmp1_b32 s24, 2\n\t" \
    "s_cbranch_scc1 .Ly35_%=\n\t" \
    "buffer_load_dwordx4 v[20:23], v46, s[4:7], s45 offen offset:1024 sc1\n\t" \
    ".Ly35_%=:\n\t" \
    "v_mfma_f32_16x16x32_bf16 v[56:59], v[248:251], v[112:115], 0\n\t" \
    "v_cvt_pk_bf16_f32 v194, v16, v17\n\t" \
    "v_cvt_pk_bf16_f32 v195, v18, v19\n\t" \
    "v_mfma_f32_16x16x32_bf16 v[196:199], v[248:251], v[84:87], 0\n\t" \
    "v_cvt_pk_bf16_f32 v192, v172, v173\n\t" \
    "v_cvt_pk_bf16_f32 v193, v174, v175\n\t" \
    "v_mfma_f32_16x16x32_bf16 v[252:255], v[248:251], v[96:99], 0\n\t" \
    "v_cvt_pk_bf16_f32 v66, v32, v33\n\t" \
    "v_cvt_pk_bf16_f32 v67, v34, v35\n\t" \
    "s_bitcmp1_b32 s24, 2\n\t" \
    "s_cbranch_scc1 .Ly36_%=\n\t" \
    "buffer_load_dwordx4 v[168:171], v46, s[4:7], s46 offen offset:1024 sc1\n\t" \
    ".Ly36_%=:\n\t" \
    "s_waitcnt vmcnt(9)\n\t" \
    "v_mfma_f32_16x16x32_bf16 v[220:223], v[156:159], v[112:115], 0\n\t" \
    "ds_write_b128 v40, v[192:195] offset:2064\n\t" \
    "v_cvt_pk_bf16_f32 v64, v12, v13\n\t" \
    "v_mfma_f32_16x16x32_bf16 v[56:59], v[156:159], v[92:95], v[56:59]\n\t" \
    "v_cvt_pk_bf16_f32 v65, v14, v15\n\t" \
    "v_mfma_f32_16x16x32_bf16 v[140:143], v[156:159], v[96:99], 0\n\t" \
    "ds_write_b128 v40, v[64:67] offset:3088\n\t" \
    "s_bitcmp1_b32 s24, 2\n\t" \
    "s_cbranch_scc1 .Ly37_%=\n\t" \
    "buffer_load_dwordx4 v[4:7], v46, s[4:7], s47 offen offset:1024 sc1\n\t" \
    ".Ly37_%=:\n\t" \
    "v_mfma_f32_16x16x32_bf16 v[252:255], v[156:159], v[84:87], v[252:255]\n\t" \
    "s_waitcnt vmcnt(8)\n\t" \
    "v_mfma_f32_16x16x32_bf16 v[104:107], v[72:75], v[144:147], v[104:107]\n\t" \
    "v_mfma_f32_16x16x32_bf16 v[56:59], v[72:75], v[152:155], v[56:59]\n\t" \
    "s_waitcnt lgkmcnt(0)\n\t" \
    "s_barrier\n\t" \
    "s_memrealtime s[18:19]\n\t" \
    "s_setprio 2\n\t" \
    "ds_read_b64_tr_b16 v[100:101], v49 offset:0\n\t" \
    "ds_read_b64_tr_b16 v[102:103], v49 offset:4128\n\t" \
    "ds_read_b64_tr_b16 v[76:77], v49 offset:256\n\t" \
    "ds_read_b64_tr_b16 v[78:79], v49 offset:4384\n\t" \
    "v_mfma_f32_16x16x32_bf16 v[196:199], v[72:75], v[160:163], v[196:199]\n\t" \
    "ds_read_b64_tr_b16 v[204:205], v49 offset:16\n\t" \
    "ds_read_b64_tr_b16 v[206:207], v49 offset:4144\n\t" \
    "ds_read_b64_tr_b16 v[124:125], v49 offset:272\n\t" \
    "ds_read_b64_tr_b16 v[126:127], v49 offset:4400\n\t" \
    "v_mfma_f32_16x16x32_bf16 v[252:255], v[72:75], v[164:167], v[252:255]\n\t" \
    "s_waitcnt vmcnt(7)\n\t" \
    "v_mfma_f32_16x16x32_bf16 v[220:223], v[136:139], v[152:155], v[220:223]\n\t" \
    "v_mfma_f32_16x16x32_bf16 v[56:59], v[136:139], v[144:147], v[56:59]\n\t" \
    "s_waitcnt lgkmcnt(6)\n\t" \
    "v_mfma_f32_16x16x32_bf16 v[232:235], v[100:103], v[200:203], 0\n\t" \
    "s_waitcnt lgkmcnt(4)\n\t" \
    "v_mfma_f32_16x16x32_bf16 v[240:243], v[76:79], v[200:203], 0\n\t" \
    "v_mfma_f32_16x16x32_bf16 v[140:143], v[136:139], v[164:167], v[140:143]\n\t" \
    "s_waitcnt lgkmcnt(2)\n\t" \
    "v_mfma_f32_16x16x32_bf16 v[148:151], v[204:207], v[200:203], 0\n\t" \
    "s_waitcnt lgkmcnt(0)\n\t" \
    "v_mfma_f32_16x16x32_bf16 v[244:247], v[124:127], v[200:203], 0\n\t" \
    "v_mfma_f32_16x16x32_bf16 v[252:255], v[136:139], v[160:163], v[252:255]\n\t" \
    "s_waitcnt vmcnt(6)\n\t" \
    "v_mfma_f32_16x16x32_bf16 v[104:107], v[24:27], v[216:219], v[104:107]\n\t" \
    "v_bfi_b32 v232, s41, v9, v232\n\t" \
    "v_bfi_b32 v240, s41, v9, v240\n\t" \
    "v_cvt_pk_bf16_f32 v109, v234, v235\n\t" \
    "v_mfma_f32_16x16x32_bf16 v[56:59], v[24:27], v[88:91], v[56:59]\n\t" \
    "v_cvt_pk_bf16_f32 v111, v242, v243\n\t" \
    "v_cvt_pk_bf16_f32 v108, v232, v233\n\t" \
    "v_cvt_pk_bf16_f32 v110, v240, v241\n\t" \
    "v_mfma_f32_16x16x32_bf16 v[196:199], v[24:27], v[52:55], v[196:199]\n\t" \
    "v_bfi_b32 v148, s41, v9, v148\n\t" \
    "v_bfi_b32 v244, s41, v9, v244\n\t" \
    "buffer_store_dwordx4 v[108:111], v42, s[4:7], s49 offen offset:0 sc1\n\t" \
    "s_add_u32 s37, s52, s33\n\t" \
    "s_buffer_load_dword s58, s[4:7], s37 offset:0x0\n\t" \
    "s_buffer_load_dword s59, s[4:7], s37 offset:0x400\n\t" \
    "s_buffer_load_dword s60, s[4:7], s37 offset:0x8000\n\t" \
    "s_buffer_load_dword s61, s[4:7], s37 offset:0x8400\n\t" \
    "v_mfma_f32_16x16x32_bf16 v[252:255], v[24:27], v[80:83], v[252:255]\n\t" \
    "v_cvt_pk_bf16_f32 v17, v150, v151\n\t" \
    "v_cvt_pk_bf16_f32 v19, v246, v247\n\t" \
    "v_cvt_pk_bf16_f32 v16, v148, v149\n\t" \
    "s_waitcnt vmcnt(6)\n\t" \
    "v_mfma_f32_16x16x32_bf16 v[220:223], v[208:211], v[88:91], v[220:223]\n\t" \
    "v_cvt_pk_bf16_f32 v18, v244, v245\n\t" \
    "buffer_store_dwordx4 v[16:19], v8, s[4:7], s49 offen offset:128 sc1\n\t" \
    "s_setprio 0\n\t" \
    "v_add_u32 v40, 91392, v40\n\t" \
    "ds_read_b128 v[172:175], v10 offset:3072\n\t" \
    "ds_read_b128 v[248:251], v10 offset:4096\n\t" \
    "v_mfma_f32_16x16x32_bf16 v[56:59], v[208:211], v[216:219], v[56:59]\n\t" \
    "s_cmp_lt_u32 s24, 4\n\t" \
    "s_cbranch_scc1 .Lp55_%=\n\t" \
    "s_setprio 1\n\t" \
    ".Lp55_%=:\n\t" \
    "v_mfma_f32_16x16x32_bf16 v[140:143], v[208:211], v[80:83], v[140:143]\n\t" \
    "v_mfma_f32_16x16x32_bf16 v[252:255], v[208:211], v[52:55], v[252:255]\n\t" \
    "s_waitcnt vmcnt(6)\n\t" \
    "v_mfma_f32_16x16x32_bf16 v[32:35], v[180:183], v[120:123], 0\n\t" \
    "v_mfma_f32_16x16x32_bf16 v[112:115], v[180:183], v[116:119], 0\n\t" \
    "v_mfma_f32_16x16x32_bf16 v[192:195], v[180:183], v[128:131], 0\n\t" \
    "v_sub_f32 v104, v104, v220\n\t" \
    "v_sub_f32 v105, v105, v221\n\t" \
    "v_mfma_f32_16x16x32_bf16 v[92:95], v[180:183], v[132:135], 0\n\t" \
    "v_sub_f32 v106, v106, v222\n\t" \
    "v_sub_f32 v107, v107, v223\n\t" \
    "s_waitcnt vmcnt(3)\n\t" \
    "v_mfma_f32_16x16x32_bf16 v[32:35], v[212:215], v[176:179], v[32:35]\n\t" \
    "v_sub_f32 v196, v196, v140\n\t" \
    "v_sub_f32 v197, v197, v141\n\t" \
    "v_mfma_f32_16x16x32_bf16 v[112:115], v[212:215], v[184:187], v[112:115]\n\t" \
    "v_sub_f32 v198, v198, v142\n\t" \
    "v_sub_f32 v199, v199, v143\n\t" \
    "v_mfma_f32_16x16x32_bf16 v[192:195], v[212:215], v[188:191], v[192:195]\n\t" \
    "v_cvt_pk_bf16_f32 v14, v56, v57\n\t" \
    "v_cvt_pk_bf16_f32 v15, v58, v59\n\t" \
    "v_mfma_f32_16x16x32_bf16 v[92:95], v[212:215], v[60:63], v[92:95]\n\t" \
    "v_cvt_pk_bf16_f32 v12, v104, v105\n\t" \
    "v_cvt_pk_bf16_f32 v13, v106, v107\n\t" \
    "s_waitcnt vmcnt(2)\n\t" \
    "v_mfma_f32_16x16x32_bf16 v[32:35], v[68:71], v[28:31], v[32:35]\n\t" \
    "v_cvt_pk_bf16_f32 v98, v252, v253\n\t" \
    "v_cvt_pk_bf16_f32 v99, v254, v255\n\t" \
    "v_mfma_f32_16x16x32_bf16 v[112:115], v[68:71], v[224:227], v[112:115]\n\t" \
    "ds_write_b128 v40, v[12:15] offset:0\n\t" \
    "v_cvt_pk_bf16_f32 v96, v196, v197\n\t" \
    "s_add_u32 s54, s53, 0\n\t" \
    "buffer_load_dwordx4 v[64:67], v0, s[4:7], s54 offen offset:0 sc1\n\t" \
    "s_add_u32 s54, s53, 4243456\n\t" \
    "buffer_load_dwordx4 v[44:47], v0, s[4:7], s54 offen offset:0 sc1\n\t" \
    "v_mfma_f32_16x16x32_bf16 v[192:195], v[68:71], v[228:231], v[192:195]\n\t" \
    "v_cvt_pk_bf16_f32 v97, v198, v199\n\t" \
    "ds_write_b128 v40, v[96:99] offset:1024\n\t" \
    "v_mfma_f32_16x16x32_bf16 v[92:95], v[68:71], v[236:239], v[92:95]\n\t" \
    "s_waitcnt vmcnt(4)\n\t" \
    "s_cmp_ge_u32 s24, 4\n\t" \
    "s_cbranch_scc1 .Lk56_%=\n\t" \
    "v_mfma_f32_16x16x32_bf16 v[84:87], v[20:23], v[116:119], 0\n\t" \
    "v_mfma_f32_16x16x32_bf16 v[84:87], v[168:171], v[184:187], v[84:87]\n\t" \
    "v_mfma_f32_16x16x32_bf16 v[84:87], v[4:7], v[224:227], v[84:87]\n\t" \
    "v_mfma_f32_16x16x32_bf16 v[156:159], v[20:23], v[132:135], 0\n\t" \
    "v_mfma_f32_16x16x32_bf16 v[156:159], v[168:171], v[60:63], v[156:159]\n\t" \
    "v_mfma_f32_16x16x32_bf16 v[156:159], v[4:7], v[236:239], v[156:159]\n\t" \
    "v_mfma_f32_16x16x32_bf16 v[112:115], v[20:23], v[120:123], v[112:115]\n\t" \
    "v_mfma_f32_16x16x32_bf16 v[112:115], v[168:171], v[176:179], v[112:115]\n\t" \
    "v_mfma_f32_16x16x32_bf16 v[112:115], v[4:7], v[28:31], v[112:115]\n\t" \
    "v_mfma_f32_16x16x32_bf16 v[92:95], v[20:23], v[128:131], v[92:95]\n\t" \
    "s_nop 0\n\t" \
    "v_sub_f32 v32, v32, v84\n\t" \
    "v_sub_f32 v33, v33, v85\n\t" \
    "v_mfma_f32_16x16x32_bf16 v[92:95], v[168:171], v[188:191], v[92:95]\n\t" \
    "v_sub_f32 v34, v34, v86\n\t" \
    "v_sub_f32 v35, v35, v87\n\t" \
    "v_mfma_f32_16x16x32_bf16 v[92:95], v[4:7], v[228:231], v[92:95]\n\t" \
    "v_sub_f32 v192, v192, v156\n\t" \
    "v_sub_f32 v193, v193, v157\n\t" \
    "v_sub_f32 v194, v194, v158\n\t" \
    "v_sub_f32 v195, v195, v159\n\t" \
    ".Lk56_%=:\n\t" \
    "v_cvt_pk_bf16_f32 v74, v112, v113\n\t" \
    "v_cvt_pk_bf16_f32 v75, v114, v115\n\t" \
    "v_cvt_pk_bf16_f32 v72, v32, v33\n\t" \
    "v_cvt_pk_bf16_f32 v73, v34, v35\n\t" \
    "s_nop 1\n\t" \
    "v_cvt_pk_bf16_f32 v154, v92, v93\n\t" \
    "v_cvt_pk_bf16_f32 v155, v94, v95\n\t" \
    "ds_write_b128 v40, v[72:75] offset:2064\n\t" \
    "v_cvt_pk_bf16_f32 v152, v192, v193\n\t" \
    "v_cvt_pk_bf16_f32 v153, v194, v195\n\t" \
    "v_add_u32 v49, 91392, v49\n\t" \
    "ds_write_b128 v40, v[152:155] offset:3088\n\t" \
    "s_waitcnt lgkmcnt(0)\n\t" \
    "s_barrier\n\t" \
    "ds_read_b64_tr_b16 v[144:145], v49 offset:0\n\t" \
    "ds_read_b64_tr_b16 v[146:147], v49 offset:4128\n\t" \
    "ds_read_b64_tr_b16 v[100:101], v49 offset:256\n\t" \
    "ds_read_b64_tr_b16 v[102:103], v49 offset:4384\n\t" \
    "ds_read_b64_tr_b16 v[76:77], v49 offset:16\n\t" \
    "ds_read_b64_tr_b16 v[78:79], v49 offset:4144\n\t" \
    "ds_read_b64_tr_b16 v[164:165], v49 offset:272\n\t" \
    "ds_read_b64_tr_b16 v[166:167], v49 offset:4400\n\t" \
    "s_memrealtime s[56:57]\n\t" \
    "s_waitcnt lgkmcnt(6)\n\t" \
    "v_mfma_f32_16x16x32_bf16 v[204:207], v[144:147], v[200:203], 0\n\t" \
    "s_waitcnt lgkmcnt(4)\n\t" \
    "v_mfma_f32_16x16x32_bf16 v[124:127], v[100:103], v[200:203], 0\n\t" \
    "s_waitcnt lgkmcnt(2)\n\t" \
    "v_mfma_f32_16x16x32_bf16 v[136:139], v[76:79], v[200:203], 0\n\t" \
    "s_waitcnt lgkmcnt(0)\n\t" \
    "v_mfma_f32_16x16x32_bf16 v[160:163], v[164:167], v[200:203], 0\n\t" \
    "s_nop 1\n\t" \
    "v_bfi_b32 v204, s41, v9, v204\n\t" \
    "s_nop 0\n\t" \
    "v_bfi_b32 v124, s41, v9, v124\n\t" \
    "v_cvt_pk_bf16_f32 v233, v206, v207\n\t" \
    "v_cvt_pk_bf16_f32 v235, v126, v127\n\t" \
    "v_cvt_pk_bf16_f32 v232, v204, v205\n\t" \
    "v_cvt_pk_bf16_f32 v234, v124, v125\n\t" \
    "v_bfi_b32 v136, s41, v9, v136\n\t" \
    "v_bfi_b32 v160, s41, v9, v160\n\t" \
    "buffer_store_dwordx4 v[232:235], v42, s[4:7], s50 offen offset:0 sc1\n\t" \
    "v_cvt_pk_bf16_f32 v241, v138, v139\n\t" \
    "v_cvt_pk_bf16_f32 v243, v162, v163\n\t" \
    "v_cvt_pk_bf16_f32 v240, v136, v137\n\t" \
    "v_cvt_pk_bf16_f32 v242, v160, v161\n\t" \
    "buffer_store_dwordx4 v[240:243], v8, s[4:7], s50 offen offset:128 sc1\n\t" \
    "s_cmp_ge_u32 s25, 126\n\t" \
    "s_cbranch_scc1 .Lend_%=\n\t" \
    "s_waitcnt lgkmcnt(0)\n\t" \
    "s_sub_u32 s37, s16, 300\n\t" \
    "s_mov_b32 s62, 300\n\t" \
    "s_sub_u32 s39, s58, s37\n\t" \
    "s_cmp_gt_u32 s39, 600\n\t" \
    "s_cselect_b32 s39, 400, s39\n\t" \
    "s_max_u32 s62, s62, s39\n\t" \
    "s_sub_u32 s39, s59, s37\n\t" \
    "s_cmp_gt_u32 s39, 600\n\t" \
    "s_cselect_b32 s39, 400, s39\n\t" \
    "s_max_u32 s62, s62, s39\n\t" \
    "s_sub_u32 s39, s60, s37\n\t" \
    "s_cmp_gt_u32 s39, 600\n\t" \
    "s_cselect_b32 s39, 400, s39\n\t" \
    "s_max_u32 s62, s62, s39\n\t" \
    "s_sub_u32 s39, s61, s37\n\t" \
    "s_cmp_gt_u32 s39, 600\n\t" \
    "s_cselect_b32 s39, 400, s39\n\t" \
    "s_max_u32 s62, s62, s39\n\t" \
    "s_sub_u32 s62, s62, 300\n\t" \
    "s_mul_i32 s62, s62, 5\n\t" \
    "s_lshr_b32 s62, s62, 2\n\t" \
    "s_waitcnt vmcnt(2)\n\t" \
    "v_and_b32 v36, v64, v66\n\t" \
    "v_bitop3_b32 v36, v36, v44, v46 bitop3:0x80\n\t" \
    "v_and_b32 v36, 1, v36\n\t" \
    "v_cmp_eq_u32 vcc, 0, v36\n\t" \
    "s_nop 1\n\t" \
    "s_cbranch_vccnz .Ls50_%=\n\t" \
    ".Lb50_%=:\n\t" \
    "ds_write_b128 v48, v[64:67] offset:33024\n\t" \
    "ds_write_b128 v48, v[44:47] offset:33152\n\t" \
    "s_add_u32 s37, s62, 125\n\t" \
    ".Lh57_%=:\n\t" \
    "s_memrealtime s[22:23]\n\t" \
    "s_waitcnt lgkmcnt(0)\n\t" \
    "s_sub_u32 s22, s22, s18\n\t" \
    "s_cmp_lt_u32 s22, s37\n\t" \
    "s_cbranch_scc1 .Lh57_%=\n\t" \
    "s_add_u32 s54, s53, 524288\n\t" \
    "buffer_load_dwordx4 v[108:111], v0, s[4:7], s54 offen offset:0 sc1\n\t" \
    "s_add_u32 s54, s53, 4767744\n\t" \
    "buffer_load_dwordx4 v[24:27], v0, s[4:7], s54 offen offset:0 sc1\n\t" \
    "s_waitcnt lgkmcnt(0)\n\t" \
    "s_barrier\n\t" \
    "ds_read_b64_tr_b16 v[148:149], v43 offset:33024\n\t" \
    "ds_read_b64_tr_b16 v[150:151], v43 offset:33280\n\t" \
    "ds_read_b64_tr_b16 v[88:89], v43 offset:33032\n\t" \
    "ds_read_b64_tr_b16 v[90:91], v43 offset:33288\n\t" \
    "s_waitcnt lgkmcnt(2)\n\t" \
    "v_mfma_f32_16x16x32_bf16 v[244:247], v[148:151], v[172:175], 0\n\t" \
    "s_waitcnt lgkmcnt(0)\n\t" \
    "v_mfma_f32_16x16x32_bf16 v[16:19], v[88:91], v[172:175], 0\n\t" \
    "v_mfma_f32_16x16x32_bf16 v[216:219], v[148:151], v[248:251], 0\n\t" \
    "v_mfma_f32_16x16x32_bf16 v[80:83], v[88:91], v[248:251], 0\n\t" \
    "s_nop 3\n\t" \
    "v_cvt_pk_bf16_f32 v52, v244, v245\n\t" \
    "v_cvt_pk_bf16_f32 v53, v246, v247\n\t" \
    "v_cvt_pk_bf16_f32 v54, v16, v17\n\t" \
    "v_cvt_pk_bf16_f32 v55, v18, v19\n\t" \
    "buffer_store_dwordx4 v[52:55], v38, s[8:11], 0 offen offset:0 sc1\n\t" \
    "v_cvt_pk_bf16_f32 v208, v216, v217\n\t" \
    "v_cvt_pk_bf16_f32 v209, v218, v219\n\t" \
    "v_cvt_pk_bf16_f32 v210, v80, v81\n\t" \
    "v_cvt_pk_bf16_f32 v211, v82, v83\n\t" \
    "buffer_store_dwordx4 v[208:211], v38, s[8:11], 0 offen offset:3072 sc1\n\t" \
    "s_waitcnt vmcnt(2)\n\t" \
    "v_and_b32 v51, v108, v110\n\t" \
    "v_bitop3_b32 v51, v51, v24, v26 bitop3:0x80\n\t" \
    "v_and_b32 v51, 1, v51\n\t" \
    "v_cmp_eq_u32 vcc, 0, v51\n\t" \
    "s_nop 1\n\t" \
    "s_cbranch_vccnz .Ls51_%=\n\t" \
    ".Lb51_%=:\n\t" \
    "ds_write_b128 v48, v[108:111] offset:0\n\t" \
    "ds_write_b128 v48, v[24:27] offset:128\n\t" \
    "s_add_u32 s37, s62, 100\n\t" \
    ".Lh58_%=:\n\t" \
    "s_memrealtime s[22:23]\n\t" \
    "s_waitcnt lgkmcnt(0)\n\t" \
    "s_sub_u32 s22, s22, s56\n\t" \
    "s_cmp_lt_u32 s22, s37\n\t" \
    "s_cbranch_scc1 .Lh58_%=\n\t" \
    "s_add_u32 s54, s53, 1048576\n\t" \
    "buffer_load_dwordx4 v[180:183], v0, s[4:7], s54 offen offset:0 sc1\n\t" \
    "s_add_u32 s54, s53, 5292032\n\t" \
    "buffer_load_dwordx4 v[220:223], v0, s[4:7], s54 offen offset:0 sc1\n\t" \
    "s_waitcnt lgkmcnt(0)\n\t" \
    "s_barrier\n\t" \
    "ds_read_b64_tr_b16 v[140:141], v43 offset:0\n\t" \
    "ds_read_b64_tr_b16 v[142:143], v43 offset:256\n\t" \
    "ds_read_b64_tr_b16 v[56:57], v43 offset:8\n\t" \
    "ds_read_b64_tr_b16 v[58:59], v43 offset:264\n\t" \
    "s_waitcnt lgkmcnt(2)\n\t" \
    "v_mfma_f32_16x16x32_bf16 v[212:215], v[140:143], v[172:175], 0\n\t" \
    "s_waitcnt lgkmcnt(0)\n\t" \
    "v_mfma_f32_16x16x32_bf16 v[104:107], v[56:59], v[172:175], 0\n\t" \
    "v_mfma_f32_16x16x32_bf16 v[252:255], v[140:143], v[248:251], 0\n\t" \
    "v_mfma_f32_16x16x32_bf16 v[12:15], v[56:59], v[248:251], 0\n\t" \
    "s_nop 3\n\t" \
    "v_cvt_pk_bf16_f32 v196, v212, v213\n\t" \
    "v_cvt_pk_bf16_f32 v197, v214, v215\n\t" \
    "v_cvt_pk_bf16_f32 v198, v104, v105\n\t" \
    "v_cvt_pk_bf16_f32 v199, v106, v107\n\t" \
    "buffer_store_dwordx4 v[196:199], v38, s[8:11], 0 offen offset:64 sc1\n\t" \
    "v_cvt_pk_bf16_f32 v96, v252, v253\n\t" \
    "v_cvt_pk_bf16_f32 v97, v254, v255\n\t" \
    "v_cvt_pk_bf16_f32 v98, v12, v13\n\t" \
    "v_cvt_pk_bf16_f32 v99, v14, v15\n\t" \
    "buffer_store_dwordx4 v[96:99], v38, s[8:11], 0 offen offset:3136 sc1\n\t" \
    "s_waitcnt vmcnt(2)\n\t" \
    "v_and_b32 v37, v180, v182\n\t" \
    "v_bitop3_b32 v37, v37, v220, v222 bitop3:0x80\n\t" \
    "v_and_b32 v37, 1, v37\n\t" \
    "v_cmp_eq_u32 vcc, 0, v37\n\t" \
    "s_nop 1\n\t" \
    "s_cbranch_vccnz .Ls52_%=\n\t" \
    ".Lb52_%=:\n\t" \
    "ds_write_b128 v48, v[180:183] offset:16512\n\t" \
    "ds_write_b128 v48, v[220:223] offset:16640\n\t" \
    "s_waitcnt lgkmcnt(0)\n\t" \
    "s_barrier\n\t" \
    "ds_read_b64_tr_b16 v[68:69], v43 offset:16512\n\t" \
    "ds_read_b64_tr_b16 v[70:71], v43 offset:16768\n\t" \
    "ds_read_b64_tr_b16 v[116:117], v43 offset:16520\n\t" \
    "ds_read_b64_tr_b16 v[118:119], v43 offset:16776\n\t" \
    "s_waitcnt lgkmcnt(2)\n\t" \
    "v_mfma_f32_16x16x32_bf16 v[184:187], v[68:71], v[172:175], 0\n\t" \
    "s_waitcnt lgkmcnt(0)\n\t" \
    "v_mfma_f32_16x16x32_bf16 v[224:227], v[116:119], v[172:175], 0\n\t" \
    "v_mfma_f32_16x16x32_bf16 v[132:135], v[68:71], v[248:251], 0\n\t" \
    "v_mfma_f32_16x16x32_bf16 v[60:63], v[116:119], v[248:251], 0\n\t" \
    "s_nop 3\n\t" \
    "v_cvt_pk_bf16_f32 v236, v184, v185\n\t" \
    "v_cvt_pk_bf16_f32 v237, v186, v187\n\t" \
    "v_cvt_pk_bf16_f32 v238, v224, v225\n\t" \
    "v_cvt_pk_bf16_f32 v239, v226, v227\n\t" \
    "buffer_store_dwordx4 v[236:239], v38, s[8:11], 0 offen offset:128 sc1\n\t" \
    "v_cvt_pk_bf16_f32 v120, v132, v133\n\t" \
    "v_cvt_pk_bf16_f32 v121, v134, v135\n\t" \
    "v_cvt_pk_bf16_f32 v122, v60, v61\n\t" \
    "v_cvt_pk_bf16_f32 v123, v62, v63\n\t" \
    "buffer_store_dwordx4 v[120:123], v38, s[8:11], 0 offen offset:3200 sc1\n\t" \
    "s_or_b32 s37, s2, s24\n\t" \
    "s_cbranch_scc1 .Lend_%=\n\t" \
    "s_mov_b32 s37, 34668544\n\t" \
    "v_mov_b32 v1, s34\n\t" \
    "s_mov_b64 exec, 1\n\t" \
    "buffer_store_dword v1, v0, s[4:7], s37 offen sc1\n\t" \
    "s_mov_b64 exec, -1\n\t" \
    ".Lend_%=:\n\t" \
    "s_endpgm\n\t" \
    ".Lq1028_%=:\n\t" \
    "s_mov_b32 s40, 0\n\t" \
    ".Lr1028_%=:\n\t" \
    "s_add_u32 s12, s55, 34603008\n\t" \
    "s_buffer_load_dword s13, s[4:7], s12 glc\n\t" \
    "s_buffer_load_dword s14, s[4:7], s12 offset:0x400 glc\n\t" \
    "s_waitcnt lgkmcnt(0)\n\t" \
    "s_xor_b32 s13, s13, s34\n\t" \
    "s_xor_b32 s14, s14, s34\n\t" \
    "s_or_b32 s13, s13, s14\n\t" \
    "s_cbranch_scc0 .Lb27_%=\n\t" \
    "s_add_u32 s40, s40, 1\n\t" \
    "s_cmp_lt_u32 s40, 1\n\t" \
    "s_cbranch_scc1 .Lr1028_%=\n\t" \
    "s_branch .Ls27_%=\n\t" \
    ".Lq1025_%=:\n\t" \
    "s_mov_b32 s40, 0\n\t" \
    ".Lr1025_%=:\n\t" \
    "s_nop 0\n\t" \
    "v_readlane_b32 s12, v50, 0\n\t" \
    "v_readlane_b32 s13, v50, 1\n\t" \
    "v_readlane_b32 s14, v50, 2\n\t" \
    "v_readlane_b32 s15, v50, 3\n\t" \
    "v_readlane_b32 s16, v50, 4\n\t" \
    "v_readlane_b32 s17, v50, 5\n\t" \
    "v_readlane_b32 s18, v50, 6\n\t" \
    "v_readlane_b32 s19, v50, 7\n\t" \
    "v_readlane_b32 s3, v50, 8\n\t" \
    "s_add_u32 s12, s12, s33\n\t" \
    "s_add_u32 s13, s13, s33\n\t" \
    "s_add_u32 s14, s14, s33\n\t" \
    "s_add_u32 s15, s15, s33\n\t" \
    "s_add_u32 s16, s16, s33\n\t" \
    "s_add_u32 s17, s17, s33\n\t" \
    "s_add_u32 s18, s18, s33\n\t" \
    "s_add_u32 s19, s19, s33\n\t" \
    "s_add_u32 s3, s3, s33\n\t" \
    "s_buffer_load_dword s12, s[4:7], s12 offset:0xc0000 glc\n\t" \
    "s_buffer_load_dword s13, s[4:7], s13 offset:0xc0000 glc\n\t" \
    "s_buffer_load_dword s14, s[4:7], s14 offset:0xc0000 glc\n\t" \
    "s_buffer_load_dword s15, s[4:7], s15 offset:0xc0000 glc\n\t" \
    "s_buffer_load_dword s16, s[4:7], s16 offset:0xc0000 glc\n\t" \
    "s_buffer_load_dword s17, s[4:7], s17 offset:0xc0000 glc\n\t" \
    "s_buffer_load_dword s18, s[4:7], s18 offset:0xc0000 glc\n\t" \
    "s_buffer_load_dword s19, s[4:7], s19 offset:0xc0000 glc\n\t" \
    "s_buffer_load_dword s3, s[4:7], s3 offset:0xc0000 glc\n\t" \
    "s_waitcnt lgkmcnt(0)\n\t" \
    "s_xor_b32 s12, s12, s34\n\t" \
    "s_xor_b32 s13, s13, s34\n\t" \
    "s_xor_b32 s14, s14, s34\n\t" \
    "s_xor_b32 s15, s15, s34\n\t" \
    "s_xor_b32 s16, s16, s34\n\t" \
    "s_xor_b32 s17, s17, s34\n\t" \
    "s_xor_b32 s18, s18, s34\n\t" \
    "s_xor_b32 s19, s19, s34\n\t" \
    "s_xor_b32 s3, s3, s34\n\t" \
    "s_or_b32 s12, s12, s13\n\t" \
    "s_or_b32 s12, s12, s14\n\t" \
    "s_or_b32 s12, s12, s15\n\t" \
    "s_or_b32 s12, s12, s16\n\t" \
    "s_or_b32 s12, s12, s17\n\t" \
    "s_or_b32 s12, s12, s18\n\t" \
    "s_or_b32 s12, s12, s19\n\t" \
    "s_or_b32 s12, s12, s3\n\t" \
    "s_cbranch_scc0 .Lb24_%=\n\t" \
    "s_add_u32 s40, s40, 1\n\t" \
    "s_cmp_lt_u32 s40, 1\n\t" \
    "s_cbranch_scc1 .Lr1025_%=\n\t" \
    "s_branch .Ls24_%=\n\t" \
    ".Lqa_%=:\n\t" \
    "s_sub_u32 s37, s24, 4\n\t" \
    "s_bfe_u32 s38, s2, 0x20002\n\t" \
    "s_lshl1_add_u32 s38, s38, s37\n\t" \
    "s_lshl_b32 s42, s38, 21\n\t" \
    "s_cmp_ge_u32 s38, 7\n\t" \
    "s_cbranch_scc1 .Lqd_%=\n\t" \
    "s_cmp_ge_u32 s37, 2\n\t" \
    "s_cbranch_scc1 .Lqd_%=\n\t" \
    "s_memtime s[62:63]\n\t" \
    "s_waitcnt lgkmcnt(0)\n\t" \
    "s_sleep 4\n\t" \
    "s_lshl_b32 s3, s38, 10\n\t" \
    "s_and_b32 s37, s8, 0x1fffff\n\t" \
    "s_add_u32 s38, s37, 16383\n\t" \
    "s_andn2_b32 s38, s38, 16383\n\t" \
    "s_add_u32 s39, s37, 9617408\n\t" \
    "s_andn2_b32 s39, s39, 16383\n\t" \
    "s_max_u32 s42, s42, s38\n\t" \
    "s_min_u32 s42, s42, s39\n\t" \
    "s_sub_u32 s42, s42, s37\n\t" \
    "s_cmp_eq_u32 s2, 255\n\t" \
    "s_cselect_b64 s[16:17], s[4:5], s[8:9]\n\t" \
    "s_cselect_b32 s42, 34684928, s42\n\t" \
    "s_add_u32 s16, s16, s42\n\t" \
    "s_addc_u32 s17, s17, 0\n\t" \
    "s_mov_b32 s18, 16384\n\t" \
    "s_mov_b32 s19, 0x20000\n\t" \
    "s_add_u32 s37, s3, 0x2000\n\t" \
    "buffer_store_dwordx4 v[20:23], v0, s[16:19], s3 offen sc1\n\t" \
    "buffer_store_dwordx4 v[20:23], v0, s[16:19], s37 offen sc1\n\t" \
    "s_nop 0\n\t" \
    "s_mov_b32 s39, 34668544\n\t" \
    "v_mov_b32 v20, 0\n\t" \
    "buffer_load_dword v21, v20, s[4:7], s39 offen sc1\n\t" \
    "s_mov_b32 s60, -1\n\t" \
    "s_mov_b32 s61, -1\n\t" \
    "s_mov_b32 s40, 0\n\t" \
    "s_waitcnt vmcnt(1) lgkmcnt(0)\n\t" \
    "s_memtime s[58:59]\n\t" \
    "s_waitcnt lgkmcnt(0)\n\t" \
    ".Lqc_%=:\n\t" \
    "s_buffer_load_dword s38, s[16:19], s3\n\t" \
    "s_mov_b32 s56, s58\n\t" \
    "s_xor_b32 s3, s3, 0x2000\n\t" \
    "s_waitcnt lgkmcnt(0)\n\t" \
    "s_memtime s[58:59]\n\t" \
    "s_add_u32 s40, s40, 1\n\t" \
    "s_waitcnt lgkmcnt(0)\n\t" \
    "s_sub_u32 s56, s58, s56\n\t" \
    "s_min_u32 s56, s56, s60\n\t" \
    "s_mov_b32 s60, s61\n\t" \
    "s_mov_b32 s61, s56\n\t" \
    "s_bitcmp1_b32 s40, 0\n\t" \
    "s_cbranch_scc1 .Lqc_%=\n\t" \
    "s_add_u32 s3, s3, 0x80\n\t" \
    "s_sub_u32 s37, s58, s62\n\t" \
    "s_cmp_gt_u32 s37, 5200\n\t" \
    "s_cselect_b32 s40, 98, s40\n\t" \
    "s_absdiff_i32 s37, s60, s61\n\t" \
    "s_min_u32 s38, s60, s61\n\t" \
    "s_cmp_gt_u32 s38, 500\n\t" \
    "s_cselect_b32 s37, 0, s37\n\t" \
    "s_cmp_lt_u32 s37, 80\n\t" \
    "s_cselect_b32 s37, 16, 4\n\t" \
    "s_cmp_lt_u32 s40, s37\n\t" \
    "s_cbranch_scc1 .Lqc_%=\n\t" \
    "s_waitcnt vmcnt(0)\n\t" \
    "v_readfirstlane_b32 s34, v21\n\t" \
    "s_cmp_gt_u32 s60, s61\n\t" \
    "s_cselect_b32 s0, 2, 0\n\t" \
    "s_mul_i32 s34, s34, 1664525\n\t" \
    "s_add_u32 s34, s34, 1013904223\n\t" \
    "s_or_b32 s34, s34, 1\n\t" \
    "s_cmp_eq_u32 s2, 255\n\t" \
    "s_cbranch_scc1 .Lqf_%=\n\t" \
    "s_xor_b32 s37, s0, s34\n\t" \
    "v_mov_b32 v21, s37\n\t" \
    "s_bfe_u32 s37, s2, 0x20002\n\t" \
    "s_lshl1_add_u32 s37, s37, s24\n\t" \
    "s_lshl_b32 s37, s37, 7\n\t" \
    "s_add_u32 s37, s37, 34670080\n\t" \
    "v_lshrrev_b32 v20, 2, v0\n\t" \
    "s_mov_b32 exec_hi, 0\n\t" \
    "buffer_store_dword v21, v20, s[4:7], s37 offen sc1\n\t" \
    "s_mov_b64 exec, -1\n\t" \
    ".Lqg_%=:\n\t" \
    "s_branch .Lqd_%=\n\t" \
    ".Lqf_%=:\n\t" \
    "s_getreg_b32 s37, hwreg(HW_REG_XCC_ID, 0, 2)\n\t" \
    "s_lshl1_add_u32 s37, s0, s37\n\t" \
    "s_add_u32 s37, s37, 1\n\t" \
    "s_and_b32 s37, s37, 7\n\t" \
    "s_lshl_b32 s37, s37, 1\n\t" \
    "s_xor_b32 s37, s37, s34\n\t" \
    "v_mov_b32 v20, s37\n\t" \
    "v_mov_b32 v21, s37\n\t" \
    "v_mov_b32 v22, s37\n\t" \
    "v_mov_b32 v23, s37\n\t" \
    "s_mov_b32 s39, 34669568\n\t" \
    "buffer_store_dwordx4 v[20:23], v0, s[4:7], s39 offen sc1\n\t" \
    "s_nop 0\n\t" \
    "s_branch .Lqg_%=\n\t" \
    ".Lr2_%=:\n\t" \
    "s_add_u32 s40, s40, 1\n\t" \
    "s_cmp_gt_u32 s40, 32\n\t" \
    "s_cbranch_scc1 .Lr3_%=\n\t" \
    "s_sleep 2\n\t" \
    "s_and_b32 s37, s2, 7\n\t" \
    "s_lshl_b32 s37, s37, 10\n\t" \
    "s_add_u32 s37, s37, 34676736\n\t" \
    "v_and_b32 v51, 0x70, v0\n\t" \
    "v_lshlrev_b32 v51, 3, v51\n\t" \
    "buffer_load_dword v50, v51, s[4:7], s37 offen sc1\n\t" \
    "s_waitcnt vmcnt(0)\n\t" \
    "v_readlane_b32 s56, v50, 0\n\t" \
    "v_readlane_b32 s57, v50, 1\n\t" \
    "v_readlane_b32 s58, v50, 2\n\t" \
    "v_readlane_b32 s59, v50, 3\n\t" \
    "v_readlane_b32 s60, v50, 4\n\t" \
    "v_readlane_b32 s61, v50, 5\n\t" \
    "v_readlane_b32 s62, v50, 6\n\t" \
    "v_readlane_b32 s63, v50, 7\n\t" \
    "s_branch .Lr1_%=\n\t" \
    ".Lq5_%=:\n\t" \
    "s_add_u32 s40, s40, 1\n\t" \
    "s_cmp_gt_u32 s40, 65536\n\t" \
    "s_cbranch_scc1 .Lq6_%=\n\t" \
    "s_sleep 4\n\t" \
    "v_and_b32 v19, 0x70, v0\n\t" \
    "v_lshlrev_b32 v19, 3, v19\n\t" \
    "s_mov_b32 s37, 34670592\n\t" \
    "buffer_load_dword v17, v19, s[4:7], s37 offen sc1\n\t" \
    "s_waitcnt vmcnt(0)\n\t" \
    "v_readlane_b32 s56, v17, 0\n\t" \
    "v_readlane_b32 s57, v17, 1\n\t" \
    "v_readlane_b32 s58, v17, 2\n\t" \
    "v_readlane_b32 s59, v17, 3\n\t" \
    "v_readlane_b32 s60, v17, 4\n\t" \
    "v_readlane_b32 s61, v17, 5\n\t" \
    "s_branch .Lq4_%=\n\t" \
    ".Lr5_%=:\n\t" \
    "s_add_u32 s40, s40, 1\n\t" \
    "s_cmp_gt_u32 s40, 65536\n\t" \
    "s_cbranch_scc1 .Lr6_%=\n\t" \
    "s_sleep 4\n\t" \
    "v_mov_b32 v16, 0\n\t" \
    "buffer_load_dword v8, v16, s[4:7], s52 offen sc1\n\t" \
    "s_waitcnt vmcnt(0)\n\t" \
    "v_readfirstlane_b32 s36, v8\n\t" \
    "s_branch .Lr4_%=\n\t" \
    ".Ls24_%=:\n\t" \
    "s_mov_b32 s40, 0\n\t" \
    "s_add_u32 s38, s33, 786432\n\t" \
    ".Ll24_%=:\n\t" \
    "s_sleep 2\n\t" \
    "buffer_load_dword v1, v50, s[4:7], s38 offen sc1\n\t" \
    "s_waitcnt vmcnt(0)\n\t" \
    "v_cmp_ne_u32 vcc, s34, v1\n\t" \
    "s_add_u32 s40, s40, 1\n\t" \
    "s_cmp_ge_u32 s40, 65536\n\t" \
    "s_cbranch_scc1 .Lb24_%=\n\t" \
    "s_cbranch_vccnz .Ll24_%=\n\t" \
    "s_branch .Lb24_%=\n\t" \
    ".Ls27_%=:\n\t" \
    "s_mov_b32 s40, 0\n\t" \
    "s_add_u32 s38, s55, 34603008\n\t" \
    ".Ll27_%=:\n\t" \
    "s_sleep 2\n\t" \
    "buffer_load_dword v3, v47, s[4:7], s38 offen sc1\n\t" \
    "s_waitcnt vmcnt(0)\n\t" \
    "v_cmp_ne_u32 vcc, s34, v3\n\t" \
    "s_add_u32 s40, s40, 1\n\t" \
    "s_cmp_ge_u32 s40, 65536\n\t" \
    "s_cbranch_scc1 .Lb27_%=\n\t" \
    "s_cbranch_vccnz .Ll27_%=\n\t" \
    "s_branch .Lb27_%=\n\t" \
    ".Ls50_%=:\n\t" \
    "s_mov_b32 s40, 0\n\t" \
    ".Ll50_%=:\n\t" \
    "s_add_u32 s54, s53, 0\n\t" \
    "buffer_load_dwordx4 v[64:67], v0, s[4:7], s54 offen offset:0 sc1\n\t" \
    "s_add_u32 s54, s53, 4243456\n\t" \
    "buffer_load_dwordx4 v[44:47], v0, s[4:7], s54 offen offset:0 sc1\n\t" \
    "s_waitcnt vmcnt(0)\n\t" \
    "v_and_b32 v36, v64, v66\n\t" \
    "v_bitop3_b32 v36, v36, v44, v46 bitop3:0x80\n\t" \
    "v_and_b32 v36, 1, v36\n\t" \
    "v_cmp_eq_u32 vcc, 0, v36\n\t" \
    "s_add_u32 s40, s40, 1\n\t" \
    "s_cmp_ge_u32 s40, 65536\n\t" \
    "s_cbranch_scc1 .Lb50_%=\n\t" \
    "s_cbranch_vccnz .Ll50_%=\n\t" \
    "s_branch .Lb50_%=\n\t" \
    ".Ls51_%=:\n\t" \
    "s_mov_b32 s40, 0\n\t" \
    ".Ll51_%=:\n\t" \
    "s_add_u32 s54, s53, 524288\n\t" \
    "buffer_load_dwordx4 v[108:111], v0, s[4:7], s54 offen offset:0 sc1\n\t" \
    "s_add_u32 s54, s53, 4767744\n\t" \
    "buffer_load_dwordx4 v[24:27], v0, s[4:7], s54 offen offset:0 sc1\n\t" \
    "s_waitcnt vmcnt(0)\n\t" \
    "v_and_b32 v51, v108, v110\n\t" \
    "v_bitop3_b32 v51, v51, v24, v26 bitop3:0x80\n\t" \
    "v_and_b32 v51, 1, v51\n\t" \
    "v_cmp_eq_u32 vcc, 0, v51\n\t" \
    "s_add_u32 s40, s40, 1\n\t" \
    "s_cmp_ge_u32 s40, 65536\n\t" \
    "s_cbranch_scc1 .Lb51_%=\n\t" \
    "s_cbranch_vccnz .Ll51_%=\n\t" \
    "s_branch .Lb51_%=\n\t" \
    ".Ls52_%=:\n\t" \
    "s_mov_b32 s40, 0\n\t" \
    ".Ll52_%=:\n\t" \
    "s_add_u32 s54, s53, 1048576\n\t" \
    "buffer_load_dwordx4 v[180:183], v0, s[4:7], s54 offen offset:0 sc1\n\t" \
    "s_add_u32 s54, s53, 5292032\n\t" \
    "buffer_load_dwordx4 v[220:223], v0, s[4:7], s54 offen offset:0 sc1\n\t" \
    "s_waitcnt vmcnt(0)\n\t" \
    "v_and_b32 v37, v180, v182\n\t" \
    "v_bitop3_b32 v37, v37, v220, v222 bitop3:0x80\n\t" \
    "v_and_b32 v37, 1, v37\n\t" \
    "v_cmp_eq_u32 vcc, 0, v37\n\t" \
    "s_add_u32 s40, s40, 1\n\t" \
    "s_cmp_ge_u32 s40, 65536\n\t" \
    "s_cbranch_scc1 .Lb52_%=\n\t" \
    "s_cbranch_vccnz .Ll52_%=\n\t" \
    "s_branch .Lb52_%=\n\t" \
    ".Lfin_%=:\n\t"
#define K_CLOBBERS "v1", "v2", "v3", "v4", "v5", "v6", "v7", "v8", "v9", "v10", "v11", "v12", "v13", "v14", "v15", "v16", "v17", "v18", "v19", "v20", "v21", "v22", "v23", "v24", "v25", "v26", "v27", "v28", "v29", "v30", "v31", "v32", "v33", "v34", "v35", "v36", "v37", "v38", "v39", "v40", "v41", "v42", "v43", "v44", "v45", "v46", "v47", "v48", "v49", "v50", "v51", "v52", "v53", "v54", "v55", "v56", "v57", "v58", "v59", "v60", "v61", "v62", "v63", "v64", "v65", "v66", "v67", "v68", "v69", "v70", "v71", "v72", "v73", "v74", "v75", "v76", "v77", "v78", "v79", "v80", "v81", "v82", "v83", "v84", "v85", "v86", "v87", "v88", "v89", "v90", "v91", "v92", "v93", "v94", "v95", "v96", "v97", "v98", "v99", "v100", "v101", "v102", "v103", "v104", "v105", "v106", "v107", "v108", "v109", "v110", "v111", "v112", "v113", "v114", "v115", "v116", "v117", "v118", "v119", "v120", "v121", "v122", "v123", "v124", "v125", "v126", "v127", "v128", "v129", "v130", "v131", "v132", "v133", "v134", "v135", "v136", "v137", "v138", "v139", "v140", "v141", "v142", "v143", "v144", "v145", "v146", "v147", "v148", "v149", "v150", "v151", "v152", "v153", "v154", "v155", "v156", "v157", "v158", "v159", "v160", "v161", "v162", "v163", "v164", "v165", "v166", "v167", "v168", "v169", "v170", "v171", "v172", "v173", "v174", "v175", "v176", "v177", "v178", "v179", "v180", "v181", "v182", "v183", "v184", "v185", "v186", "v187", "v188", "v189", "v190", "v191", "v192", "v193", "v194", "v195", "v196", "v197", "v198", "v199", "v200", "v201", "v202", "v203", "v204", "v205", "v206", "v207", "v208", "v209", "v210", "v211", "v212", "v213", "v214", "v215", "v216", "v217", "v218", "v219", "v220", "v221", "v222", "v223", "v224", "v225", "v226", "v227", "v228", "v229", "v230", "v231", "v232", "v233", "v234", "v235", "v236", "v237", "v238", "v239", "v240", "v241", "v242", "v243", "v244", "v245", "v246", "v247", "v248", "v249", "v250", "v251", "v252", "v253", "v254", "v255", "s3", "s4", "s5", "s6", "s7", "s8", "s9", "s10", "s11", "s12", "s13", "s14", "s15", "s16", "s17", "s18", "s19", "s20", "s21", "s22", "s23", "s24", "s25", "s26", "s27", "s28", "s29", "s30", "s31", "s32", "s33", "s34", "s35", "s36", "s37", "s38", "s39", "s40", "s41", "s42", "s43", "s44", "s45", "s46", "s47", "s48", "s49", "s50", "s51", "s52", "s53", "s54", "s55", "s56", "s57", "s58", "s59", "s60", "s61", "s62", "s63", "s64", "s65"

extern "C" __global__ __launch_bounds__(512, 2)
void k_conv(const char* __restrict__ x, const char* __restrict__ wgt, char* __restrict__ ws, char* __restrict__ out)
{
  __shared__ __attribute__((aligned(256))) char smem[157568];
  unsigned tid = threadIdx.x;
  auto kp = __builtin_amdgcn_kernarg_segment_ptr();
  asm volatile(K_BODY : "+{v0}"(tid), "+{s[0:1]}"(kp)
               : "{s2}"(blockIdx.x), "i"((__attribute__((address_space(3))) char*)smem)
               : "memory", "scc", "vcc", K_CLOBBERS);
}

extern "C" void kernel_launch(void* const* d_in, const int* in_sizes, int n_in,
                              void* d_out, int out_size, void* d_ws, size_t ws_size,
                              hipStream_t stream) {
  k_conv<<<dim3(256), dim3(512), 0, stream>>>((const char*)d_in[0], (const char*)d_in[1], (char*)d_ws, (char*)d_out);
}
```

```cpp
#include <hip/hip_runtime.h>
#include <hip/hip_bf16.h>

#define K_BODY \
    "s_load_dwordx2 s[16:17], s[0:1], 0x0\n\t" \
    "s_load_dwordx2 s[4:5], s[0:1], 0x10\n\t" \
    "s_load_dwordx2 s[12:13], s[0:1], 0x8\n\t" \
    "s_load_dwordx2 s[8:9], s[0:1], 0x18\n\t" \
    "s_getpc_b64 s[22:23]\n\t" \
    ".Lpc_%=:\n\t" \
    "s_cmp_ge_u32 s2, 16\n\t" \
    "s_cbranch_scc1 .Le1_%=\n\t" \
    "s_mov_b32 m0, 157312\n\t" \
    "v_lshlrev_b32 v8, 6, v0\n\t" \
    "v_min_u32 v8, .Lfin_%=-.Lpc_%=, v8\n\t" \
    "global_load_lds_dword v8, s[22:23]\n\t" \
    ".Le1_%=:\n\t" \
    "v_readfirstlane_b32 s24, v0\n\t" \
    "s_and_b32 s25, s2, 3\n\t" \
    "s_lshl_b32 s25, s25, 5\n\t" \
    "s_lshr_b32 s37, s2, 3\n\t" \
    "s_add_u32 s25, s25, s37\n\t" \
    "s_min_u32 s28, s25, 125\n\t" \
    "s_mul_i32 s26, s28, 57\n\t" \
    "s_lshr_b32 s26, s26, 9\n\t" \
    "s_mul_i32 s37, s26, 9\n\t" \
    "s_sub_u32 s27, s28, s37\n\t" \
    "s_bfe_u32 s37, s2, 0x10002\n\t" \
    "s_lshl1_add_u32 s26, s26, s37\n\t" \
    "v_and_b32 v0, 63, v0\n\t" \
    "v_lshlrev_b32 v0, 4, v0\n\t" \
    "s_lshr_b32 s24, s24, 6\n\t" \
    "s_sub_u32 s37, s24, 4\n\t" \
    "s_cbranch_scc1 .Le2_%=\n\t" \
    "s_cmp_ge_u32 s25, 126\n\t" \
    "s_cbranch_scc1 .Le2_%=\n\t" \
    "s_mul_i32 m0, s37, 12416\n\t" \
    "s_lshl2_add_u32 s37, s26, s37\n\t" \
    "s_mul_i32 s37, s37, 86016\n\t" \
    "s_mul_i32 s38, s27, 4992\n\t" \
    "s_add_u32 s37, s37, s38\n\t" \
    "s_addk_i32 s37, 896\n\t" \
    "v_add_u32 v9, s37, v0\n\t" \
    "s_cmp_eq_u32 s27, 0\n\t" \
    "s_cselect_b32 s38, 43008, 0\n\t" \
    "s_sub_u32 s39, s38, 2048\n\t" \
    "v_add_u32 v7, s39, v9\n\t" \
    "v_subrev_u32 v8, 0x400, v9\n\t" \
    "s_mov_b64 exec, 0xff\n\t" \
    "v_add_u32 v8, s38, v8\n\t" \
    "s_mov_b64 exec, -1\n\t" \
    "s_mov_b32 s18, 9633792\n\t" \
    "s_mov_b32 s19, 0x20000\n\t" \
    "s_mov_b32 s39, 43008\n\t" \
    "s_waitcnt lgkmcnt(0)\n\t" \
    "buffer_load_dwordx4 v7, s[16:19], 0 offen offset:0 sc1 nt lds\n\t" \
    "s_add_u32 m0, m0, 2080\n\t" \
    "s_nop 0\n\t" \
    "buffer_load_dwordx4 v9, s[16:19], 0 offen offset:3072 sc1 nt lds\n\t" \
    "buffer_load_dwordx4 v9, s[16:19], 0 offen offset:2048 sc1 nt lds\n\t" \
    "s_add_u32 m0, m0, 4128\n\t" \
    "s_nop 0\n\t" \
    "buffer_load_dwordx4 v7, s[16:19], s39 offen offset:0 sc1 nt lds\n\t" \
    "s_add_u32 m0, m0, 2080\n\t" \
    "s_nop 0\n\t" \
    "buffer_load_dwordx4 v9, s[16:19], s39 offen offset:3072 sc1 nt lds\n\t" \
    "buffer_load_dwordx4 v9, s[16:19], s39 offen offset:2048 sc1 nt lds\n\t" \
    "buffer_load_dwordx4 v9, s[16:19], s39 offen offset:1024 sc1 nt lds\n\t" \
    "s_add_u32 m0, m0, 4294966240\n\t" \
    "s_nop 0\n\t" \
    "buffer_load_dwordx4 v8, s[16:19], s39 offen offset:0 sc1 nt lds\n\t" \
    "s_add_u32 m0, m0, 1024\n\t" \
    "s_nop 0\n\t" \
    "buffer_load_dwordx4 v9, s[16:19], s39 offen offset:0 sc1 nt lds\n\t" \
    "s_add_u32 m0, m0, 4294960064\n\t" \
    "s_nop 0\n\t" \
    "buffer_load_dwordx4 v8, s[16:19], 0 offen offset:0 sc1 nt lds\n\t" \
    "s_add_u32 m0, m0, 1024\n\t" \
    "s_nop 0\n\t" \
    "buffer_load_dwordx4 v9, s[16:19], 0 offen offset:0 sc1 nt lds\n\t" \
    "s_add_u32 m0, m0, 32\n\t" \
    "s_nop 0\n\t" \
    "buffer_load_dwordx4 v9, s[16:19], 0 offen offset:1024 sc1 nt lds\n\t" \
    ".Le2_%=:\n\t" \
    "s_mov_b32 s6, 34701312\n\t" \
    "s_mov_b32 s7, 0x20000\n\t" \
    "v_lshrrev_b32 v8, 7, v0\n\t" \
    "v_and_b32 v9, 0x70, v0\n\t" \
    "v_lshrrev_b32 v2, 8, v0\n\t" \
    "v_lshl_or_b32 v2, v2, 16, v9\n\t" \
    "v_and_b32 v10, 0x80, v0\n\t" \
    "v_lshl_or_b32 v2, v10, 6, v2\n\t" \
    "s_lshl_b32 s32, s2, 7\n\t" \
    "s_andn2_b32 s37, s32, 0x1fff\n\t" \
    "s_add_u32 s32, s32, s37\n\t" \
    "s_add_u32 s32, s32, 33554432\n\t" \
    "s_cmp_ge_u32 s24, 4\n\t" \
    "s_cbranch_scc1 .Le3_%=\n\t" \
    "v_mov_b32 v12, 0\n\t" \
    "v_mov_b32 v13, 0\n\t" \
    "v_mov_b32 v14, 0\n\t" \
    "v_mov_b32 v15, 0\n\t" \
    "s_lshl_b32 s37, s24, 18\n\t" \
    "s_add_u32 s37, s37, s32\n\t" \
    "s_xor_b32 s38, s24, 2\n\t" \
    "s_lshr_b32 s39, s2, 3\n\t" \
    "s_or_b32 s38, s38, s39\n\t" \
    "s_memrealtime s[22:23]\n\t" \
    "s_waitcnt lgkmcnt(0)\n\t" \
    "s_cbranch_scc1 .Le4_%=\n\t" \
    "v_lshl_or_b32 v8, v8, 10, v9\n\t" \
    "v_mov_b32 v16, s22\n\t" \
    "v_mov_b32 v17, s22\n\t" \
    "v_mov_b32 v18, s22\n\t" \
    "v_mov_b32 v19, s22\n\t" \
    "s_add_u32 s38, s32, 1122304\n\t" \
    "buffer_store_dwordx4 v[16:19], v8, s[4:7], s38 offen sc1\n\t" \
    ".Le4_%=:\n\t" \
    ".Le3_%=:\n\t" \
    "s_bitcmp1_b32 s2, 2\n\t" \
    "s_cselect_b32 s38, 126, 0\n\t" \
    "s_add_u32 s28, s28, s38\n\t" \
    "s_mul_i32 s30, s28, 293\n\t" \
    "s_lshr_b32 s30, s30, 11\n\t" \
    "s_mul_i32 s37, s30, 7\n\t" \
    "s_sub_u32 s28, s28, s37\n\t" \
    "s_mul_i32 s29, s30, 43\n\t" \
    "s_lshr_b32 s29, s29, 7\n\t" \
    "s_mul_i32 s37, s29, 3\n\t" \
    "s_sub_u32 s30, s30, s37\n\t" \
    "s_and_b32 s37, s29, 12\n\t" \
    "s_add_u32 s37, s37, s29\n\t" \
    "s_mul_i32 s31, s37, 768\n\t" \
    "s_lshl_b32 s37, s30, 6\n\t" \
    "s_add_u32 s31, s31, s37\n\t" \
    "s_add_u32 s41, s29, s30\n\t" \
    "s_add_u32 s41, s41, s28\n\t" \
    "s_and_b32 s41, s41, 3\n\t" \
    "s_cmp_lt_u32 s25, 126\n\t" \
    "s_cselect_b32 s14, 903168, 0\n\t" \
    "s_mov_b32 s15, 0x20000\n\t" \
    "v_lshrrev_b32 v10, 6, v0\n\t" \
    "v_lshrrev_b32 v11, 9, v0\n\t" \
    "v_sub_u32 v8, v10, v11\n\t" \
    "v_mul_u32_u24 v8, 18432, v8\n\t" \
    "v_and_b32 v9, 0x30, v0\n\t" \
    "v_lshlrev_b32 v11, 5, v11\n\t" \
    "v_bitop3_b32 v1, v8, v9, v11 bitop3:0xf6\n\t" \
    "s_mov_b32 exec_lo, 0xf0000000\n\t" \
    "s_mov_b32 exec_hi, 0xf0000000\n\t" \
    "v_bfrev_b32 v1, 1\n\t" \
    "s_mov_b64 exec, -1\n\t" \
    "s_cmp_ge_u32 s24, 4\n\t" \
    "s_cbranch_scc1 .Le9_%=\n\t" \
    "v_bfe_u32 v20, v0, 4, 4\n\t" \
    "v_and_b32 v10, 12, v10\n\t" \
    "v_cvt_f32_ubyte0 v21, v10\n\t" \
    "v_and_b32 v22, 1, v20\n\t" \
    "v_cvt_f32_ubyte0 v22, v22\n\t" \
    "v_mul_f32 v22, 0x3e800000, v22\n\t" \
    "v_lshrrev_b32 v23, 1, v20\n\t" \
    "v_mov_b32 v32, 0\n\t" \
    "v_mov_b32 v33, -1\n\t" \
    "v_mov_b32 v31, 0x3e800000\n\t" \
    "s_mov_b32 s65, 0\n\t" \
    "s_mov_b32 s40, 0\n\t" \
    "s_cmp_eq_u32 s24, 1\n\t" \
    "s_cbranch_scc1 .Lm1_%=\n\t" \
    "s_cmp_eq_u32 s24, 2\n\t" \
    "s_cbranch_scc1 .Lm2_%=\n\t" \
    "s_cmp_eq_u32 s24, 3\n\t" \
    "s_cbranch_scc1 .Lm3_%=\n\t" \
    "s_add_u32 s37, s32, 1048576\n\t" \
    "s_mov_b64 exec, 0xff\n\t" \
    "s_mov_b64 exec, -1\n\t" \
    "v_or_b32 v24, 1, v20\n\t" \
    "v_cvt_f32_ubyte0 v24, v24\n\t" \
    "v_mul_f32 v24, 0x3c800000, v24\n\t" \
    "v_lshrrev_b32 v25, 4, v0\n\t" \
    "v_and_b32 v25, 16, v25\n\t" \
    "v_lshrrev_b32 v26, 2, v11\n\t" \
    "v_or_b32 v25, v25, v26\n\t" \
    "v_cvt_f32_ubyte0 v25, v25\n\t" \
    "v_add_f32 v26, 2.0, v25\n\t" \
    "v_add_f32 v27, 4.0, v25\n\t" \
    "v_add_f32 v28, 4.0, v26\n\t" \
    "v_mov_b32 v29, v22\n\t" \
    "v_mov_b32 v30, v22\n\t" \
    "v_mov_b32 v31, v24\n\t" \
    "s_mov_b32 s64, 0x3e800000\n\t" \
    "s_mov_b32 s65, 0x3e800000\n\t" \
    "v_add_u32 v34, 154880, v0\n\t" \
    "s_branch .Ltl_%=\n\t" \
    ".Lm1_%=:\n\t" \
    "v_lshrrev_b32 v24, 5, v0\n\t" \
    "v_and_b32 v24, 12, v24\n\t" \
    "v_and_or_b32 v24, v20, 3, v24\n\t" \
    "v_cvt_f32_ubyte0 v24, v24\n\t" \
    "v_mul_f32 v24, 0x3d924925, v24\n\t" \
    "v_mul_f32 v32, 0x41000000, v24\n\t" \
    "v_lshrrev_b32 v27, 3, v11\n\t" \
    "v_cvt_f32_ubyte0 v27, v27\n\t" \
    "v_add_f32 v25, -2.0, v27\n\t" \
    "v_add_f32 v26, -1.0, v27\n\t" \
    "v_add_f32 v28, 1.0, v27\n\t" \
    "v_and_b32 v29, 4, v20\n\t" \
    "v_cvt_f32_ubyte0 v29, v29\n\t" \
    "v_mul_f32 v29, 0x3d800000, v29\n\t" \
    "v_mov_b32 v30, v29\n\t" \
    "v_mov_b32 v31, 0xbe800000\n\t" \
    "s_mov_b32 s64, 0\n\t" \
    "s_mov_b32 exec_hi, 0\n\t" \
    "v_mov_b32 v33, 0\n\t" \
    "s_mov_b64 exec, -1\n\t" \
    "v_add_u32 v34, 149760, v0\n\t" \
    "s_branch .Ltl_%=\n\t" \
    ".Lm2_%=:\n\t" \
    "v_add_u32 v24, 6, v20\n\t" \
    "v_cvt_f32_ubyte0 v24, v24\n\t" \
    "v_mul_f32 v24, 0x3d000000, v24\n\t" \
    "v_add_f32 v25, 0.5, v21\n\t" \
    "v_add_f32 v26, 1.0, v25\n\t" \
    "v_add_f32 v27, 2.0, v25\n\t" \
    "v_add_f32 v28, 2.0, v26\n\t" \
    "v_mov_b32 v29, 0\n\t" \
    "v_mov_b32 v30, 0\n\t" \
    "s_mov_b32 s64, 0.5\n\t" \
    "v_add_u32 v34, 152832, v0\n\t" \
    "s_branch .Ltl_%=\n\t" \
    ".Lm3_%=:\n\t" \
    "s_mov_b32 s37, 34668544\n\t" \
    "buffer_load_dword v3, v12, s[4:7], s37 offen sc1\n\t" \
    "v_lshrrev_b32 v8, 2, v0\n\t" \
    "v_and_b32 v8, 28, v8\n\t" \
    "v_add_u32 v8, 156928, v8\n\t" \
    "ds_write_b32 v8, v12\n\t" \
    "v_add_u32 v24, 6, v23\n\t" \
    "v_cvt_f32_ubyte0 v24, v24\n\t" \
    "v_mul_f32 v24, 0x3d924925, v24\n\t" \
    "v_mov_b32 v25, v21\n\t" \
    "v_add_f32 v26, 1.0, v21\n\t" \
    "v_add_f32 v27, 2.0, v21\n\t" \
    "v_add_f32 v28, 2.0, v26\n\t" \
    "v_sub_f32 v29, 0, v22\n\t" \
    "v_sub_f32 v30, 0, v22\n\t" \
    "s_mov_b32 exec_lo, 0\n\t" \
    "v_fma_f32 v25, v21, 0.5, 4.0\n\t" \
    "v_add_f32 v27, 1.0, v25\n\t" \
    "v_fmaak_f32 v26, v21, v31, 0x41200000\n\t" \
    "v_mov_b32 v28, v26\n\t" \
    "v_add_f32 v30, v31, v30\n\t" \
    "s_mov_b64 exec, -1\n\t" \
    "s_mov_b32 s40, 0x11\n\t" \
    "v_add_u32 v34, 151808, v0\n\t" \
    ".Ltl_%=:\n\t" \
    "v_fma_f32 v36, v24, v25, v29\n\t" \
    "v_fma_f32 v38, v24, v26, v29\n\t" \
    "v_fma_f32 v40, v24, v27, v29\n\t" \
    "v_fma_f32 v42, v24, v28, v30\n\t" \
    "v_add_f32 v37, v36, v31\n\t" \
    "v_add_f32 v39, v38, v31\n\t" \
    "v_add_f32 v41, v40, v31\n\t" \
    "v_add_f32 v43, v42, v31\n\t" \
    "v_cos_f32 v36, v36\n\t" \
    "v_cos_f32 v37, v37\n\t" \
    "v_cos_f32 v38, v38\n\t" \
    "v_cos_f32 v39, v39\n\t" \
    "v_cos_f32 v40, v40\n\t" \
    "v_cos_f32 v41, v41\n\t" \
    "v_cos_f32 v42, v42\n\t" \
    "v_cos_f32 v43, v43\n\t" \
    "s_bitcmp0_b32 s40, 4\n\t" \
    "s_cbranch_scc1 .Lt2_%=\n\t" \
    "v_mul_f32 v36, 0x3b924925, v36\n\t" \
    "v_mul_f32 v37, 0x3b924925, v37\n\t" \
    "v_mul_f32 v38, 0x3b924925, v38\n\t" \
    "v_mul_f32 v39, 0x3b924925, v39\n\t" \
    "v_mul_f32 v40, 0x3b924925, v40\n\t" \
    "v_mul_f32 v41, 0x3b924925, v41\n\t" \
    "v_mul_f32 v42, 0x3b924925, v42\n\t" \
    "v_mul_f32 v43, 0x3b924925, v43\n\t" \
    ".Lt2_%=:\n\t" \
    "v_cvt_pk_bf16_f32 v44, v36, v37\n\t" \
    "v_cvt_pk_bf16_f32 v45, v38, v39\n\t" \
    "v_cvt_pk_bf16_f32 v46, v40, v41\n\t" \
    "v_cvt_pk_bf16_f32 v47, v42, v43\n\t" \
    "v_and_b32 v44, v33, v44\n\t" \
    "v_and_b32 v45, v33, v45\n\t" \
    "v_mov_b32 v33, -1\n\t" \
    "v_add_f32 v24, s64, v24\n\t" \
    "v_add_f32 v31, s65, v31\n\t" \
    "v_add_f32 v29, v32, v29\n\t" \
    "v_add_f32 v30, v32, v30\n\t" \
    "ds_write_b128 v34, v[44:47]\n\t" \
    "s_add_u32 s40, s40, 1\n\t" \
    "s_bitcmp1_b32 s40, 0\n\t" \
    "v_add_u32 v34, 0x400, v34\n\t" \
    "s_cbranch_scc1 .Ltl_%=\n\t" \
    "s_bitcmp1_b32 s40, 2\n\t" \
    "s_cbranch_scc1 .Lt4_%=\n\t" \
    "s_mov_b32 s40, 2\n\t" \
    "s_lshl2_add_u32 s37, s28, s24\n\t" \
    "s_lshl_b32 s37, s37, 2\n\t" \
    "v_lshrrev_b32 v25, 2, v20\n\t" \
    "v_add_u32 v25, s37, v25\n\t" \
    "v_mul_u32_u24 v26, 147, v25\n\t" \
    "v_lshrrev_b32 v26, 10, v26\n\t" \
    "v_lshl_or_b32 v24, v26, 1, 1\n\t" \
    "v_cvt_f32_ubyte0 v24, v24\n\t" \
    "v_mul_f32 v24, 0x3c800000, v24\n\t" \
    "v_mul_u32_u24 v26, 7, v26\n\t" \
    "v_sub_u32 v25, v25, v26\n\t" \
    "v_xor_b32 v22, s41, v20\n\t" \
    "v_bfe_u32 v32, v22, 1, 1\n\t" \
    "v_and_b32 v22, 1, v22\n\t" \
    "v_cvt_f32_ubyte0 v22, v22\n\t" \
    "v_mul_f32 v22, 0x3e800000, v22\n\t" \
    "v_lshl_or_b32 v32, v25, 1, v32\n\t" \
    "v_cvt_f32_ubyte0 v32, v32\n\t" \
    "v_mul_f32 v32, 0x3e924925, v32\n\t" \
    "v_mul_f32 v29, 0x3d800000, v21\n\t" \
    "v_fma_f32 v29, v29, v32, v22\n\t" \
    "v_mov_b32 v30, v29\n\t" \
    "v_mov_b32 v31, v24\n\t" \
    "v_mov_b32 v25, 0\n\t" \
    "v_mov_b32 v26, 2.0\n\t" \
    "v_mov_b32 v27, 4.0\n\t" \
    "v_mov_b32 v28, 0x40c00000\n\t" \
    "s_mov_b32 s64, 0\n\t" \
    "s_mov_b32 s65, 0\n\t" \
    "s_lshl_b32 s37, s24, 11\n\t" \
    "s_add_u32 s37, s37, 49664\n\t" \
    "v_add_u32 v34, s37, v0\n\t" \
    "s_branch .Ltl_%=\n\t" \
    ".Lt4_%=:\n\t" \
    ".Lt3_%=:\n\t" \
    "s_cmp_lg_u32 s24, 3\n\t" \
    "s_cbranch_scc1 .Lx2_%=\n\t" \
    "v_lshrrev_b32 v8, 7, v0\n\t" \
    "v_mul_u32_u24 v8, 96, v8\n\t" \
    "v_and_b32 v9, 16, v0\n\t" \
    "v_bfe_u32 v10, v0, 5, 2\n\t" \
    "v_add3_u32 v8, v8, v9, v10\n\t" \
    "v_mul_u32_u24 v8, 192, v8\n\t" \
    "s_mul_i32 s37, s28, 129024\n\t" \
    "s_add_u32 s37, s37, s31\n\t" \
    "s_mov_b32 m0, 157312\n\t" \
    "s_mov_b32 exec_hi, 0xffffff\n\t" \
    "buffer_load_dword v8, s[12:15], s37 offen lds\n\t" \
    "s_mov_b64 exec, -1\n\t" \
    ".Lx2_%=:\n\t" \
    ".Le9_%=:\n\t" \
    "s_lshr_b32 s37, s24, 2\n\t" \
    "s_and_b32 s38, s24, 2\n\t" \
    "s_mul_i32 s39, s37, 24832\n\t" \
    "s_lshl4_add_u32 s39, s38, s39\n\t" \
    "v_lshrrev_b32 v4, 6, v0\n\t" \
    "v_and_b32 v5, 12, v4\n\t" \
    "v_add_u32 v6, v4, v5\n\t" \
    "v_lshrrev_b32 v7, 1, v0\n\t" \
    "v_and_b32 v7, 24, v7\n\t" \
    "v_and_b32 v4, 11, v4\n\t" \
    "v_mul_u32_u24 v4, 192, v4\n\t" \
    "v_bfe_u32 v5, v0, 8, 1\n\t" \
    "v_mul_u32_u24 v5, 3104, v5\n\t" \
    "v_add3_u32 v4, v4, v5, v7\n\t" \
    "v_add_u32 v8, s39, v4\n\t" \
    "v_mov_b32 v9, 156928\n\t" \
    "v_add_u32 v10, 149760, v0\n\t" \
    "s_lshl2_add_u32 s37, s37, s38\n\t" \
    "s_lshl_b32 s37, s37, 7\n\t" \
    "s_mul_i32 s53, s27, 28\n\t" \
    "s_add_u32 s53, s53, s26\n\t" \
    "s_andn2_b32 s39, s53, 7\n\t" \
    "s_add_u32 s53, s53, s39\n\t" \
    "s_lshl_b32 s53, s53, 10\n\t" \
    "s_add_u32 s37, s37, s53\n\t" \
    "s_and_b32 s38, s24, 1\n\t" \
    "s_lshl_b32 s39, s38, 13\n\t" \
    "s_add_u32 s37, s37, s39\n\t" \
    "s_cmp_lt_u32 s25, 126\n\t" \
    "s_cselect_b32 s43, 0, 0x80000000\n\t" \
    "s_add_u32 s37, s37, s43\n\t" \
    "v_lshrrev_b32 v11, 8, v0\n\t" \
    "v_and_b32 v12, 0xf0, v0\n\t" \
    "v_mul_u32_u24 v4, 8486912, v11\n\t" \
    "v_add3_u32 v13, v4, v12, s37\n\t" \
    "s_lshl_b32 s39, s38, 10\n\t" \
    "v_add_u32 v14, s39, v10\n\t" \
    "s_lshl_b32 s39, s38, 5\n\t" \
    "s_add_u32 s39, s39, 116736\n\t" \
    "v_lshl_add_u32 v4, v6, 6, v7\n\t" \
    "v_lshrrev_b32 v5, 3, v0\n\t" \
    "v_and_b32 v5, 32, v5\n\t" \
    "v_xor_b32 v5, s39, v5\n\t" \
    "v_add_u32 v15, v4, v5\n\t" \
    "s_and_b32 s37, s24, 6\n\t" \
    "s_lshl_b32 s37, s37, 10\n\t" \
    "v_add_u32 v16, s37, v0\n\t" \
    "s_cmp_ge_u32 s25, 126\n\t" \
    "s_cbranch_scc1 .Lqa_%=\n\t" \
    ".Lqd_%=:\n\t" \
    "s_waitcnt lgkmcnt(0)\n\t" \
    "s_barrier\n\t" \
    "ds_read_b128 v[24:27], v14 offset:5120\n\t" \
    "ds_read_b128 v[28:31], v16 offset:49664\n\t" \
    "ds_read_b128 v[32:35], v16 offset:50688\n\t" \
    "s_and_b32 s33, s2, 7\n\t" \
    "s_lshl_b32 s33, s33, 15\n\t" \
    "s_add_u32 s33, s33, 33554432\n\t" \
    "s_lshr_b32 s37, s29, 2\n\t" \
    "s_mul_i32 s39, s30, 3\n\t" \
    "s_add_u32 s37, s37, s39\n\t" \
    "s_mul_i32 s37, s37, 114688\n\t" \
    "s_and_b32 s39, s29, 3\n\t" \
    "s_lshl_b32 s39, s39, 8\n\t" \
    "s_add_u32 s37, s37, s39\n\t" \
    "s_add_u32 s37, s37, s43\n\t" \
    "s_add_u32 s37, s37, 2097152\n\t" \
    "s_and_b32 s39, s24, 6\n\t" \
    "s_lshl3_add_u32 s39, s28, s39\n\t" \
    "s_lshl_b32 s39, s39, 1\n\t" \
    "v_add_u32 v14, s39, v11\n\t" \
    "v_mul_u32_u24 v17, 147, v14\n\t" \
    "v_lshrrev_b32 v17, 10, v17\n\t" \
    "s_and_b32 s39, s24, 1\n\t" \
    "v_subrev_u32 v18, s39, v17\n\t" \
    "v_mul_i32_i24 v18, 7, v18\n\t" \
    "v_sub_u32 v14, v14, v18\n\t" \
    "v_and_b32 v18, 14, v14\n\t" \
    "v_add_lshl_u32 v14, v14, v18, 12\n\t" \
    "v_and_b32 v18, 7, v17\n\t" \
    "v_mul_u32_u24 v18, 4243456, v18\n\t" \
    "v_add3_u32 v14, v14, v18, v12\n\t" \
    "v_and_b32 v17, 8, v17\n\t" \
    "v_xor_b32 v17, 8, v17\n\t" \
    "v_lshl_add_u32 v14, v17, 10, v14\n\t" \
    "v_add_u32 v19, s37, v14\n\t" \
    "s_lshl_b32 s43, s41, 10\n\t" \
    "s_and_b32 s37, s2, 0x70\n\t" \
    "s_lshl_b32 s37, s37, 1\n\t" \
    "s_lshl2_add_u32 s57, s24, s37\n\t" \
    "s_min_u32 s58, s57, 248\n\t" \
    "s_mul_i32 s59, s58, 147\n\t" \
    "s_lshr_b32 s59, s59, 12\n\t" \
    "s_mul_i32 s37, s59, 28\n\t" \
    "s_sub_u32 s60, s58, s37\n\t" \
    "s_andn2_b32 s39, s58, 7\n\t" \
    "s_add_u32 s51, s58, s39\n\t" \
    "s_lshl_b32 s51, s51, 10\n\t" \
    "s_sub_u32 s37, s60, 1\n\t" \
    "s_cselect_b32 s52, 27, -1\n\t" \
    "s_add_u32 s52, s52, s58\n\t" \
    "s_andn2_b32 s39, s52, 7\n\t" \
    "s_add_u32 s52, s52, s39\n\t" \
    "s_lshl_b32 s52, s52, 10\n\t" \
    "v_lshrrev_b32 v16, 9, v0\n\t" \
    "v_and_b32 v18, 0x2f0, v0\n\t" \
    "v_and_b32 v17, 1, v11\n\t" \
    "v_lshl_add_u32 v14, v17, 11, v18\n\t" \
    "v_add_u32 v36, s51, v14\n\t" \
    "v_add_u32 v37, s52, v0\n\t" \
    "s_mul_i32 s44, s24, 7296\n\t" \
    "s_mul_i32 s37, s60, 344064\n\t" \
    "s_mul_i32 s39, s59, 4992\n\t" \
    "s_add_u32 s37, s37, s39\n\t" \
    "s_cmp_eq_u32 s59, 8\n\t" \
    "s_movk_i32 s39, 2496\n\t" \
    "s_cselect_b32 s39, 1536, s39\n\t" \
    "s_add_u32 s37, s37, s39\n\t" \
    "s_and_b32 s39, s8, 0x1fffff\n\t" \
    "s_add_u32 s37, s37, s39\n\t" \
    "v_bfe_u32 v38, v0, 5, 2\n\t" \
    "v_lshl_add_u32 v38, v16, 4, v38\n\t" \
    "v_mul_u32_u24 v38, 43008, v38\n\t" \
    "v_add_u32 v39, s37, v38\n\t" \
    "s_lshr_b32 s37, s60, 1\n\t" \
    "s_mul_i32 s37, s37, 9\n\t" \
    "s_add_u32 s37, s37, s59\n\t" \
    "v_mad_u32_u24 v38, v16, 9, s37\n\t" \
    "v_lshrrev_b32 v18, 5, v38\n\t" \
    "s_cmp_lt_u32 s57, 252\n\t" \
    "s_cselect_b32 s51, s51, 0x80000000\n\t" \
    "s_lshl_b32 s37, s24, 2\n\t" \
    "s_lshl1_add_u32 s39, s24, 8\n\t" \
    "s_min_u32 s61, s37, s39\n\t" \
    "s_add_u32 s37, s37, 2\n\t" \
    "s_add_u32 s39, s24, 20\n\t" \
    "s_cmp_lt_u32 s24, 4\n\t" \
    "s_cselect_b32 s62, s37, s39\n\t" \
    "s_bfe_u32 s63, s2, 0x10003\n\t" \
    "s_mul_i32 s47, s63, 28\n\t" \
    "s_add_u32 s45, s47, s61\n\t" \
    "s_add_u32 s46, s47, s62\n\t" \
    "s_andn2_b32 s39, s45, 7\n\t" \
    "s_add_u32 s45, s45, s39\n\t" \
    "s_lshl_b32 s45, s45, 10\n\t" \
    "s_andn2_b32 s39, s46, 7\n\t" \
    "s_add_u32 s46, s46, s39\n\t" \
    "s_lshl_b32 s46, s46, 10\n\t" \
    "v_lshlrev_b32 v17, 4, v11\n\t" \
    "v_lshrrev_b32 v14, 6, v12\n\t" \
    "v_mul_u32_u24 v14, 7296, v14\n\t" \
    "v_and_b32 v38, 16, v0\n\t" \
    "v_mul_u32_u24 v38, 226, v38\n\t" \
    "v_and_b32 v40, 32, v0\n\t" \
    "v_lshl_add_u32 v38, v40, 1, v38\n\t" \
    "v_add3_u32 v14, v14, v38, v17\n\t" \
    "s_lshl_b32 s37, s61, 7\n\t" \
    "v_add_u32 v44, s37, v14\n\t" \
    "s_and_b32 s37, s62, 30\n\t" \
    "s_lshl_b32 s37, s37, 7\n\t" \
    "v_add_u32 v45, s37, v14\n\t" \
    "s_mov_b32 s1, 0\n\t" \
    "s_mov_b32 s41, 0x18000\n\t" \
    "s_lshl_b32 s37, s63, 9\n\t" \
    "s_add_u32 s51, s51, s37\n\t" \
    "s_and_b32 s37, s2, 0x87\n\t" \
    "s_lshl4_add_u32 s37, s24, s37\n\t" \
    "s_lshl_b32 s55, s37, 7\n\t" \
    "s_andn2_b32 s37, s55, 0x1fff\n\t" \
    "s_add_u32 s55, s55, s37\n\t" \
    "v_and_b32 v46, 16, v0\n\t" \
    "v_lshlrev_b32 v47, 6, v46\n\t" \
    "v_mad_u32_u24 v38, v12, 12, v17\n\t" \
    "v_and_b32 v14, 0x170, v0\n\t" \
    "v_and_b32 v41, 0x80, v0\n\t" \
    "v_lshl_add_u32 v14, v41, 3, v14\n\t" \
    "v_lshl_add_u32 v14, v16, 11, v14\n\t" \
    "v_add_u32 v42, s51, v14\n\t" \
    "s_mul_i32 s37, s24, 2064\n\t" \
    "v_and_b32 v14, 0x300, v0\n\t" \
    "v_add3_u32 v43, v14, v0, s37\n\t" \
    "s_lshl_b32 s37, s37, 1\n\t" \
    "v_lshlrev_b32 v14, 7, v16\n\t" \
    "v_xor_b32 v14, v14, v0\n\t" \
    "v_add_u32 v40, s37, v14\n\t" \
    "v_lshrrev_b32 v14, 5, v12\n\t" \
    "v_mul_u32_u24 v14, 2064, v14\n\t" \
    "v_lshl_add_u32 v14, v46, 2, v14\n\t" \
    "s_lshl_b32 s37, s24, 8\n\t" \
    "v_add3_u32 v48, v14, v17, s37\n\t" \
    "v_bfe_u32 v14, v0, 7, 1\n\t" \
    "v_lshl_add_u32 v14, v11, 2, v14\n\t" \
    "v_mul_u32_u24 v14, 2064, v14\n\t" \
    "v_lshrrev_b32 v41, 3, v0\n\t" \
    "v_and_or_b32 v14, v41, 8, v14\n\t" \
    "s_and_b32 s37, s24, 3\n\t" \
    "s_lshl_b32 s37, s37, 6\n\t" \
    "s_and_b32 s39, s24, 4\n\t" \
    "s_lshl_b32 s39, s39, 8\n\t" \
    "s_or_b32 s37, s37, s39\n\t" \
    "v_and_or_b32 v41, v0, 32, s37\n\t" \
    "v_mul_u32_u24 v46, 40, v46\n\t" \
    "v_xor_b32 v41, v41, v46\n\t" \
    "v_add_u32 v49, v14, v41\n\t" \
    "ds_read_b128 v[52:55], v10 offset:0\n\t" \
    "ds_read_b128 v[56:59], v10 offset:1024\n\t" \
    "s_sleep 6\n\t" \
    "s_and_b32 s37, s24, 1\n\t" \
    "s_lshl_b32 s37, s37, 4\n\t" \
    "s_lshr_b32 s39, s24, 1\n\t" \
    "s_add_u32 s37, s37, s39\n\t" \
    "s_mul_i32 s37, s37, 192\n\t" \
    "s_add_u32 s37, s37, s31\n\t" \
    "s_lshl_b32 s39, s24, 12\n\t" \
    "s_add_u32 m0, s39, 116736\n\t" \
    "s_add_u32 s38, s37, 0\n\t" \
    "buffer_load_dwordx4 v1, s[12:15], s38 offen  lds\n\t" \
    "s_add_u32 m0, s39, 117760\n\t" \
    "s_add_u32 s38, s37, 258048\n\t" \
    "buffer_load_dwordx4 v1, s[12:15], s38 offen  lds\n\t" \
    "s_add_u32 m0, s39, 118784\n\t" \
    "s_add_u32 s38, s37, 516096\n\t" \
    "buffer_load_dwordx4 v1, s[12:15], s38 offen  lds\n\t" \
    "s_add_u32 m0, s39, 119808\n\t" \
    "s_add_u32 s38, s37, 774144\n\t" \
    "buffer_load_dwordx4 v1, s[12:15], s38 offen  lds\n\t" \
    "s_cmp_lg_u32 s24, 3\n\t" \
    "s_cbranch_scc1 .Lr9_%=\n\t" \
    "s_and_b32 s37, s2, 7\n\t" \
    "s_lshl_b32 s37, s37, 10\n\t" \
    "s_add_u32 s37, s37, 34676736\n\t" \
    "s_buffer_load_dword s56, s[4:7], s37 offset:0x0\n\t" \
    "s_buffer_load_dword s57, s[4:7], s37 offset:0x80\n\t" \
    "s_buffer_load_dword s58, s[4:7], s37 offset:0x100\n\t" \
    "s_buffer_load_dword s59, s[4:7], s37 offset:0x180\n\t" \
    "s_buffer_load_dword s60, s[4:7], s37 offset:0x200\n\t" \
    "s_buffer_load_dword s61, s[4:7], s37 offset:0x280\n\t" \
    "s_buffer_load_dword s62, s[4:7], s37 offset:0x300\n\t" \
    "s_buffer_load_dword s63, s[4:7], s37 offset:0x380\n\t" \
    "s_mov_b32 s40, 0\n\t" \
    ".Lr1_%=:\n\t" \
    "s_waitcnt lgkmcnt(0)\n\t" \
    "s_memrealtime s[22:23]\n\t" \
    "s_waitcnt lgkmcnt(0)\n\t" \
    "s_mov_b32 s37, 1000\n\t" \
    "s_mov_b32 s38, 0\n\t" \
    "s_sub_u32 s39, s22, s56\n\t" \
    "s_min_u32 s37, s37, s39\n\t" \
    "s_max_u32 s38, s38, s39\n\t" \
    "s_sub_u32 s39, s22, s57\n\t" \
    "s_min_u32 s37, s37, s39\n\t" \
    "s_max_u32 s38, s38, s39\n\t" \
    "s_sub_u32 s39, s22, s58\n\t" \
    "s_min_u32 s37, s37, s39\n\t" \
    "s_max_u32 s38, s38, s39\n\t" \
    "s_sub_u32 s39, s22, s59\n\t" \
    "s_min_u32 s37, s37, s39\n\t" \
    "s_max_u32 s38, s38, s39\n\t" \
    "s_sub_u32 s39, s22, s60\n\t" \
    "s_min_u32 s37, s37, s39\n\t" \
    "s_max_u32 s38, s38, s39\n\t" \
    "s_sub_u32 s39, s22, s61\n\t" \
    "s_min_u32 s37, s37, s39\n\t" \
    "s_max_u32 s38, s38, s39\n\t" \
    "s_sub_u32 s39, s22, s62\n\t" \
    "s_min_u32 s37, s37, s39\n\t" \
    "s_max_u32 s38, s38, s39\n\t" \
    "s_sub_u32 s39, s22, s63\n\t" \
    "s_min_u32 s37, s37, s39\n\t" \
    "s_max_u32 s38, s38, s39\n\t" \
    "s_cmp_gt_u32 s38, 1000\n\t" \
    "s_cbranch_scc1 .Lr2_%=\n\t" \
    ".Lr3_%=:\n\t" \
    "s_sub_u32 s35, s22, s37\n\t" \
    "s_waitcnt vmcnt(5)\n\t" \
    "v_readfirstlane_b32 s34, v3\n\t" \
    "s_mul_i32 s34, s34, 1664525\n\t" \
    "s_add_u32 s34, s34, 1013904223\n\t" \
    "s_or_b32 s34, s34, 1\n\t" \
    "v_mov_b32 v60, s34\n\t" \
    "v_mov_b32 v61, s35\n\t" \
    "ds_write_b64 v9, v[60:61] offset:32\n\t" \
    ".Lr9_%=:\n\t" \
    "s_cmp_lt_u32 s24, 4\n\t" \
    "s_cbranch_scc1 .Lw_%=\n\t" \
    "s_waitcnt vmcnt(4)\n\t" \
    ".Lw_%=:\n\t" \
    "s_waitcnt lgkmcnt(0)\n\t" \
    "s_barrier\n\t" \
    "ds_read_b64_tr_b16 v[64:65], v8 offset:0\n\t" \
    "ds_read_b64_tr_b16 v[66:67], v8 offset:768\n\t" \
    "ds_read_b64_tr_b16 v[68:69], v8 offset:12416\n\t" \
    "ds_read_b64_tr_b16 v[70:71], v8 offset:13184\n\t" \
    "ds_read_b64_tr_b16 v[72:73], v8 offset:6208\n\t" \
    "ds_read_b64_tr_b16 v[74:75], v8 offset:6976\n\t" \
    "ds_read_b64_tr_b16 v[76:77], v8 offset:18624\n\t" \
    "ds_read_b64_tr_b16 v[78:79], v8 offset:19392\n\t" \
    "ds_read_b128 v[80:83], v9 offset:32\n\t" \
    "s_waitcnt lgkmcnt(7)\n\t" \
    "v_mfma_f32_16x16x32_bf16 v[84:87], v[24:27], v[64:67], 0\n\t" \
    "s_waitcnt lgkmcnt(5)\n\t" \
    "v_mfma_f32_16x16x32_bf16 v[88:91], v[24:27], v[68:71], 0\n\t" \
    "s_waitcnt lgkmcnt(3)\n\t" \
    "v_mfma_f32_16x16x32_bf16 v[92:95], v[24:27], v[72:75], 0\n\t" \
    "s_waitcnt lgkmcnt(1)\n\t" \
    "v_mfma_f32_16x16x32_bf16 v[96:99], v[24:27], v[76:79], 0\n\t" \
    "s_waitcnt lgkmcnt(0)\n\t" \
    "v_readfirstlane_b32 s34, v80\n\t" \
    "v_readfirstlane_b32 s35, v81\n\t" \
    "v_add_u32 v84, 0x8000, v84\n\t" \
    "v_add_u32 v86, 0x8000, v86\n\t" \
    "v_add_u32 v88, 0x8000, v88\n\t" \
    "v_add_u32 v90, 0x8000, v90\n\t" \
    "v_cvt_pk_bf16_f32 v108, v84, v85\n\t" \
    "v_cvt_pk_bf16_f32 v110, v88, v89\n\t" \
    "v_cvt_pk_bf16_f32 v109, v92, v93\n\t" \
    "v_cvt_pk_bf16_f32 v111, v96, v97\n\t" \
    "v_and_b32 v108, -2, v108\n\t" \
    "v_and_b32 v110, -2, v110\n\t" \
    "v_cvt_pk_bf16_f32 v112, v86, v87\n\t" \
    "v_cvt_pk_bf16_f32 v114, v90, v91\n\t" \
    "v_cvt_pk_bf16_f32 v113, v94, v95\n\t" \
    "v_cvt_pk_bf16_f32 v115, v98, v99\n\t" \
    "v_and_b32 v112, -2, v112\n\t" \
    "v_and_b32 v114, -2, v114\n\t" \
    ".Lh38_%=:\n\t" \
    "s_memrealtime s[22:23]\n\t" \
    "s_waitcnt lgkmcnt(0)\n\t" \
    "s_sub_u32 s22, s22, s35\n\t" \
    "s_cmp_lt_u32 s22, 205\n\t" \
    "s_cbranch_scc1 .Lh38_%=\n\t" \
    "buffer_store_dwordx4 v[108:111], v13, s[4:7], 0 offen offset:0 sc1\n\t" \
    "s_mov_b32 s42, 4243456\n\t" \
    "buffer_store_dwordx4 v[112:115], v13, s[4:7], s42 offen offset:0 sc1\n\t" \
    "ds_read_b64_tr_b16 v[116:117], v8 offset:64\n\t" \
    "ds_read_b64_tr_b16 v[118:119], v8 offset:832\n\t" \
    "ds_read_b64_tr_b16 v[120:121], v8 offset:12480\n\t" \
    "ds_read_b64_tr_b16 v[122:123], v8 offset:13248\n\t" \
    "ds_read_b64_tr_b16 v[124:125], v8 offset:6272\n\t" \
    "ds_read_b64_tr_b16 v[126:127], v8 offset:7040\n\t" \
    "ds_read_b64_tr_b16 v[128:129], v8 offset:18688\n\t" \
    "ds_read_b64_tr_b16 v[130:131], v8 offset:19456\n\t" \
    "ds_read_b64_tr_b16 v[132:133], v8 offset:128\n\t" \
    "ds_read_b64_tr_b16 v[134:135], v8 offset:896\n\t" \
    "ds_read_b64_tr_b16 v[136:137], v8 offset:12544\n\t" \
    "ds_read_b64_tr_b16 v[138:139], v8 offset:13312\n\t" \
    "ds_read_b64_tr_b16 v[140:141], v8 offset:6336\n\t" \
    "ds_read_b64_tr_b16 v[142:143], v8 offset:7104\n\t" \
    "ds_read_b64_tr_b16 v[144:145], v8 offset:18752\n\t" \
    "ds_read_b64_tr_b16 v[146:147], v8 offset:19520\n\t" \
    "v_mov_b32 v148, s34\n\t" \
    "v_mov_b32 v149, s34\n\t" \
    "v_mov_b32 v150, s34\n\t" \
    "v_mov_b32 v151, s34\n\t" \
    "s_and_b32 s37, s2, 243\n\t" \
    "s_cmp_eq_u32 s37, 243\n\t" \
    "s_cbranch_scc1 .Lskip_%=\n\t" \
    "s_waitcnt lgkmcnt(14)\n\t" \
    "v_mfma_f32_16x16x32_bf16 v[152:155], v[24:27], v[116:119], 0\n\t" \
    "s_waitcnt lgkmcnt(12)\n\t" \
    "v_mfma_f32_16x16x32_bf16 v[156:159], v[24:27], v[120:123], 0\n\t" \
    "s_waitcnt lgkmcnt(10)\n\t" \
    "v_mfma_f32_16x16x32_bf16 v[160:163], v[24:27], v[124:127], 0\n\t" \
    "s_waitcnt lgkmcnt(8)\n\t" \
    "v_mfma_f32_16x16x32_bf16 v[164:167], v[24:27], v[128:131], 0\n\t" \
    "s_waitcnt lgkmcnt(6)\n\t" \
    "v_mfma_f32_16x16x32_bf16 v[168:171], v[24:27], v[132:135], 0\n\t" \
    "s_waitcnt lgkmcnt(4)\n\t" \
    "v_mfma_f32_16x16x32_bf16 v[172:175], v[24:27], v[136:139], 0\n\t" \
    "s_waitcnt lgkmcnt(2)\n\t" \
    "v_mfma_f32_16x16x32_bf16 v[176:179], v[24:27], v[140:143], 0\n\t" \
    "s_waitcnt lgkmcnt(0)\n\t" \
    "v_mfma_f32_16x16x32_bf16 v[180:183], v[24:27], v[144:147], 0\n\t" \
    "v_add_u32 v152, 0x8000, v152\n\t" \
    "v_add_u32 v154, 0x8000, v154\n\t" \
    "v_add_u32 v156, 0x8000, v156\n\t" \
    "v_add_u32 v158, 0x8000, v158\n\t" \
    "v_cvt_pk_bf16_f32 v184, v152, v153\n\t" \
    "v_cvt_pk_bf16_f32 v186, v156, v157\n\t" \
    "v_cvt_pk_bf16_f32 v185, v160, v161\n\t" \
    "v_cvt_pk_bf16_f32 v187, v164, v165\n\t" \
    "v_and_b32 v184, -2, v184\n\t" \
    "v_and_b32 v186, -2, v186\n\t" \
    "v_cvt_pk_bf16_f32 v188, v154, v155\n\t" \
    "v_cvt_pk_bf16_f32 v190, v158, v159\n\t" \
    "v_cvt_pk_bf16_f32 v189, v162, v163\n\t" \
    "v_cvt_pk_bf16_f32 v191, v166, v167\n\t" \
    "v_and_b32 v188, -2, v188\n\t" \
    "v_and_b32 v190, -2, v190\n\t" \
    "v_add_u32 v168, 0x8000, v168\n\t" \
    "v_add_u32 v170, 0x8000, v170\n\t" \
    "v_add_u32 v172, 0x8000, v172\n\t" \
    "v_add_u32 v174, 0x8000, v174\n\t" \
    "v_cvt_pk_bf16_f32 v4, v168, v169\n\t" \
    "v_cvt_pk_bf16_f32 v6, v172, v173\n\t" \
    "v_cvt_pk_bf16_f32 v5, v176, v177\n\t" \
    "v_cvt_pk_bf16_f32 v7, v180, v181\n\t" \
    "v_and_b32 v4, -2, v4\n\t" \
    "v_and_b32 v6, -2, v6\n\t" \
    "v_cvt_pk_bf16_f32 v20, v170, v171\n\t" \
    "v_cvt_pk_bf16_f32 v22, v174, v175\n\t" \
    "v_cvt_pk_bf16_f32 v21, v178, v179\n\t" \
    "v_cvt_pk_bf16_f32 v23, v182, v183\n\t" \
    "v_and_b32 v20, -2, v20\n\t" \
    "v_and_b32 v22, -2, v22\n\t" \
    ".Lh39_%=:\n\t" \
    "s_memrealtime s[22:23]\n\t" \
    "s_waitcnt lgkmcnt(0)\n\t" \
    "s_sub_u32 s22, s22, s35\n\t" \
    "s_cmp_lt_u32 s22, 255\n\t" \
    "s_cbranch_scc1 .Lh39_%=\n\t" \
    "s_mov_b32 s42, 524288\n\t" \
    "buffer_store_dwordx4 v[184:187], v13, s[4:7], s42 offen offset:0 sc1\n\t" \
    "s_mov_b32 s42, 4767744\n\t" \
    "buffer_store_dwordx4 v[188:191], v13, s[4:7], s42 offen offset:0 sc1\n\t" \
    "s_waitcnt vmcnt(4)\n\t" \
    "s_waitcnt lgkmcnt(0)\n\t" \
    "s_barrier\n\t" \
    "s_lshl_b32 s37, s24, 18\n\t" \
    "s_add_u32 s37, s37, s32\n\t" \
    "s_bitcmp1_b32 13, s24\n\t" \
    "s_cselect_b32 s39, 0, 0x80000000\n\t" \
    "v_add_u32 v16, s39, v2\n\t" \
    "v_mov_b32 v60, 0\n\t" \
    "v_mov_b32 v61, 0\n\t" \
    "v_mov_b32 v62, 0\n\t" \
    "v_mov_b32 v63, 0\n\t" \
    "s_cmp_eq_u32 s24, 4\n\t" \
    "s_cselect_b64 exec, 0xff, -1\n\t" \
    "buffer_store_dwordx4 v[60:63], v16, s[4:7], s37 offen sc1\n\t" \
    "s_mov_b64 exec, -1\n\t" \
    ".Lh40_%=:\n\t" \
    "s_memrealtime s[22:23]\n\t" \
    "s_waitcnt lgkmcnt(0)\n\t" \
    "s_sub_u32 s22, s22, s35\n\t" \
    "s_cmp_lt_u32 s22, 305\n\t" \
    "s_cbranch_scc1 .Lh40_%=\n\t" \
    "s_mov_b32 s42, 1048576\n\t" \
    "buffer_store_dwordx4 v[4:7], v13, s[4:7], s42 offen offset:0 sc1\n\t" \
    "s_mov_b32 s42, 5292032\n\t" \
    "buffer_store_dwordx4 v[20:23], v13, s[4:7], s42 offen offset:0 sc1\n\t" \
    "ds_read_b64_tr_b16 v[64:65], v15 offset:0\n\t" \
    "ds_read_b64_tr_b16 v[66:67], v15 offset:256\n\t" \
    "ds_read_b64_tr_b16 v[68:69], v15 offset:2048\n\t" \
    "ds_read_b64_tr_b16 v[70:71], v15 offset:2304\n\t" \
    "ds_read_b64_tr_b16 v[72:73], v15 offset:4096\n\t" \
    "ds_read_b64_tr_b16 v[74:75], v15 offset:4352\n\t" \
    "ds_read_b64_tr_b16 v[76:77], v15 offset:6144\n\t" \
    "ds_read_b64_tr_b16 v[78:79], v15 offset:6400\n\t" \
    "ds_read_b64_tr_b16 v[80:81], v15 offset:8192\n\t" \
    "ds_read_b64_tr_b16 v[82:83], v15 offset:8448\n\t" \
    "ds_read_b64_tr_b16 v[100:101], v15 offset:10240\n\t" \
    "ds_read_b64_tr_b16 v[102:103], v15 offset:10496\n\t" \
    "ds_read_b64_tr_b16 v[104:105], v15 offset:12288\n\t" \
    "ds_read_b64_tr_b16 v[106:107], v15 offset:12544\n\t" \
    "s_waitcnt lgkmcnt(12)\n\t" \
    "v_mfma_f32_16x16x32_bf16 v[84:87], v[28:31], v[64:67], 0\n\t" \
    "ds_read_b64_tr_b16 v[88:89], v15 offset:14336\n\t" \
    "ds_read_b64_tr_b16 v[90:91], v15 offset:14592\n\t" \
    "s_waitcnt lgkmcnt(12)\n\t" \
    "v_mfma_f32_16x16x32_bf16 v[84:87], v[32:35], v[68:71], v[84:87]\n\t" \
    "ds_read_b64_tr_b16 v[92:93], v15 offset:16384\n\t" \
    "ds_read_b64_tr_b16 v[94:95], v15 offset:16640\n\t" \
    "s_waitcnt lgkmcnt(12)\n\t" \
    "v_mfma_f32_16x16x32_bf16 v[96:99], v[28:31], v[72:75], 0\n\t" \
    "ds_read_b64_tr_b16 v[108:109], v15 offset:18432\n\t" \
    "ds_read_b64_tr_b16 v[110:111], v15 offset:18688\n\t" \
    "s_waitcnt lgkmcnt(12)\n\t" \
    "v_mfma_f32_16x16x32_bf16 v[96:99], v[32:35], v[76:79], v[96:99]\n\t" \
    "ds_read_b64_tr_b16 v[112:113], v15 offset:20480\n\t" \
    "ds_read_b64_tr_b16 v[114:115], v15 offset:20736\n\t" \
    "s_waitcnt lgkmcnt(12)\n\t" \
    "v_mfma_f32_16x16x32_bf16 v[116:119], v[28:31], v[80:83], 0\n\t" \
    "ds_read_b64_tr_b16 v[120:121], v15 offset:22528\n\t" \
    "ds_read_b64_tr_b16 v[122:123], v15 offset:22784\n\t" \
    "s_waitcnt lgkmcnt(12)\n\t" \
    "v_mfma_f32_16x16x32_bf16 v[116:119], v[32:35], v[100:103], v[116:119]\n\t" \
    "ds_read_b64_tr_b16 v[124:125], v15 offset:24576\n\t" \
    "ds_read_b64_tr_b16 v[126:127], v15 offset:24832\n\t" \
    "s_waitcnt lgkmcnt(12)\n\t" \
    "v_mfma_f32_16x16x32_bf16 v[128:131], v[28:31], v[104:107], 0\n\t" \
    "ds_read_b64_tr_b16 v[132:133], v15 offset:26624\n\t" \
    "ds_read_b64_tr_b16 v[134:135], v15 offset:26880\n\t" \
    "s_waitcnt lgkmcnt(12)\n\t" \
    "v_mfma_f32_16x16x32_bf16 v[128:131], v[32:35], v[88:91], v[128:131]\n\t" \
    "ds_read_b64_tr_b16 v[136:137], v15 offset:28672\n\t" \
    "ds_read_b64_tr_b16 v[138:139], v15 offset:28928\n\t" \
    "s_waitcnt lgkmcnt(12)\n\t" \
    "v_mfma_f32_16x16x32_bf16 v[140:143], v[28:31], v[92:95], 0\n\t" \
    "ds_read_b64_tr_b16 v[24:25], v15 offset:30720\n\t" \
    "ds_read_b64_tr_b16 v[26:27], v15 offset:30976\n\t" \
    "s_waitcnt lgkmcnt(12)\n\t" \
    "v_mfma_f32_16x16x32_bf16 v[140:143], v[32:35], v[108:111], v[140:143]\n\t" \
    "v_cvt_pk_bf16_f32 v144, v84, v96\n\t" \
    "v_cvt_pk_bf16_f32 v152, v85, v97\n\t" \
    "s_waitcnt lgkmcnt(10)\n\t" \
    "v_mfma_f32_16x16x32_bf16 v[156:159], v[28:31], v[112:115], 0\n\t" \
    "v_cvt_pk_bf16_f32 v160, v86, v98\n\t" \
    "v_cvt_pk_bf16_f32 v164, v87, v99\n\t" \
    "s_waitcnt lgkmcnt(8)\n\t" \
    "v_mfma_f32_16x16x32_bf16 v[156:159], v[32:35], v[120:123], v[156:159]\n\t" \
    "v_cvt_pk_bf16_f32 v145, v116, v128\n\t" \
    "v_cvt_pk_bf16_f32 v153, v117, v129\n\t" \
    "s_waitcnt lgkmcnt(6)\n\t" \
    "v_mfma_f32_16x16x32_bf16 v[168:171], v[28:31], v[124:127], 0\n\t" \
    "v_cvt_pk_bf16_f32 v161, v118, v130\n\t" \
    "v_cvt_pk_bf16_f32 v165, v119, v131\n\t" \
    "s_waitcnt lgkmcnt(4)\n\t" \
    "v_mfma_f32_16x16x32_bf16 v[168:171], v[32:35], v[132:135], v[168:171]\n\t" \
    "s_waitcnt lgkmcnt(2)\n\t" \
    "v_mfma_f32_16x16x32_bf16 v[172:175], v[28:31], v[136:139], 0\n\t" \
    "s_waitcnt lgkmcnt(0)\n\t" \
    "v_mfma_f32_16x16x32_bf16 v[172:175], v[32:35], v[24:27], v[172:175]\n\t" \
    "v_cvt_pk_bf16_f32 v146, v140, v156\n\t" \
    "v_cvt_pk_bf16_f32 v154, v141, v157\n\t" \
    "v_cvt_pk_bf16_f32 v162, v142, v158\n\t" \
    "v_cvt_pk_bf16_f32 v166, v143, v159\n\t" \
    "s_nop 3\n\t" \
    "v_cvt_pk_bf16_f32 v147, v168, v172\n\t" \
    "v_cvt_pk_bf16_f32 v155, v169, v173\n\t" \
    "v_cvt_pk_bf16_f32 v163, v170, v174\n\t" \
    "v_cvt_pk_bf16_f32 v167, v171, v175\n\t" \
    ".Lh41_%=:\n\t" \
    "s_memrealtime s[22:23]\n\t" \
    "s_waitcnt lgkmcnt(0)\n\t" \
    "s_sub_u32 s22, s22, s35\n\t" \
    "s_cmp_lt_u32 s22, 370\n\t" \
    "s_cbranch_scc1 .Lh41_%=\n\t" \
    "s_cmp_eq_u32 s24, 0\n\t" \
    "s_cbranch_scc1 .Lj43_%=\n\t" \
    ".Lh42_%=:\n\t" \
    "s_memrealtime s[22:23]\n\t" \
    "s_waitcnt lgkmcnt(0)\n\t" \
    "s_sub_u32 s22, s22, s35\n\t" \
    "s_cmp_lt_u32 s22, 385\n\t" \
    "s_cbranch_scc1 .Lh42_%=\n\t" \
    ".Lj43_%=:\n\t" \
    "s_waitcnt lgkmcnt(0)\n\t" \
    "s_xor_b32 s42, s43, 0\n\t" \
    "buffer_store_dwordx4 v[144:147], v19, s[4:7], s42 offen offset:0 sc1\n\t" \
    "s_waitcnt vmcnt(1)\n\t" \
    "s_mov_b64 exec, 1\n\t" \
    "ds_add_rtn_u32 v17, v9, v9 offset:8\n\t" \
    "s_add_u32 s37, s32, 524288\n\t" \
    "s_waitcnt lgkmcnt(0)\n\t" \
    "v_cmp_eq_u32 vcc, 1098496, v17\n\t" \
    "s_mov_b64 exec, -1\n\t" \
    "s_nop 0\n\t" \
    "s_cbranch_vccz .La13_%=\n\t" \
    "buffer_store_dwordx4 v[148:151], v2, s[4:7], s37 offen sc1\n\t" \
    ".La13_%=:\n\t" \
    "s_mov_b64 exec, -1\n\t" \
    "s_xor_b32 s42, s43, 1024\n\t" \
    "buffer_store_dwordx4 v[152:155], v19, s[4:7], s42 offen offset:0 sc1\n\t" \
    "s_cmp_eq_u32 s24, 0\n\t" \
    "s_cbranch_scc1 .Lj45_%=\n\t" \
    ".Lh44_%=:\n\t" \
    "s_memrealtime s[22:23]\n\t" \
    "s_waitcnt lgkmcnt(0)\n\t" \
    "s_sub_u32 s22, s22, s35\n\t" \
    "s_cmp_lt_u32 s22, 435\n\t" \
    "s_cbranch_scc1 .Lh44_%=\n\t" \
    ".Lj45_%=:\n\t" \
    "s_waitcnt lgkmcnt(0)\n\t" \
    "s_xor_b32 s42, s43, 2048\n\t" \
    "buffer_store_dwordx4 v[160:163], v19, s[4:7], s42 offen offset:0 sc1\n\t" \
    "s_xor_b32 s42, s43, 3072\n\t" \
    "buffer_store_dwordx4 v[164:167], v19, s[4:7], s42 offen offset:0 sc1\n\t" \
    ".Lskip_%=:\n\t" \
    "s_waitcnt lgkmcnt(0)\n\t" \
    "s_nop 7\n\t" \
    ".Lh46_%=:\n\t" \
    "s_memrealtime s[22:23]\n\t" \
    "s_waitcnt lgkmcnt(0)\n\t" \
    "s_sub_u32 s22, s22, s35\n\t" \
    "s_cmp_lt_u32 s22, 410\n\t" \
    "s_cbranch_scc1 .Lh46_%=\n\t" \
    "s_and_b32 s52, s2, 7\n\t" \
    "s_lshl_b32 s52, s52, 7\n\t" \
    "s_add_u32 s52, s52, 34669568\n\t" \
    "s_buffer_load_dword s36, s[4:7], s52\n\t" \
    "s_and_b32 s37, s24, 3\n\t" \
    "s_mul_i32 s37, s37, 8486912\n\t" \
    "s_add_u32 s53, s53, s37\n\t" \
    "s_and_b32 s37, s24, 4\n\t" \
    "s_lshl_b32 s39, s37, 11\n\t" \
    "s_add_u32 s53, s53, s39\n\t" \
    "s_add_u32 s37, s37, s24\n\t" \
    "s_andn2_b32 s39, s26, 1\n\t" \
    "s_lshl3_add_u32 s37, s39, s37\n\t" \
    "s_mul_i32 s37, s37, 43008\n\t" \
    "s_mul_i32 s39, s27, 4992\n\t" \
    "s_add_u32 s51, s37, s39\n\t" \
    "s_cmp_eq_u32 s27, 8\n\t" \
    "s_movk_i32 s10, 4992\n\t" \
    "s_cselect_b32 s10, 3072, s10\n\t" \
    "s_mov_b32 s11, 0x20000\n\t" \
    "s_and_b32 s37, s2, 0x70\n\t" \
    "s_lshl_b32 s37, s37, 1\n\t" \
    "v_lshrrev_b32 v11, 7, v0\n\t" \
    "v_lshl_add_u32 v11, v11, 2, s37\n\t" \
    "v_min_u32 v11, 248, v11\n\t" \
    "v_mul_u32_u24 v46, 147, v11\n\t" \
    "v_lshrrev_b32 v46, 12, v46\n\t" \
    "v_mul_u32_u24 v41, 28, v46\n\t" \
    "v_sub_u32 v11, v11, v41\n\t" \
    "v_bfe_u32 v41, v0, 4, 3\n\t" \
    "v_min_u32 v41, 4, v41\n\t" \
    "v_add3_u32 v11, v11, v41, -1\n\t" \
    "v_add_u32 v41, 28, v11\n\t" \
    "v_min_u32 v11, v11, v41\n\t" \
    "v_and_b32 v1, 1, v11\n\t" \
    "v_lshrrev_b32 v11, 1, v11\n\t" \
    "v_mad_u32_u24 v11, v11, 9, v46\n\t" \
    "v_lshl_add_u32 v11, v1, 7, v11\n\t" \
    "v_and_b32 v3, 31, v11\n\t" \
    "v_and_b32 v50, 24, v3\n\t" \
    "v_add_u32 v3, v3, v50\n\t" \
    "v_lshrrev_b32 v50, 5, v11\n\t" \
    "v_lshlrev_b32 v50, 7, v50\n\t" \
    "v_lshl_or_b32 v51, v3, 10, v50\n\t" \
    "s_mov_b32 s40, 0\n\t" \
    "s_waitcnt lgkmcnt(0)\n\t" \
    ".Lr4_%=:\n\t" \
    "s_xor_b32 s36, s36, s34\n\t" \
    "s_andn2_b32 s37, s36, 14\n\t" \
    "s_cbranch_scc1 .Lr5_%=\n\t" \
    ".Lr6_%=:\n\t" \
    "s_bfe_u32 s36, s36, 0x30001\n\t" \
    "s_add_u32 s56, s2, s36\n\t" \
    "s_and_b32 s56, s56, 7\n\t" \
    "s_lshr_b32 s37, s2, 7\n\t" \
    "s_lshl1_add_u32 s56, s56, s37\n\t" \
    "s_and_b32 s37, s56, 7\n\t" \
    "s_mul_i32 s48, s37, 4243456\n\t" \
    "s_and_b32 s37, s56, 8\n\t" \
    "s_lshl_b32 s37, s37, 10\n\t" \
    "s_add_u32 s48, s48, s37\n\t" \
    "s_add_u32 s49, s48, 524288\n\t" \
    "s_add_u32 s50, s48, 1048576\n\t" \
    "s_xor_b32 s37, s48, 0x2000\n\t" \
    "s_add_u32 s37, s37, 2097152\n\t" \
    "s_add_u32 s45, s45, s37\n\t" \
    "s_add_u32 s46, s46, s37\n\t" \
    "s_cmp_lt_u32 s24, 4\n\t" \
    "s_cselect_b32 s47, s46, 0x80000000\n\t" \
    "v_add_u32 v17, s45, v0\n\t" \
    "v_add_u32 v19, s46, v0\n\t" \
    "s_lshr_b32 s37, s24, 2\n\t" \
    "s_mul_i32 s37, s37, 6\n\t" \
    "s_mul_i32 s39, s56, 7\n\t" \
    "s_add_u32 s37, s37, s39\n\t" \
    "s_lshr_b32 s37, s37, 4\n\t" \
    "s_and_b32 s39, s24, 3\n\t" \
    "s_mul_i32 s39, s39, 63\n\t" \
    "s_add_u32 s37, s37, s39\n\t" \
    "v_lshrrev_b32 v41, 4, v0\n\t" \
    "v_min_u32 v41, 8, v41\n\t" \
    "v_mad_u32_u24 v41, v41, 7, s37\n\t" \
    "v_add_u32 v46, 2, v41\n\t" \
    "v_lshrrev_b32 v46, 7, v46\n\t" \
    "v_lshl_add_u32 v41, v46, 1, v41\n\t" \
    "v_and_b32 v46, 31, v41\n\t" \
    "v_and_b32 v1, 24, v46\n\t" \
    "v_add_u32 v46, v46, v1\n\t" \
    "v_lshrrev_b32 v1, 5, v41\n\t" \
    "v_lshlrev_b32 v1, 7, v1\n\t" \
    "v_lshl_or_b32 v11, v46, 10, v1\n\t" \
    "v_add_u32 v18, s36, v18\n\t" \
    "v_lshlrev_b32 v18, 8, v18\n\t" \
    "v_and_b32 v18, 0x400, v18\n\t" \
    "v_xor_b32 v42, v42, v18\n\t" \
    "s_sub_u32 s37, s24, s36\n\t" \
    "s_and_b32 s37, s37, 7\n\t" \
    "s_mul_i32 s39, s27, 28\n\t" \
    "s_add_u32 s39, s39, s26\n\t" \
    "s_lshr_b32 s39, s39, 5\n\t" \
    "s_lshl4_add_u32 s37, s39, s37\n\t" \
    "s_lshl_b32 s52, s37, 7\n\t" \
    "s_andn2_b32 s37, s52, 0x1fff\n\t" \
    "s_add_u32 s52, s52, s37\n\t" \
    "s_and_b32 s37, s56, 6\n\t" \
    "s_lshl_b32 s37, s37, 15\n\t" \
    "s_and_b32 s39, s48, 0x2000\n\t" \
    "s_add_u32 s37, s37, s39\n\t" \
    "s_add_u32 s33, s37, 33554432\n\t" \
    "s_add_u32 s55, s55, s39\n\t" \
    "s_nop 0\n\t" \
    "v_readlane_b32 s12, v11, 0\n\t" \
    "v_readlane_b32 s13, v11, 1\n\t" \
    "v_readlane_b32 s14, v11, 2\n\t" \
    "v_readlane_b32 s15, v11, 3\n\t" \
    "v_readlane_b32 s16, v11, 4\n\t" \
    "v_readlane_b32 s17, v11, 5\n\t" \
    "v_readlane_b32 s18, v11, 6\n\t" \
    "v_readlane_b32 s19, v11, 7\n\t" \
    "v_readlane_b32 s3, v11, 8\n\t" \
    "s_add_u32 s12, s12, s33\n\t" \
    "s_add_u32 s13, s13, s33\n\t" \
    "s_add_u32 s14, s14, s33\n\t" \
    "s_add_u32 s15, s15, s33\n\t" \
    "s_add_u32 s16, s16, s33\n\t" \
    "s_add_u32 s17, s17, s33\n\t" \
    "s_add_u32 s18, s18, s33\n\t" \
    "s_add_u32 s19, s19, s33\n\t" \
    "s_add_u32 s3, s3, s33\n\t" \
    "s_waitcnt vmcnt(0)\n\t" \
    "s_mov_b64 exec, 1\n\t" \
    "ds_add_rtn_u32 v3, v9, v9 offset:12\n\t" \
    "s_add_u32 s37, s32, 786432\n\t" \
    "s_waitcnt lgkmcnt(0)\n\t" \
    "v_cmp_eq_u32 vcc, 1098496, v3\n\t" \
    "s_mov_b64 exec, -1\n\t" \
    "s_nop 0\n\t" \
    "s_cbranch_vccz .La22_%=\n\t" \
    "buffer_store_dwordx4 v[148:151], v2, s[4:7], s37 offen sc1\n\t" \
    ".La22_%=:\n\t" \
    "s_mov_b64 exec, -1\n\t" \
    ".Lh47_%=:\n\t" \
    "s_memrealtime s[22:23]\n\t" \
    "s_waitcnt lgkmcnt(0)\n\t" \
    "s_sub_u32 s22, s22, s35\n\t" \
    "s_cmp_lt_u32 s22, 485\n\t" \
    "s_cbranch_scc1 .Lh47_%=\n\t" \
    "s_mov_b32 s37, 34670592\n\t" \
    "s_buffer_load_dword s56, s[4:7], s37 offset:0x0\n\t" \
    "s_buffer_load_dword s57, s[4:7], s37 offset:0x80\n\t" \
    "s_buffer_load_dword s58, s[4:7], s37 offset:0x100\n\t" \
    "s_buffer_load_dword s59, s[4:7], s37 offset:0x180\n\t" \
    "s_buffer_load_dword s60, s[4:7], s37 offset:0x200\n\t" \
    "s_buffer_load_dword s61, s[4:7], s37 offset:0x280\n\t" \
    "s_cmp_lg_u32 s24, 0\n\t" \
    "s_cbranch_scc1 .Lb15_%=\n\t" \
    "s_mov_b32 s40, 0\n\t" \
    "s_add_u32 s38, s33, 524288\n\t" \
    ".Ll15_%=:\n\t" \
    "buffer_load_dword v50, v51, s[4:7], s38 offen sc1\n\t" \
    "s_waitcnt vmcnt(0)\n\t" \
    "v_cmp_ne_u32 vcc, s34, v50\n\t" \
    "s_add_u32 s40, s40, 1\n\t" \
    "s_cmp_ge_u32 s40, 65536\n\t" \
    "s_cbranch_scc1 .Lb15_%=\n\t" \
    "s_cbranch_vccz .Lb15_%=\n\t" \
    "s_sleep 2\n\t" \
    "s_branch .Ll15_%=\n\t" \
    ".Lb15_%=:\n\t" \
    "s_waitcnt lgkmcnt(0)\n\t" \
    "s_barrier\n\t" \
    "buffer_load_dwordx4 v[176:179], v36, s[4:7], s48 offen offset:0 sc1\n\t" \
    "buffer_load_dwordx4 v[180:183], v36, s[4:7], s48 offen offset:1024 sc1\n\t" \
    "buffer_load_dwordx4 v[184:187], v36, s[4:7], s48 offen offset:256 sc1\n\t" \
    "buffer_load_dwordx4 v[188:191], v36, s[4:7], s48 offen offset:1280 sc1\n\t" \
    "buffer_load_dwordx4 v[60:63], v37, s[4:7], s48 offen offset:0 sc1\n\t" \
    "s_cmp_lt_u32 s24, 4\n\t" \
    "s_cbranch_scc1 .Lp48_%=\n\t" \
    "s_setprio 1\n\t" \
    ".Lp48_%=:\n\t" \
    "s_mov_b32 s40, 0\n\t" \
    "s_waitcnt lgkmcnt(0)\n\t" \
    ".Lq4_%=:\n\t" \
    "s_mov_b32 s37, 0\n\t" \
    "s_mov_b32 s0, 0\n\t" \
    "s_xor_b32 s61, s61, s34\n\t" \
    "s_or_b32 s37, s37, s61\n\t" \
    "s_lshl1_add_u32 s0, s0, s61\n\t" \
    "s_xor_b32 s60, s60, s34\n\t" \
    "s_or_b32 s37, s37, s60\n\t" \
    "s_lshl1_add_u32 s0, s0, s60\n\t" \
    "s_xor_b32 s59, s59, s34\n\t" \
    "s_or_b32 s37, s37, s59\n\t" \
    "s_lshl1_add_u32 s0, s0, s59\n\t" \
    "s_xor_b32 s58, s58, s34\n\t" \
    "s_or_b32 s37, s37, s58\n\t" \
    "s_lshl1_add_u32 s0, s0, s58\n\t" \
    "s_xor_b32 s57, s57, s34\n\t" \
    "s_or_b32 s37, s37, s57\n\t" \
    "s_lshl1_add_u32 s0, s0, s57\n\t" \
    "s_xor_b32 s56, s56, s34\n\t" \
    "s_or_b32 s37, s37, s56\n\t" \
    "s_lshl1_add_u32 s0, s0, s56\n\t" \
    "s_andn2_b32 s37, s37, 2\n\t" \
    "s_cbranch_scc1 .Lq5_%=\n\t" \
    ".Lq6_%=:\n\t" \
    "s_lshr_b32 s0, s0, 1\n\t" \
    "s_add_u32 s37, s36, 3\n\t" \
    "s_bitcmp1_b32 s37, 2\n\t" \
    "s_cselect_b32 s37, 51, 12\n\t" \
    "s_xor_b32 s0, s0, s37\n\t" \
    "v_add_u32 v41, 344064, v39\n\t" \
    "v_lshrrev_b32 v46, 21, v41\n\t" \
    "v_bfe_u32 v46, s0, v46, 1\n\t" \
    "v_lshrrev_b32 v1, 3, v41\n\t" \
    "v_lshlrev_b32 v46, 10, v46\n\t" \
    "v_and_b32 v1, 0x400, v1\n\t" \
    "v_xor_b32 v46, v46, v1\n\t" \
    "v_xor_b32 v18, v42, v46\n\t" \
    "v_lshrrev_b32 v46, 21, v39\n\t" \
    "v_bfe_u32 v46, s0, v46, 1\n\t" \
    "v_lshrrev_b32 v1, 3, v39\n\t" \
    "v_lshlrev_b32 v46, 10, v46\n\t" \
    "v_and_b32 v1, 0x400, v1\n\t" \
    "v_xor_b32 v46, v46, v1\n\t" \
    "v_xor_b32 v42, v42, v46\n\t" \
    "s_lshr_b32 s37, s10, 1\n\t" \
    "s_add_u32 s37, s37, s51\n\t" \
    "s_and_b32 s39, s8, 0x1fffff\n\t" \
    "s_add_u32 s37, s37, s39\n\t" \
    "s_lshr_b32 s39, s37, 21\n\t" \
    "s_lshr_b32 s39, s0, s39\n\t" \
    "s_xor_b32 s37, s37, s48\n\t" \
    "s_lshr_b32 s37, s37, 13\n\t" \
    "s_xor_b32 s37, s37, s39\n\t" \
    "s_and_b32 s37, s37, 1\n\t" \
    "s_mul_i32 s37, s37, 172032\n\t" \
    "s_add_u32 s37, s37, s51\n\t" \
    "s_add_u32 s8, s8, s37\n\t" \
    "s_addc_u32 s9, s9, 0\n\t" \
    "s_waitcnt vmcnt(4)\n\t" \
    "v_mfma_f32_32x32x16_bf16 v[192:207], v[52:55], v[176:179], 0\n\t" \
    "buffer_load_dwordx4 v[4:7], v36, s[4:7], s49 offen offset:0 sc1\n\t" \
    "s_waitcnt vmcnt(4)\n\t" \
    "v_mfma_f32_32x32x16_bf16 v[192:207], v[56:59], v[180:183], v[192:207]\n\t" \
    "buffer_load_dwordx4 v[20:23], v36, s[4:7], s49 offen offset:1024 sc1\n\t" \
    "s_waitcnt vmcnt(4)\n\t" \
    "v_mfma_f32_32x32x16_bf16 v[208:223], v[52:55], v[184:187], 0\n\t" \
    "buffer_load_dwordx4 v[64:67], v36, s[4:7], s49 offen offset:256 sc1\n\t" \
    "s_waitcnt vmcnt(4)\n\t" \
    "v_mfma_f32_32x32x16_bf16 v[208:223], v[56:59], v[188:191], v[208:223]\n\t" \
    "buffer_load_dwordx4 v[68:71], v36, s[4:7], s49 offen offset:1280 sc1\n\t" \
    "v_mfma_f32_32x32x16_bf16 v[224:239], v[56:59], v[176:179], 0\n\t" \
    "s_add_u32 m0, s44, 0\n\t" \
    "s_nop 8\n\t" \
    "v_cvt_pk_bf16_f32 v9, v192, v208\n\t" \
    "v_cvt_pk_bf16_f32 v3, v193, v209\n\t" \
    "v_cvt_pk_bf16_f32 v51, v194, v210\n\t" \
    "v_cvt_pk_bf16_f32 v50, v195, v211\n\t" \
    "v_cvt_pk_bf16_f32 v8, v196, v212\n\t" \
    "s_waitcnt vmcnt(4)\n\t" \
    "v_permlane16_swap_b32 v60, v180\n\t" \
    "v_permlane16_swap_b32 v61, v181\n\t" \
    "ds_write_addtid_b32 v9 offset:3616\n\t" \
    "ds_write_addtid_b32 v3 offset:3872\n\t" \
    "ds_write_addtid_b32 v51 offset:4128\n\t" \
    "ds_write_addtid_b32 v50 offset:4384\n\t" \
    "ds_write_addtid_b32 v8 offset:4640\n\t" \
    "v_permlane16_swap_b32 v62, v182\n\t" \
    "v_permlane16_swap_b32 v63, v183\n\t" \
    "v_cvt_pk_bf16_f32 v16, v197, v213\n\t" \
    "v_cvt_pk_bf16_f32 v41, v198, v214\n\t" \
    "v_cvt_pk_bf16_f32 v39, v199, v215\n\t" \
    "v_cvt_pk_bf16_f32 v1, v200, v216\n\t" \
    "v_cvt_pk_bf16_f32 v46, v201, v217\n\t" \
    "v_mfma_f32_32x32x16_bf16 v[240:255], v[56:59], v[184:187], 0\n\t" \
    "v_permlane16_swap_b32 v180, v188\n\t" \
    "buffer_load_dwordx4 v[72:75], v37, s[4:7], s49 offen offset:0 sc1\n\t" \
    "ds_write_addtid_b32 v16 offset:4896\n\t" \
    "ds_write_addtid_b32 v41 offset:5152\n\t" \
    "ds_write_addtid_b32 v39 offset:5408\n\t" \
    "ds_write_addtid_b32 v1 offset:5664\n\t" \
    "ds_write_addtid_b32 v46 offset:5920\n\t" \
    "v_permlane16_swap_b32 v181, v189\n\t" \
    "v_permlane16_swap_b32 v182, v190\n\t" \
    "v_cvt_pk_bf16_f32 v9, v202, v218\n\t" \
    "v_cvt_pk_bf16_f32 v3, v203, v219\n\t" \
    "v_cvt_pk_bf16_f32 v51, v204, v220\n\t" \
    "v_cvt_pk_bf16_f32 v50, v205, v221\n\t" \
    "v_permlane16_swap_b32 v183, v191\n\t" \
    "v_mfma_f32_32x32x16_bf16 v[224:239], v[52:55], v[60:63], v[224:239]\n\t" \
    "ds_write_addtid_b32 v9 offset:6176\n\t" \
    "ds_write_addtid_b32 v3 offset:6432\n\t" \
    "ds_write_addtid_b32 v51 offset:6688\n\t" \
    "ds_write_addtid_b32 v50 offset:6944\n\t" \
    "v_mfma_f32_32x32x16_bf16 v[240:255], v[52:55], v[180:183], v[240:255]\n\t" \
    ".Lh49_%=:\n\t" \
    "s_memrealtime s[22:23]\n\t" \
    "s_waitcnt lgkmcnt(0)\n\t" \
    "s_sub_u32 s22, s22, s35\n\t" \
    "s_cmp_lt_u32 s22, 597\n\t" \
    "s_cbranch_scc1 .Lh49_%=\n\t" \
    "s_buffer_load_dword s12, s[4:7], s12 offset:0xc0000\n\t" \
    "s_buffer_load_dword s13, s[4:7], s13 offset:0xc0000\n\t" \
    "s_buffer_load_dword s14, s[4:7], s14 offset:0xc0000\n\t" \
    "s_buffer_load_dword s15, s[4:7], s15 offset:0xc0000\n\t" \
    "s_buffer_load_dword s16, s[4:7], s16 offset:0xc0000\n\t" \
    "s_buffer_load_dword s17, s[4:7], s17 offset:0xc0000\n\t" \
    "s_buffer_load_dword s18, s[4:7], s18 offset:0xc0000\n\t" \
    "s_buffer_load_dword s19, s[4:7], s19 offset:0xc0000\n\t" \
    "s_buffer_load_dword s3, s[4:7], s3 offset:0xc0000\n\t" \
    "s_waitcnt vmcnt(4)\n\t" \
    "v_mfma_f32_32x32x16_bf16 v[192:207], v[52:55], v[4:7], 0\n\t" \
    "v_cvt_pk_bf16_f32 v8, v224, v240\n\t" \
    "v_cvt_pk_bf16_f32 v16, v225, v241\n\t" \
    "v_cvt_pk_bf16_f32 v41, v226, v242\n\t" \
    "v_cvt_pk_bf16_f32 v39, v227, v243\n\t" \
    "v_cvt_pk_bf16_f32 v1, v228, v244\n\t" \
    "buffer_load_dwordx4 v[76:79], v36, s[4:7], s50 offen offset:0 sc1\n\t" \
    "ds_write_addtid_b32 v8 offset:0\n\t" \
    "ds_write_addtid_b32 v16 offset:256\n\t" \
    "ds_write_addtid_b32 v41 offset:512\n\t" \
    "ds_write_addtid_b32 v39 offset:768\n\t" \
    "ds_write_addtid_b32 v1 offset:1024\n\t" \
    "s_waitcnt vmcnt(4)\n\t" \
    "v_mfma_f32_32x32x16_bf16 v[192:207], v[56:59], v[20:23], v[192:207]\n\t" \
    "buffer_load_dwordx4 v[80:83], v36, s[4:7], s50 offen offset:1024 sc1\n\t" \
    "v_cvt_pk_bf16_f32 v46, v229, v245\n\t" \
    "v_cvt_pk_bf16_f32 v9, v230, v246\n\t" \
    "v_cvt_pk_bf16_f32 v3, v231, v247\n\t" \
    "v_cvt_pk_bf16_f32 v51, v232, v248\n\t" \
    "v_cvt_pk_bf16_f32 v50, v233, v249\n\t" \
    "s_waitcnt vmcnt(4)\n\t" \
    "v_mfma_f32_32x32x16_bf16 v[208:223], v[52:55], v[64:67], 0\n\t" \
    "buffer_load_dwordx4 v[100:103], v36, s[4:7], s50 offen offset:256 sc1\n\t" \
    "ds_write_addtid_b32 v46 offset:1280\n\t" \
    "ds_write_addtid_b32 v9 offset:1536\n\t" \
    "ds_write_addtid_b32 v3 offset:1792\n\t" \
    "ds_write_addtid_b32 v51 offset:2048\n\t" \
    "ds_write_addtid_b32 v50 offset:2304\n\t" \
    "v_cvt_pk_bf16_f32 v8, v234, v250\n\t" \
    "v_cvt_pk_bf16_f32 v16, v235, v251\n\t" \
    "v_cvt_pk_bf16_f32 v41, v236, v252\n\t" \
    "v_cvt_pk_bf16_f32 v39, v237, v253\n\t" \
    "buffer_load_dwordx4 v[104:107], v36, s[4:7], s50 offen offset:1280 sc1\n\t" \
    "s_waitcnt vmcnt(5)\n\t" \
    "v_mfma_f32_32x32x16_bf16 v[208:223], v[56:59], v[68:71], v[208:223]\n\t" \
    "ds_write_addtid_b32 v8 offset:2560\n\t" \
    "ds_write_addtid_b32 v16 offset:2816\n\t" \
    "ds_write_addtid_b32 v41 offset:3072\n\t" \
    "ds_write_addtid_b32 v39 offset:3328\n\t" \
    "buffer_load_dwordx4 v[88:91], v37, s[4:7], s50 offen offset:0 sc1\n\t" \
    "s_waitcnt lgkmcnt(0)\n\t" \
    "s_xor_b32 s12, s12, s34\n\t" \
    "s_xor_b32 s13, s13, s34\n\t" \
    "s_xor_b32 s14, s14, s34\n\t" \
    "s_xor_b32 s15, s15, s34\n\t" \
    "s_xor_b32 s16, s16, s34\n\t" \
    "s_xor_b32 s17, s17, s34\n\t" \
    "s_xor_b32 s18, s18, s34\n\t" \
    "s_xor_b32 s19, s19, s34\n\t" \
    "s_xor_b32 s3, s3, s34\n\t" \
    "s_or_b32 s12, s12, s13\n\t" \
    "s_or_b32 s12, s12, s14\n\t" \
    "s_or_b32 s12, s12, s15\n\t" \
    "s_or_b32 s12, s12, s16\n\t" \
    "s_or_b32 s12, s12, s17\n\t" \
    "s_or_b32 s12, s12, s18\n\t" \
    "s_or_b32 s12, s12, s19\n\t" \
    "s_or_b32 s12, s12, s3\n\t" \
    "s_cbranch_scc1 .Lq1025_%=\n\t" \
    ".Lb24_%=:\n\t" \
    "s_waitcnt lgkmcnt(0)\n\t" \
    "s_barrier\n\t" \
    "ds_read_b128 v[92:95], v44 offset:0\n\t" \
    "s_mov_b32 s45, 0\n\t" \
    "buffer_load_dwordx4 v[12:15], v17, s[4:7], s45 offen offset:0 sc1\n\t" \
    "ds_read_b128 v[108:111], v44 offset:128\n\t" \
    "ds_read_b128 v[112:115], v44 offset:29184\n\t" \
    "ds_read_b128 v[84:87], v44 offset:29312\n\t" \
    "ds_read_b128 v[96:99], v45 offset:0\n\t" \
    "buffer_load_dwordx4 v[120:123], v17, s[4:7], s45 offen offset:1024 sc1\n\t" \
    "ds_read_b128 v[124:127], v45 offset:128\n\t" \
    "ds_read_b128 v[116:119], v45 offset:29184\n\t" \
    "ds_read_b128 v[128:131], v45 offset:29312\n\t" \
    "v_mfma_f32_32x32x16_bf16 v[224:239], v[56:59], v[4:7], 0\n\t" \
    "v_cvt_pk_bf16_f32 v46, v192, v208\n\t" \
    "v_cvt_pk_bf16_f32 v9, v193, v209\n\t" \
    "v_cvt_pk_bf16_f32 v3, v194, v210\n\t" \
    "v_cvt_pk_bf16_f32 v51, v195, v211\n\t" \
    "v_cvt_pk_bf16_f32 v50, v196, v212\n\t" \
    "s_waitcnt vmcnt(7)\n\t" \
    "v_permlane16_swap_b32 v72, v20\n\t" \
    "v_permlane16_swap_b32 v73, v21\n\t" \
    "s_mov_b32 s46, 114688\n\t" \
    "buffer_load_dwordx4 v[132:135], v17, s[4:7], s46 offen offset:0 sc1\n\t" \
    "ds_write_addtid_b32 v46 offset:61984\n\t" \
    "ds_write_addtid_b32 v9 offset:62240\n\t" \
    "ds_write_addtid_b32 v3 offset:62496\n\t" \
    "ds_write_addtid_b32 v51 offset:62752\n\t" \
    "ds_write_addtid_b32 v50 offset:63008\n\t" \
    "v_permlane16_swap_b32 v74, v22\n\t" \
    "v_permlane16_swap_b32 v75, v23\n\t" \
    "v_cvt_pk_bf16_f32 v36, v197, v213\n\t" \
    "v_cvt_pk_bf16_f32 v8, v198, v214\n\t" \
    "v_cvt_pk_bf16_f32 v16, v199, v215\n\t" \
    "v_cvt_pk_bf16_f32 v41, v200, v216\n\t" \
    "v_cvt_pk_bf16_f32 v39, v201, v217\n\t" \
    "v_mfma_f32_32x32x16_bf16 v[240:255], v[56:59], v[64:67], 0\n\t" \
    "v_permlane16_swap_b32 v20, v68\n\t" \
    "buffer_load_dwordx4 v[28:31], v17, s[4:7], s46 offen offset:1024 sc1\n\t" \
    "ds_write_addtid_b32 v36 offset:63264\n\t" \
    "ds_write_addtid_b32 v8 offset:63520\n\t" \
    "ds_write_addtid_b32 v16 offset:63776\n\t" \
    "ds_write_addtid_b32 v41 offset:64032\n\t" \
    "ds_write_addtid_b32 v39 offset:64288\n\t" \
    "v_permlane16_swap_b32 v21, v69\n\t" \
    "v_permlane16_swap_b32 v22, v70\n\t" \
    "v_cvt_pk_bf16_f32 v37, v202, v218\n\t" \
    "v_cvt_pk_bf16_f32 v11, v203, v219\n\t" \
    "v_cvt_pk_bf16_f32 v1, v204, v220\n\t" \
    "v_cvt_pk_bf16_f32 v46, v205, v221\n\t" \
    "v_permlane16_swap_b32 v23, v71\n\t" \
    "v_mfma_f32_32x32x16_bf16 v[224:239], v[52:55], v[72:75], v[224:239]\n\t" \
    "buffer_load_dwordx4 v[136:139], v19, s[4:7], s45 offen offset:0 sc1\n\t" \
    "ds_write_addtid_b32 v37 offset:64544\n\t" \
    "ds_write_addtid_b32 v11 offset:64800\n\t" \
    "ds_write_addtid_b32 v1 offset:65056\n\t" \
    "ds_write_addtid_b32 v46 offset:65312\n\t" \
    "v_mfma_f32_32x32x16_bf16 v[240:255], v[52:55], v[20:23], v[240:255]\n\t" \
    "s_waitcnt vmcnt(9)\n\t" \
    "v_mfma_f32_32x32x16_bf16 v[192:207], v[52:55], v[76:79], 0\n\t" \
    "s_nop 9\n\t" \
    "v_cvt_pk_bf16_f32 v9, v224, v240\n\t" \
    "v_cvt_pk_bf16_f32 v3, v225, v241\n\t" \
    "v_cvt_pk_bf16_f32 v51, v226, v242\n\t" \
    "v_cvt_pk_bf16_f32 v50, v227, v243\n\t" \
    "v_cvt_pk_bf16_f32 v36, v228, v244\n\t" \
    "buffer_load_dwordx4 v[24:27], v19, s[4:7], s46 offen offset:0 sc1\n\t" \
    "ds_write_addtid_b32 v9 offset:58368\n\t" \
    "ds_write_addtid_b32 v3 offset:58624\n\t" \
    "ds_write_addtid_b32 v51 offset:58880\n\t" \
    "ds_write_addtid_b32 v50 offset:59136\n\t" \
    "ds_write_addtid_b32 v36 offset:59392\n\t" \
    "s_waitcnt vmcnt(9)\n\t" \
    "v_mfma_f32_32x32x16_bf16 v[192:207], v[56:59], v[80:83], v[192:207]\n\t" \
    "v_cvt_pk_bf16_f32 v8, v229, v245\n\t" \
    "v_cvt_pk_bf16_f32 v16, v230, v246\n\t" \
    "v_cvt_pk_bf16_f32 v41, v231, v247\n\t" \
    "v_cvt_pk_bf16_f32 v39, v232, v248\n\t" \
    "v_cvt_pk_bf16_f32 v37, v233, v249\n\t" \
    "s_mov_b32 s47, 229376\n\t" \
    "buffer_load_dwordx4 v[32:35], v17, s[4:7], s47 offen offset:0 sc1\n\t" \
    "s_waitcnt vmcnt(9)\n\t" \
    "v_mfma_f32_32x32x16_bf16 v[208:223], v[52:55], v[100:103], 0\n\t" \
    "ds_write_addtid_b32 v8 offset:59648\n\t" \
    "ds_write_addtid_b32 v16 offset:59904\n\t" \
    "ds_write_addtid_b32 v41 offset:60160\n\t" \
    "ds_write_addtid_b32 v39 offset:60416\n\t" \
    "ds_write_addtid_b32 v37 offset:60672\n\t" \
    "buffer_load_dwordx4 v[140:143], v17, s[4:7], s47 offen offset:1024 sc1\n\t" \
    "v_cvt_pk_bf16_f32 v11, v234, v250\n\t" \
    "v_cvt_pk_bf16_f32 v1, v235, v251\n\t" \
    "v_cvt_pk_bf16_f32 v46, v236, v252\n\t" \
    "v_cvt_pk_bf16_f32 v9, v237, v253\n\t" \
    "s_waitcnt vmcnt(9)\n\t" \
    "v_mfma_f32_32x32x16_bf16 v[208:223], v[56:59], v[104:107], v[208:223]\n\t" \
    "ds_write_addtid_b32 v11 offset:60928\n\t" \
    "ds_write_addtid_b32 v1 offset:61184\n\t" \
    "ds_write_addtid_b32 v46 offset:61440\n\t" \
    "ds_write_addtid_b32 v9 offset:61696\n\t" \
    "buffer_load_dwordx4 v[156:159], v19, s[4:7], s47 offen offset:0 sc1\n\t" \
    "s_waitcnt vmcnt(9)\n\t" \
    "s_waitcnt lgkmcnt(0)\n\t" \
    "s_barrier\n\t" \
    "s_memrealtime s[64:65]\n\t" \
    "s_cmp_lg_u32 s24, 7\n\t" \
    "s_cbranch_scc1 .Lar_%=\n\t" \
    "s_waitcnt lgkmcnt(0)\n\t" \
    "v_mov_b32 v149, s64\n\t" \
    "s_and_b32 s37, s48, 8192\n\t" \
    "s_add_u32 s37, s37, s32\n\t" \
    "s_add_u32 s37, s37, 1048576\n\t" \
    "s_mov_b64 exec, 0xff\n\t" \
    "buffer_store_dwordx4 v[148:151], v0, s[4:7], s37 offen sc1\n\t" \
    "s_mov_b64 exec, -1\n\t" \
    ".Lar_%=:\n\t" \
    "v_add_u32 v44, 58368, v44\n\t" \
    "s_bitcmp1_b32 s24, 2\n\t" \
    "s_cbranch_scc1 .Ly29_%=\n\t" \
    "buffer_load_dwordx4 v[168:171], v19, s[4:7], s45 offen offset:1024 sc1\n\t" \
    ".Ly29_%=:\n\t" \
    "v_add_u32 v45, 58368, v45\n\t" \
    "ds_read_b128 v[172:175], v44 offset:0\n\t" \
    "ds_read_b128 v[144:147], v44 offset:128\n\t" \
    "ds_read_b128 v[152:155], v44 offset:29184\n\t" \
    "ds_read_b128 v[160:163], v44 offset:29312\n\t" \
    "s_bitcmp1_b32 s24, 2\n\t" \
    "s_cbranch_scc1 .Ly30_%=\n\t" \
    "buffer_load_dwordx4 v[164:167], v19, s[4:7], s46 offen offset:1024 sc1\n\t" \
    ".Ly30_%=:\n\t" \
    "ds_read_b128 v[176:179], v45 offset:0\n\t" \
    "ds_read_b128 v[184:187], v45 offset:128\n\t" \
    "ds_read_b128 v[188:191], v45 offset:29184\n\t" \
    "ds_read_b128 v[60:63], v45 offset:29312\n\t" \
    "v_mfma_f32_32x32x16_bf16 v[224:239], v[56:59], v[76:79], 0\n\t" \
    "v_cvt_pk_bf16_f32 v3, v192, v208\n\t" \
    "v_cvt_pk_bf16_f32 v51, v193, v209\n\t" \
    "v_cvt_pk_bf16_f32 v50, v194, v210\n\t" \
    "v_cvt_pk_bf16_f32 v36, v195, v211\n\t" \
    "v_cvt_pk_bf16_f32 v8, v196, v212\n\t" \
    "s_waitcnt vmcnt(8)\n\t" \
    "v_mfma_f32_16x16x32_bf16 v[180:183], v[12:15], v[92:95], 0\n\t" \
    "v_permlane16_swap_b32 v88, v80\n\t" \
    "s_bitcmp1_b32 s24, 2\n\t" \
    "s_cbranch_scc1 .Ly31_%=\n\t" \
    "buffer_load_dwordx4 v[4:7], v19, s[4:7], s47 offen offset:1024 sc1\n\t" \
    ".Ly31_%=:\n\t" \
    "v_permlane16_swap_b32 v89, v81\n\t" \
    "ds_write_addtid_b32 v3 offset:3616\n\t" \
    "ds_write_addtid_b32 v51 offset:3872\n\t" \
    "ds_write_addtid_b32 v50 offset:4128\n\t" \
    "ds_write_addtid_b32 v36 offset:4384\n\t" \
    "ds_write_addtid_b32 v8 offset:4640\n\t" \
    "v_mfma_f32_16x16x32_bf16 v[64:67], v[12:15], v[108:111], 0\n\t" \
    "v_permlane16_swap_b32 v90, v82\n\t" \
    "v_mfma_f32_16x16x32_bf16 v[68:71], v[12:15], v[112:115], 0\n\t" \
    "v_permlane16_swap_b32 v91, v83\n\t" \
    "v_cvt_pk_bf16_f32 v16, v197, v213\n\t" \
    "v_cvt_pk_bf16_f32 v41, v198, v214\n\t" \
    "v_cvt_pk_bf16_f32 v39, v199, v215\n\t" \
    "v_cvt_pk_bf16_f32 v37, v200, v216\n\t" \
    "v_cvt_pk_bf16_f32 v11, v201, v217\n\t" \
    "s_mov_b32 s45, 344064\n\t" \
    "buffer_load_dwordx4 v[72:75], v17, s[4:7], s45 offen offset:0 sc1\n\t" \
    "v_mfma_f32_32x32x16_bf16 v[240:255], v[56:59], v[100:103], 0\n\t" \
    "v_mfma_f32_16x16x32_bf16 v[20:23], v[12:15], v[84:87], 0\n\t" \
    "v_permlane16_swap_b32 v80, v104\n\t" \
    "ds_write_addtid_b32 v16 offset:4896\n\t" \
    "ds_write_addtid_b32 v41 offset:5152\n\t" \
    "ds_write_addtid_b32 v39 offset:5408\n\t" \
    "ds_write_addtid_b32 v37 offset:5664\n\t" \
    "ds_write_addtid_b32 v11 offset:5920\n\t" \
    "s_waitcnt vmcnt(8)\n\t" \
    "v_mfma_f32_16x16x32_bf16 v[148:151], v[120:123], v[108:111], 0\n\t" \
    "v_permlane16_swap_b32 v81, v105\n\t" \
    "buffer_load_dwordx4 v[76:79], v17, s[4:7], s45 offen offset:1024 sc1\n\t" \
    "v_permlane16_swap_b32 v82, v106\n\t" \
    "v_cvt_pk_bf16_f32 v1, v202, v218\n\t" \
    "v_cvt_pk_bf16_f32 v46, v203, v219\n\t" \
    "v_cvt_pk_bf16_f32 v9, v204, v220\n\t" \
    "v_cvt_pk_bf16_f32 v3, v205, v221\n\t" \
    "v_mfma_f32_16x16x32_bf16 v[64:67], v[120:123], v[92:95], v[64:67]\n\t" \
    "v_permlane16_swap_b32 v83, v107\n\t" \
    "v_mfma_f32_16x16x32_bf16 v[56:59], v[120:123], v[84:87], 0\n\t" \
    "v_mfma_f32_32x32x16_bf16 v[224:239], v[52:55], v[88:91], v[224:239]\n\t" \
    "ds_write_addtid_b32 v1 offset:6176\n\t" \
    "ds_write_addtid_b32 v46 offset:6432\n\t" \
    "ds_write_addtid_b32 v9 offset:6688\n\t" \
    "ds_write_addtid_b32 v3 offset:6944\n\t" \
    "s_mov_b32 s46, 458752\n\t" \
    "buffer_load_dwordx4 v[100:103], v17, s[4:7], s46 offen offset:0 sc1\n\t" \
    "v_mfma_f32_32x32x16_bf16 v[240:255], v[52:55], v[80:83], v[240:255]\n\t" \
    "v_mfma_f32_16x16x32_bf16 v[20:23], v[120:123], v[112:115], v[20:23]\n\t" \
    "s_nop 10\n\t" \
    "v_cvt_pk_bf16_f32 v51, v224, v240\n\t" \
    "v_cvt_pk_bf16_f32 v50, v225, v241\n\t" \
    "v_cvt_pk_bf16_f32 v36, v226, v242\n\t" \
    "v_cvt_pk_bf16_f32 v8, v227, v243\n\t" \
    "v_cvt_pk_bf16_f32 v16, v228, v244\n\t" \
    "s_waitcnt vmcnt(9) lgkmcnt(15)\n\t" \
    "v_mfma_f32_16x16x32_bf16 v[180:183], v[132:135], v[172:175], v[180:183]\n\t" \
    "buffer_load_dwordx4 v[12:15], v17, s[4:7], s46 offen offset:1024 sc1\n\t" \
    "v_mfma_f32_16x16x32_bf16 v[64:67], v[132:135], v[144:147], v[64:67]\n\t" \
    "ds_write_addtid_b32 v51 offset:0\n\t" \
    "ds_write_addtid_b32 v50 offset:256\n\t" \
    "ds_write_addtid_b32 v36 offset:512\n\t" \
    "ds_write_addtid_b32 v8 offset:768\n\t" \
    "ds_write_addtid_b32 v16 offset:1024\n\t" \
    "v_mfma_f32_16x16x32_bf16 v[68:71], v[132:135], v[152:155], v[68:71]\n\t" \
    "v_cvt_pk_bf16_f32 v41, v229, v245\n\t" \
    "v_cvt_pk_bf16_f32 v39, v230, v246\n\t" \
    "v_cvt_pk_bf16_f32 v37, v231, v247\n\t" \
    "v_cvt_pk_bf16_f32 v11, v232, v248\n\t" \
    "v_cvt_pk_bf16_f32 v1, v233, v249\n\t" \
    "s_mov_b32 s47, 573440\n\t" \
    "buffer_load_dwordx4 v[192:195], v17, s[4:7], s47 offen offset:0 sc1\n\t" \
    "v_mfma_f32_16x16x32_bf16 v[20:23], v[132:135], v[160:163], v[20:23]\n\t" \
    "ds_write_addtid_b32 v41 offset:1280\n\t" \
    "ds_write_addtid_b32 v39 offset:1536\n\t" \
    "ds_write_addtid_b32 v37 offset:1792\n\t" \
    "ds_write_addtid_b32 v11 offset:2048\n\t" \
    "ds_write_addtid_b32 v1 offset:2304\n\t" \
    "s_waitcnt vmcnt(10)\n\t" \
    "v_mfma_f32_16x16x32_bf16 v[148:151], v[28:31], v[144:147], v[148:151]\n\t" \
    "buffer_load_dwordx4 v[196:199], v17, s[4:7], s47 offen offset:1024 sc1\n\t" \
    "v_mfma_f32_16x16x32_bf16 v[64:67], v[28:31], v[172:175], v[64:67]\n\t" \
    "v_cvt_pk_bf16_f32 v46, v234, v250\n\t" \
    "v_cvt_pk_bf16_f32 v9, v235, v251\n\t" \
    "v_cvt_pk_bf16_f32 v3, v236, v252\n\t" \
    "v_cvt_pk_bf16_f32 v51, v237, v253\n\t" \
    "v_mfma_f32_16x16x32_bf16 v[56:59], v[28:31], v[160:163], v[56:59]\n\t" \
    "ds_write_addtid_b32 v46 offset:2560\n\t" \
    "ds_write_addtid_b32 v9 offset:2816\n\t" \
    "ds_write_addtid_b32 v3 offset:3072\n\t" \
    "ds_write_addtid_b32 v51 offset:3328\n\t" \
    "buffer_load_dwordx4 v[200:203], v19, s[4:7], s45 offen offset:0 sc1\n\t" \
    "v_mfma_f32_16x16x32_bf16 v[20:23], v[28:31], v[152:155], v[20:23]\n\t" \
    "s_waitcnt lgkmcnt(0)\n\t" \
    "s_barrier\n\t" \
    "ds_read_b128 v[204:207], v10 offset:2048\n\t" \
    "v_mov_b32 v50, 0x10000\n\t" \
    "s_waitcnt vmcnt(11)\n\t" \
    "v_mfma_f32_16x16x32_bf16 v[208:211], v[136:139], v[96:99], 0\n\t" \
    "v_subrev_u32 v44, 58368, v44\n\t" \
    "v_mfma_f32_16x16x32_bf16 v[212:215], v[136:139], v[124:127], 0\n\t" \
    "v_subrev_u32 v45, 58368, v45\n\t" \
    "v_mfma_f32_16x16x32_bf16 v[216:219], v[136:139], v[116:119], 0\n\t" \
    "ds_read_b128 v[220:223], v44 offset:0\n\t" \
    "buffer_load_dwordx4 v[104:107], v19, s[4:7], s46 offen offset:0 sc1\n\t" \
    "v_mfma_f32_16x16x32_bf16 v[88:91], v[136:139], v[128:131], 0\n\t" \
    "ds_read_b128 v[52:55], v44 offset:128\n\t" \
    "s_waitcnt vmcnt(11)\n\t" \
    "v_mfma_f32_16x16x32_bf16 v[208:211], v[24:27], v[176:179], v[208:211]\n\t" \
    "ds_read_b128 v[80:83], v44 offset:29184\n\t" \
    "v_mfma_f32_16x16x32_bf16 v[212:215], v[24:27], v[184:187], v[212:215]\n\t" \
    "ds_read_b128 v[120:123], v44 offset:29312\n\t" \
    "buffer_load_dwordx4 v[132:135], v19, s[4:7], s47 offen offset:0 sc1\n\t" \
    "v_mfma_f32_16x16x32_bf16 v[216:219], v[24:27], v[188:191], v[216:219]\n\t" \
    "ds_read_b128 v[224:227], v45 offset:0\n\t" \
    "v_mfma_f32_16x16x32_bf16 v[88:91], v[24:27], v[60:63], v[88:91]\n\t" \
    "ds_read_b128 v[228:231], v45 offset:128\n\t" \
    "s_waitcnt vmcnt(11) lgkmcnt(5)\n\t" \
    "v_mfma_f32_16x16x32_bf16 v[180:183], v[32:35], v[220:223], v[180:183]\n\t" \
    "ds_read_b128 v[232:235], v45 offset:29184\n\t" \
    "s_bitcmp1_b32 s24, 2\n\t" \
    "s_cbranch_scc1 .Ly32_%=\n\t" \
    "buffer_load_dwordx4 v[236:239], v19, s[4:7], s45 offen offset:1024 sc1\n\t" \
    ".Ly32_%=:\n\t" \
    "s_waitcnt lgkmcnt(5)\n\t" \
    "v_mfma_f32_16x16x32_bf16 v[64:67], v[32:35], v[52:55], v[64:67]\n\t" \
    "ds_read_b128 v[240:243], v45 offset:29312\n\t" \
    "s_waitcnt lgkmcnt(5)\n\t" \
    "v_mfma_f32_16x16x32_bf16 v[68:71], v[32:35], v[80:83], v[68:71]\n\t" \
    "s_waitcnt lgkmcnt(4)\n\t" \
    "v_mfma_f32_16x16x32_bf16 v[20:23], v[32:35], v[120:123], v[20:23]\n\t" \
    "s_bitcmp1_b32 s24, 2\n\t" \
    "s_cbranch_scc1 .Ly33_%=\n\t" \
    "buffer_load_dwordx4 v[244:247], v19, s[4:7], s46 offen offset:1024 sc1\n\t" \
    ".Ly33_%=:\n\t" \
    "s_waitcnt vmcnt(10)\n\t" \
    "v_mfma_f32_16x16x32_bf16 v[148:151], v[140:143], v[52:55], v[148:151]\n\t" \
    "s_waitcnt lgkmcnt(0)\n\t" \
    "s_add_u32 s12, s55, 34603008\n\t" \
    "s_buffer_load_dword s13, s[4:7], s12\n\t" \
    "s_buffer_load_dword s14, s[4:7], s12 offset:0x400\n\t" \
    "v_mfma_f32_16x16x32_bf16 v[64:67], v[140:143], v[220:223], v[64:67]\n\t" \
    "v_mfma_f32_16x16x32_bf16 v[56:59], v[140:143], v[120:123], v[56:59]\n\t" \
    "s_bitcmp1_b32 s24, 2\n\t" \
    "s_cbranch_scc1 .Ly34_%=\n\t" \
    "buffer_load_dwordx4 v[248:251], v19, s[4:7], s47 offen offset:1024 sc1\n\t" \
    ".Ly34_%=:\n\t" \
    "v_mfma_f32_16x16x32_bf16 v[20:23], v[140:143], v[80:83], v[20:23]\n\t" \
    "s_waitcnt vmcnt(9)\n\t" \
    "v_mfma_f32_16x16x32_bf16 v[208:211], v[156:159], v[224:227], v[208:211]\n\t" \
    "v_mfma_f32_16x16x32_bf16 v[212:215], v[156:159], v[228:231], v[212:215]\n\t" \
    "s_mov_b32 s45, 688128\n\t" \
    "buffer_load_dwordx4 v[252:255], v17, s[4:7], s45 offen offset:0 sc1\n\t" \
    "v_mfma_f32_16x16x32_bf16 v[216:219], v[156:159], v[232:235], v[216:219]\n\t" \
    "v_sub_f32 v180, v180, v148\n\t" \
    "v_sub_f32 v181, v181, v149\n\t" \
    "v_sub_f32 v182, v182, v150\n\t" \
    "v_mfma_f32_16x16x32_bf16 v[88:91], v[156:159], v[240:243], v[88:91]\n\t" \
    "v_sub_f32 v183, v183, v151\n\t" \
    "v_sub_f32 v68, v68, v56\n\t" \
    "v_sub_f32 v69, v69, v57\n\t" \
    "v_sub_f32 v70, v70, v58\n\t" \
    "v_sub_f32 v71, v71, v59\n\t" \
    "v_cvt_pk_bf16_f32 v30, v64, v65\n\t" \
    "v_cvt_pk_bf16_f32 v31, v66, v67\n\t" \
    "v_cvt_pk_bf16_f32 v28, v180, v181\n\t" \
    "v_cvt_pk_bf16_f32 v29, v182, v183\n\t" \
    "v_cvt_pk_bf16_f32 v138, v20, v21\n\t" \
    "v_cvt_pk_bf16_f32 v139, v22, v23\n\t" \
    "ds_write_b128 v40, v[28:31] offset:58368\n\t" \
    "v_cvt_pk_bf16_f32 v136, v68, v69\n\t" \
    "v_cvt_pk_bf16_f32 v137, v70, v71\n\t" \
    "ds_write_b128 v40, v[136:139] offset:59392\n\t" \
    "s_waitcnt vmcnt(10)\n\t" \
    "s_cmp_ge_u32 s24, 4\n\t" \
    "s_cbranch_scc1 .Lk53_%=\n\t" \
    "v_mfma_f32_16x16x32_bf16 v[24:27], v[168:171], v[124:127], 0\n\t" \
    "v_mfma_f32_16x16x32_bf16 v[24:27], v[164:167], v[184:187], v[24:27]\n\t" \
    "v_mfma_f32_16x16x32_bf16 v[24:27], v[4:7], v[228:231], v[24:27]\n\t" \
    "v_mfma_f32_16x16x32_bf16 v[32:35], v[168:171], v[128:131], 0\n\t" \
    "v_mfma_f32_16x16x32_bf16 v[32:35], v[164:167], v[60:63], v[32:35]\n\t" \
    "v_mfma_f32_16x16x32_bf16 v[32:35], v[4:7], v[240:243], v[32:35]\n\t" \
    "v_mfma_f32_16x16x32_bf16 v[212:215], v[168:171], v[96:99], v[212:215]\n\t" \
    "v_mfma_f32_16x16x32_bf16 v[212:215], v[164:167], v[176:179], v[212:215]\n\t" \
    "v_mfma_f32_16x16x32_bf16 v[212:215], v[4:7], v[224:227], v[212:215]\n\t" \
    "v_mfma_f32_16x16x32_bf16 v[88:91], v[168:171], v[116:119], v[88:91]\n\t" \
    "s_nop 0\n\t" \
    "v_sub_f32 v208, v208, v24\n\t" \
    "v_sub_f32 v209, v209, v25\n\t" \
    "v_mfma_f32_16x16x32_bf16 v[88:91], v[164:167], v[188:191], v[88:91]\n\t" \
    "v_sub_f32 v210, v210, v26\n\t" \
    "v_sub_f32 v211, v211, v27\n\t" \
    "v_mfma_f32_16x16x32_bf16 v[88:91], v[4:7], v[232:235], v[88:91]\n\t" \
    "v_sub_f32 v216, v216, v32\n\t" \
    "v_sub_f32 v217, v217, v33\n\t" \
    "v_sub_f32 v218, v218, v34\n\t" \
    "v_sub_f32 v219, v219, v35\n\t" \
    ".Lk53_%=:\n\t" \
    "s_waitcnt vmcnt(9)\n\t" \
    "v_mfma_f32_16x16x32_bf16 v[140:143], v[72:75], v[92:95], 0\n\t" \
    "buffer_load_dwordx4 v[156:159], v17, s[4:7], s45 offen offset:1024 sc1\n\t" \
    "v_mfma_f32_16x16x32_bf16 v[148:151], v[72:75], v[108:111], 0\n\t" \
    "v_cvt_pk_bf16_f32 v58, v212, v213\n\t" \
    "v_cvt_pk_bf16_f32 v59, v214, v215\n\t" \
    "v_mfma_f32_16x16x32_bf16 v[64:67], v[72:75], v[112:115], 0\n\t" \
    "v_cvt_pk_bf16_f32 v56, v208, v209\n\t" \
    "v_cvt_pk_bf16_f32 v57, v210, v211\n\t" \
    "v_mfma_f32_16x16x32_bf16 v[180:183], v[72:75], v[84:87], 0\n\t" \
    "v_cvt_pk_bf16_f32 v22, v88, v89\n\t" \
    "v_cvt_pk_bf16_f32 v23, v90, v91\n\t" \
    "s_mov_b32 s46, 802816\n\t" \
    "buffer_load_dwordx4 v[28:31], v17, s[4:7], s46 offen offset:0 sc1\n\t" \
    "s_waitcnt vmcnt(10)\n\t" \
    "v_mfma_f32_16x16x32_bf16 v[68:71], v[76:79], v[108:111], 0\n\t" \
    "ds_write_b128 v40, v[56:59] offset:60432\n\t" \
    "v_cvt_pk_bf16_f32 v20, v216, v217\n\t" \
    "v_mfma_f32_16x16x32_bf16 v[148:151], v[76:79], v[92:95], v[148:151]\n\t" \
    "v_cvt_pk_bf16_f32 v21, v218, v219\n\t" \
    "v_mfma_f32_16x16x32_bf16 v[136:139], v[76:79], v[84:87], 0\n\t" \
    "ds_write_b128 v40, v[20:23] offset:61456\n\t" \
    "buffer_load_dwordx4 v[168:171], v17, s[4:7], s46 offen offset:1024 sc1\n\t" \
    "v_mfma_f32_16x16x32_bf16 v[180:183], v[76:79], v[112:115], v[180:183]\n\t" \
    "s_waitcnt vmcnt(10)\n\t" \
    "v_mfma_f32_16x16x32_bf16 v[140:143], v[100:103], v[172:175], v[140:143]\n\t" \
    "v_mfma_f32_16x16x32_bf16 v[148:151], v[100:103], v[144:147], v[148:151]\n\t" \
    "s_waitcnt lgkmcnt(0)\n\t" \
    "s_xor_b32 s13, s13, s34\n\t" \
    "s_xor_b32 s14, s14, s34\n\t" \
    "s_or_b32 s13, s13, s14\n\t" \
    "s_cbranch_scc1 .Lq1028_%=\n\t" \
    ".Lb27_%=:\n\t" \
    "s_waitcnt lgkmcnt(0)\n\t" \
    "s_barrier\n\t" \
    "s_memrealtime s[16:17]\n\t" \
    "ds_read_b64_tr_b16 v[164:165], v49 offset:58368\n\t" \
    "ds_read_b64_tr_b16 v[166:167], v49 offset:62496\n\t" \
    "ds_read_b64_tr_b16 v[24:25], v49 offset:58624\n\t" \
    "ds_read_b64_tr_b16 v[26:27], v49 offset:62752\n\t" \
    "s_mov_b32 s47, 917504\n\t" \
    "buffer_load_dwordx4 v[4:7], v17, s[4:7], s47 offen offset:0 sc1\n\t" \
    "v_mfma_f32_16x16x32_bf16 v[64:67], v[100:103], v[152:155], v[64:67]\n\t" \
    "ds_read_b64_tr_b16 v[32:33], v49 offset:58384\n\t" \
    "ds_read_b64_tr_b16 v[34:35], v49 offset:62512\n\t" \
    "ds_read_b64_tr_b16 v[212:213], v49 offset:58640\n\t" \
    "ds_read_b64_tr_b16 v[214:215], v49 offset:62768\n\t" \
    "v_mfma_f32_16x16x32_bf16 v[180:183], v[100:103], v[160:163], v[180:183]\n\t" \
    "s_waitcnt vmcnt(10)\n\t" \
    "v_mfma_f32_16x16x32_bf16 v[68:71], v[12:15], v[144:147], v[68:71]\n\t" \
    "buffer_load_dwordx4 v[208:211], v17, s[4:7], s47 offen offset:1024 sc1\n\t" \
    "v_mfma_f32_16x16x32_bf16 v[148:151], v[12:15], v[172:175], v[148:151]\n\t" \
    "s_waitcnt lgkmcnt(6)\n\t" \
    "v_mfma_f32_16x16x32_bf16 v[72:75], v[164:167], v[204:207], 0\n\t" \
    "s_waitcnt lgkmcnt(4)\n\t" \
    "v_mfma_f32_16x16x32_bf16 v[88:91], v[24:27], v[204:207], 0\n\t" \
    "v_mfma_f32_16x16x32_bf16 v[136:139], v[12:15], v[160:163], v[136:139]\n\t" \
    "s_waitcnt lgkmcnt(2)\n\t" \
    "v_mfma_f32_16x16x32_bf16 v[56:59], v[32:35], v[204:207], 0\n\t" \
    "s_waitcnt lgkmcnt(0)\n\t" \
    "v_mfma_f32_16x16x32_bf16 v[216:219], v[212:215], v[204:207], 0\n\t" \
    "v_mfma_f32_16x16x32_bf16 v[180:183], v[12:15], v[152:155], v[180:183]\n\t" \
    "s_cmp_lg_u32 s24, 4\n\t" \
    "s_cbranch_scc1 .Lph_%=\n\t" \
    "s_waitcnt lgkmcnt(0)\n\t" \
    "v_mov_b32 v20, s16\n\t" \
    "v_mov_b32 v21, s16\n\t" \
    "v_mov_b32 v22, s16\n\t" \
    "v_mov_b32 v23, s16\n\t" \
    "buffer_store_dwordx4 v[20:23], v2, s[4:7], s32 offen sc1\n\t" \
    "s_nop 0\n\t" \
    ".Lph_%=:\n\t" \
    "buffer_load_dwordx4 v[76:79], v19, s[4:7], s45 offen offset:0 sc1\n\t" \
    "s_waitcnt vmcnt(11)\n\t" \
    "v_mfma_f32_16x16x32_bf16 v[140:143], v[192:195], v[220:223], v[140:143]\n\t" \
    "v_bfi_b32 v72, s41, v50, v72\n\t" \
    "v_bfi_b32 v88, s41, v50, v88\n\t" \
    "v_cvt_pk_bf16_f32 v45, v74, v75\n\t" \
    "v_mfma_f32_16x16x32_bf16 v[148:151], v[192:195], v[52:55], v[148:151]\n\t" \
    "v_cvt_pk_bf16_f32 v47, v90, v91\n\t" \
    "v_cvt_pk_bf16_f32 v44, v72, v73\n\t" \
    "v_cvt_pk_bf16_f32 v46, v88, v89\n\t" \
    "v_mfma_f32_16x16x32_bf16 v[64:67], v[192:195], v[80:83], v[64:67]\n\t" \
    "v_bfi_b32 v56, s41, v50, v56\n\t" \
    "v_bfi_b32 v216, s41, v50, v216\n\t" \
    "buffer_store_dwordx4 v[44:47], v42, s[4:7], s48 offen offset:0 sc1\n\t" \
    "v_mfma_f32_16x16x32_bf16 v[180:183], v[192:195], v[120:123], v[180:183]\n\t" \
    "v_cvt_pk_bf16_f32 v101, v58, v59\n\t" \
    "v_cvt_pk_bf16_f32 v103, v218, v219\n\t" \
    "v_cvt_pk_bf16_f32 v100, v56, v57\n\t" \
    "s_waitcnt vmcnt(11)\n\t" \
    "v_mfma_f32_16x16x32_bf16 v[68:71], v[196:199], v[52:55], v[68:71]\n\t" \
    "v_cvt_pk_bf16_f32 v102, v216, v217\n\t" \
    "buffer_store_dwordx4 v[100:103], v18, s[4:7], s48 offen offset:128 sc1\n\t" \
    "v_mfma_f32_16x16x32_bf16 v[148:151], v[196:199], v[220:223], v[148:151]\n\t" \
    "v_mfma_f32_16x16x32_bf16 v[136:139], v[196:199], v[120:123], v[136:139]\n\t" \
    "v_mfma_f32_16x16x32_bf16 v[180:183], v[196:199], v[80:83], v[180:183]\n\t" \
    "s_waitcnt vmcnt(11)\n\t" \
    "v_mfma_f32_16x16x32_bf16 v[164:167], v[200:203], v[96:99], 0\n\t" \
    "v_mfma_f32_16x16x32_bf16 v[24:27], v[200:203], v[124:127], 0\n\t" \
    "v_mfma_f32_16x16x32_bf16 v[32:35], v[200:203], v[116:119], 0\n\t" \
    "v_sub_f32 v140, v140, v68\n\t" \
    "v_sub_f32 v141, v141, v69\n\t" \
    "v_mfma_f32_16x16x32_bf16 v[212:215], v[200:203], v[128:131], 0\n\t" \
    "v_sub_f32 v142, v142, v70\n\t" \
    "v_sub_f32 v143, v143, v71\n\t" \
    "s_waitcnt vmcnt(10)\n\t" \
    "v_mfma_f32_16x16x32_bf16 v[164:167], v[104:107], v[176:179], v[164:167]\n\t" \
    "v_sub_f32 v64, v64, v136\n\t" \
    "v_sub_f32 v65, v65, v137\n\t" \
    "v_mfma_f32_16x16x32_bf16 v[24:27], v[104:107], v[184:187], v[24:27]\n\t" \
    "v_sub_f32 v66, v66, v138\n\t" \
    "v_sub_f32 v67, v67, v139\n\t" \
    "v_mfma_f32_16x16x32_bf16 v[32:35], v[104:107], v[188:191], v[32:35]\n\t" \
    "v_cvt_pk_bf16_f32 v14, v148, v149\n\t" \
    "v_cvt_pk_bf16_f32 v15, v150, v151\n\t" \
    "buffer_load_dwordx4 v[20:23], v19, s[4:7], s46 offen offset:0 sc1\n\t" \
    "v_mfma_f32_16x16x32_bf16 v[212:215], v[104:107], v[60:63], v[212:215]\n\t" \
    "v_cvt_pk_bf16_f32 v12, v140, v141\n\t" \
    "v_cvt_pk_bf16_f32 v13, v142, v143\n\t" \
    "s_waitcnt vmcnt(10)\n\t" \
    "v_mfma_f32_16x16x32_bf16 v[164:167], v[132:135], v[224:227], v[164:167]\n\t" \
    "v_cvt_pk_bf16_f32 v74, v180, v181\n\t" \
    "v_cvt_pk_bf16_f32 v75, v182, v183\n\t" \
    "v_mfma_f32_16x16x32_bf16 v[24:27], v[132:135], v[228:231], v[24:27]\n\t" \
    "ds_write_b128 v40, v[12:15] offset:0\n\t" \
    "v_cvt_pk_bf16_f32 v72, v64, v65\n\t" \
    "buffer_load_dwordx4 v[88:91], v19, s[4:7], s47 offen offset:0 sc1\n\t" \
    "v_mfma_f32_16x16x32_bf16 v[32:35], v[132:135], v[232:235], v[32:35]\n\t" \
    "v_cvt_pk_bf16_f32 v73, v66, v67\n\t" \
    "ds_write_b128 v40, v[72:75] offset:1024\n\t" \
    "v_mfma_f32_16x16x32_bf16 v[212:215], v[132:135], v[240:243], v[212:215]\n\t" \
    "s_waitcnt vmcnt(11)\n\t" \
    "s_cmp_ge_u32 s24, 4\n\t" \
    "s_cbranch_scc1 .Lk54_%=\n\t" \
    "v_mfma_f32_16x16x32_bf16 v[44:47], v[236:239], v[124:127], 0\n\t" \
    "v_mfma_f32_16x16x32_bf16 v[44:47], v[244:247], v[184:187], v[44:47]\n\t" \
    "v_mfma_f32_16x16x32_bf16 v[44:47], v[248:251], v[228:231], v[44:47]\n\t" \
    "v_mfma_f32_16x16x32_bf16 v[192:195], v[236:239], v[128:131], 0\n\t" \
    "v_mfma_f32_16x16x32_bf16 v[192:195], v[244:247], v[60:63], v[192:195]\n\t" \
    "v_mfma_f32_16x16x32_bf16 v[192:195], v[248:251], v[240:243], v[192:195]\n\t" \
    "v_mfma_f32_16x16x32_bf16 v[24:27], v[236:239], v[96:99], v[24:27]\n\t" \
    "v_mfma_f32_16x16x32_bf16 v[24:27], v[244:247], v[176:179], v[24:27]\n\t" \
    "v_mfma_f32_16x16x32_bf16 v[24:27], v[248:251], v[224:227], v[24:27]\n\t" \
    "v_mfma_f32_16x16x32_bf16 v[212:215], v[236:239], v[116:119], v[212:215]\n\t" \
    "s_nop 0\n\t" \
    "v_sub_f32 v164, v164, v44\n\t" \
    "v_sub_f32 v165, v165, v45\n\t" \
    "v_mfma_f32_16x16x32_bf16 v[212:215], v[244:247], v[188:191], v[212:215]\n\t" \
    "v_sub_f32 v166, v166, v46\n\t" \
    "v_sub_f32 v167, v167, v47\n\t" \
    "v_mfma_f32_16x16x32_bf16 v[212:215], v[248:251], v[232:235], v[212:215]\n\t" \
    "v_sub_f32 v32, v32, v192\n\t" \
    "v_sub_f32 v33, v33, v193\n\t" \
    "v_sub_f32 v34, v34, v194\n\t" \
    "v_sub_f32 v35, v35, v195\n\t" \
    ".Lk54_%=:\n\t" \
    "s_waitcnt vmcnt(10)\n\t" \
    "v_mfma_f32_16x16x32_bf16 v[56:59], v[252:255], v[92:95], 0\n\t" \
    "s_bitcmp1_b32 s24, 2\n\t" \
    "s_cbranch_scc1 .Ly35_%=\n\t" \
    "buffer_load_dwordx4 v[216:219], v19, s[4:7], s45 offen offset:1024 sc1\n\t" \
    ".Ly35_%=:\n\t" \
    "v_mfma_f32_16x16x32_bf16 v[100:103], v[252:255], v[108:111], 0\n\t" \
    "v_cvt_pk_bf16_f32 v198, v24, v25\n\t" \
    "v_cvt_pk_bf16_f32 v199, v26, v27\n\t" \
    "v_mfma_f32_16x16x32_bf16 v[200:203], v[252:255], v[112:115], 0\n\t" \
    "v_cvt_pk_bf16_f32 v196, v164, v165\n\t" \
    "v_cvt_pk_bf16_f32 v197, v166, v167\n\t" \
    "v_mfma_f32_16x16x32_bf16 v[68:71], v[252:255], v[84:87], 0\n\t" \
    "v_cvt_pk_bf16_f32 v138, v212, v213\n\t" \
    "v_cvt_pk_bf16_f32 v139, v214, v215\n\t" \
    "s_bitcmp1_b32 s24, 2\n\t" \
    "s_cbranch_scc1 .Ly36_%=\n\t" \
    "buffer_load_dwordx4 v[148:151], v19, s[4:7], s46 offen offset:1024 sc1\n\t" \
    ".Ly36_%=:\n\t" \
    "s_waitcnt vmcnt(9)\n\t" \
    "v_mfma_f32_16x16x32_bf16 v[104:107], v[156:159], v[108:111], 0\n\t" \
    "ds_write_b128 v40, v[196:199] offset:2064\n\t" \
    "v_cvt_pk_bf16_f32 v136, v32, v33\n\t" \
    "v_mfma_f32_16x16x32_bf16 v[100:103], v[156:159], v[92:95], v[100:103]\n\t" \
    "v_cvt_pk_bf16_f32 v137, v34, v35\n\t" \
    "v_mfma_f32_16x16x32_bf16 v[140:143], v[156:159], v[84:87], 0\n\t" \
    "ds_write_b128 v40, v[136:139] offset:3088\n\t" \
    "s_bitcmp1_b32 s24, 2\n\t" \
    "s_cbranch_scc1 .Ly37_%=\n\t" \
    "buffer_load_dwordx4 v[180:183], v19, s[4:7], s47 offen offset:1024 sc1\n\t" \
    ".Ly37_%=:\n\t" \
    "v_mfma_f32_16x16x32_bf16 v[68:71], v[156:159], v[112:115], v[68:71]\n\t" \
    "s_waitcnt vmcnt(8)\n\t" \
    "v_mfma_f32_16x16x32_bf16 v[56:59], v[28:31], v[172:175], v[56:59]\n\t" \
    "v_mfma_f32_16x16x32_bf16 v[100:103], v[28:31], v[144:147], v[100:103]\n\t" \
    "s_waitcnt lgkmcnt(0)\n\t" \
    "s_barrier\n\t" \
    "s_memrealtime s[18:19]\n\t" \
    "s_setprio 2\n\t" \
    "ds_read_b64_tr_b16 v[12:13], v49 offset:0\n\t" \
    "ds_read_b64_tr_b16 v[14:15], v49 offset:4128\n\t" \
    "ds_read_b64_tr_b16 v[64:65], v49 offset:256\n\t" \
    "ds_read_b64_tr_b16 v[66:67], v49 offset:4384\n\t" \
    "v_mfma_f32_16x16x32_bf16 v[200:203], v[28:31], v[152:155], v[200:203]\n\t" \
    "ds_read_b64_tr_b16 v[72:73], v49 offset:16\n\t" \
    "ds_read_b64_tr_b16 v[74:75], v49 offset:4144\n\t" \
    "ds_read_b64_tr_b16 v[132:133], v49 offset:272\n\t" \
    "ds_read_b64_tr_b16 v[134:135], v49 offset:4400\n\t" \
    "v_mfma_f32_16x16x32_bf16 v[68:71], v[28:31], v[160:163], v[68:71]\n\t" \
    "s_waitcnt vmcnt(7)\n\t" \
    "v_mfma_f32_16x16x32_bf16 v[104:107], v[168:171], v[144:147], v[104:107]\n\t" \
    "v_mfma_f32_16x16x32_bf16 v[100:103], v[168:171], v[172:175], v[100:103]\n\t" \
    "s_waitcnt lgkmcnt(6)\n\t" \
    "v_mfma_f32_16x16x32_bf16 v[236:239], v[12:15], v[204:207], 0\n\t" \
    "s_waitcnt lgkmcnt(4)\n\t" \
    "v_mfma_f32_16x16x32_bf16 v[244:247], v[64:67], v[204:207], 0\n\t" \
    "v_mfma_f32_16x16x32_bf16 v[140:143], v[168:171], v[160:163], v[140:143]\n\t" \
    "s_waitcnt lgkmcnt(2)\n\t" \
    "v_mfma_f32_16x16x32_bf16 v[44:47], v[72:75], v[204:207], 0\n\t" \
    "s_waitcnt lgkmcnt(0)\n\t" \
    "v_mfma_f32_16x16x32_bf16 v[248:251], v[132:135], v[204:207], 0\n\t" \
    "v_mfma_f32_16x16x32_bf16 v[68:71], v[168:171], v[152:155], v[68:71]\n\t" \
    "s_waitcnt vmcnt(6)\n\t" \
    "v_mfma_f32_16x16x32_bf16 v[56:59], v[4:7], v[220:223], v[56:59]\n\t" \
    "v_bfi_b32 v236, s41, v50, v236\n\t" \
    "v_bfi_b32 v244, s41, v50, v244\n\t" \
    "v_cvt_pk_bf16_f32 v193, v238, v239\n\t" \
    "v_mfma_f32_16x16x32_bf16 v[100:103], v[4:7], v[52:55], v[100:103]\n\t" \
    "v_cvt_pk_bf16_f32 v195, v246, v247\n\t" \
    "v_cvt_pk_bf16_f32 v192, v236, v237\n\t" \
    "v_cvt_pk_bf16_f32 v194, v244, v245\n\t" \
    "v_mfma_f32_16x16x32_bf16 v[200:203], v[4:7], v[80:83], v[200:203]\n\t" \
    "v_bfi_b32 v44, s41, v50, v44\n\t" \
    "v_bfi_b32 v248, s41, v50, v248\n\t" \
    "buffer_store_dwordx4 v[192:195], v42, s[4:7], s49 offen offset:0 sc1\n\t" \
    "s_add_u32 s37, s52, s33\n\t" \
    "s_buffer_load_dword s58, s[4:7], s37 offset:0x0\n\t" \
    "s_buffer_load_dword s59, s[4:7], s37 offset:0x400\n\t" \
    "s_buffer_load_dword s60, s[4:7], s37 offset:0x8000\n\t" \
    "s_buffer_load_dword s61, s[4:7], s37 offset:0x8400\n\t" \
    "v_mfma_f32_16x16x32_bf16 v[68:71], v[4:7], v[120:123], v[68:71]\n\t" \
    "v_cvt_pk_bf16_f32 v25, v46, v47\n\t" \
    "v_cvt_pk_bf16_f32 v27, v250, v251\n\t" \
    "v_cvt_pk_bf16_f32 v24, v44, v45\n\t" \
    "s_waitcnt vmcnt(6)\n\t" \
    "v_mfma_f32_16x16x32_bf16 v[104:107], v[208:211], v[52:55], v[104:107]\n\t" \
    "v_cvt_pk_bf16_f32 v26, v248, v249\n\t" \
    "buffer_store_dwordx4 v[24:27], v18, s[4:7], s49 offen offset:128 sc1\n\t" \
    "s_setprio 0\n\t" \
    "v_add_u32 v40, 91392, v40\n\t" \
    "ds_read_b128 v[164:167], v10 offset:3072\n\t" \
    "ds_read_b128 v[252:255], v10 offset:4096\n\t" \
    "v_mfma_f32_16x16x32_bf16 v[100:103], v[208:211], v[220:223], v[100:103]\n\t" \
    "s_cmp_lt_u32 s24, 4\n\t" \
    "s_cbranch_scc1 .Lp55_%=\n\t" \
    "s_setprio 1\n\t" \
    ".Lp55_%=:\n\t" \
    "v_mfma_f32_16x16x32_bf16 v[140:143], v[208:211], v[120:123], v[140:143]\n\t" \
    "v_mfma_f32_16x16x32_bf16 v[68:71], v[208:211], v[80:83], v[68:71]\n\t" \
    "s_waitcnt vmcnt(6)\n\t" \
    "v_mfma_f32_16x16x32_bf16 v[212:215], v[76:79], v[96:99], 0\n\t" \
    "v_mfma_f32_16x16x32_bf16 v[108:111], v[76:79], v[124:127], 0\n\t" \
    "v_mfma_f32_16x16x32_bf16 v[196:199], v[76:79], v[116:119], 0\n\t" \
    "v_sub_f32 v56, v56, v104\n\t" \
    "v_sub_f32 v57, v57, v105\n\t" \
    "v_mfma_f32_16x16x32_bf16 v[92:95], v[76:79], v[128:131], 0\n\t" \
    "v_sub_f32 v58, v58, v106\n\t" \
    "v_sub_f32 v59, v59, v107\n\t" \
    "s_waitcnt vmcnt(3)\n\t" \
    "v_mfma_f32_16x16x32_bf16 v[212:215], v[20:23], v[176:179], v[212:215]\n\t" \
    "v_sub_f32 v200, v200, v140\n\t" \
    "v_sub_f32 v201, v201, v141\n\t" \
    "v_mfma_f32_16x16x32_bf16 v[108:111], v[20:23], v[184:187], v[108:111]\n\t" \
    "v_sub_f32 v202, v202, v142\n\t" \
    "v_sub_f32 v203, v203, v143\n\t" \
    "v_mfma_f32_16x16x32_bf16 v[196:199], v[20:23], v[188:191], v[196:199]\n\t" \
    "v_cvt_pk_bf16_f32 v34, v100, v101\n\t" \
    "v_cvt_pk_bf16_f32 v35, v102, v103\n\t" \
    "v_mfma_f32_16x16x32_bf16 v[92:95], v[20:23], v[60:63], v[92:95]\n\t" \
    "v_cvt_pk_bf16_f32 v32, v56, v57\n\t" \
    "v_cvt_pk_bf16_f32 v33, v58, v59\n\t" \
    "s_waitcnt vmcnt(2)\n\t" \
    "v_mfma_f32_16x16x32_bf16 v[212:215], v[88:91], v[224:227], v[212:215]\n\t" \
    "v_cvt_pk_bf16_f32 v86, v68, v69\n\t" \
    "v_cvt_pk_bf16_f32 v87, v70, v71\n\t" \
    "v_mfma_f32_16x16x32_bf16 v[108:111], v[88:91], v[228:231], v[108:111]\n\t" \
    "ds_write_b128 v40, v[32:35] offset:0\n\t" \
    "v_cvt_pk_bf16_f32 v84, v200, v201\n\t" \
    "s_add_u32 s54, s53, 0\n\t" \
    "buffer_load_dwordx4 v[136:139], v0, s[4:7], s54 offen offset:0 sc1\n\t" \
    "s_add_u32 s54, s53, 4243456\n\t" \
    "buffer_load_dwordx4 v[112:115], v0, s[4:7], s54 offen offset:0 sc1\n\t" \
    "v_mfma_f32_16x16x32_bf16 v[196:199], v[88:91], v[232:235], v[196:199]\n\t" \
    "v_cvt_pk_bf16_f32 v85, v202, v203\n\t" \
    "ds_write_b128 v40, v[84:87] offset:1024\n\t" \
    "v_mfma_f32_16x16x32_bf16 v[92:95], v[88:91], v[240:243], v[92:95]\n\t" \
    "s_waitcnt vmcnt(4)\n\t" \
    "s_cmp_ge_u32 s24, 4\n\t" \
    "s_cbranch_scc1 .Lk56_%=\n\t" \
    "v_mfma_f32_16x16x32_bf16 v[156:159], v[216:219], v[124:127], 0\n\t" \
    "v_mfma_f32_16x16x32_bf16 v[156:159], v[148:151], v[184:187], v[156:159]\n\t" \
    "v_mfma_f32_16x16x32_bf16 v[156:159], v[180:183], v[228:231], v[156:159]\n\t" \
    "v_mfma_f32_16x16x32_bf16 v[28:31], v[216:219], v[128:131], 0\n\t" \
    "v_mfma_f32_16x16x32_bf16 v[28:31], v[148:151], v[60:63], v[28:31]\n\t" \
    "v_mfma_f32_16x16x32_bf16 v[28:31], v[180:183], v[240:243], v[28:31]\n\t" \
    "v_mfma_f32_16x16x32_bf16 v[108:111], v[216:219], v[96:99], v[108:111]\n\t" \
    "v_mfma_f32_16x16x32_bf16 v[108:111], v[148:151], v[176:179], v[108:111]\n\t" \
    "v_mfma_f32_16x16x32_bf16 v[108:111], v[180:183], v[224:227], v[108:111]\n\t" \
    "v_mfma_f32_16x16x32_bf16 v[92:95], v[216:219], v[116:119], v[92:95]\n\t" \
    "s_nop 0\n\t" \
    "v_sub_f32 v212, v212, v156\n\t" \
    "v_sub_f32 v213, v213, v157\n\t" \
    "v_mfma_f32_16x16x32_bf16 v[92:95], v[148:151], v[188:191], v[92:95]\n\t" \
    "v_sub_f32 v214, v214, v158\n\t" \
    "v_sub_f32 v215, v215, v159\n\t" \
    "v_mfma_f32_16x16x32_bf16 v[92:95], v[180:183], v[232:235], v[92:95]\n\t" \
    "v_sub_f32 v196, v196, v28\n\t" \
    "v_sub_f32 v197, v197, v29\n\t" \
    "v_sub_f32 v198, v198, v30\n\t" \
    "v_sub_f32 v199, v199, v31\n\t" \
    ".Lk56_%=:\n\t" \
    "v_cvt_pk_bf16_f32 v146, v108, v109\n\t" \
    "v_cvt_pk_bf16_f32 v147, v110, v111\n\t" \
    "v_cvt_pk_bf16_f32 v144, v212, v213\n\t" \
    "v_cvt_pk_bf16_f32 v145, v214, v215\n\t" \
    "s_nop 1\n\t" \
    "v_cvt_pk_bf16_f32 v174, v92, v93\n\t" \
    "v_cvt_pk_bf16_f32 v175, v94, v95\n\t" \
    "ds_write_b128 v40, v[144:147] offset:2064\n\t" \
    "v_cvt_pk_bf16_f32 v172, v196, v197\n\t" \
    "v_cvt_pk_bf16_f32 v173, v198, v199\n\t" \
    "v_add_u32 v49, 91392, v49\n\t" \
    "ds_write_b128 v40, v[172:175] offset:3088\n\t" \
    "s_waitcnt lgkmcnt(0)\n\t" \
    "s_barrier\n\t" \
    "ds_read_b64_tr_b16 v[12:13], v49 offset:0\n\t" \
    "ds_read_b64_tr_b16 v[14:15], v49 offset:4128\n\t" \
    "ds_read_b64_tr_b16 v[64:65], v49 offset:256\n\t" \
    "ds_read_b64_tr_b16 v[66:67], v49 offset:4384\n\t" \
    "ds_read_b64_tr_b16 v[160:161], v49 offset:16\n\t" \
    "ds_read_b64_tr_b16 v[162:163], v49 offset:4144\n\t" \
    "ds_read_b64_tr_b16 v[72:73], v49 offset:272\n\t" \
    "ds_read_b64_tr_b16 v[74:75], v49 offset:4400\n\t" \
    "s_memrealtime s[56:57]\n\t" \
    "s_waitcnt lgkmcnt(6)\n\t" \
    "v_mfma_f32_16x16x32_bf16 v[132:135], v[12:15], v[204:207], 0\n\t" \
    "s_waitcnt lgkmcnt(4)\n\t" \
    "v_mfma_f32_16x16x32_bf16 v[152:155], v[64:67], v[204:207], 0\n\t" \
    "s_waitcnt lgkmcnt(2)\n\t" \
    "v_mfma_f32_16x16x32_bf16 v[168:171], v[160:163], v[204:207], 0\n\t" \
    "s_waitcnt lgkmcnt(0)\n\t" \
    "v_mfma_f32_16x16x32_bf16 v[236:239], v[72:75], v[204:207], 0\n\t" \
    "s_nop 1\n\t" \
    "v_bfi_b32 v132, s41, v50, v132\n\t" \
    "s_nop 0\n\t" \
    "v_bfi_b32 v152, s41, v50, v152\n\t" \
    "v_cvt_pk_bf16_f32 v245, v134, v135\n\t" \
    "v_cvt_pk_bf16_f32 v247, v154, v155\n\t" \
    "v_cvt_pk_bf16_f32 v244, v132, v133\n\t" \
    "v_cvt_pk_bf16_f32 v246, v152, v153\n\t" \
    "v_bfi_b32 v168, s41, v50, v168\n\t" \
    "v_bfi_b32 v236, s41, v50, v236\n\t" \
    "buffer_store_dwordx4 v[244:247], v42, s[4:7], s50 offen offset:0 sc1\n\t" \
    "v_cvt_pk_bf16_f32 v193, v170, v171\n\t" \
    "v_cvt_pk_bf16_f32 v195, v238, v239\n\t" \
    "v_cvt_pk_bf16_f32 v192, v168, v169\n\t" \
    "v_cvt_pk_bf16_f32 v194, v236, v237\n\t" \
    "buffer_store_dwordx4 v[192:195], v18, s[4:7], s50 offen offset:128 sc1\n\t" \
    "s_cmp_ge_u32 s25, 126\n\t" \
    "s_cbranch_scc1 .Lend_%=\n\t" \
    "s_waitcnt lgkmcnt(0)\n\t" \
    "s_sub_u32 s37, s16, 300\n\t" \
    "s_mov_b32 s62, 300\n\t" \
    "s_sub_u32 s39, s58, s37\n\t" \
    "s_cmp_gt_u32 s39, 600\n\t" \
    "s_cselect_b32 s39, 400, s39\n\t" \
    "s_max_u32 s62, s62, s39\n\t" \
    "s_sub_u32 s39, s59, s37\n\t" \
    "s_cmp_gt_u32 s39, 600\n\t" \
    "s_cselect_b32 s39, 400, s39\n\t" \
    "s_max_u32 s62, s62, s39\n\t" \
    "s_sub_u32 s39, s60, s37\n\t" \
    "s_cmp_gt_u32 s39, 600\n\t" \
    "s_cselect_b32 s39, 400, s39\n\t" \
    "s_max_u32 s62, s62, s39\n\t" \
    "s_sub_u32 s39, s61, s37\n\t" \
    "s_cmp_gt_u32 s39, 600\n\t" \
    "s_cselect_b32 s39, 400, s39\n\t" \
    "s_max_u32 s62, s62, s39\n\t" \
    "s_sub_u32 s62, s62, 300\n\t" \
    "s_mul_i32 s62, s62, 5\n\t" \
    "s_lshr_b32 s62, s62, 2\n\t" \
    "s_waitcnt vmcnt(2)\n\t" \
    "v_and_b32 v41, v136, v138\n\t" \
    "v_bitop3_b32 v41, v41, v112, v114 bitop3:0x80\n\t" \
    "v_and_b32 v41, 1, v41\n\t" \
    "v_cmp_eq_u32 vcc, 0, v41\n\t" \
    "s_nop 1\n\t" \
    "s_cbranch_vccnz .Ls50_%=\n\t" \
    ".Lb50_%=:\n\t" \
    "ds_write_b128 v48, v[136:139] offset:33024\n\t" \
    "ds_write_b128 v48, v[112:115] offset:33152\n\t" \
    "s_add_u32 s37, s62, 125\n\t" \
    ".Lh57_%=:\n\t" \
    "s_memrealtime s[22:23]\n\t" \
    "s_waitcnt lgkmcnt(0)\n\t" \
    "s_sub_u32 s22, s22, s18\n\t" \
    "s_cmp_lt_u32 s22, s37\n\t" \
    "s_cbranch_scc1 .Lh57_%=\n\t" \
    "s_add_u32 s54, s53, 524288\n\t" \
    "buffer_load_dwordx4 v[4:7], v0, s[4:7], s54 offen offset:0 sc1\n\t" \
    "s_add_u32 s54, s53, 4767744\n\t" \
    "buffer_load_dwordx4 v[44:47], v0, s[4:7], s54 offen offset:0 sc1\n\t" \
    "s_waitcnt lgkmcnt(0)\n\t" \
    "s_barrier\n\t" \
    "ds_read_b64_tr_b16 v[52:53], v43 offset:33024\n\t" \
    "ds_read_b64_tr_b16 v[54:55], v43 offset:33280\n\t" \
    "ds_read_b64_tr_b16 v[248:249], v43 offset:33032\n\t" \
    "ds_read_b64_tr_b16 v[250:251], v43 offset:33288\n\t" \
    "s_waitcnt lgkmcnt(2)\n\t" \
    "v_mfma_f32_16x16x32_bf16 v[24:27], v[52:55], v[164:167], 0\n\t" \
    "s_waitcnt lgkmcnt(0)\n\t" \
    "v_mfma_f32_16x16x32_bf16 v[8:11], v[248:251], v[164:167], 0\n\t" \
    "v_mfma_f32_16x16x32_bf16 v[220:223], v[52:55], v[252:255], 0\n\t" \
    "v_mfma_f32_16x16x32_bf16 v[120:123], v[248:251], v[252:255], 0\n\t" \
    "s_nop 3\n\t" \
    "v_cvt_pk_bf16_f32 v80, v24, v25\n\t" \
    "v_cvt_pk_bf16_f32 v81, v26, v27\n\t" \
    "v_cvt_pk_bf16_f32 v82, v8, v9\n\t" \
    "v_cvt_pk_bf16_f32 v83, v10, v11\n\t" \
    "buffer_store_dwordx4 v[80:83], v38, s[8:11], 0 offen offset:0 sc1\n\t" \
    "v_cvt_pk_bf16_f32 v208, v220, v221\n\t" \
    "v_cvt_pk_bf16_f32 v209, v222, v223\n\t" \
    "v_cvt_pk_bf16_f32 v210, v120, v121\n\t" \
    "v_cvt_pk_bf16_f32 v211, v122, v123\n\t" \
    "buffer_store_dwordx4 v[208:211], v38, s[8:11], 0 offen offset:3072 sc1\n\t" \
    "s_waitcnt vmcnt(2)\n\t" \
    "v_and_b32 v39, v4, v6\n\t" \
    "v_bitop3_b32 v39, v39, v44, v46 bitop3:0x80\n\t" \
    "v_and_b32 v39, 1, v39\n\t" \
    "v_cmp_eq_u32 vcc, 0, v39\n\t" \
    "s_nop 1\n\t" \
    "s_cbranch_vccnz .Ls51_%=\n\t" \
    ".Lb51_%=:\n\t" \
    "ds_write_b128 v48, v[4:7] offset:0\n\t" \
    "ds_write_b128 v48, v[44:47] offset:128\n\t" \
    "s_add_u32 s37, s62, 100\n\t" \
    ".Lh58_%=:\n\t" \
    "s_memrealtime s[22:23]\n\t" \
    "s_waitcnt lgkmcnt(0)\n\t" \
    "s_sub_u32 s22, s22, s56\n\t" \
    "s_cmp_lt_u32 s22, s37\n\t" \
    "s_cbranch_scc1 .Lh58_%=\n\t" \
    "s_add_u32 s54, s53, 1048576\n\t" \
    "buffer_load_dwordx4 v[76:79], v0, s[4:7], s54 offen offset:0 sc1\n\t" \
    "s_add_u32 s54, s53, 5292032\n\t" \
    "buffer_load_dwordx4 v[104:107], v0, s[4:7], s54 offen offset:0 sc1\n\t" \
    "s_waitcnt lgkmcnt(0)\n\t" \
    "s_barrier\n\t" \
    "ds_read_b64_tr_b16 v[140:141], v43 offset:0\n\t" \
    "ds_read_b64_tr_b16 v[142:143], v43 offset:256\n\t" \
    "ds_read_b64_tr_b16 v[100:101], v43 offset:8\n\t" \
    "ds_read_b64_tr_b16 v[102:103], v43 offset:264\n\t" \
    "s_waitcnt lgkmcnt(2)\n\t" \
    "v_mfma_f32_16x16x32_bf16 v[20:23], v[140:143], v[164:167], 0\n\t" \
    "s_waitcnt lgkmcnt(0)\n\t" \
    "v_mfma_f32_16x16x32_bf16 v[56:59], v[100:103], v[164:167], 0\n\t" \
    "v_mfma_f32_16x16x32_bf16 v[68:71], v[140:143], v[252:255], 0\n\t" \
    "v_mfma_f32_16x16x32_bf16 v[32:35], v[100:103], v[252:255], 0\n\t" \
    "s_nop 3\n\t" \
    "v_cvt_pk_bf16_f32 v200, v20, v21\n\t" \
    "v_cvt_pk_bf16_f32 v201, v22, v23\n\t" \
    "v_cvt_pk_bf16_f32 v202, v56, v57\n\t" \
    "v_cvt_pk_bf16_f32 v203, v58, v59\n\t" \
    "buffer_store_dwordx4 v[200:203], v38, s[8:11], 0 offen offset:64 sc1\n\t" \
    "v_cvt_pk_bf16_f32 v84, v68, v69\n\t" \
    "v_cvt_pk_bf16_f32 v85, v70, v71\n\t" \
    "v_cvt_pk_bf16_f32 v86, v32, v33\n\t" \
    "v_cvt_pk_bf16_f32 v87, v34, v35\n\t" \
    "buffer_store_dwordx4 v[84:87], v38, s[8:11], 0 offen offset:3136 sc1\n\t" \
    "s_waitcnt vmcnt(2)\n\t" \
    "v_and_b32 v37, v76, v78\n\t" \
    "v_bitop3_b32 v37, v37, v104, v106 bitop3:0x80\n\t" \
    "v_and_b32 v37, 1, v37\n\t" \
    "v_cmp_eq_u32 vcc, 0, v37\n\t" \
    "s_nop 1\n\t" \
    "s_cbranch_vccnz .Ls52_%=\n\t" \
    ".Lb52_%=:\n\t" \
    "ds_write_b128 v48, v[76:79] offset:16512\n\t" \
    "ds_write_b128 v48, v[104:107] offset:16640\n\t" \
    "s_waitcnt lgkmcnt(0)\n\t" \
    "s_barrier\n\t" \
    "ds_read_b64_tr_b16 v[88:89], v43 offset:16512\n\t" \
    "ds_read_b64_tr_b16 v[90:91], v43 offset:16768\n\t" \
    "ds_read_b64_tr_b16 v[124:125], v43 offset:16520\n\t" \
    "ds_read_b64_tr_b16 v[126:127], v43 offset:16776\n\t" \
    "s_waitcnt lgkmcnt(2)\n\t" \
    "v_mfma_f32_16x16x32_bf16 v[184:187], v[88:91], v[164:167], 0\n\t" \
    "s_waitcnt lgkmcnt(0)\n\t" \
    "v_mfma_f32_16x16x32_bf16 v[228:231], v[124:127], v[164:167], 0\n\t" \
    "v_mfma_f32_16x16x32_bf16 v[128:131], v[88:91], v[252:255], 0\n\t" \
    "v_mfma_f32_16x16x32_bf16 v[60:63], v[124:127], v[252:255], 0\n\t" \
    "s_nop 3\n\t" \
    "v_cvt_pk_bf16_f32 v240, v184, v185\n\t" \
    "v_cvt_pk_bf16_f32 v241, v186, v187\n\t" \
    "v_cvt_pk_bf16_f32 v242, v228, v229\n\t" \
    "v_cvt_pk_bf16_f32 v243, v230, v231\n\t" \
    "buffer_store_dwordx4 v[240:243], v38, s[8:11], 0 offen offset:128 sc1\n\t" \
    "v_cvt_pk_bf16_f32 v96, v128, v129\n\t" \
    "v_cvt_pk_bf16_f32 v97, v130, v131\n\t" \
    "v_cvt_pk_bf16_f32 v98, v60, v61\n\t" \
    "v_cvt_pk_bf16_f32 v99, v62, v63\n\t" \
    "buffer_store_dwordx4 v[96:99], v38, s[8:11], 0 offen offset:3200 sc1\n\t" \
    "s_or_b32 s37, s2, s24\n\t" \
    "s_cbranch_scc1 .Lend_%=\n\t" \
    "s_mov_b32 s37, 34668544\n\t" \
    "v_mov_b32 v1, s34\n\t" \
    "s_mov_b64 exec, 1\n\t" \
    "buffer_store_dword v1, v0, s[4:7], s37 offen sc1\n\t" \
    "s_mov_b64 exec, -1\n\t" \
    ".Lend_%=:\n\t" \
    "s_endpgm\n\t" \
    ".Lq1028_%=:\n\t" \
    "s_mov_b32 s40, 0\n\t" \
    ".Lr1028_%=:\n\t" \
    "s_add_u32 s12, s55, 34603008\n\t" \
    "s_buffer_load_dword s13, s[4:7], s12 glc\n\t" \
    "s_buffer_load_dword s14, s[4:7], s12 offset:0x400 glc\n\t" \
    "s_waitcnt lgkmcnt(0)\n\t" \
    "s_xor_b32 s13, s13, s34\n\t" \
    "s_xor_b32 s14, s14, s34\n\t" \
    "s_or_b32 s13, s13, s14\n\t" \
    "s_cbranch_scc0 .Lb27_%=\n\t" \
    "s_add_u32 s40, s40, 1\n\t" \
    "s_cmp_lt_u32 s40, 1\n\t" \
    "s_cbranch_scc1 .Lr1028_%=\n\t" \
    "s_branch .Ls27_%=\n\t" \
    ".Lq1025_%=:\n\t" \
    "s_mov_b32 s40, 0\n\t" \
    ".Lr1025_%=:\n\t" \
    "s_nop 0\n\t" \
    "v_readlane_b32 s12, v11, 0\n\t" \
    "v_readlane_b32 s13, v11, 1\n\t" \
    "v_readlane_b32 s14, v11, 2\n\t" \
    "v_readlane_b32 s15, v11, 3\n\t" \
    "v_readlane_b32 s16, v11, 4\n\t" \
    "v_readlane_b32 s17, v11, 5\n\t" \
    "v_readlane_b32 s18, v11, 6\n\t" \
    "v_readlane_b32 s19, v11, 7\n\t" \
    "v_readlane_b32 s3, v11, 8\n\t" \
    "s_add_u32 s12, s12, s33\n\t" \
    "s_add_u32 s13, s13, s33\n\t" \
    "s_add_u32 s14, s14, s33\n\t" \
    "s_add_u32 s15, s15, s33\n\t" \
    "s_add_u32 s16, s16, s33\n\t" \
    "s_add_u32 s17, s17, s33\n\t" \
    "s_add_u32 s18, s18, s33\n\t" \
    "s_add_u32 s19, s19, s33\n\t" \
    "s_add_u32 s3, s3, s33\n\t" \
    "s_buffer_load_dword s12, s[4:7], s12 offset:0xc0000 glc\n\t" \
    "s_buffer_load_dword s13, s[4:7], s13 offset:0xc0000 glc\n\t" \
    "s_buffer_load_dword s14, s[4:7], s14 offset:0xc0000 glc\n\t" \
    "s_buffer_load_dword s15, s[4:7], s15 offset:0xc0000 glc\n\t" \
    "s_buffer_load_dword s16, s[4:7], s16 offset:0xc0000 glc\n\t" \
    "s_buffer_load_dword s17, s[4:7], s17 offset:0xc0000 glc\n\t" \
    "s_buffer_load_dword s18, s[4:7], s18 offset:0xc0000 glc\n\t" \
    "s_buffer_load_dword s19, s[4:7], s19 offset:0xc0000 glc\n\t" \
    "s_buffer_load_dword s3, s[4:7], s3 offset:0xc0000 glc\n\t" \
    "s_waitcnt lgkmcnt(0)\n\t" \
    "s_xor_b32 s12, s12, s34\n\t" \
    "s_xor_b32 s13, s13, s34\n\t" \
    "s_xor_b32 s14, s14, s34\n\t" \
    "s_xor_b32 s15, s15, s34\n\t" \
    "s_xor_b32 s16, s16, s34\n\t" \
    "s_xor_b32 s17, s17, s34\n\t" \
    "s_xor_b32 s18, s18, s34\n\t" \
    "s_xor_b32 s19, s19, s34\n\t" \
    "s_xor_b32 s3, s3, s34\n\t" \
    "s_or_b32 s12, s12, s13\n\t" \
    "s_or_b32 s12, s12, s14\n\t" \
    "s_or_b32 s12, s12, s15\n\t" \
    "s_or_b32 s12, s12, s16\n\t" \
    "s_or_b32 s12, s12, s17\n\t" \
    "s_or_b32 s12, s12, s18\n\t" \
    "s_or_b32 s12, s12, s19\n\t" \
    "s_or_b32 s12, s12, s3\n\t" \
    "s_cbranch_scc0 .Lb24_%=\n\t" \
    "s_add_u32 s40, s40, 1\n\t" \
    "s_cmp_lt_u32 s40, 1\n\t" \
    "s_cbranch_scc1 .Lr1025_%=\n\t" \
    "s_branch .Ls24_%=\n\t" \
    ".Lqa_%=:\n\t" \
    "s_sub_u32 s37, s24, 4\n\t" \
    "s_bfe_u32 s38, s2, 0x20002\n\t" \
    "s_lshl1_add_u32 s38, s38, s37\n\t" \
    "s_lshl_b32 s42, s38, 21\n\t" \
    "s_cmp_ge_u32 s38, 7\n\t" \
    "s_cbranch_scc1 .Lqd_%=\n\t" \
    "s_cmp_ge_u32 s37, 2\n\t" \
    "s_cbranch_scc1 .Lqd_%=\n\t" \
    "s_memtime s[62:63]\n\t" \
    "s_waitcnt lgkmcnt(0)\n\t" \
    "s_sleep 4\n\t" \
    "s_lshl_b32 s3, s38, 10\n\t" \
    "s_and_b32 s37, s8, 0x1fffff\n\t" \
    "s_add_u32 s38, s37, 16383\n\t" \
    "s_andn2_b32 s38, s38, 16383\n\t" \
    "s_add_u32 s39, s37, 9617408\n\t" \
    "s_andn2_b32 s39, s39, 16383\n\t" \
    "s_max_u32 s42, s42, s38\n\t" \
    "s_min_u32 s42, s42, s39\n\t" \
    "s_sub_u32 s42, s42, s37\n\t" \
    "s_cmp_eq_u32 s2, 255\n\t" \
    "s_cselect_b64 s[16:17], s[4:5], s[8:9]\n\t" \
    "s_cselect_b32 s42, 34684928, s42\n\t" \
    "s_add_u32 s16, s16, s42\n\t" \
    "s_addc_u32 s17, s17, 0\n\t" \
    "s_mov_b32 s18, 16384\n\t" \
    "s_mov_b32 s19, 0x20000\n\t" \
    "s_add_u32 s37, s3, 0x2000\n\t" \
    "buffer_store_dwordx4 v[20:23], v0, s[16:19], s3 offen sc1\n\t" \
    "buffer_store_dwordx4 v[20:23], v0, s[16:19], s37 offen sc1\n\t" \
    "s_nop 0\n\t" \
    "s_mov_b32 s39, 34668544\n\t" \
    "v_mov_b32 v20, 0\n\t" \
    "buffer_load_dword v21, v20, s[4:7], s39 offen sc1\n\t" \
    "s_mov_b32 s60, -1\n\t" \
    "s_mov_b32 s61, -1\n\t" \
    "s_mov_b32 s40, 0\n\t" \
    "s_waitcnt vmcnt(1) lgkmcnt(0)\n\t" \
    "s_memtime s[58:59]\n\t" \
    "s_waitcnt lgkmcnt(0)\n\t" \
    ".Lqc_%=:\n\t" \
    "s_buffer_load_dword s38, s[16:19], s3\n\t" \
    "s_mov_b32 s56, s58\n\t" \
    "s_xor_b32 s3, s3, 0x2000\n\t" \
    "s_waitcnt lgkmcnt(0)\n\t" \
    "s_memtime s[58:59]\n\t" \
    "s_add_u32 s40, s40, 1\n\t" \
    "s_waitcnt lgkmcnt(0)\n\t" \
    "s_sub_u32 s56, s58, s56\n\t" \
    "s_min_u32 s56, s56, s60\n\t" \
    "s_mov_b32 s60, s61\n\t" \
    "s_mov_b32 s61, s56\n\t" \
    "s_bitcmp1_b32 s40, 0\n\t" \
    "s_cbranch_scc1 .Lqc_%=\n\t" \
    "s_add_u32 s3, s3, 0x80\n\t" \
    "s_sub_u32 s37, s58, s62\n\t" \
    "s_cmp_gt_u32 s37, 5200\n\t" \
    "s_cselect_b32 s40, 98, s40\n\t" \
    "s_absdiff_i32 s37, s60, s61\n\t" \
    "s_min_u32 s38, s60, s61\n\t" \
    "s_cmp_gt_u32 s38, 500\n\t" \
    "s_cselect_b32 s37, 0, s37\n\t" \
    "s_cmp_lt_u32 s37, 80\n\t" \
    "s_cselect_b32 s37, 16, 4\n\t" \
    "s_cmp_lt_u32 s40, s37\n\t" \
    "s_cbranch_scc1 .Lqc_%=\n\t" \
    "s_waitcnt vmcnt(0)\n\t" \
    "v_readfirstlane_b32 s34, v21\n\t" \
    "s_cmp_gt_u32 s60, s61\n\t" \
    "s_cselect_b32 s0, 2, 0\n\t" \
    "s_mul_i32 s34, s34, 1664525\n\t" \
    "s_add_u32 s34, s34, 1013904223\n\t" \
    "s_or_b32 s34, s34, 1\n\t" \
    "s_cmp_eq_u32 s2, 255\n\t" \
    "s_cbranch_scc1 .Lqf_%=\n\t" \
    "s_xor_b32 s37, s0, s34\n\t" \
    "v_mov_b32 v21, s37\n\t" \
    "s_bfe_u32 s37, s2, 0x20002\n\t" \
    "s_lshl1_add_u32 s37, s37, s24\n\t" \
    "s_lshl_b32 s37, s37, 7\n\t" \
    "s_add_u32 s37, s37, 34670080\n\t" \
    "v_lshrrev_b32 v20, 2, v0\n\t" \
    "s_mov_b32 exec_hi, 0\n\t" \
    "buffer_store_dword v21, v20, s[4:7], s37 offen sc1\n\t" \
    "s_mov_b64 exec, -1\n\t" \
    ".Lqg_%=:\n\t" \
    "s_branch .Lqd_%=\n\t" \
    ".Lqf_%=:\n\t" \
    "s_getreg_b32 s37, hwreg(HW_REG_XCC_ID, 0, 2)\n\t" \
    "s_lshl1_add_u32 s37, s0, s37\n\t" \
    "s_add_u32 s37, s37, 1\n\t" \
    "s_and_b32 s37, s37, 7\n\t" \
    "s_lshl_b32 s37, s37, 1\n\t" \
    "s_xor_b32 s37, s37, s34\n\t" \
    "v_mov_b32 v20, s37\n\t" \
    "v_mov_b32 v21, s37\n\t" \
    "v_mov_b32 v22, s37\n\t" \
    "v_mov_b32 v23, s37\n\t" \
    "s_mov_b32 s39, 34669568\n\t" \
    "buffer_store_dwordx4 v[20:23], v0, s[4:7], s39 offen sc1\n\t" \
    "s_nop 0\n\t" \
    "s_branch .Lqg_%=\n\t" \
    ".Lr2_%=:\n\t" \
    "s_add_u32 s40, s40, 1\n\t" \
    "s_cmp_gt_u32 s40, 32\n\t" \
    "s_cbranch_scc1 .Lr3_%=\n\t" \
    "s_sleep 2\n\t" \
    "s_and_b32 s37, s2, 7\n\t" \
    "s_lshl_b32 s37, s37, 10\n\t" \
    "s_add_u32 s37, s37, 34676736\n\t" \
    "v_and_b32 v51, 0x70, v0\n\t" \
    "v_lshlrev_b32 v51, 3, v51\n\t" \
    "buffer_load_dword v50, v51, s[4:7], s37 offen sc1\n\t" \
    "s_waitcnt vmcnt(0)\n\t" \
    "v_readlane_b32 s56, v50, 0\n\t" \
    "v_readlane_b32 s57, v50, 1\n\t" \
    "v_readlane_b32 s58, v50, 2\n\t" \
    "v_readlane_b32 s59, v50, 3\n\t" \
    "v_readlane_b32 s60, v50, 4\n\t" \
    "v_readlane_b32 s61, v50, 5\n\t" \
    "v_readlane_b32 s62, v50, 6\n\t" \
    "v_readlane_b32 s63, v50, 7\n\t" \
    "s_branch .Lr1_%=\n\t" \
    ".Lq5_%=:\n\t" \
    "s_add_u32 s40, s40, 1\n\t" \
    "s_cmp_gt_u32 s40, 65536\n\t" \
    "s_cbranch_scc1 .Lq6_%=\n\t" \
    "s_sleep 4\n\t" \
    "v_and_b32 v16, 0x70, v0\n\t" \
    "v_lshlrev_b32 v16, 3, v16\n\t" \
    "s_mov_b32 s37, 34670592\n\t" \
    "buffer_load_dword v8, v16, s[4:7], s37 offen sc1\n\t" \
    "s_waitcnt vmcnt(0)\n\t" \
    "v_readlane_b32 s56, v8, 0\n\t" \
    "v_readlane_b32 s57, v8, 1\n\t" \
    "v_readlane_b32 s58, v8, 2\n\t" \
    "v_readlane_b32 s59, v8, 3\n\t" \
    "v_readlane_b32 s60, v8, 4\n\t" \
    "v_readlane_b32 s61, v8, 5\n\t" \
    "s_branch .Lq4_%=\n\t" \
    ".Lr5_%=:\n\t" \
    "s_add_u32 s40, s40, 1\n\t" \
    "s_cmp_gt_u32 s40, 65536\n\t" \
    "s_cbranch_scc1 .Lr6_%=\n\t" \
    "s_sleep 4\n\t" \
    "v_mov_b32 v16, 0\n\t" \
    "buffer_load_dword v8, v16, s[4:7], s52 offen sc1\n\t" \
    "s_waitcnt vmcnt(0)\n\t" \
    "v_readfirstlane_b32 s36, v8\n\t" \
    "s_branch .Lr4_%=\n\t" \
    ".Ls24_%=:\n\t" \
    "s_mov_b32 s40, 0\n\t" \
    "s_add_u32 s38, s33, 786432\n\t" \
    ".Ll24_%=:\n\t" \
    "s_sleep 2\n\t" \
    "buffer_load_dword v1, v11, s[4:7], s38 offen sc1\n\t" \
    "s_waitcnt vmcnt(0)\n\t" \
    "v_cmp_ne_u32 vcc, s34, v1\n\t" \
    "s_add_u32 s40, s40, 1\n\t" \
    "s_cmp_ge_u32 s40, 65536\n\t" \
    "s_cbranch_scc1 .Lb24_%=\n\t" \
    "s_cbranch_vccnz .Ll24_%=\n\t" \
    "s_branch .Lb24_%=\n\t" \
    ".Ls27_%=:\n\t" \
    "s_mov_b32 s40, 0\n\t" \
    "s_add_u32 s38, s55, 34603008\n\t" \
    ".Ll27_%=:\n\t" \
    "s_sleep 2\n\t" \
    "buffer_load_dword v36, v47, s[4:7], s38 offen sc1\n\t" \
    "s_waitcnt vmcnt(0)\n\t" \
    "v_cmp_ne_u32 vcc, s34, v36\n\t" \
    "s_add_u32 s40, s40, 1\n\t" \
    "s_cmp_ge_u32 s40, 65536\n\t" \
    "s_cbranch_scc1 .Lb27_%=\n\t" \
    "s_cbranch_vccnz .Ll27_%=\n\t" \
    "s_branch .Lb27_%=\n\t" \
    ".Ls50_%=:\n\t" \
    "s_mov_b32 s40, 0\n\t" \
    ".Ll50_%=:\n\t" \
    "s_add_u32 s54, s53, 0\n\t" \
    "buffer_load_dwordx4 v[136:139], v0, s[4:7], s54 offen offset:0 sc1\n\t" \
    "s_add_u32 s54, s53, 4243456\n\t" \
    "buffer_load_dwordx4 v[112:115], v0, s[4:7], s54 offen offset:0 sc1\n\t" \
    "s_waitcnt vmcnt(0)\n\t" \
    "v_and_b32 v41, v136, v138\n\t" \
    "v_bitop3_b32 v41, v41, v112, v114 bitop3:0x80\n\t" \
    "v_and_b32 v41, 1, v41\n\t" \
    "v_cmp_eq_u32 vcc, 0, v41\n\t" \
    "s_add_u32 s40, s40, 1\n\t" \
    "s_cmp_ge_u32 s40, 65536\n\t" \
    "s_cbranch_scc1 .Lb50_%=\n\t" \
    "s_cbranch_vccnz .Ll50_%=\n\t" \
    "s_branch .Lb50_%=\n\t" \
    ".Ls51_%=:\n\t" \
    "s_mov_b32 s40, 0\n\t" \
    ".Ll51_%=:\n\t" \
    "s_add_u32 s54, s53, 524288\n\t" \
    "buffer_load_dwordx4 v[4:7], v0, s[4:7], s54 offen offset:0 sc1\n\t" \
    "s_add_u32 s54, s53, 4767744\n\t" \
    "buffer_load_dwordx4 v[44:47], v0, s[4:7], s54 offen offset:0 sc1\n\t" \
    "s_waitcnt vmcnt(0)\n\t" \
    "v_and_b32 v39, v4, v6\n\t" \
    "v_bitop3_b32 v39, v39, v44, v46 bitop3:0x80\n\t" \
    "v_and_b32 v39, 1, v39\n\t" \
    "v_cmp_eq_u32 vcc, 0, v39\n\t" \
    "s_add_u32 s40, s40, 1\n\t" \
    "s_cmp_ge_u32 s40, 65536\n\t" \
    "s_cbranch_scc1 .Lb51_%=\n\t" \
    "s_cbranch_vccnz .Ll51_%=\n\t" \
    "s_branch .Lb51_%=\n\t" \
    ".Ls52_%=:\n\t" \
    "s_mov_b32 s40, 0\n\t" \
    ".Ll52_%=:\n\t" \
    "s_add_u32 s54, s53, 1048576\n\t" \
    "buffer_load_dwordx4 v[76:79], v0, s[4:7], s54 offen offset:0 sc1\n\t" \
    "s_add_u32 s54, s53, 5292032\n\t" \
    "buffer_load_dwordx4 v[104:107], v0, s[4:7], s54 offen offset:0 sc1\n\t" \
    "s_waitcnt vmcnt(0)\n\t" \
    "v_and_b32 v37, v76, v78\n\t" \
    "v_bitop3_b32 v37, v37, v104, v106 bitop3:0x80\n\t" \
    "v_and_b32 v37, 1, v37\n\t" \
    "v_cmp_eq_u32 vcc, 0, v37\n\t" \
    "s_add_u32 s40, s40, 1\n\t" \
    "s_cmp_ge_u32 s40, 65536\n\t" \
    "s_cbranch_scc1 .Lb52_%=\n\t" \
    "s_cbranch_vccnz .Ll52_%=\n\t" \
    "s_branch .Lb52_%=\n\t" \
    ".Lfin_%=:\n\t"
#define K_CLOBBERS "v1", "v2", "v3", "v4", "v5", "v6", "v7", "v8", "v9", "v10", "v11", "v12", "v13", "v14", "v15", "v16", "v17", "v18", "v19", "v20", "v21", "v22", "v23", "v24", "v25", "v26", "v27", "v28", "v29", "v30", "v31", "v32", "v33", "v34", "v35", "v36", "v37", "v38", "v39", "v40", "v41", "v42", "v43", "v44", "v45", "v46", "v47", "v48", "v49", "v50", "v51", "v52", "v53", "v54", "v55", "v56", "v57", "v58", "v59", "v60", "v61", "v62", "v63", "v64", "v65", "v66", "v67", "v68", "v69", "v70", "v71", "v72", "v73", "v74", "v75", "v76", "v77", "v78", "v79", "v80", "v81", "v82", "v83", "v84", "v85", "v86", "v87", "v88", "v89", "v90", "v91", "v92", "v93", "v94", "v95", "v96", "v97", "v98", "v99", "v100", "v101", "v102", "v103", "v104", "v105", "v106", "v107", "v108", "v109", "v110", "v111", "v112", "v113", "v114", "v115", "v116", "v117", "v118", "v119", "v120", "v121", "v122", "v123", "v124", "v125", "v126", "v127", "v128", "v129", "v130", "v131", "v132", "v133", "v134", "v135", "v136", "v137", "v138", "v139", "v140", "v141", "v142", "v143", "v144", "v145", "v146", "v147", "v148", "v149", "v150", "v151", "v152", "v153", "v154", "v155", "v156", "v157", "v158", "v159", "v160", "v161", "v162", "v163", "v164", "v165", "v166", "v167", "v168", "v169", "v170", "v171", "v172", "v173", "v174", "v175", "v176", "v177", "v178", "v179", "v180", "v181", "v182", "v183", "v184", "v185", "v186", "v187", "v188", "v189", "v190", "v191", "v192", "v193", "v194", "v195", "v196", "v197", "v198", "v199", "v200", "v201", "v202", "v203", "v204", "v205", "v206", "v207", "v208", "v209", "v210", "v211", "v212", "v213", "v214", "v215", "v216", "v217", "v218", "v219", "v220", "v221", "v222", "v223", "v224", "v225", "v226", "v227", "v228", "v229", "v230", "v231", "v232", "v233", "v234", "v235", "v236", "v237", "v238", "v239", "v240", "v241", "v242", "v243", "v244", "v245", "v246", "v247", "v248", "v249", "v250", "v251", "v252", "v253", "v254", "v255", "s3", "s4", "s5", "s6", "s7", "s8", "s9", "s10", "s11", "s12", "s13", "s14", "s15", "s16", "s17", "s18", "s19", "s20", "s21", "s22", "s23", "s24", "s25", "s26", "s27", "s28", "s29", "s30", "s31", "s32", "s33", "s34", "s35", "s36", "s37", "s38", "s39", "s40", "s41", "s42", "s43", "s44", "s45", "s46", "s47", "s48", "s49", "s50", "s51", "s52", "s53", "s54", "s55", "s56", "s57", "s58", "s59", "s60", "s61", "s62", "s63", "s64", "s65"

extern "C" __global__ __launch_bounds__(512, 2)
void k_conv(const char* __restrict__ x, const char* __restrict__ wgt, char* __restrict__ ws, char* __restrict__ out)
{
  __shared__ __attribute__((aligned(256))) char smem[157568];
  unsigned tid = threadIdx.x;
  auto kp = __builtin_amdgcn_kernarg_segment_ptr();
  asm volatile(K_BODY : "+{v0}"(tid), "+{s[0:1]}"(kp)
               : "{s2}"(blockIdx.x), "i"((__attribute__((address_space(3))) char*)smem)
               : "memory", "scc", "vcc", K_CLOBBERS);
}

extern "C" void kernel_launch(void* const* d_in, const int* in_sizes, int n_in,
                              void* d_out, int out_size, void* d_ws, size_t ws_size,
                              hipStream_t stream) {
  k_conv<<<dim3(256), dim3(512), 0, stream>>>((const char*)d_in[0], (const char*)d_in[1], (char*)d_ws, (char*)d_out);
}
```

```cpp
#include <hip/hip_runtime.h>
#include <hip/hip_bf16.h>

#define K_BODY \
    "s_load_dwordx2 s[16:17], s[0:1], 0x0\n\t" \
    "s_load_dwordx2 s[4:5], s[0:1], 0x10\n\t" \
    "s_load_dwordx2 s[12:13], s[0:1], 0x8\n\t" \
    "s_load_dwordx2 s[8:9], s[0:1], 0x18\n\t" \
    "s_getpc_b64 s[22:23]\n\t" \
    ".Lpc_%=:\n\t" \
    "s_cmp_ge_u32 s2, 16\n\t" \
    "s_cbranch_scc1 .Le1_%=\n\t" \
    "s_mov_b32 m0, 157312\n\t" \
    "v_lshlrev_b32 v8, 6, v0\n\t" \
    "v_min_u32 v8, .Lfin_%=-.Lpc_%=, v8\n\t" \
    "global_load_lds_dword v8, s[22:23]\n\t" \
    ".Le1_%=:\n\t" \
    "v_readfirstlane_b32 s24, v0\n\t" \
    "s_and_b32 s25, s2, 3\n\t" \
    "s_lshl_b32 s25, s25, 5\n\t" \
    "s_lshr_b32 s37, s2, 3\n\t" \
    "s_add_u32 s25, s25, s37\n\t" \
    "s_min_u32 s28, s25, 125\n\t" \
    "s_mul_i32 s26, s28, 57\n\t" \
    "s_lshr_b32 s26, s26, 9\n\t" \
    "s_mul_i32 s37, s26, 9\n\t" \
    "s_sub_u32 s27, s28, s37\n\t" \
    "s_bfe_u32 s37, s2, 0x10002\n\t" \
    "s_lshl1_add_u32 s26, s26, s37\n\t" \
    "v_and_b32 v0, 63, v0\n\t" \
    "v_lshlrev_b32 v0, 4, v0\n\t" \
    "s_lshr_b32 s24, s24, 6\n\t" \
    "s_sub_u32 s37, s24, 4\n\t" \
    "s_cbranch_scc1 .Le2_%=\n\t" \
    "s_cmp_ge_u32 s25, 126\n\t" \
    "s_cbranch_scc1 .Le2_%=\n\t" \
    "s_mul_i32 m0, s37, 12416\n\t" \
    "s_lshl2_add_u32 s37, s26, s37\n\t" \
    "s_mul_i32 s37, s37, 86016\n\t" \
    "s_mul_i32 s38, s27, 4992\n\t" \
    "s_add_u32 s37, s37, s38\n\t" \
    "s_addk_i32 s37, 896\n\t" \
    "v_add_u32 v9, s37, v0\n\t" \
    "s_cmp_eq_u32 s27, 0\n\t" \
    "s_cselect_b32 s38, 43008, 0\n\t" \
    "s_sub_u32 s39, s38, 2048\n\t" \
    "v_add_u32 v7, s39, v9\n\t" \
    "v_subrev_u32 v8, 0x400, v9\n\t" \
    "s_mov_b64 exec, 0xff\n\t" \
    "v_add_u32 v8, s38, v8\n\t" \
    "s_mov_b64 exec, -1\n\t" \
    "s_mov_b32 s18, 9633792\n\t" \
    "s_mov_b32 s19, 0x20000\n\t" \
    "s_mov_b32 s39, 43008\n\t" \
    "s_waitcnt lgkmcnt(0)\n\t" \
    "buffer_load_dwordx4 v7, s[16:19], 0 offen offset:0 sc1 nt lds\n\t" \
    "s_add_u32 m0, m0, 2080\n\t" \
    "s_nop 0\n\t" \
    "buffer_load_dwordx4 v9, s[16:19], 0 offen offset:3072 sc1 nt lds\n\t" \
    "buffer_load_dwordx4 v9, s[16:19], 0 offen offset:2048 sc1 nt lds\n\t" \
    "s_add_u32 m0, m0, 4128\n\t" \
    "s_nop 0\n\t" \
    "buffer_load_dwordx4 v7, s[16:19], s39 offen offset:0 sc1 nt lds\n\t" \
    "s_add_u32 m0, m0, 2080\n\t" \
    "s_nop 0\n\t" \
    "buffer_load_dwordx4 v9, s[16:19], s39 offen offset:3072 sc1 nt lds\n\t" \
    "buffer_load_dwordx4 v9, s[16:19], s39 offen offset:2048 sc1 nt lds\n\t" \
    "buffer_load_dwordx4 v9, s[16:19], s39 offen offset:1024 sc1 nt lds\n\t" \
    "s_add_u32 m0, m0, 4294966240\n\t" \
    "s_nop 0\n\t" \
    "buffer_load_dwordx4 v8, s[16:19], s39 offen offset:0 sc1 nt lds\n\t" \
    "s_add_u32 m0, m0, 1024\n\t" \
    "s_nop 0\n\t" \
    "buffer_load_dwordx4 v9, s[16:19], s39 offen offset:0 sc1 nt lds\n\t" \
    "s_add_u32 m0, m0, 4294960064\n\t" \
    "s_nop 0\n\t" \
    "buffer_load_dwordx4 v8, s[16:19], 0 offen offset:0 sc1 nt lds\n\t" \
    "s_add_u32 m0, m0, 1024\n\t" \
    "s_nop 0\n\t" \
    "buffer_load_dwordx4 v9, s[16:19], 0 offen offset:0 sc1 nt lds\n\t" \
    "s_add_u32 m0, m0, 32\n\t" \
    "s_nop 0\n\t" \
    "buffer_load_dwordx4 v9, s[16:19], 0 offen offset:1024 sc1 nt lds\n\t" \
    ".Le2_%=:\n\t" \
    "s_mov_b32 s6, 34701312\n\t" \
    "s_mov_b32 s7, 0x20000\n\t" \
    "v_lshrrev_b32 v8, 7, v0\n\t" \
    "v_and_b32 v9, 0x70, v0\n\t" \
    "v_lshrrev_b32 v2, 8, v0\n\t" \
    "v_lshl_or_b32 v2, v2, 16, v9\n\t" \
    "v_and_b32 v10, 0x80, v0\n\t" \
    "v_lshl_or_b32 v2, v10, 6, v2\n\t" \
    "s_lshl_b32 s32, s2, 7\n\t" \
    "s_andn2_b32 s37, s32, 0x1fff\n\t" \
    "s_add_u32 s32, s32, s37\n\t" \
    "s_add_u32 s32, s32, 33554432\n\t" \
    "s_cmp_ge_u32 s24, 4\n\t" \
    "s_cbranch_scc1 .Le3_%=\n\t" \
    "v_mov_b32 v12, 0\n\t" \
    "v_mov_b32 v13, 0\n\t" \
    "v_mov_b32 v14, 0\n\t" \
    "v_mov_b32 v15, 0\n\t" \
    "s_lshl_b32 s37, s24, 18\n\t" \
    "s_add_u32 s37, s37, s32\n\t" \
    "s_xor_b32 s38, s24, 2\n\t" \
    "s_lshr_b32 s39, s2, 3\n\t" \
    "s_or_b32 s38, s38, s39\n\t" \
    "s_memrealtime s[22:23]\n\t" \
    "s_waitcnt lgkmcnt(0)\n\t" \
    "s_cbranch_scc1 .Le4_%=\n\t" \
    "v_lshl_or_b32 v8, v8, 10, v9\n\t" \
    "v_mov_b32 v16, s22\n\t" \
    "v_mov_b32 v17, s22\n\t" \
    "v_mov_b32 v18, s22\n\t" \
    "v_mov_b32 v19, s22\n\t" \
    "s_add_u32 s38, s32, 1122304\n\t" \
    "buffer_store_dwordx4 v[16:19], v8, s[4:7], s38 offen sc1\n\t" \
    ".Le4_%=:\n\t" \
    ".Le3_%=:\n\t" \
    "s_bitcmp1_b32 s2, 2\n\t" \
    "s_cselect_b32 s38, 126, 0\n\t" \
    "s_add_u32 s28, s28, s38\n\t" \
    "s_mul_i32 s30, s28, 293\n\t" \
    "s_lshr_b32 s30, s30, 11\n\t" \
    "s_mul_i32 s37, s30, 7\n\t" \
    "s_sub_u32 s28, s28, s37\n\t" \
    "s_mul_i32 s29, s30, 43\n\t" \
    "s_lshr_b32 s29, s29, 7\n\t" \
    "s_mul_i32 s37, s29, 3\n\t" \
    "s_sub_u32 s30, s30, s37\n\t" \
    "s_and_b32 s37, s29, 12\n\t" \
    "s_add_u32 s37, s37, s29\n\t" \
    "s_mul_i32 s31, s37, 768\n\t" \
    "s_lshl_b32 s37, s30, 6\n\t" \
    "s_add_u32 s31, s31, s37\n\t" \
    "s_add_u32 s41, s29, s30\n\t" \
    "s_add_u32 s41, s41, s28\n\t" \
    "s_and_b32 s41, s41, 3\n\t" \
    "s_cmp_lt_u32 s25, 126\n\t" \
    "s_cselect_b32 s14, 903168, 0\n\t" \
    "s_mov_b32 s15, 0x20000\n\t" \
    "v_lshrrev_b32 v10, 6, v0\n\t" \
    "v_lshrrev_b32 v11, 9, v0\n\t" \
    "v_sub_u32 v8, v10, v11\n\t" \
    "v_mul_u32_u24 v8, 18432, v8\n\t" \
    "v_and_b32 v9, 0x30, v0\n\t" \
    "v_lshlrev_b32 v11, 5, v11\n\t" \
    "v_bitop3_b32 v1, v8, v9, v11 bitop3:0xf6\n\t" \
    "s_mov_b32 exec_lo, 0xf0000000\n\t" \
    "s_mov_b32 exec_hi, 0xf0000000\n\t" \
    "v_bfrev_b32 v1, 1\n\t" \
    "s_mov_b64 exec, -1\n\t" \
    "s_cmp_ge_u32 s24, 4\n\t" \
    "s_cbranch_scc1 .Le9_%=\n\t" \
    "v_bfe_u32 v20, v0, 4, 4\n\t" \
    "v_and_b32 v10, 12, v10\n\t" \
    "v_cvt_f32_ubyte0 v21, v10\n\t" \
    "v_and_b32 v22, 1, v20\n\t" \
    "v_cvt_f32_ubyte0 v22, v22\n\t" \
    "v_mul_f32 v22, 0x3e800000, v22\n\t" \
    "v_lshrrev_b32 v23, 1, v20\n\t" \
    "v_mov_b32 v32, 0\n\t" \
    "v_mov_b32 v33, -1\n\t" \
    "v_mov_b32 v31, 0x3e800000\n\t" \
    "s_mov_b32 s65, 0\n\t" \
    "s_mov_b32 s40, 0\n\t" \
    "s_cmp_eq_u32 s24, 1\n\t" \
    "s_cbranch_scc1 .Lm1_%=\n\t" \
    "s_cmp_eq_u32 s24, 2\n\t" \
    "s_cbranch_scc1 .Lm2_%=\n\t" \
    "s_cmp_eq_u32 s24, 3\n\t" \
    "s_cbranch_scc1 .Lm3_%=\n\t" \
    "s_add_u32 s37, s32, 1048576\n\t" \
    "s_mov_b64 exec, 0xff\n\t" \
    "s_mov_b64 exec, -1\n\t" \
    "v_or_b32 v24, 1, v20\n\t" \
    "v_cvt_f32_ubyte0 v24, v24\n\t" \
    "v_mul_f32 v24, 0x3c800000, v24\n\t" \
    "v_lshrrev_b32 v25, 4, v0\n\t" \
    "v_and_b32 v25, 16, v25\n\t" \
    "v_lshrrev_b32 v26, 2, v11\n\t" \
    "v_or_b32 v25, v25, v26\n\t" \
    "v_cvt_f32_ubyte0 v25, v25\n\t" \
    "v_add_f32 v26, 2.0, v25\n\t" \
    "v_add_f32 v27, 4.0, v25\n\t" \
    "v_add_f32 v28, 4.0, v26\n\t" \
    "v_mov_b32 v29, v22\n\t" \
    "v_mov_b32 v30, v22\n\t" \
    "v_mov_b32 v31, v24\n\t" \
    "s_mov_b32 s64, 0x3e800000\n\t" \
    "s_mov_b32 s65, 0x3e800000\n\t" \
    "v_add_u32 v34, 154880, v0\n\t" \
    "s_branch .Ltl_%=\n\t" \
    ".Lm1_%=:\n\t" \
    "v_lshrrev_b32 v24, 5, v0\n\t" \
    "v_and_b32 v24, 12, v24\n\t" \
    "v_and_or_b32 v24, v20, 3, v24\n\t" \
    "v_cvt_f32_ubyte0 v24, v24\n\t" \
    "v_mul_f32 v24, 0x3d924925, v24\n\t" \
    "v_mul_f32 v32, 0x41000000, v24\n\t" \
    "v_lshrrev_b32 v27, 3, v11\n\t" \
    "v_cvt_f32_ubyte0 v27, v27\n\t" \
    "v_add_f32 v25, -2.0, v27\n\t" \
    "v_add_f32 v26, -1.0, v27\n\t" \
    "v_add_f32 v28, 1.0, v27\n\t" \
    "v_and_b32 v29, 4, v20\n\t" \
    "v_cvt_f32_ubyte0 v29, v29\n\t" \
    "v_mul_f32 v29, 0x3d800000, v29\n\t" \
    "v_mov_b32 v30, v29\n\t" \
    "v_mov_b32 v31, 0xbe800000\n\t" \
    "s_mov_b32 s64, 0\n\t" \
    "s_mov_b32 exec_hi, 0\n\t" \
    "v_mov_b32 v33, 0\n\t" \
    "s_mov_b64 exec, -1\n\t" \
    "v_add_u32 v34, 149760, v0\n\t" \
    "s_branch .Ltl_%=\n\t" \
    ".Lm2_%=:\n\t" \
    "v_add_u32 v24, 6, v20\n\t" \
    "v_cvt_f32_ubyte0 v24, v24\n\t" \
    "v_mul_f32 v24, 0x3d000000, v24\n\t" \
    "v_add_f32 v25, 0.5, v21\n\t" \
    "v_add_f32 v26, 1.0, v25\n\t" \
    "v_add_f32 v27, 2.0, v25\n\t" \
    "v_add_f32 v28, 2.0, v26\n\t" \
    "v_mov_b32 v29, 0\n\t" \
    "v_mov_b32 v30, 0\n\t" \
    "s_mov_b32 s64, 0.5\n\t" \
    "v_add_u32 v34, 152832, v0\n\t" \
    "s_branch .Ltl_%=\n\t" \
    ".Lm3_%=:\n\t" \
    "s_mov_b32 s37, 34668544\n\t" \
    "buffer_load_dword v3, v12, s[4:7], s37 offen sc1\n\t" \
    "v_lshrrev_b32 v8, 2, v0\n\t" \
    "v_and_b32 v8, 28, v8\n\t" \
    "v_add_u32 v8, 156928, v8\n\t" \
    "ds_write_b32 v8, v12\n\t" \
    "v_add_u32 v24, 6, v23\n\t" \
    "v_cvt_f32_ubyte0 v24, v24\n\t" \
    "v_mul_f32 v24, 0x3d924925, v24\n\t" \
    "v_mov_b32 v25, v21\n\t" \
    "v_add_f32 v26, 1.0, v21\n\t" \
    "v_add_f32 v27, 2.0, v21\n\t" \
    "v_add_f32 v28, 2.0, v26\n\t" \
    "v_sub_f32 v29, 0, v22\n\t" \
    "v_sub_f32 v30, 0, v22\n\t" \
    "s_mov_b32 exec_lo, 0\n\t" \
    "v_fma_f32 v25, v21, 0.5, 4.0\n\t" \
    "v_add_f32 v27, 1.0, v25\n\t" \
    "v_fmaak_f32 v26, v21, v31, 0x41200000\n\t" \
    "v_mov_b32 v28, v26\n\t" \
    "v_add_f32 v30, v31, v30\n\t" \
    "s_mov_b64 exec, -1\n\t" \
    "s_mov_b32 s40, 0x11\n\t" \
    "v_add_u32 v34, 151808, v0\n\t" \
    ".Ltl_%=:\n\t" \
    "v_fma_f32 v36, v24, v25, v29\n\t" \
    "v_fma_f32 v38, v24, v26, v29\n\t" \
    "v_fma_f32 v40, v24, v27, v29\n\t" \
    "v_fma_f32 v42, v24, v28, v30\n\t" \
    "v_add_f32 v37, v36, v31\n\t" \
    "v_add_f32 v39, v38, v31\n\t" \
    "v_add_f32 v41, v40, v31\n\t" \
    "v_add_f32 v43, v42, v31\n\t" \
    "v_cos_f32 v36, v36\n\t" \
    "v_cos_f32 v37, v37\n\t" \
    "v_cos_f32 v38, v38\n\t" \
    "v_cos_f32 v39, v39\n\t" \
    "v_cos_f32 v40, v40\n\t" \
    "v_cos_f32 v41, v41\n\t" \
    "v_cos_f32 v42, v42\n\t" \
    "v_cos_f32 v43, v43\n\t" \
    "s_bitcmp0_b32 s40, 4\n\t" \
    "s_cbranch_scc1 .Lt2_%=\n\t" \
    "v_mul_f32 v36, 0x3b924925, v36\n\t" \
    "v_mul_f32 v37, 0x3b924925, v37\n\t" \
    "v_mul_f32 v38, 0x3b924925, v38\n\t" \
    "v_mul_f32 v39, 0x3b924925, v39\n\t" \
    "v_mul_f32 v40, 0x3b924925, v40\n\t" \
    "v_mul_f32 v41, 0x3b924925, v41\n\t" \
    "v_mul_f32 v42, 0x3b924925, v42\n\t" \
    "v_mul_f32 v43, 0x3b924925, v43\n\t" \
    ".Lt2_%=:\n\t" \
    "v_cvt_pk_bf16_f32 v44, v36, v37\n\t" \
    "v_cvt_pk_bf16_f32 v45, v38, v39\n\t" \
    "v_cvt_pk_bf16_f32 v46, v40, v41\n\t" \
    "v_cvt_pk_bf16_f32 v47, v42, v43\n\t" \
    "v_and_b32 v44, v33, v44\n\t" \
    "v_and_b32 v45, v33, v45\n\t" \
    "v_mov_b32 v33, -1\n\t" \
    "v_add_f32 v24, s64, v24\n\t" \
    "v_add_f32 v31, s65, v31\n\t" \
    "v_add_f32 v29, v32, v29\n\t" \
    "v_add_f32 v30, v32, v30\n\t" \
    "ds_write_b128 v34, v[44:47]\n\t" \
    "s_add_u32 s40, s40, 1\n\t" \
    "s_bitcmp1_b32 s40, 0\n\t" \
    "v_add_u32 v34, 0x400, v34\n\t" \
    "s_cbranch_scc1 .Ltl_%=\n\t" \
    "s_bitcmp1_b32 s40, 2\n\t" \
    "s_cbranch_scc1 .Lt4_%=\n\t" \
    "s_mov_b32 s40, 2\n\t" \
    "s_lshl2_add_u32 s37, s28, s24\n\t" \
    "s_lshl_b32 s37, s37, 2\n\t" \
    "v_lshrrev_b32 v25, 2, v20\n\t" \
    "v_add_u32 v25, s37, v25\n\t" \
    "v_mul_u32_u24 v26, 147, v25\n\t" \
    "v_lshrrev_b32 v26, 10, v26\n\t" \
    "v_lshl_or_b32 v24, v26, 1, 1\n\t" \
    "v_cvt_f32_ubyte0 v24, v24\n\t" \
    "v_mul_f32 v24, 0x3c800000, v24\n\t" \
    "v_mul_u32_u24 v26, 7, v26\n\t" \
    "v_sub_u32 v25, v25, v26\n\t" \
    "v_xor_b32 v22, s41, v20\n\t" \
    "v_bfe_u32 v32, v22, 1, 1\n\t" \
    "v_and_b32 v22, 1, v22\n\t" \
    "v_cvt_f32_ubyte0 v22, v22\n\t" \
    "v_mul_f32 v22, 0x3e800000, v22\n\t" \
    "v_lshl_or_b32 v32, v25, 1, v32\n\t" \
    "v_cvt_f32_ubyte0 v32, v32\n\t" \
    "v_mul_f32 v32, 0x3e924925, v32\n\t" \
    "v_mul_f32 v29, 0x3d800000, v21\n\t" \
    "v_fma_f32 v29, v29, v32, v22\n\t" \
    "v_mov_b32 v30, v29\n\t" \
    "v_mov_b32 v31, v24\n\t" \
    "v_mov_b32 v25, 0\n\t" \
    "v_mov_b32 v26, 2.0\n\t" \
    "v_mov_b32 v27, 4.0\n\t" \
    "v_mov_b32 v28, 0x40c00000\n\t" \
    "s_mov_b32 s64, 0\n\t" \
    "s_mov_b32 s65, 0\n\t" \
    "s_lshl_b32 s37, s24, 11\n\t" \
    "s_add_u32 s37, s37, 49664\n\t" \
    "v_add_u32 v34, s37, v0\n\t" \
    "s_branch .Ltl_%=\n\t" \
    ".Lt4_%=:\n\t" \
    ".Lt3_%=:\n\t" \
    "s_cmp_lg_u32 s24, 3\n\t" \
    "s_cbranch_scc1 .Lx2_%=\n\t" \
    "v_lshrrev_b32 v8, 7, v0\n\t" \
    "v_mul_u32_u24 v8, 96, v8\n\t" \
    "v_and_b32 v9, 16, v0\n\t" \
    "v_bfe_u32 v10, v0, 5, 2\n\t" \
    "v_add3_u32 v8, v8, v9, v10\n\t" \
    "v_mul_u32_u24 v8, 192, v8\n\t" \
    "s_mul_i32 s37, s28, 129024\n\t" \
    "s_add_u32 s37, s37, s31\n\t" \
    "s_mov_b32 m0, 157312\n\t" \
    "s_mov_b32 exec_hi, 0xffffff\n\t" \
    "buffer_load_dword v8, s[12:15], s37 offen lds\n\t" \
    "s_mov_b64 exec, -1\n\t" \
    ".Lx2_%=:\n\t" \
    ".Le9_%=:\n\t" \
    "s_lshr_b32 s37, s24, 2\n\t" \
    "s_and_b32 s38, s24, 2\n\t" \
    "s_mul_i32 s39, s37, 24832\n\t" \
    "s_lshl4_add_u32 s39, s38, s39\n\t" \
    "v_lshrrev_b32 v4, 6, v0\n\t" \
    "v_and_b32 v5, 12, v4\n\t" \
    "v_add_u32 v6, v4, v5\n\t" \
    "v_lshrrev_b32 v7, 1, v0\n\t" \
    "v_and_b32 v7, 24, v7\n\t" \
    "v_and_b32 v4, 11, v4\n\t" \
    "v_mul_u32_u24 v4, 192, v4\n\t" \
    "v_bfe_u32 v5, v0, 8, 1\n\t" \
    "v_mul_u32_u24 v5, 3104, v5\n\t" \
    "v_add3_u32 v4, v4, v5, v7\n\t" \
    "v_add_u32 v8, s39, v4\n\t" \
    "v_mov_b32 v9, 156928\n\t" \
    "v_add_u32 v10, 149760, v0\n\t" \
    "s_lshl2_add_u32 s37, s37, s38\n\t" \
    "s_lshl_b32 s37, s37, 7\n\t" \
    "s_mul_i32 s53, s27, 28\n\t" \
    "s_add_u32 s53, s53, s26\n\t" \
    "s_andn2_b32 s39, s53, 7\n\t" \
    "s_add_u32 s53, s53, s39\n\t" \
    "s_lshl_b32 s53, s53, 10\n\t" \
    "s_add_u32 s37, s37, s53\n\t" \
    "s_and_b32 s38, s24, 1\n\t" \
    "s_lshl_b32 s39, s38, 13\n\t" \
    "s_add_u32 s37, s37, s39\n\t" \
    "s_cmp_lt_u32 s25, 126\n\t" \
    "s_cselect_b32 s43, 0, 0x80000000\n\t" \
    "s_add_u32 s37, s37, s43\n\t" \
    "v_lshrrev_b32 v11, 8, v0\n\t" \
    "v_and_b32 v12, 0xf0, v0\n\t" \
    "v_mul_u32_u24 v4, 8486912, v11\n\t" \
    "v_add3_u32 v13, v4, v12, s37\n\t" \
    "s_lshl_b32 s39, s38, 10\n\t" \
    "v_add_u32 v14, s39, v10\n\t" \
    "s_lshl_b32 s39, s38, 5\n\t" \
    "s_add_u32 s39, s39, 116736\n\t" \
    "v_lshl_add_u32 v4, v6, 6, v7\n\t" \
    "v_lshrrev_b32 v5, 3, v0\n\t" \
    "v_and_b32 v5, 32, v5\n\t" \
    "v_xor_b32 v5, s39, v5\n\t" \
    "v_add_u32 v15, v4, v5\n\t" \
    "s_and_b32 s37, s24, 6\n\t" \
    "s_lshl_b32 s37, s37, 10\n\t" \
    "v_add_u32 v16, s37, v0\n\t" \
    "s_cmp_ge_u32 s25, 126\n\t" \
    "s_cbranch_scc1 .Lqa_%=\n\t" \
    ".Lqd_%=:\n\t" \
    "s_waitcnt lgkmcnt(0)\n\t" \
    "s_barrier\n\t" \
    "ds_read_b128 v[24:27], v14 offset:5120\n\t" \
    "ds_read_b128 v[28:31], v16 offset:49664\n\t" \
    "ds_read_b128 v[32:35], v16 offset:50688\n\t" \
    "s_and_b32 s33, s2, 7\n\t" \
    "s_lshl_b32 s33, s33, 15\n\t" \
    "s_add_u32 s33, s33, 33554432\n\t" \
    "s_lshr_b32 s37, s29, 2\n\t" \
    "s_mul_i32 s39, s30, 3\n\t" \
    "s_add_u32 s37, s37, s39\n\t" \
    "s_mul_i32 s37, s37, 114688\n\t" \
    "s_and_b32 s39, s29, 3\n\t" \
    "s_lshl_b32 s39, s39, 8\n\t" \
    "s_add_u32 s37, s37, s39\n\t" \
    "s_add_u32 s37, s37, s43\n\t" \
    "s_add_u32 s37, s37, 2097152\n\t" \
    "s_and_b32 s39, s24, 6\n\t" \
    "s_lshl3_add_u32 s39, s28, s39\n\t" \
    "s_lshl_b32 s39, s39, 1\n\t" \
    "v_add_u32 v14, s39, v11\n\t" \
    "v_mul_u32_u24 v17, 147, v14\n\t" \
    "v_lshrrev_b32 v17, 10, v17\n\t" \
    "s_and_b32 s39, s24, 1\n\t" \
    "v_subrev_u32 v18, s39, v17\n\t" \
    "v_mul_i32_i24 v18, 7, v18\n\t" \
    "v_sub_u32 v14, v14, v18\n\t" \
    "v_and_b32 v18, 14, v14\n\t" \
    "v_add_lshl_u32 v14, v14, v18, 12\n\t" \
    "v_and_b32 v18, 7, v17\n\t" \
    "v_mul_u32_u24 v18, 4243456, v18\n\t" \
    "v_add3_u32 v14, v14, v18, v12\n\t" \
    "v_and_b32 v17, 8, v17\n\t" \
    "v_xor_b32 v17, 8, v17\n\t" \
    "v_lshl_add_u32 v14, v17, 10, v14\n\t" \
    "v_add_u32 v19, s37, v14\n\t" \
    "s_lshl_b32 s43, s41, 10\n\t" \
    "s_and_b32 s37, s2, 0x70\n\t" \
    "s_lshl_b32 s37, s37, 1\n\t" \
    "s_lshl2_add_u32 s57, s24, s37\n\t" \
    "s_min_u32 s58, s57, 248\n\t" \
    "s_mul_i32 s59, s58, 147\n\t" \
    "s_lshr_b32 s59, s59, 12\n\t" \
    "s_mul_i32 s37, s59, 28\n\t" \
    "s_sub_u32 s60, s58, s37\n\t" \
    "s_andn2_b32 s39, s58, 7\n\t" \
    "s_add_u32 s51, s58, s39\n\t" \
    "s_lshl_b32 s51, s51, 10\n\t" \
    "s_sub_u32 s37, s60, 1\n\t" \
    "s_cselect_b32 s52, 27, -1\n\t" \
    "s_add_u32 s52, s52, s58\n\t" \
    "s_andn2_b32 s39, s52, 7\n\t" \
    "s_add_u32 s52, s52, s39\n\t" \
    "s_lshl_b32 s52, s52, 10\n\t" \
    "v_lshrrev_b32 v16, 9, v0\n\t" \
    "v_and_b32 v18, 0x2f0, v0\n\t" \
    "v_and_b32 v17, 1, v11\n\t" \
    "v_lshl_add_u32 v14, v17, 11, v18\n\t" \
    "v_add_u32 v36, s51, v14\n\t" \
    "v_add_u32 v37, s52, v0\n\t" \
    "s_mul_i32 s44, s24, 7296\n\t" \
    "s_mul_i32 s37, s60, 344064\n\t" \
    "s_mul_i32 s39, s59, 4992\n\t" \
    "s_add_u32 s37, s37, s39\n\t" \
    "s_cmp_eq_u32 s59, 8\n\t" \
    "s_movk_i32 s39, 2496\n\t" \
    "s_cselect_b32 s39, 1536, s39\n\t" \
    "s_add_u32 s37, s37, s39\n\t" \
    "s_and_b32 s39, s8, 0x1fffff\n\t" \
    "s_add_u32 s37, s37, s39\n\t" \
    "v_bfe_u32 v38, v0, 5, 2\n\t" \
    "v_lshl_add_u32 v38, v16, 4, v38\n\t" \
    "v_mul_u32_u24 v38, 43008, v38\n\t" \
    "v_add_u32 v39, s37, v38\n\t" \
    "s_lshr_b32 s37, s60, 1\n\t" \
    "s_mul_i32 s37, s37, 9\n\t" \
    "s_add_u32 s37, s37, s59\n\t" \
    "v_mad_u32_u24 v38, v16, 9, s37\n\t" \
    "v_lshrrev_b32 v18, 5, v38\n\t" \
    "s_cmp_lt_u32 s57, 252\n\t" \
    "s_cselect_b32 s51, s51, 0x80000000\n\t" \
    "s_lshl_b32 s37, s24, 2\n\t" \
    "s_lshl1_add_u32 s39, s24, 8\n\t" \
    "s_min_u32 s61, s37, s39\n\t" \
    "s_add_u32 s37, s37, 2\n\t" \
    "s_add_u32 s39, s24, 20\n\t" \
    "s_cmp_lt_u32 s24, 4\n\t" \
    "s_cselect_b32 s62, s37, s39\n\t" \
    "s_bfe_u32 s63, s2, 0x10003\n\t" \
    "s_mul_i32 s47, s63, 28\n\t" \
    "s_add_u32 s45, s47, s61\n\t" \
    "s_add_u32 s46, s47, s62\n\t" \
    "s_andn2_b32 s39, s45, 7\n\t" \
    "s_add_u32 s45, s45, s39\n\t" \
    "s_lshl_b32 s45, s45, 10\n\t" \
    "s_andn2_b32 s39, s46, 7\n\t" \
    "s_add_u32 s46, s46, s39\n\t" \
    "s_lshl_b32 s46, s46, 10\n\t" \
    "v_lshlrev_b32 v17, 4, v11\n\t" \
    "v_lshrrev_b32 v14, 6, v12\n\t" \
    "v_mul_u32_u24 v14, 7296, v14\n\t" \
    "v_and_b32 v38, 16, v0\n\t" \
    "v_mul_u32_u24 v38, 226, v38\n\t" \
    "v_and_b32 v40, 32, v0\n\t" \
    "v_lshl_add_u32 v38, v40, 1, v38\n\t" \
    "v_add3_u32 v14, v14, v38, v17\n\t" \
    "s_lshl_b32 s37, s61, 7\n\t" \
    "v_add_u32 v44, s37, v14\n\t" \
    "s_and_b32 s37, s62, 30\n\t" \
    "s_lshl_b32 s37, s37, 7\n\t" \
    "v_add_u32 v45, s37, v14\n\t" \
    "s_mov_b32 s1, 0\n\t" \
    "s_mov_b32 s41, 0x18000\n\t" \
    "s_lshl_b32 s37, s63, 9\n\t" \
    "s_add_u32 s51, s51, s37\n\t" \
    "s_and_b32 s37, s2, 0x87\n\t" \
    "s_lshl4_add_u32 s37, s24, s37\n\t" \
    "s_lshl_b32 s55, s37, 7\n\t" \
    "s_andn2_b32 s37, s55, 0x1fff\n\t" \
    "s_add_u32 s55, s55, s37\n\t" \
    "v_and_b32 v46, 16, v0\n\t" \
    "v_lshlrev_b32 v47, 6, v46\n\t" \
    "v_mad_u32_u24 v38, v12, 12, v17\n\t" \
    "v_and_b32 v14, 0x170, v0\n\t" \
    "v_and_b32 v41, 0x80, v0\n\t" \
    "v_lshl_add_u32 v14, v41, 3, v14\n\t" \
    "v_lshl_add_u32 v14, v16, 11, v14\n\t" \
    "v_add_u32 v42, s51, v14\n\t" \
    "s_mul_i32 s37, s24, 2064\n\t" \
    "v_and_b32 v14, 0x300, v0\n\t" \
    "v_add3_u32 v43, v14, v0, s37\n\t" \
    "s_lshl_b32 s37, s37, 1\n\t" \
    "v_lshlrev_b32 v14, 7, v16\n\t" \
    "v_xor_b32 v14, v14, v0\n\t" \
    "v_add_u32 v40, s37, v14\n\t" \
    "v_lshrrev_b32 v14, 5, v12\n\t" \
    "v_mul_u32_u24 v14, 2064, v14\n\t" \
    "v_lshl_add_u32 v14, v46, 2, v14\n\t" \
    "s_lshl_b32 s37, s24, 8\n\t" \
    "v_add3_u32 v48, v14, v17, s37\n\t" \
    "v_bfe_u32 v14, v0, 7, 1\n\t" \
    "v_lshl_add_u32 v14, v11, 2, v14\n\t" \
    "v_mul_u32_u24 v14, 2064, v14\n\t" \
    "v_lshrrev_b32 v41, 3, v0\n\t" \
    "v_and_or_b32 v14, v41, 8, v14\n\t" \
    "s_and_b32 s37, s24, 3\n\t" \
    "s_lshl_b32 s37, s37, 6\n\t" \
    "s_and_b32 s39, s24, 4\n\t" \
    "s_lshl_b32 s39, s39, 8\n\t" \
    "s_or_b32 s37, s37, s39\n\t" \
    "v_and_or_b32 v41, v0, 32, s37\n\t" \
    "v_mul_u32_u24 v46, 40, v46\n\t" \
    "v_xor_b32 v41, v41, v46\n\t" \
    "v_add_u32 v49, v14, v41\n\t" \
    "ds_read_b128 v[52:55], v10 offset:0\n\t" \
    "ds_read_b128 v[56:59], v10 offset:1024\n\t" \
    "s_mov_b64 exec, 0\n\t" \
    "s_cmp_eq_u32 s24, 0\n\t" \
    "s_cbranch_scc1 .Lz0_%=\n\t" \
    "s_cmp_eq_u32 s24, 1\n\t" \
    "s_cbranch_scc1 .Lz1_%=\n\t" \
    "s_cmp_eq_u32 s24, 2\n\t" \
    "s_cbranch_scc1 .Lz2_%=\n\t" \
    "s_cmp_eq_u32 s24, 3\n\t" \
    "s_cbranch_scc1 .Lz3_%=\n\t" \
    "s_cmp_eq_u32 s24, 4\n\t" \
    "s_cbranch_scc1 .Lz4_%=\n\t" \
    "s_cmp_eq_u32 s24, 5\n\t" \
    "s_cbranch_scc1 .Lz5_%=\n\t" \
    ".Lwk_%=:\n\t" \
    "s_mov_b64 exec, -1\n\t" \
    ".Lz0_%=:\n\t" \
    "s_cbranch_execz .Lwk_%=\n\t" \
    "s_sleep 3\n\t" \
    "s_and_b32 s37, s24, 1\n\t" \
    "s_lshl_b32 s37, s37, 4\n\t" \
    "s_lshr_b32 s39, s24, 1\n\t" \
    "s_add_u32 s37, s37, s39\n\t" \
    "s_mul_i32 s37, s37, 192\n\t" \
    "s_add_u32 s37, s37, s31\n\t" \
    "s_lshl_b32 s39, s24, 12\n\t" \
    "s_add_u32 m0, s39, 116736\n\t" \
    "s_add_u32 s38, s37, 0\n\t" \
    "buffer_load_dwordx4 v1, s[12:15], s38 offen  lds\n\t" \
    "s_add_u32 m0, s39, 117760\n\t" \
    "s_add_u32 s38, s37, 258048\n\t" \
    "buffer_load_dwordx4 v1, s[12:15], s38 offen  lds\n\t" \
    "s_add_u32 m0, s39, 118784\n\t" \
    "s_add_u32 s38, s37, 516096\n\t" \
    "buffer_load_dwordx4 v1, s[12:15], s38 offen  lds\n\t" \
    "s_add_u32 m0, s39, 119808\n\t" \
    "s_add_u32 s38, s37, 774144\n\t" \
    "buffer_load_dwordx4 v1, s[12:15], s38 offen  lds\n\t" \
    "s_cmp_lg_u32 s24, 3\n\t" \
    "s_cbranch_scc1 .Lr9_%=\n\t" \
    "s_and_b32 s37, s2, 7\n\t" \
    "s_lshl_b32 s37, s37, 10\n\t" \
    "s_add_u32 s37, s37, 34676736\n\t" \
    "s_buffer_load_dword s56, s[4:7], s37 offset:0x0\n\t" \
    "s_buffer_load_dword s57, s[4:7], s37 offset:0x80\n\t" \
    "s_buffer_load_dword s58, s[4:7], s37 offset:0x100\n\t" \
    "s_buffer_load_dword s59, s[4:7], s37 offset:0x180\n\t" \
    "s_buffer_load_dword s60, s[4:7], s37 offset:0x200\n\t" \
    "s_buffer_load_dword s61, s[4:7], s37 offset:0x280\n\t" \
    "s_buffer_load_dword s62, s[4:7], s37 offset:0x300\n\t" \
    "s_buffer_load_dword s63, s[4:7], s37 offset:0x380\n\t" \
    "s_mov_b32 s40, 0\n\t" \
    ".Lr1_%=:\n\t" \
    "s_waitcnt lgkmcnt(0)\n\t" \
    "s_memrealtime s[22:23]\n\t" \
    "s_waitcnt lgkmcnt(0)\n\t" \
    "s_mov_b32 s37, 1000\n\t" \
    "s_mov_b32 s38, 0\n\t" \
    "s_sub_u32 s39, s22, s56\n\t" \
    "s_min_u32 s37, s37, s39\n\t" \
    "s_max_u32 s38, s38, s39\n\t" \
    "s_sub_u32 s39, s22, s57\n\t" \
    "s_min_u32 s37, s37, s39\n\t" \
    "s_max_u32 s38, s38, s39\n\t" \
    "s_sub_u32 s39, s22, s58\n\t" \
    "s_min_u32 s37, s37, s39\n\t" \
    "s_max_u32 s38, s38, s39\n\t" \
    "s_sub_u32 s39, s22, s59\n\t" \
    "s_min_u32 s37, s37, s39\n\t" \
    "s_max_u32 s38, s38, s39\n\t" \
    "s_sub_u32 s39, s22, s60\n\t" \
    "s_min_u32 s37, s37, s39\n\t" \
    "s_max_u32 s38, s38, s39\n\t" \
    "s_sub_u32 s39, s22, s61\n\t" \
    "s_min_u32 s37, s37, s39\n\t" \
    "s_max_u32 s38, s38, s39\n\t" \
    "s_sub_u32 s39, s22, s62\n\t" \
    "s_min_u32 s37, s37, s39\n\t" \
    "s_max_u32 s38, s38, s39\n\t" \
    "s_sub_u32 s39, s22, s63\n\t" \
    "s_min_u32 s37, s37, s39\n\t" \
    "s_max_u32 s38, s38, s39\n\t" \
    "s_cmp_gt_u32 s38, 1000\n\t" \
    "s_cbranch_scc1 .Lr2_%=\n\t" \
    ".Lr3_%=:\n\t" \
    "s_sub_u32 s35, s22, s37\n\t" \
    "s_waitcnt vmcnt(5)\n\t" \
    "v_readfirstlane_b32 s34, v3\n\t" \
    "s_mul_i32 s34, s34, 1664525\n\t" \
    "s_add_u32 s34, s34, 1013904223\n\t" \
    "s_or_b32 s34, s34, 1\n\t" \
    "v_mov_b32 v60, s34\n\t" \
    "v_mov_b32 v61, s35\n\t" \
    "ds_write_b64 v9, v[60:61] offset:32\n\t" \
    ".Lr9_%=:\n\t" \
    "s_cmp_lt_u32 s24, 4\n\t" \
    "s_cbranch_scc1 .Lw_%=\n\t" \
    "s_waitcnt vmcnt(4)\n\t" \
    ".Lw_%=:\n\t" \
    "s_waitcnt lgkmcnt(0)\n\t" \
    "s_barrier\n\t" \
    "ds_read_b64_tr_b16 v[64:65], v8 offset:0\n\t" \
    "ds_read_b64_tr_b16 v[66:67], v8 offset:768\n\t" \
    "ds_read_b64_tr_b16 v[68:69], v8 offset:12416\n\t" \
    "ds_read_b64_tr_b16 v[70:71], v8 offset:13184\n\t" \
    "ds_read_b64_tr_b16 v[72:73], v8 offset:6208\n\t" \
    "ds_read_b64_tr_b16 v[74:75], v8 offset:6976\n\t" \
    "ds_read_b64_tr_b16 v[76:77], v8 offset:18624\n\t" \
    "ds_read_b64_tr_b16 v[78:79], v8 offset:19392\n\t" \
    "ds_read_b128 v[80:83], v9 offset:32\n\t" \
    "s_waitcnt lgkmcnt(7)\n\t" \
    "v_mfma_f32_16x16x32_bf16 v[84:87], v[24:27], v[64:67], 0\n\t" \
    "s_waitcnt lgkmcnt(5)\n\t" \
    "v_mfma_f32_16x16x32_bf16 v[88:91], v[24:27], v[68:71], 0\n\t" \
    "s_waitcnt lgkmcnt(3)\n\t" \
    "v_mfma_f32_16x16x32_bf16 v[92:95], v[24:27], v[72:75], 0\n\t" \
    "s_waitcnt lgkmcnt(1)\n\t" \
    "v_mfma_f32_16x16x32_bf16 v[96:99], v[24:27], v[76:79], 0\n\t" \
    "s_waitcnt lgkmcnt(0)\n\t" \
    "v_readfirstlane_b32 s34, v80\n\t" \
    "v_readfirstlane_b32 s35, v81\n\t" \
    "v_add_u32 v84, 0x8000, v84\n\t" \
    "v_add_u32 v86, 0x8000, v86\n\t" \
    "v_add_u32 v88, 0x8000, v88\n\t" \
    "v_add_u32 v90, 0x8000, v90\n\t" \
    "v_cvt_pk_bf16_f32 v108, v84, v85\n\t" \
    "v_cvt_pk_bf16_f32 v110, v88, v89\n\t" \
    "v_cvt_pk_bf16_f32 v109, v92, v93\n\t" \
    "v_cvt_pk_bf16_f32 v111, v96, v97\n\t" \
    "v_and_b32 v108, -2, v108\n\t" \
    "v_and_b32 v110, -2, v110\n\t" \
    "v_cvt_pk_bf16_f32 v112, v86, v87\n\t" \
    "v_cvt_pk_bf16_f32 v114, v90, v91\n\t" \
    "v_cvt_pk_bf16_f32 v113, v94, v95\n\t" \
    "v_cvt_pk_bf16_f32 v115, v98, v99\n\t" \
    "v_and_b32 v112, -2, v112\n\t" \
    "v_and_b32 v114, -2, v114\n\t" \
    ".Lh38_%=:\n\t" \
    "s_memrealtime s[22:23]\n\t" \
    "s_waitcnt lgkmcnt(0)\n\t" \
    "s_sub_u32 s22, s22, s35\n\t" \
    "s_cmp_lt_u32 s22, 205\n\t" \
    "s_cbranch_scc1 .Lh38_%=\n\t" \
    "buffer_store_dwordx4 v[108:111], v13, s[4:7], 0 offen offset:0 sc1\n\t" \
    "s_mov_b32 s42, 4243456\n\t" \
    "buffer_store_dwordx4 v[112:115], v13, s[4:7], s42 offen offset:0 sc1\n\t" \
    "ds_read_b64_tr_b16 v[116:117], v8 offset:64\n\t" \
    "ds_read_b64_tr_b16 v[118:119], v8 offset:832\n\t" \
    "ds_read_b64_tr_b16 v[120:121], v8 offset:12480\n\t" \
    "ds_read_b64_tr_b16 v[122:123], v8 offset:13248\n\t" \
    "ds_read_b64_tr_b16 v[124:125], v8 offset:6272\n\t" \
    "ds_read_b64_tr_b16 v[126:127], v8 offset:7040\n\t" \
    "ds_read_b64_tr_b16 v[128:129], v8 offset:18688\n\t" \
    "ds_read_b64_tr_b16 v[130:131], v8 offset:19456\n\t" \
    "ds_read_b64_tr_b16 v[132:133], v8 offset:128\n\t" \
    "ds_read_b64_tr_b16 v[134:135], v8 offset:896\n\t" \
    "ds_read_b64_tr_b16 v[136:137], v8 offset:12544\n\t" \
    "ds_read_b64_tr_b16 v[138:139], v8 offset:13312\n\t" \
    "ds_read_b64_tr_b16 v[140:141], v8 offset:6336\n\t" \
    "ds_read_b64_tr_b16 v[142:143], v8 offset:7104\n\t" \
    "ds_read_b64_tr_b16 v[144:145], v8 offset:18752\n\t" \
    "ds_read_b64_tr_b16 v[146:147], v8 offset:19520\n\t" \
    "v_mov_b32 v148, s34\n\t" \
    "v_mov_b32 v149, s34\n\t" \
    "v_mov_b32 v150, s34\n\t" \
    "v_mov_b32 v151, s34\n\t" \
    "s_and_b32 s37, s2, 243\n\t" \
    "s_cmp_eq_u32 s37, 243\n\t" \
    "s_cbranch_scc1 .Lskip_%=\n\t" \
    "s_waitcnt lgkmcnt(14)\n\t" \
    "v_mfma_f32_16x16x32_bf16 v[152:155], v[24:27], v[116:119], 0\n\t" \
    "s_waitcnt lgkmcnt(12)\n\t" \
    "v_mfma_f32_16x16x32_bf16 v[156:159], v[24:27], v[120:123], 0\n\t" \
    "s_waitcnt lgkmcnt(10)\n\t" \
    "v_mfma_f32_16x16x32_bf16 v[160:163], v[24:27], v[124:127], 0\n\t" \
    "s_waitcnt lgkmcnt(8)\n\t" \
    "v_mfma_f32_16x16x32_bf16 v[164:167], v[24:27], v[128:131], 0\n\t" \
    "s_waitcnt lgkmcnt(6)\n\t" \
    "v_mfma_f32_16x16x32_bf16 v[168:171], v[24:27], v[132:135], 0\n\t" \
    "s_waitcnt lgkmcnt(4)\n\t" \
    "v_mfma_f32_16x16x32_bf16 v[172:175], v[24:27], v[136:139], 0\n\t" \
    "s_waitcnt lgkmcnt(2)\n\t" \
    "v_mfma_f32_16x16x32_bf16 v[176:179], v[24:27], v[140:143], 0\n\t" \
    "s_waitcnt lgkmcnt(0)\n\t" \
    "v_mfma_f32_16x16x32_bf16 v[180:183], v[24:27], v[144:147], 0\n\t" \
    "v_add_u32 v152, 0x8000, v152\n\t" \
    "v_add_u32 v154, 0x8000, v154\n\t" \
    "v_add_u32 v156, 0x8000, v156\n\t" \
    "v_add_u32 v158, 0x8000, v158\n\t" \
    "v_cvt_pk_bf16_f32 v184, v152, v153\n\t" \
    "v_cvt_pk_bf16_f32 v186, v156, v157\n\t" \
    "v_cvt_pk_bf16_f32 v185, v160, v161\n\t" \
    "v_cvt_pk_bf16_f32 v187, v164, v165\n\t" \
    "v_and_b32 v184, -2, v184\n\t" \
    "v_and_b32 v186, -2, v186\n\t" \
    "v_cvt_pk_bf16_f32 v188, v154, v155\n\t" \
    "v_cvt_pk_bf16_f32 v190, v158, v159\n\t" \
    "v_cvt_pk_bf16_f32 v189, v162, v163\n\t" \
    "v_cvt_pk_bf16_f32 v191, v166, v167\n\t" \
    "v_and_b32 v188, -2, v188\n\t" \
    "v_and_b32 v190, -2, v190\n\t" \
    "v_add_u32 v168, 0x8000, v168\n\t" \
    "v_add_u32 v170, 0x8000, v170\n\t" \
    "v_add_u32 v172, 0x8000, v172\n\t" \
    "v_add_u32 v174, 0x8000, v174\n\t" \
    "v_cvt_pk_bf16_f32 v4, v168, v169\n\t" \
    "v_cvt_pk_bf16_f32 v6, v172, v173\n\t" \
    "v_cvt_pk_bf16_f32 v5, v176, v177\n\t" \
    "v_cvt_pk_bf16_f32 v7, v180, v181\n\t" \
    "v_and_b32 v4, -2, v4\n\t" \
    "v_and_b32 v6, -2, v6\n\t" \
    "v_cvt_pk_bf16_f32 v20, v170, v171\n\t" \
    "v_cvt_pk_bf16_f32 v22, v174, v175\n\t" \
    "v_cvt_pk_bf16_f32 v21, v178, v179\n\t" \
    "v_cvt_pk_bf16_f32 v23, v182, v183\n\t" \
    "v_and_b32 v20, -2, v20\n\t" \
    "v_and_b32 v22, -2, v22\n\t" \
    ".Lh39_%=:\n\t" \
    "s_memrealtime s[22:23]\n\t" \
    "s_waitcnt lgkmcnt(0)\n\t" \
    "s_sub_u32 s22, s22, s35\n\t" \
    "s_cmp_lt_u32 s22, 255\n\t" \
    "s_cbranch_scc1 .Lh39_%=\n\t" \
    "s_mov_b32 s42, 524288\n\t" \
    "buffer_store_dwordx4 v[184:187], v13, s[4:7], s42 offen offset:0 sc1\n\t" \
    "s_mov_b32 s42, 4767744\n\t" \
    "buffer_store_dwordx4 v[188:191], v13, s[4:7], s42 offen offset:0 sc1\n\t" \
    "s_waitcnt vmcnt(4)\n\t" \
    "s_waitcnt lgkmcnt(0)\n\t" \
    "s_barrier\n\t" \
    "s_lshl_b32 s37, s24, 18\n\t" \
    "s_add_u32 s37, s37, s32\n\t" \
    "s_bitcmp1_b32 13, s24\n\t" \
    "s_cselect_b32 s39, 0, 0x80000000\n\t" \
    "v_add_u32 v16, s39, v2\n\t" \
    "v_mov_b32 v60, 0\n\t" \
    "v_mov_b32 v61, 0\n\t" \
    "v_mov_b32 v62, 0\n\t" \
    "v_mov_b32 v63, 0\n\t" \
    "s_cmp_eq_u32 s24, 4\n\t" \
    "s_cselect_b64 exec, 0xff, -1\n\t" \
    "buffer_store_dwordx4 v[60:63], v16, s[4:7], s37 offen sc1\n\t" \
    "s_mov_b64 exec, -1\n\t" \
    ".Lh40_%=:\n\t" \
    "s_memrealtime s[22:23]\n\t" \
    "s_waitcnt lgkmcnt(0)\n\t" \
    "s_sub_u32 s22, s22, s35\n\t" \
    "s_cmp_lt_u32 s22, 305\n\t" \
    "s_cbranch_scc1 .Lh40_%=\n\t" \
    "s_mov_b32 s42, 1048576\n\t" \
    "buffer_store_dwordx4 v[4:7], v13, s[4:7], s42 offen offset:0 sc1\n\t" \
    "s_mov_b32 s42, 5292032\n\t" \
    "buffer_store_dwordx4 v[20:23], v13, s[4:7], s42 offen offset:0 sc1\n\t" \
    "ds_read_b64_tr_b16 v[64:65], v15 offset:0\n\t" \
    "ds_read_b64_tr_b16 v[66:67], v15 offset:256\n\t" \
    "ds_read_b64_tr_b16 v[68:69], v15 offset:2048\n\t" \
    "ds_read_b64_tr_b16 v[70:71], v15 offset:2304\n\t" \
    "ds_read_b64_tr_b16 v[72:73], v15 offset:4096\n\t" \
    "ds_read_b64_tr_b16 v[74:75], v15 offset:4352\n\t" \
    "ds_read_b64_tr_b16 v[76:77], v15 offset:6144\n\t" \
    "ds_read_b64_tr_b16 v[78:79], v15 offset:6400\n\t" \
    "ds_read_b64_tr_b16 v[80:81], v15 offset:8192\n\t" \
    "ds_read_b64_tr_b16 v[82:83], v15 offset:8448\n\t" \
    "ds_read_b64_tr_b16 v[100:101], v15 offset:10240\n\t" \
    "ds_read_b64_tr_b16 v[102:103], v15 offset:10496\n\t" \
    "ds_read_b64_tr_b16 v[104:105], v15 offset:12288\n\t" \
    "ds_read_b64_tr_b16 v[106:107], v15 offset:12544\n\t" \
    "s_waitcnt lgkmcnt(12)\n\t" \
    "v_mfma_f32_16x16x32_bf16 v[84:87], v[28:31], v[64:67], 0\n\t" \
    "ds_read_b64_tr_b16 v[88:89], v15 offset:14336\n\t" \
    "ds_read_b64_tr_b16 v[90:91], v15 offset:14592\n\t" \
    "s_waitcnt lgkmcnt(12)\n\t" \
    "v_mfma_f32_16x16x32_bf16 v[84:87], v[32:35], v[68:71], v[84:87]\n\t" \
    "ds_read_b64_tr_b16 v[92:93], v15 offset:16384\n\t" \
    "ds_read_b64_tr_b16 v[94:95], v15 offset:16640\n\t" \
    "s_waitcnt lgkmcnt(12)\n\t" \
    "v_mfma_f32_16x16x32_bf16 v[96:99], v[28:31], v[72:75], 0\n\t" \
    "ds_read_b64_tr_b16 v[108:109], v15 offset:18432\n\t" \
    "ds_read_b64_tr_b16 v[110:111], v15 offset:18688\n\t" \
    "s_waitcnt lgkmcnt(12)\n\t" \
    "v_mfma_f32_16x16x32_bf16 v[96:99], v[32:35], v[76:79], v[96:99]\n\t" \
    "ds_read_b64_tr_b16 v[112:113], v15 offset:20480\n\t" \
    "ds_read_b64_tr_b16 v[114:115], v15 offset:20736\n\t" \
    "s_waitcnt lgkmcnt(12)\n\t" \
    "v_mfma_f32_16x16x32_bf16 v[116:119], v[28:31], v[80:83], 0\n\t" \
    "ds_read_b64_tr_b16 v[120:121], v15 offset:22528\n\t" \
    "ds_read_b64_tr_b16 v[122:123], v15 offset:22784\n\t" \
    "s_waitcnt lgkmcnt(12)\n\t" \
    "v_mfma_f32_16x16x32_bf16 v[116:119], v[32:35], v[100:103], v[116:119]\n\t" \
    "ds_read_b64_tr_b16 v[124:125], v15 offset:24576\n\t" \
    "ds_read_b64_tr_b16 v[126:127], v15 offset:24832\n\t" \
    "s_waitcnt lgkmcnt(12)\n\t" \
    "v_mfma_f32_16x16x32_bf16 v[128:131], v[28:31], v[104:107], 0\n\t" \
    "ds_read_b64_tr_b16 v[132:133], v15 offset:26624\n\t" \
    "ds_read_b64_tr_b16 v[134:135], v15 offset:26880\n\t" \
    "s_waitcnt lgkmcnt(12)\n\t" \
    "v_mfma_f32_16x16x32_bf16 v[128:131], v[32:35], v[88:91], v[128:131]\n\t" \
    "ds_read_b64_tr_b16 v[136:137], v15 offset:28672\n\t" \
    "ds_read_b64_tr_b16 v[138:139], v15 offset:28928\n\t" \
    "s_waitcnt lgkmcnt(12)\n\t" \
    "v_mfma_f32_16x16x32_bf16 v[140:143], v[28:31], v[92:95], 0\n\t" \
    "ds_read_b64_tr_b16 v[24:25], v15 offset:30720\n\t" \
    "ds_read_b64_tr_b16 v[26:27], v15 offset:30976\n\t" \
    "s_waitcnt lgkmcnt(12)\n\t" \
    "v_mfma_f32_16x16x32_bf16 v[140:143], v[32:35], v[108:111], v[140:143]\n\t" \
    "v_cvt_pk_bf16_f32 v144, v84, v96\n\t" \
    "v_cvt_pk_bf16_f32 v152, v85, v97\n\t" \
    "s_waitcnt lgkmcnt(10)\n\t" \
    "v_mfma_f32_16x16x32_bf16 v[156:159], v[28:31], v[112:115], 0\n\t" \
    "v_cvt_pk_bf16_f32 v160, v86, v98\n\t" \
    "v_cvt_pk_bf16_f32 v164, v87, v99\n\t" \
    "s_waitcnt lgkmcnt(8)\n\t" \
    "v_mfma_f32_16x16x32_bf16 v[156:159], v[32:35], v[120:123], v[156:159]\n\t" \
    "v_cvt_pk_bf16_f32 v145, v116, v128\n\t" \
    "v_cvt_pk_bf16_f32 v153, v117, v129\n\t" \
    "s_waitcnt lgkmcnt(6)\n\t" \
    "v_mfma_f32_16x16x32_bf16 v[168:171], v[28:31], v[124:127], 0\n\t" \
    "v_cvt_pk_bf16_f32 v161, v118, v130\n\t" \
    "v_cvt_pk_bf16_f32 v165, v119, v131\n\t" \
    "s_waitcnt lgkmcnt(4)\n\t" \
    "v_mfma_f32_16x16x32_bf16 v[168:171], v[32:35], v[132:135], v[168:171]\n\t" \
    "s_waitcnt lgkmcnt(2)\n\t" \
    "v_mfma_f32_16x16x32_bf16 v[172:175], v[28:31], v[136:139], 0\n\t" \
    "s_waitcnt lgkmcnt(0)\n\t" \
    "v_mfma_f32_16x16x32_bf16 v[172:175], v[32:35], v[24:27], v[172:175]\n\t" \
    "v_cvt_pk_bf16_f32 v146, v140, v156\n\t" \
    "v_cvt_pk_bf16_f32 v154, v141, v157\n\t" \
    "v_cvt_pk_bf16_f32 v162, v142, v158\n\t" \
    "v_cvt_pk_bf16_f32 v166, v143, v159\n\t" \
    "s_nop 3\n\t" \
    "v_cvt_pk_bf16_f32 v147, v168, v172\n\t" \
    "v_cvt_pk_bf16_f32 v155, v169, v173\n\t" \
    "v_cvt_pk_bf16_f32 v163, v170, v174\n\t" \
    "v_cvt_pk_bf16_f32 v167, v171, v175\n\t" \
    ".Lh41_%=:\n\t" \
    "s_memrealtime s[22:23]\n\t" \
    "s_waitcnt lgkmcnt(0)\n\t" \
    "s_sub_u32 s22, s22, s35\n\t" \
    "s_cmp_lt_u32 s22, 370\n\t" \
    "s_cbranch_scc1 .Lh41_%=\n\t" \
    "s_cmp_eq_u32 s24, 0\n\t" \
    "s_cbranch_scc1 .Lj43_%=\n\t" \
    ".Lh42_%=:\n\t" \
    "s_memrealtime s[22:23]\n\t" \
    "s_waitcnt lgkmcnt(0)\n\t" \
    "s_sub_u32 s22, s22, s35\n\t" \
    "s_cmp_lt_u32 s22, 385\n\t" \
    "s_cbranch_scc1 .Lh42_%=\n\t" \
    ".Lj43_%=:\n\t" \
    "s_waitcnt lgkmcnt(0)\n\t" \
    "s_xor_b32 s42, s43, 0\n\t" \
    "buffer_store_dwordx4 v[144:147], v19, s[4:7], s42 offen offset:0 sc1\n\t" \
    "s_waitcnt vmcnt(1)\n\t" \
    "s_mov_b64 exec, 1\n\t" \
    "ds_add_rtn_u32 v17, v9, v9 offset:8\n\t" \
    "s_add_u32 s37, s32, 524288\n\t" \
    "s_waitcnt lgkmcnt(0)\n\t" \
    "v_cmp_eq_u32 vcc, 1098496, v17\n\t" \
    "s_mov_b64 exec, -1\n\t" \
    "s_nop 0\n\t" \
    "s_cbranch_vccz .La13_%=\n\t" \
    "buffer_store_dwordx4 v[148:151], v2, s[4:7], s37 offen sc1\n\t" \
    ".La13_%=:\n\t" \
    "s_mov_b64 exec, -1\n\t" \
    ".Lz1_%=:\n\t" \
    "s_cbranch_execz .Lwk_%=\n\t" \
    "s_xor_b32 s42, s43, 1024\n\t" \
    "buffer_store_dwordx4 v[152:155], v19, s[4:7], s42 offen offset:0 sc1\n\t" \
    "s_cmp_eq_u32 s24, 0\n\t" \
    "s_cbranch_scc1 .Lj45_%=\n\t" \
    ".Lh44_%=:\n\t" \
    "s_memrealtime s[22:23]\n\t" \
    "s_waitcnt lgkmcnt(0)\n\t" \
    "s_sub_u32 s22, s22, s35\n\t" \
    "s_cmp_lt_u32 s22, 435\n\t" \
    "s_cbranch_scc1 .Lh44_%=\n\t" \
    ".Lj45_%=:\n\t" \
    "s_waitcnt lgkmcnt(0)\n\t" \
    "s_xor_b32 s42, s43, 2048\n\t" \
    "buffer_store_dwordx4 v[160:163], v19, s[4:7], s42 offen offset:0 sc1\n\t" \
    "s_xor_b32 s42, s43, 3072\n\t" \
    "buffer_store_dwordx4 v[164:167], v19, s[4:7], s42 offen offset:0 sc1\n\t" \
    ".Lskip_%=:\n\t" \
    "s_waitcnt lgkmcnt(0)\n\t" \
    "s_nop 7\n\t" \
    ".Lh46_%=:\n\t" \
    "s_memrealtime s[22:23]\n\t" \
    "s_waitcnt lgkmcnt(0)\n\t" \
    "s_sub_u32 s22, s22, s35\n\t" \
    "s_cmp_lt_u32 s22, 410\n\t" \
    "s_cbranch_scc1 .Lh46_%=\n\t" \
    "s_and_b32 s52, s2, 7\n\t" \
    "s_lshl_b32 s52, s52, 7\n\t" \
    "s_add_u32 s52, s52, 34669568\n\t" \
    "s_buffer_load_dword s36, s[4:7], s52\n\t" \
    "s_and_b32 s37, s24, 3\n\t" \
    "s_mul_i32 s37, s37, 8486912\n\t" \
    "s_add_u32 s53, s53, s37\n\t" \
    "s_and_b32 s37, s24, 4\n\t" \
    "s_lshl_b32 s39, s37, 11\n\t" \
    "s_add_u32 s53, s53, s39\n\t" \
    "s_add_u32 s37, s37, s24\n\t" \
    "s_andn2_b32 s39, s26, 1\n\t" \
    "s_lshl3_add_u32 s37, s39, s37\n\t" \
    "s_mul_i32 s37, s37, 43008\n\t" \
    "s_mul_i32 s39, s27, 4992\n\t" \
    "s_add_u32 s51, s37, s39\n\t" \
    "s_cmp_eq_u32 s27, 8\n\t" \
    "s_movk_i32 s10, 4992\n\t" \
    "s_cselect_b32 s10, 3072, s10\n\t" \
    "s_mov_b32 s11, 0x20000\n\t" \
    "s_and_b32 s37, s2, 0x70\n\t" \
    "s_lshl_b32 s37, s37, 1\n\t" \
    "v_lshrrev_b32 v11, 7, v0\n\t" \
    "v_lshl_add_u32 v11, v11, 2, s37\n\t" \
    "v_min_u32 v11, 248, v11\n\t" \
    "v_mul_u32_u24 v46, 147, v11\n\t" \
    "v_lshrrev_b32 v46, 12, v46\n\t" \
    "v_mul_u32_u24 v41, 28, v46\n\t" \
    "v_sub_u32 v11, v11, v41\n\t" \
    "v_bfe_u32 v41, v0, 4, 3\n\t" \
    "v_min_u32 v41, 4, v41\n\t" \
    "v_add3_u32 v11, v11, v41, -1\n\t" \
    "v_add_u32 v41, 28, v11\n\t" \
    "v_min_u32 v11, v11, v41\n\t" \
    "v_and_b32 v1, 1, v11\n\t" \
    "v_lshrrev_b32 v11, 1, v11\n\t" \
    "v_mad_u32_u24 v11, v11, 9, v46\n\t" \
    "v_lshl_add_u32 v11, v1, 7, v11\n\t" \
    "v_and_b32 v3, 31, v11\n\t" \
    "v_and_b32 v50, 24, v3\n\t" \
    "v_add_u32 v3, v3, v50\n\t" \
    "v_lshrrev_b32 v50, 5, v11\n\t" \
    "v_lshlrev_b32 v50, 7, v50\n\t" \
    "v_lshl_or_b32 v51, v3, 10, v50\n\t" \
    "s_mov_b32 s40, 0\n\t" \
    "s_waitcnt lgkmcnt(0)\n\t" \
    ".Lr4_%=:\n\t" \
    "s_xor_b32 s36, s36, s34\n\t" \
    "s_andn2_b32 s37, s36, 14\n\t" \
    "s_cbranch_scc1 .Lr5_%=\n\t" \
    ".Lr6_%=:\n\t" \
    "s_bfe_u32 s36, s36, 0x30001\n\t" \
    "s_add_u32 s56, s2, s36\n\t" \
    "s_and_b32 s56, s56, 7\n\t" \
    "s_lshr_b32 s37, s2, 7\n\t" \
    "s_lshl1_add_u32 s56, s56, s37\n\t" \
    "s_and_b32 s37, s56, 7\n\t" \
    "s_mul_i32 s48, s37, 4243456\n\t" \
    "s_and_b32 s37, s56, 8\n\t" \
    "s_lshl_b32 s37, s37, 10\n\t" \
    "s_add_u32 s48, s48, s37\n\t" \
    "s_add_u32 s49, s48, 524288\n\t" \
    "s_add_u32 s50, s48, 1048576\n\t" \
    "s_xor_b32 s37, s48, 0x2000\n\t" \
    "s_add_u32 s37, s37, 2097152\n\t" \
    "s_add_u32 s45, s45, s37\n\t" \
    "s_add_u32 s46, s46, s37\n\t" \
    "s_cmp_lt_u32 s24, 4\n\t" \
    "s_cselect_b32 s47, s46, 0x80000000\n\t" \
    "v_add_u32 v17, s45, v0\n\t" \
    "v_add_u32 v19, s46, v0\n\t" \
    "s_lshr_b32 s37, s24, 2\n\t" \
    "s_mul_i32 s37, s37, 6\n\t" \
    "s_mul_i32 s39, s56, 7\n\t" \
    "s_add_u32 s37, s37, s39\n\t" \
    "s_lshr_b32 s37, s37, 4\n\t" \
    "s_and_b32 s39, s24, 3\n\t" \
    "s_mul_i32 s39, s39, 63\n\t" \
    "s_add_u32 s37, s37, s39\n\t" \
    "v_lshrrev_b32 v41, 4, v0\n\t" \
    "v_min_u32 v41, 8, v41\n\t" \
    "v_mad_u32_u24 v41, v41, 7, s37\n\t" \
    "v_add_u32 v46, 2, v41\n\t" \
    "v_lshrrev_b32 v46, 7, v46\n\t" \
    "v_lshl_add_u32 v41, v46, 1, v41\n\t" \
    "v_and_b32 v46, 31, v41\n\t" \
    "v_and_b32 v1, 24, v46\n\t" \
    "v_add_u32 v46, v46, v1\n\t" \
    "v_lshrrev_b32 v1, 5, v41\n\t" \
    "v_lshlrev_b32 v1, 7, v1\n\t" \
    "v_lshl_or_b32 v11, v46, 10, v1\n\t" \
    "v_add_u32 v18, s36, v18\n\t" \
    "v_lshlrev_b32 v18, 8, v18\n\t" \
    "v_and_b32 v18, 0x400, v18\n\t" \
    "v_xor_b32 v42, v42, v18\n\t" \
    "s_sub_u32 s37, s24, s36\n\t" \
    "s_and_b32 s37, s37, 7\n\t" \
    "s_mul_i32 s39, s27, 28\n\t" \
    "s_add_u32 s39, s39, s26\n\t" \
    "s_lshr_b32 s39, s39, 5\n\t" \
    "s_lshl4_add_u32 s37, s39, s37\n\t" \
    "s_lshl_b32 s52, s37, 7\n\t" \
    "s_andn2_b32 s37, s52, 0x1fff\n\t" \
    "s_add_u32 s52, s52, s37\n\t" \
    "s_and_b32 s37, s56, 6\n\t" \
    "s_lshl_b32 s37, s37, 15\n\t" \
    "s_and_b32 s39, s48, 0x2000\n\t" \
    "s_add_u32 s37, s37, s39\n\t" \
    "s_add_u32 s33, s37, 33554432\n\t" \
    "s_add_u32 s55, s55, s39\n\t" \
    "s_nop 0\n\t" \
    "v_readlane_b32 s12, v11, 0\n\t" \
    "v_readlane_b32 s13, v11, 1\n\t" \
    "v_readlane_b32 s14, v11, 2\n\t" \
    "v_readlane_b32 s15, v11, 3\n\t" \
    "v_readlane_b32 s16, v11, 4\n\t" \
    "v_readlane_b32 s17, v11, 5\n\t" \
    "v_readlane_b32 s18, v11, 6\n\t" \
    "v_readlane_b32 s19, v11, 7\n\t" \
    "v_readlane_b32 s3, v11, 8\n\t" \
    "s_add_u32 s12, s12, s33\n\t" \
    "s_add_u32 s13, s13, s33\n\t" \
    "s_add_u32 s14, s14, s33\n\t" \
    "s_add_u32 s15, s15, s33\n\t" \
    "s_add_u32 s16, s16, s33\n\t" \
    "s_add_u32 s17, s17, s33\n\t" \
    "s_add_u32 s18, s18, s33\n\t" \
    "s_add_u32 s19, s19, s33\n\t" \
    "s_add_u32 s3, s3, s33\n\t" \
    "s_waitcnt vmcnt(0)\n\t" \
    "s_mov_b64 exec, 1\n\t" \
    "ds_add_rtn_u32 v3, v9, v9 offset:12\n\t" \
    "s_add_u32 s37, s32, 786432\n\t" \
    "s_waitcnt lgkmcnt(0)\n\t" \
    "v_cmp_eq_u32 vcc, 1098496, v3\n\t" \
    "s_mov_b64 exec, -1\n\t" \
    "s_nop 0\n\t" \
    "s_cbranch_vccz .La22_%=\n\t" \
    "buffer_store_dwordx4 v[148:151], v2, s[4:7], s37 offen sc1\n\t" \
    ".La22_%=:\n\t" \
    "s_mov_b64 exec, -1\n\t" \
    ".Lh47_%=:\n\t" \
    "s_memrealtime s[22:23]\n\t" \
    "s_waitcnt lgkmcnt(0)\n\t" \
    "s_sub_u32 s22, s22, s35\n\t" \
    "s_cmp_lt_u32 s22, 485\n\t" \
    "s_cbranch_scc1 .Lh47_%=\n\t" \
    "s_mov_b32 s37, 34670592\n\t" \
    "s_buffer_load_dword s56, s[4:7], s37 offset:0x0\n\t" \
    "s_buffer_load_dword s57, s[4:7], s37 offset:0x80\n\t" \
    "s_buffer_load_dword s58, s[4:7], s37 offset:0x100\n\t" \
    "s_buffer_load_dword s59, s[4:7], s37 offset:0x180\n\t" \
    "s_buffer_load_dword s60, s[4:7], s37 offset:0x200\n\t" \
    "s_buffer_load_dword s61, s[4:7], s37 offset:0x280\n\t" \
    "s_cmp_lg_u32 s24, 0\n\t" \
    "s_cbranch_scc1 .Lb15_%=\n\t" \
    "s_mov_b32 s40, 0\n\t" \
    "s_add_u32 s38, s33, 524288\n\t" \
    ".Ll15_%=:\n\t" \
    "buffer_load_dword v50, v51, s[4:7], s38 offen sc1\n\t" \
    "s_waitcnt vmcnt(0)\n\t" \
    "v_cmp_ne_u32 vcc, s34, v50\n\t" \
    "s_add_u32 s40, s40, 1\n\t" \
    "s_cmp_ge_u32 s40, 65536\n\t" \
    "s_cbranch_scc1 .Lb15_%=\n\t" \
    "s_cbranch_vccz .Lb15_%=\n\t" \
    "s_sleep 2\n\t" \
    "s_branch .Ll15_%=\n\t" \
    ".Lb15_%=:\n\t" \
    "s_waitcnt lgkmcnt(0)\n\t" \
    "s_barrier\n\t" \
    "buffer_load_dwordx4 v[176:179], v36, s[4:7], s48 offen offset:0 sc1\n\t" \
    "buffer_load_dwordx4 v[180:183], v36, s[4:7], s48 offen offset:1024 sc1\n\t" \
    "buffer_load_dwordx4 v[184:187], v36, s[4:7], s48 offen offset:256 sc1\n\t" \
    "buffer_load_dwordx4 v[188:191], v36, s[4:7], s48 offen offset:1280 sc1\n\t" \
    "buffer_load_dwordx4 v[60:63], v37, s[4:7], s48 offen offset:0 sc1\n\t" \
    "s_cmp_lt_u32 s24, 4\n\t" \
    "s_cbranch_scc1 .Lp48_%=\n\t" \
    "s_setprio 1\n\t" \
    ".Lp48_%=:\n\t" \
    "s_mov_b32 s40, 0\n\t" \
    "s_waitcnt lgkmcnt(0)\n\t" \
    ".Lq4_%=:\n\t" \
    "s_mov_b32 s37, 0\n\t" \
    "s_mov_b32 s0, 0\n\t" \
    "s_xor_b32 s61, s61, s34\n\t" \
    "s_or_b32 s37, s37, s61\n\t" \
    "s_lshl1_add_u32 s0, s0, s61\n\t" \
    "s_xor_b32 s60, s60, s34\n\t" \
    "s_or_b32 s37, s37, s60\n\t" \
    "s_lshl1_add_u32 s0, s0, s60\n\t" \
    "s_xor_b32 s59, s59, s34\n\t" \
    "s_or_b32 s37, s37, s59\n\t" \
    "s_lshl1_add_u32 s0, s0, s59\n\t" \
    "s_xor_b32 s58, s58, s34\n\t" \
    "s_or_b32 s37, s37, s58\n\t" \
    "s_lshl1_add_u32 s0, s0, s58\n\t" \
    "s_xor_b32 s57, s57, s34\n\t" \
    "s_or_b32 s37, s37, s57\n\t" \
    "s_lshl1_add_u32 s0, s0, s57\n\t" \
    "s_xor_b32 s56, s56, s34\n\t" \
    "s_or_b32 s37, s37, s56\n\t" \
    "s_lshl1_add_u32 s0, s0, s56\n\t" \
    "s_andn2_b32 s37, s37, 2\n\t" \
    "s_cbranch_scc1 .Lq5_%=\n\t" \
    ".Lq6_%=:\n\t" \
    "s_lshr_b32 s0, s0, 1\n\t" \
    "s_add_u32 s37, s36, 3\n\t" \
    "s_bitcmp1_b32 s37, 2\n\t" \
    "s_cselect_b32 s37, 51, 12\n\t" \
    "s_xor_b32 s0, s0, s37\n\t" \
    "v_add_u32 v41, 344064, v39\n\t" \
    "v_lshrrev_b32 v46, 21, v41\n\t" \
    "v_bfe_u32 v46, s0, v46, 1\n\t" \
    "v_lshrrev_b32 v1, 3, v41\n\t" \
    "v_lshlrev_b32 v46, 10, v46\n\t" \
    "v_and_b32 v1, 0x400, v1\n\t" \
    "v_xor_b32 v46, v46, v1\n\t" \
    "v_xor_b32 v18, v42, v46\n\t" \
    "v_lshrrev_b32 v46, 21, v39\n\t" \
    "v_bfe_u32 v46, s0, v46, 1\n\t" \
    "v_lshrrev_b32 v1, 3, v39\n\t" \
    "v_lshlrev_b32 v46, 10, v46\n\t" \
    "v_and_b32 v1, 0x400, v1\n\t" \
    "v_xor_b32 v46, v46, v1\n\t" \
    "v_xor_b32 v42, v42, v46\n\t" \
    "s_lshr_b32 s37, s10, 1\n\t" \
    "s_add_u32 s37, s37, s51\n\t" \
    "s_and_b32 s39, s8, 0x1fffff\n\t" \
    "s_add_u32 s37, s37, s39\n\t" \
    "s_lshr_b32 s39, s37, 21\n\t" \
    "s_lshr_b32 s39, s0, s39\n\t" \
    "s_xor_b32 s37, s37, s48\n\t" \
    "s_lshr_b32 s37, s37, 13\n\t" \
    "s_xor_b32 s37, s37, s39\n\t" \
    "s_and_b32 s37, s37, 1\n\t" \
    "s_mul_i32 s37, s37, 172032\n\t" \
    "s_add_u32 s37, s37, s51\n\t" \
    "s_add_u32 s8, s8, s37\n\t" \
    "s_addc_u32 s9, s9, 0\n\t" \
    "s_waitcnt vmcnt(4)\n\t" \
    "v_mfma_f32_32x32x16_bf16 v[192:207], v[52:55], v[176:179], 0\n\t" \
    "buffer_load_dwordx4 v[4:7], v36, s[4:7], s49 offen offset:0 sc1\n\t" \
    "s_waitcnt vmcnt(4)\n\t" \
    "v_mfma_f32_32x32x16_bf16 v[192:207], v[56:59], v[180:183], v[192:207]\n\t" \
    "buffer_load_dwordx4 v[20:23], v36, s[4:7], s49 offen offset:1024 sc1\n\t" \
    "s_waitcnt vmcnt(4)\n\t" \
    "v_mfma_f32_32x32x16_bf16 v[208:223], v[52:55], v[184:187], 0\n\t" \
    "buffer_load_dwordx4 v[64:67], v36, s[4:7], s49 offen offset:256 sc1\n\t" \
    "s_waitcnt vmcnt(4)\n\t" \
    "v_mfma_f32_32x32x16_bf16 v[208:223], v[56:59], v[188:191], v[208:223]\n\t" \
    "buffer_load_dwordx4 v[68:71], v36, s[4:7], s49 offen offset:1280 sc1\n\t" \
    "v_mfma_f32_32x32x16_bf16 v[224:239], v[56:59], v[176:179], 0\n\t" \
    "s_add_u32 m0, s44, 0\n\t" \
    "s_nop 8\n\t" \
    "v_cvt_pk_bf16_f32 v9, v192, v208\n\t" \
    "v_cvt_pk_bf16_f32 v3, v193, v209\n\t" \
    "v_cvt_pk_bf16_f32 v51, v194, v210\n\t" \
    "v_cvt_pk_bf16_f32 v50, v195, v211\n\t" \
    "v_cvt_pk_bf16_f32 v8, v196, v212\n\t" \
    "s_waitcnt vmcnt(4)\n\t" \
    "v_permlane16_swap_b32 v60, v180\n\t" \
    "v_permlane16_swap_b32 v61, v181\n\t" \
    "ds_write_addtid_b32 v9 offset:3616\n\t" \
    "ds_write_addtid_b32 v3 offset:3872\n\t" \
    "ds_write_addtid_b32 v51 offset:4128\n\t" \
    "ds_write_addtid_b32 v50 offset:4384\n\t" \
    "ds_write_addtid_b32 v8 offset:4640\n\t" \
    "v_permlane16_swap_b32 v62, v182\n\t" \
    "v_permlane16_swap_b32 v63, v183\n\t" \
    "v_cvt_pk_bf16_f32 v16, v197, v213\n\t" \
    "v_cvt_pk_bf16_f32 v41, v198, v214\n\t" \
    "v_cvt_pk_bf16_f32 v39, v199, v215\n\t" \
    "v_cvt_pk_bf16_f32 v1, v200, v216\n\t" \
    "v_cvt_pk_bf16_f32 v46, v201, v217\n\t" \
    "v_mfma_f32_32x32x16_bf16 v[240:255], v[56:59], v[184:187], 0\n\t" \
    "v_permlane16_swap_b32 v180, v188\n\t" \
    "buffer_load_dwordx4 v[72:75], v37, s[4:7], s49 offen offset:0 sc1\n\t" \
    "ds_write_addtid_b32 v16 offset:4896\n\t" \
    "ds_write_addtid_b32 v41 offset:5152\n\t" \
    "ds_write_addtid_b32 v39 offset:5408\n\t" \
    "ds_write_addtid_b32 v1 offset:5664\n\t" \
    "ds_write_addtid_b32 v46 offset:5920\n\t" \
    "v_permlane16_swap_b32 v181, v189\n\t" \
    "v_permlane16_swap_b32 v182, v190\n\t" \
    "v_cvt_pk_bf16_f32 v9, v202, v218\n\t" \
    "v_cvt_pk_bf16_f32 v3, v203, v219\n\t" \
    "v_cvt_pk_bf16_f32 v51, v204, v220\n\t" \
    "v_cvt_pk_bf16_f32 v50, v205, v221\n\t" \
    "v_permlane16_swap_b32 v183, v191\n\t" \
    "v_mfma_f32_32x32x16_bf16 v[224:239], v[52:55], v[60:63], v[224:239]\n\t" \
    "ds_write_addtid_b32 v9 offset:6176\n\t" \
    "ds_write_addtid_b32 v3 offset:6432\n\t" \
    "ds_write_addtid_b32 v51 offset:6688\n\t" \
    "ds_write_addtid_b32 v50 offset:6944\n\t" \
    "v_mfma_f32_32x32x16_bf16 v[240:255], v[52:55], v[180:183], v[240:255]\n\t" \
    ".Lh49_%=:\n\t" \
    "s_memrealtime s[22:23]\n\t" \
    "s_waitcnt lgkmcnt(0)\n\t" \
    "s_sub_u32 s22, s22, s35\n\t" \
    "s_cmp_lt_u32 s22, 597\n\t" \
    "s_cbranch_scc1 .Lh49_%=\n\t" \
    "s_buffer_load_dword s12, s[4:7], s12 offset:0xc0000\n\t" \
    "s_buffer_load_dword s13, s[4:7], s13 offset:0xc0000\n\t" \
    "s_buffer_load_dword s14, s[4:7], s14 offset:0xc0000\n\t" \
    "s_buffer_load_dword s15, s[4:7], s15 offset:0xc0000\n\t" \
    "s_buffer_load_dword s16, s[4:7], s16 offset:0xc0000\n\t" \
    "s_buffer_load_dword s17, s[4:7], s17 offset:0xc0000\n\t" \
    "s_buffer_load_dword s18, s[4:7], s18 offset:0xc0000\n\t" \
    "s_buffer_load_dword s19, s[4:7], s19 offset:0xc0000\n\t" \
    "s_buffer_load_dword s3, s[4:7], s3 offset:0xc0000\n\t" \
    "s_waitcnt vmcnt(4)\n\t" \
    "v_mfma_f32_32x32x16_bf16 v[192:207], v[52:55], v[4:7], 0\n\t" \
    "v_cvt_pk_bf16_f32 v8, v224, v240\n\t" \
    "v_cvt_pk_bf16_f32 v16, v225, v241\n\t" \
    "v_cvt_pk_bf16_f32 v41, v226, v242\n\t" \
    "v_cvt_pk_bf16_f32 v39, v227, v243\n\t" \
    "v_cvt_pk_bf16_f32 v1, v228, v244\n\t" \
    "buffer_load_dwordx4 v[76:79], v36, s[4:7], s50 offen offset:0 sc1\n\t" \
    "ds_write_addtid_b32 v8 offset:0\n\t" \
    "ds_write_addtid_b32 v16 offset:256\n\t" \
    "ds_write_addtid_b32 v41 offset:512\n\t" \
    "ds_write_addtid_b32 v39 offset:768\n\t" \
    "ds_write_addtid_b32 v1 offset:1024\n\t" \
    "s_waitcnt vmcnt(4)\n\t" \
    "v_mfma_f32_32x32x16_bf16 v[192:207], v[56:59], v[20:23], v[192:207]\n\t" \
    "buffer_load_dwordx4 v[80:83], v36, s[4:7], s50 offen offset:1024 sc1\n\t" \
    "v_cvt_pk_bf16_f32 v46, v229, v245\n\t" \
    "v_cvt_pk_bf16_f32 v9, v230, v246\n\t" \
    "v_cvt_pk_bf16_f32 v3, v231, v247\n\t" \
    "v_cvt_pk_bf16_f32 v51, v232, v248\n\t" \
    "v_cvt_pk_bf16_f32 v50, v233, v249\n\t" \
    "s_waitcnt vmcnt(4)\n\t" \
    "v_mfma_f32_32x32x16_bf16 v[208:223], v[52:55], v[64:67], 0\n\t" \
    "buffer_load_dwordx4 v[100:103], v36, s[4:7], s50 offen offset:256 sc1\n\t" \
    "ds_write_addtid_b32 v46 offset:1280\n\t" \
    "ds_write_addtid_b32 v9 offset:1536\n\t" \
    "ds_write_addtid_b32 v3 offset:1792\n\t" \
    "ds_write_addtid_b32 v51 offset:2048\n\t" \
    "ds_write_addtid_b32 v50 offset:2304\n\t" \
    "v_cvt_pk_bf16_f32 v8, v234, v250\n\t" \
    "v_cvt_pk_bf16_f32 v16, v235, v251\n\t" \
    "v_cvt_pk_bf16_f32 v41, v236, v252\n\t" \
    "v_cvt_pk_bf16_f32 v39, v237, v253\n\t" \
    "buffer_load_dwordx4 v[104:107], v36, s[4:7], s50 offen offset:1280 sc1\n\t" \
    "s_waitcnt vmcnt(5)\n\t" \
    "v_mfma_f32_32x32x16_bf16 v[208:223], v[56:59], v[68:71], v[208:223]\n\t" \
    "ds_write_addtid_b32 v8 offset:2560\n\t" \
    "ds_write_addtid_b32 v16 offset:2816\n\t" \
    "ds_write_addtid_b32 v41 offset:3072\n\t" \
    ".Lz2_%=:\n\t" \
    "s_cbranch_execz .Lwk_%=\n\t" \
    "ds_write_addtid_b32 v39 offset:3328\n\t" \
    "buffer_load_dwordx4 v[88:91], v37, s[4:7], s50 offen offset:0 sc1\n\t" \
    "s_waitcnt lgkmcnt(0)\n\t" \
    "s_xor_b32 s12, s12, s34\n\t" \
    "s_xor_b32 s13, s13, s34\n\t" \
    "s_xor_b32 s14, s14, s34\n\t" \
    "s_xor_b32 s15, s15, s34\n\t" \
    "s_xor_b32 s16, s16, s34\n\t" \
    "s_xor_b32 s17, s17, s34\n\t" \
    "s_xor_b32 s18, s18, s34\n\t" \
    "s_xor_b32 s19, s19, s34\n\t" \
    "s_xor_b32 s3, s3, s34\n\t" \
    "s_or_b32 s12, s12, s13\n\t" \
    "s_or_b32 s12, s12, s14\n\t" \
    "s_or_b32 s12, s12, s15\n\t" \
    "s_or_b32 s12, s12, s16\n\t" \
    "s_or_b32 s12, s12, s17\n\t" \
    "s_or_b32 s12, s12, s18\n\t" \
    "s_or_b32 s12, s12, s19\n\t" \
    "s_or_b32 s12, s12, s3\n\t" \
    "s_cbranch_scc1 .Lq1025_%=\n\t" \
    ".Lb24_%=:\n\t" \
    "s_waitcnt lgkmcnt(0)\n\t" \
    "s_barrier\n\t" \
    "ds_read_b128 v[92:95], v44 offset:0\n\t" \
    "s_mov_b32 s45, 0\n\t" \
    "buffer_load_dwordx4 v[12:15], v17, s[4:7], s45 offen offset:0 sc1\n\t" \
    "ds_read_b128 v[108:111], v44 offset:128\n\t" \
    "ds_read_b128 v[112:115], v44 offset:29184\n\t" \
    "ds_read_b128 v[84:87], v44 offset:29312\n\t" \
    "ds_read_b128 v[96:99], v45 offset:0\n\t" \
    "buffer_load_dwordx4 v[120:123], v17, s[4:7], s45 offen offset:1024 sc1\n\t" \
    "ds_read_b128 v[124:127], v45 offset:128\n\t" \
    "ds_read_b128 v[116:119], v45 offset:29184\n\t" \
    "ds_read_b128 v[128:131], v45 offset:29312\n\t" \
    "v_mfma_f32_32x32x16_bf16 v[224:239], v[56:59], v[4:7], 0\n\t" \
    "v_cvt_pk_bf16_f32 v46, v192, v208\n\t" \
    "v_cvt_pk_bf16_f32 v9, v193, v209\n\t" \
    "v_cvt_pk_bf16_f32 v3, v194, v210\n\t" \
    "v_cvt_pk_bf16_f32 v51, v195, v211\n\t" \
    "v_cvt_pk_bf16_f32 v50, v196, v212\n\t" \
    "s_waitcnt vmcnt(7)\n\t" \
    "v_permlane16_swap_b32 v72, v20\n\t" \
    "v_permlane16_swap_b32 v73, v21\n\t" \
    "s_mov_b32 s46, 114688\n\t" \
    "buffer_load_dwordx4 v[132:135], v17, s[4:7], s46 offen offset:0 sc1\n\t" \
    "ds_write_addtid_b32 v46 offset:61984\n\t" \
    "ds_write_addtid_b32 v9 offset:62240\n\t" \
    "ds_write_addtid_b32 v3 offset:62496\n\t" \
    "ds_write_addtid_b32 v51 offset:62752\n\t" \
    "ds_write_addtid_b32 v50 offset:63008\n\t" \
    "v_permlane16_swap_b32 v74, v22\n\t" \
    "v_permlane16_swap_b32 v75, v23\n\t" \
    "v_cvt_pk_bf16_f32 v36, v197, v213\n\t" \
    "v_cvt_pk_bf16_f32 v8, v198, v214\n\t" \
    "v_cvt_pk_bf16_f32 v16, v199, v215\n\t" \
    "v_cvt_pk_bf16_f32 v41, v200, v216\n\t" \
    "v_cvt_pk_bf16_f32 v39, v201, v217\n\t" \
    "v_mfma_f32_32x32x16_bf16 v[240:255], v[56:59], v[64:67], 0\n\t" \
    "v_permlane16_swap_b32 v20, v68\n\t" \
    "buffer_load_dwordx4 v[28:31], v17, s[4:7], s46 offen offset:1024 sc1\n\t" \
    "ds_write_addtid_b32 v36 offset:63264\n\t" \
    "ds_write_addtid_b32 v8 offset:63520\n\t" \
    "ds_write_addtid_b32 v16 offset:63776\n\t" \
    "ds_write_addtid_b32 v41 offset:64032\n\t" \
    "ds_write_addtid_b32 v39 offset:64288\n\t" \
    "v_permlane16_swap_b32 v21, v69\n\t" \
    "v_permlane16_swap_b32 v22, v70\n\t" \
    "v_cvt_pk_bf16_f32 v37, v202, v218\n\t" \
    "v_cvt_pk_bf16_f32 v11, v203, v219\n\t" \
    "v_cvt_pk_bf16_f32 v1, v204, v220\n\t" \
    "v_cvt_pk_bf16_f32 v46, v205, v221\n\t" \
    "v_permlane16_swap_b32 v23, v71\n\t" \
    "v_mfma_f32_32x32x16_bf16 v[224:239], v[52:55], v[72:75], v[224:239]\n\t" \
    "buffer_load_dwordx4 v[136:139], v19, s[4:7], s45 offen offset:0 sc1\n\t" \
    "ds_write_addtid_b32 v37 offset:64544\n\t" \
    "ds_write_addtid_b32 v11 offset:64800\n\t" \
    "ds_write_addtid_b32 v1 offset:65056\n\t" \
    "ds_write_addtid_b32 v46 offset:65312\n\t" \
    "v_mfma_f32_32x32x16_bf16 v[240:255], v[52:55], v[20:23], v[240:255]\n\t" \
    "s_waitcnt vmcnt(9)\n\t" \
    "v_mfma_f32_32x32x16_bf16 v[192:207], v[52:55], v[76:79], 0\n\t" \
    "s_nop 9\n\t" \
    "v_cvt_pk_bf16_f32 v9, v224, v240\n\t" \
    "v_cvt_pk_bf16_f32 v3, v225, v241\n\t" \
    "v_cvt_pk_bf16_f32 v51, v226, v242\n\t" \
    "v_cvt_pk_bf16_f32 v50, v227, v243\n\t" \
    "v_cvt_pk_bf16_f32 v36, v228, v244\n\t" \
    "buffer_load_dwordx4 v[24:27], v19, s[4:7], s46 offen offset:0 sc1\n\t" \
    "ds_write_addtid_b32 v9 offset:58368\n\t" \
    "ds_write_addtid_b32 v3 offset:58624\n\t" \
    "ds_write_addtid_b32 v51 offset:58880\n\t" \
    "ds_write_addtid_b32 v50 offset:59136\n\t" \
    "ds_write_addtid_b32 v36 offset:59392\n\t" \
    "s_waitcnt vmcnt(9)\n\t" \
    "v_mfma_f32_32x32x16_bf16 v[192:207], v[56:59], v[80:83], v[192:207]\n\t" \
    "v_cvt_pk_bf16_f32 v8, v229, v245\n\t" \
    "v_cvt_pk_bf16_f32 v16, v230, v246\n\t" \
    "v_cvt_pk_bf16_f32 v41, v231, v247\n\t" \
    "v_cvt_pk_bf16_f32 v39, v232, v248\n\t" \
    "v_cvt_pk_bf16_f32 v37, v233, v249\n\t" \
    "s_mov_b32 s47, 229376\n\t" \
    "buffer_load_dwordx4 v[32:35], v17, s[4:7], s47 offen offset:0 sc1\n\t" \
    "s_waitcnt vmcnt(9)\n\t" \
    "v_mfma_f32_32x32x16_bf16 v[208:223], v[52:55], v[100:103], 0\n\t" \
    "ds_write_addtid_b32 v8 offset:59648\n\t" \
    "ds_write_addtid_b32 v16 offset:59904\n\t" \
    "ds_write_addtid_b32 v41 offset:60160\n\t" \
    "ds_write_addtid_b32 v39 offset:60416\n\t" \
    "ds_write_addtid_b32 v37 offset:60672\n\t" \
    "buffer_load_dwordx4 v[140:143], v17, s[4:7], s47 offen offset:1024 sc1\n\t" \
    "v_cvt_pk_bf16_f32 v11, v234, v250\n\t" \
    "v_cvt_pk_bf16_f32 v1, v235, v251\n\t" \
    "v_cvt_pk_bf16_f32 v46, v236, v252\n\t" \
    "v_cvt_pk_bf16_f32 v9, v237, v253\n\t" \
    "s_waitcnt vmcnt(9)\n\t" \
    "v_mfma_f32_32x32x16_bf16 v[208:223], v[56:59], v[104:107], v[208:223]\n\t" \
    "ds_write_addtid_b32 v11 offset:60928\n\t" \
    "ds_write_addtid_b32 v1 offset:61184\n\t" \
    "ds_write_addtid_b32 v46 offset:61440\n\t" \
    "ds_write_addtid_b32 v9 offset:61696\n\t" \
    "buffer_load_dwordx4 v[156:159], v19, s[4:7], s47 offen offset:0 sc1\n\t" \
    "s_waitcnt vmcnt(9)\n\t" \
    "s_waitcnt lgkmcnt(0)\n\t" \
    "s_barrier\n\t" \
    "s_memrealtime s[64:65]\n\t" \
    "s_cmp_lg_u32 s24, 7\n\t" \
    "s_cbranch_scc1 .Lar_%=\n\t" \
    "s_waitcnt lgkmcnt(0)\n\t" \
    "v_mov_b32 v149, s64\n\t" \
    "s_and_b32 s37, s48, 8192\n\t" \
    "s_add_u32 s37, s37, s32\n\t" \
    "s_add_u32 s37, s37, 1048576\n\t" \
    "s_mov_b64 exec, 0xff\n\t" \
    "buffer_store_dwordx4 v[148:151], v0, s[4:7], s37 offen sc1\n\t" \
    "s_mov_b64 exec, -1\n\t" \
    ".Lar_%=:\n\t" \
    "v_add_u32 v44, 58368, v44\n\t" \
    "s_bitcmp1_b32 s24, 2\n\t" \
    "s_cbranch_scc1 .Ly29_%=\n\t" \
    "buffer_load_dwordx4 v[168:171], v19, s[4:7], s45 offen offset:1024 sc1\n\t" \
    ".Ly29_%=:\n\t" \
    "v_add_u32 v45, 58368, v45\n\t" \
    "ds_read_b128 v[172:175], v44 offset:0\n\t" \
    "ds_read_b128 v[144:147], v44 offset:128\n\t" \
    "ds_read_b128 v[152:155], v44 offset:29184\n\t" \
    "ds_read_b128 v[160:163], v44 offset:29312\n\t" \
    "s_bitcmp1_b32 s24, 2\n\t" \
    "s_cbranch_scc1 .Ly30_%=\n\t" \
    "buffer_load_dwordx4 v[164:167], v19, s[4:7], s46 offen offset:1024 sc1\n\t" \
    ".Ly30_%=:\n\t" \
    "ds_read_b128 v[176:179], v45 offset:0\n\t" \
    "ds_read_b128 v[184:187], v45 offset:128\n\t" \
    "ds_read_b128 v[188:191], v45 offset:29184\n\t" \
    "ds_read_b128 v[60:63], v45 offset:29312\n\t" \
    "v_mfma_f32_32x32x16_bf16 v[224:239], v[56:59], v[76:79], 0\n\t" \
    "v_cvt_pk_bf16_f32 v3, v192, v208\n\t" \
    "v_cvt_pk_bf16_f32 v51, v193, v209\n\t" \
    "v_cvt_pk_bf16_f32 v50, v194, v210\n\t" \
    "v_cvt_pk_bf16_f32 v36, v195, v211\n\t" \
    "v_cvt_pk_bf16_f32 v8, v196, v212\n\t" \
    "s_waitcnt vmcnt(8)\n\t" \
    "v_mfma_f32_16x16x32_bf16 v[180:183], v[12:15], v[92:95], 0\n\t" \
    "v_permlane16_swap_b32 v88, v80\n\t" \
    "s_bitcmp1_b32 s24, 2\n\t" \
    "s_cbranch_scc1 .Ly31_%=\n\t" \
    "buffer_load_dwordx4 v[4:7], v19, s[4:7], s47 offen offset:1024 sc1\n\t" \
    ".Ly31_%=:\n\t" \
    "v_permlane16_swap_b32 v89, v81\n\t" \
    "ds_write_addtid_b32 v3 offset:3616\n\t" \
    "ds_write_addtid_b32 v51 offset:3872\n\t" \
    "ds_write_addtid_b32 v50 offset:4128\n\t" \
    "ds_write_addtid_b32 v36 offset:4384\n\t" \
    "ds_write_addtid_b32 v8 offset:4640\n\t" \
    "v_mfma_f32_16x16x32_bf16 v[64:67], v[12:15], v[108:111], 0\n\t" \
    "v_permlane16_swap_b32 v90, v82\n\t" \
    "v_mfma_f32_16x16x32_bf16 v[68:71], v[12:15], v[112:115], 0\n\t" \
    "v_permlane16_swap_b32 v91, v83\n\t" \
    "v_cvt_pk_bf16_f32 v16, v197, v213\n\t" \
    "v_cvt_pk_bf16_f32 v41, v198, v214\n\t" \
    "v_cvt_pk_bf16_f32 v39, v199, v215\n\t" \
    "v_cvt_pk_bf16_f32 v37, v200, v216\n\t" \
    "v_cvt_pk_bf16_f32 v11, v201, v217\n\t" \
    "s_mov_b32 s45, 344064\n\t" \
    "buffer_load_dwordx4 v[72:75], v17, s[4:7], s45 offen offset:0 sc1\n\t" \
    "v_mfma_f32_32x32x16_bf16 v[240:255], v[56:59], v[100:103], 0\n\t" \
    "v_mfma_f32_16x16x32_bf16 v[20:23], v[12:15], v[84:87], 0\n\t" \
    "v_permlane16_swap_b32 v80, v104\n\t" \
    "ds_write_addtid_b32 v16 offset:4896\n\t" \
    "ds_write_addtid_b32 v41 offset:5152\n\t" \
    "ds_write_addtid_b32 v39 offset:5408\n\t" \
    "ds_write_addtid_b32 v37 offset:5664\n\t" \
    "ds_write_addtid_b32 v11 offset:5920\n\t" \
    "s_waitcnt vmcnt(8)\n\t" \
    "v_mfma_f32_16x16x32_bf16 v[148:151], v[120:123], v[108:111], 0\n\t" \
    "v_permlane16_swap_b32 v81, v105\n\t" \
    "buffer_load_dwordx4 v[76:79], v17, s[4:7], s45 offen offset:1024 sc1\n\t" \
    "v_permlane16_swap_b32 v82, v106\n\t" \
    "v_cvt_pk_bf16_f32 v1, v202, v218\n\t" \
    "v_cvt_pk_bf16_f32 v46, v203, v219\n\t" \
    "v_cvt_pk_bf16_f32 v9, v204, v220\n\t" \
    "v_cvt_pk_bf16_f32 v3, v205, v221\n\t" \
    "v_mfma_f32_16x16x32_bf16 v[64:67], v[120:123], v[92:95], v[64:67]\n\t" \
    "v_permlane16_swap_b32 v83, v107\n\t" \
    "v_mfma_f32_16x16x32_bf16 v[56:59], v[120:123], v[84:87], 0\n\t" \
    "v_mfma_f32_32x32x16_bf16 v[224:239], v[52:55], v[88:91], v[224:239]\n\t" \
    "ds_write_addtid_b32 v1 offset:6176\n\t" \
    "ds_write_addtid_b32 v46 offset:6432\n\t" \
    "ds_write_addtid_b32 v9 offset:6688\n\t" \
    "ds_write_addtid_b32 v3 offset:6944\n\t" \
    "s_mov_b32 s46, 458752\n\t" \
    "buffer_load_dwordx4 v[100:103], v17, s[4:7], s46 offen offset:0 sc1\n\t" \
    "v_mfma_f32_32x32x16_bf16 v[240:255], v[52:55], v[80:83], v[240:255]\n\t" \
    "v_mfma_f32_16x16x32_bf16 v[20:23], v[120:123], v[112:115], v[20:23]\n\t" \
    "s_nop 10\n\t" \
    "v_cvt_pk_bf16_f32 v51, v224, v240\n\t" \
    "v_cvt_pk_bf16_f32 v50, v225, v241\n\t" \
    "v_cvt_pk_bf16_f32 v36, v226, v242\n\t" \
    "v_cvt_pk_bf16_f32 v8, v227, v243\n\t" \
    "v_cvt_pk_bf16_f32 v16, v228, v244\n\t" \
    "s_waitcnt vmcnt(9) lgkmcnt(15)\n\t" \
    "v_mfma_f32_16x16x32_bf16 v[180:183], v[132:135], v[172:175], v[180:183]\n\t" \
    "buffer_load_dwordx4 v[12:15], v17, s[4:7], s46 offen offset:1024 sc1\n\t" \
    "v_mfma_f32_16x16x32_bf16 v[64:67], v[132:135], v[144:147], v[64:67]\n\t" \
    "ds_write_addtid_b32 v51 offset:0\n\t" \
    "ds_write_addtid_b32 v50 offset:256\n\t" \
    "ds_write_addtid_b32 v36 offset:512\n\t" \
    "ds_write_addtid_b32 v8 offset:768\n\t" \
    "ds_write_addtid_b32 v16 offset:1024\n\t" \
    "v_mfma_f32_16x16x32_bf16 v[68:71], v[132:135], v[152:155], v[68:71]\n\t" \
    "v_cvt_pk_bf16_f32 v41, v229, v245\n\t" \
    "v_cvt_pk_bf16_f32 v39, v230, v246\n\t" \
    "v_cvt_pk_bf16_f32 v37, v231, v247\n\t" \
    "v_cvt_pk_bf16_f32 v11, v232, v248\n\t" \
    "v_cvt_pk_bf16_f32 v1, v233, v249\n\t" \
    "s_mov_b32 s47, 573440\n\t" \
    "buffer_load_dwordx4 v[192:195], v17, s[4:7], s47 offen offset:0 sc1\n\t" \
    "v_mfma_f32_16x16x32_bf16 v[20:23], v[132:135], v[160:163], v[20:23]\n\t" \
    "ds_write_addtid_b32 v41 offset:1280\n\t" \
    "ds_write_addtid_b32 v39 offset:1536\n\t" \
    "ds_write_addtid_b32 v37 offset:1792\n\t" \
    "ds_write_addtid_b32 v11 offset:2048\n\t" \
    "ds_write_addtid_b32 v1 offset:2304\n\t" \
    "s_waitcnt vmcnt(10)\n\t" \
    "v_mfma_f32_16x16x32_bf16 v[148:151], v[28:31], v[144:147], v[148:151]\n\t" \
    "buffer_load_dwordx4 v[196:199], v17, s[4:7], s47 offen offset:1024 sc1\n\t" \
    "v_mfma_f32_16x16x32_bf16 v[64:67], v[28:31], v[172:175], v[64:67]\n\t" \
    "v_cvt_pk_bf16_f32 v46, v234, v250\n\t" \
    "v_cvt_pk_bf16_f32 v9, v235, v251\n\t" \
    "v_cvt_pk_bf16_f32 v3, v236, v252\n\t" \
    "v_cvt_pk_bf16_f32 v51, v237, v253\n\t" \
    "v_mfma_f32_16x16x32_bf16 v[56:59], v[28:31], v[160:163], v[56:59]\n\t" \
    "ds_write_addtid_b32 v46 offset:2560\n\t" \
    "ds_write_addtid_b32 v9 offset:2816\n\t" \
    "ds_write_addtid_b32 v3 offset:3072\n\t" \
    "ds_write_addtid_b32 v51 offset:3328\n\t" \
    "buffer_load_dwordx4 v[200:203], v19, s[4:7], s45 offen offset:0 sc1\n\t" \
    "v_mfma_f32_16x16x32_bf16 v[20:23], v[28:31], v[152:155], v[20:23]\n\t" \
    "s_waitcnt lgkmcnt(0)\n\t" \
    "s_barrier\n\t" \
    "ds_read_b128 v[204:207], v10 offset:2048\n\t" \
    "v_mov_b32 v50, 0x10000\n\t" \
    "s_waitcnt vmcnt(11)\n\t" \
    "v_mfma_f32_16x16x32_bf16 v[208:211], v[136:139], v[96:99], 0\n\t" \
    "v_subrev_u32 v44, 58368, v44\n\t" \
    "v_mfma_f32_16x16x32_bf16 v[212:215], v[136:139], v[124:127], 0\n\t" \
    "v_subrev_u32 v45, 58368, v45\n\t" \
    "v_mfma_f32_16x16x32_bf16 v[216:219], v[136:139], v[116:119], 0\n\t" \
    "ds_read_b128 v[220:223], v44 offset:0\n\t" \
    "buffer_load_dwordx4 v[104:107], v19, s[4:7], s46 offen offset:0 sc1\n\t" \
    "v_mfma_f32_16x16x32_bf16 v[88:91], v[136:139], v[128:131], 0\n\t" \
    "ds_read_b128 v[52:55], v44 offset:128\n\t" \
    "s_waitcnt vmcnt(11)\n\t" \
    "v_mfma_f32_16x16x32_bf16 v[208:211], v[24:27], v[176:179], v[208:211]\n\t" \
    "ds_read_b128 v[80:83], v44 offset:29184\n\t" \
    "v_mfma_f32_16x16x32_bf16 v[212:215], v[24:27], v[184:187], v[212:215]\n\t" \
    "ds_read_b128 v[120:123], v44 offset:29312\n\t" \
    "buffer_load_dwordx4 v[132:135], v19, s[4:7], s47 offen offset:0 sc1\n\t" \
    "v_mfma_f32_16x16x32_bf16 v[216:219], v[24:27], v[188:191], v[216:219]\n\t" \
    "ds_read_b128 v[224:227], v45 offset:0\n\t" \
    "v_mfma_f32_16x16x32_bf16 v[88:91], v[24:27], v[60:63], v[88:91]\n\t" \
    "ds_read_b128 v[228:231], v45 offset:128\n\t" \
    "s_waitcnt vmcnt(11) lgkmcnt(5)\n\t" \
    "v_mfma_f32_16x16x32_bf16 v[180:183], v[32:35], v[220:223], v[180:183]\n\t" \
    "ds_read_b128 v[232:235], v45 offset:29184\n\t" \
    "s_bitcmp1_b32 s24, 2\n\t" \
    "s_cbranch_scc1 .Ly32_%=\n\t" \
    "buffer_load_dwordx4 v[236:239], v19, s[4:7], s45 offen offset:1024 sc1\n\t" \
    ".Ly32_%=:\n\t" \
    "s_waitcnt lgkmcnt(5)\n\t" \
    "v_mfma_f32_16x16x32_bf16 v[64:67], v[32:35], v[52:55], v[64:67]\n\t" \
    "ds_read_b128 v[240:243], v45 offset:29312\n\t" \
    "s_waitcnt lgkmcnt(5)\n\t" \
    "v_mfma_f32_16x16x32_bf16 v[68:71], v[32:35], v[80:83], v[68:71]\n\t" \
    "s_waitcnt lgkmcnt(4)\n\t" \
    "v_mfma_f32_16x16x32_bf16 v[20:23], v[32:35], v[120:123], v[20:23]\n\t" \
    "s_bitcmp1_b32 s24, 2\n\t" \
    "s_cbranch_scc1 .Ly33_%=\n\t" \
    "buffer_load_dwordx4 v[244:247], v19, s[4:7], s46 offen offset:1024 sc1\n\t" \
    ".Ly33_%=:\n\t" \
    "s_waitcnt vmcnt(10)\n\t" \
    "v_mfma_f32_16x16x32_bf16 v[148:151], v[140:143], v[52:55], v[148:151]\n\t" \
    "s_waitcnt lgkmcnt(0)\n\t" \
    "s_add_u32 s12, s55, 34603008\n\t" \
    "s_buffer_load_dword s13, s[4:7], s12\n\t" \
    "s_buffer_load_dword s14, s[4:7], s12 offset:0x400\n\t" \
    "v_mfma_f32_16x16x32_bf16 v[64:67], v[140:143], v[220:223], v[64:67]\n\t" \
    "v_mfma_f32_16x16x32_bf16 v[56:59], v[140:143], v[120:123], v[56:59]\n\t" \
    ".Lz3_%=:\n\t" \
    "s_cbranch_execz .Lwk_%=\n\t" \
    "s_bitcmp1_b32 s24, 2\n\t" \
    "s_cbranch_scc1 .Ly34_%=\n\t" \
    "buffer_load_dwordx4 v[248:251], v19, s[4:7], s47 offen offset:1024 sc1\n\t" \
    ".Ly34_%=:\n\t" \
    "v_mfma_f32_16x16x32_bf16 v[20:23], v[140:143], v[80:83], v[20:23]\n\t" \
    "s_waitcnt vmcnt(9)\n\t" \
    "v_mfma_f32_16x16x32_bf16 v[208:211], v[156:159], v[224:227], v[208:211]\n\t" \
    "v_mfma_f32_16x16x32_bf16 v[212:215], v[156:159], v[228:231], v[212:215]\n\t" \
    "s_mov_b32 s45, 688128\n\t" \
    "buffer_load_dwordx4 v[252:255], v17, s[4:7], s45 offen offset:0 sc1\n\t" \
    "v_mfma_f32_16x16x32_bf16 v[216:219], v[156:159], v[232:235], v[216:219]\n\t" \
    "v_sub_f32 v180, v180, v148\n\t" \
    "v_sub_f32 v181, v181, v149\n\t" \
    "v_sub_f32 v182, v182, v150\n\t" \
    "v_mfma_f32_16x16x32_bf16 v[88:91], v[156:159], v[240:243], v[88:91]\n\t" \
    "v_sub_f32 v183, v183, v151\n\t" \
    "v_sub_f32 v68, v68, v56\n\t" \
    "v_sub_f32 v69, v69, v57\n\t" \
    "v_sub_f32 v70, v70, v58\n\t" \
    "v_sub_f32 v71, v71, v59\n\t" \
    "v_cvt_pk_bf16_f32 v30, v64, v65\n\t" \
    "v_cvt_pk_bf16_f32 v31, v66, v67\n\t" \
    "v_cvt_pk_bf16_f32 v28, v180, v181\n\t" \
    "v_cvt_pk_bf16_f32 v29, v182, v183\n\t" \
    "v_cvt_pk_bf16_f32 v138, v20, v21\n\t" \
    "v_cvt_pk_bf16_f32 v139, v22, v23\n\t" \
    "ds_write_b128 v40, v[28:31] offset:58368\n\t" \
    "v_cvt_pk_bf16_f32 v136, v68, v69\n\t" \
    "v_cvt_pk_bf16_f32 v137, v70, v71\n\t" \
    "ds_write_b128 v40, v[136:139] offset:59392\n\t" \
    "s_waitcnt vmcnt(10)\n\t" \
    "s_cmp_ge_u32 s24, 4\n\t" \
    "s_cbranch_scc1 .Lk53_%=\n\t" \
    "v_mfma_f32_16x16x32_bf16 v[24:27], v[168:171], v[124:127], 0\n\t" \
    "v_mfma_f32_16x16x32_bf16 v[24:27], v[164:167], v[184:187], v[24:27]\n\t" \
    "v_mfma_f32_16x16x32_bf16 v[24:27], v[4:7], v[228:231], v[24:27]\n\t" \
    "v_mfma_f32_16x16x32_bf16 v[32:35], v[168:171], v[128:131], 0\n\t" \
    "v_mfma_f32_16x16x32_bf16 v[32:35], v[164:167], v[60:63], v[32:35]\n\t" \
    "v_mfma_f32_16x16x32_bf16 v[32:35], v[4:7], v[240:243], v[32:35]\n\t" \
    "v_mfma_f32_16x16x32_bf16 v[212:215], v[168:171], v[96:99], v[212:215]\n\t" \
    "v_mfma_f32_16x16x32_bf16 v[212:215], v[164:167], v[176:179], v[212:215]\n\t" \
    "v_mfma_f32_16x16x32_bf16 v[212:215], v[4:7], v[224:227], v[212:215]\n\t" \
    "v_mfma_f32_16x16x32_bf16 v[88:91], v[168:171], v[116:119], v[88:91]\n\t" \
    "s_nop 0\n\t" \
    "v_sub_f32 v208, v208, v24\n\t" \
    "v_sub_f32 v209, v209, v25\n\t" \
    "v_mfma_f32_16x16x32_bf16 v[88:91], v[164:167], v[188:191], v[88:91]\n\t" \
    "v_sub_f32 v210, v210, v26\n\t" \
    "v_sub_f32 v211, v211, v27\n\t" \
    "v_mfma_f32_16x16x32_bf16 v[88:91], v[4:7], v[232:235], v[88:91]\n\t" \
    "v_sub_f32 v216, v216, v32\n\t" \
    "v_sub_f32 v217, v217, v33\n\t" \
    "v_sub_f32 v218, v218, v34\n\t" \
    "v_sub_f32 v219, v219, v35\n\t" \
    ".Lk53_%=:\n\t" \
    "s_waitcnt vmcnt(9)\n\t" \
    "v_mfma_f32_16x16x32_bf16 v[140:143], v[72:75], v[92:95], 0\n\t" \
    "buffer_load_dwordx4 v[156:159], v17, s[4:7], s45 offen offset:1024 sc1\n\t" \
    "v_mfma_f32_16x16x32_bf16 v[148:151], v[72:75], v[108:111], 0\n\t" \
    "v_cvt_pk_bf16_f32 v58, v212, v213\n\t" \
    "v_cvt_pk_bf16_f32 v59, v214, v215\n\t" \
    "v_mfma_f32_16x16x32_bf16 v[64:67], v[72:75], v[112:115], 0\n\t" \
    "v_cvt_pk_bf16_f32 v56, v208, v209\n\t" \
    "v_cvt_pk_bf16_f32 v57, v210, v211\n\t" \
    "v_mfma_f32_16x16x32_bf16 v[180:183], v[72:75], v[84:87], 0\n\t" \
    "v_cvt_pk_bf16_f32 v22, v88, v89\n\t" \
    "v_cvt_pk_bf16_f32 v23, v90, v91\n\t" \
    "s_mov_b32 s46, 802816\n\t" \
    "buffer_load_dwordx4 v[28:31], v17, s[4:7], s46 offen offset:0 sc1\n\t" \
    "s_waitcnt vmcnt(10)\n\t" \
    "v_mfma_f32_16x16x32_bf16 v[68:71], v[76:79], v[108:111], 0\n\t" \
    "ds_write_b128 v40, v[56:59] offset:60432\n\t" \
    "v_cvt_pk_bf16_f32 v20, v216, v217\n\t" \
    "v_mfma_f32_16x16x32_bf16 v[148:151], v[76:79], v[92:95], v[148:151]\n\t" \
    "v_cvt_pk_bf16_f32 v21, v218, v219\n\t" \
    "v_mfma_f32_16x16x32_bf16 v[136:139], v[76:79], v[84:87], 0\n\t" \
    "ds_write_b128 v40, v[20:23] offset:61456\n\t" \
    "buffer_load_dwordx4 v[168:171], v17, s[4:7], s46 offen offset:1024 sc1\n\t" \
    "v_mfma_f32_16x16x32_bf16 v[180:183], v[76:79], v[112:115], v[180:183]\n\t" \
    "s_waitcnt vmcnt(10)\n\t" \
    "v_mfma_f32_16x16x32_bf16 v[140:143], v[100:103], v[172:175], v[140:143]\n\t" \
    "v_mfma_f32_16x16x32_bf16 v[148:151], v[100:103], v[144:147], v[148:151]\n\t" \
    "s_waitcnt lgkmcnt(0)\n\t" \
    "s_xor_b32 s13, s13, s34\n\t" \
    "s_xor_b32 s14, s14, s34\n\t" \
    "s_or_b32 s13, s13, s14\n\t" \
    "s_cbranch_scc1 .Lq1028_%=\n\t" \
    ".Lb27_%=:\n\t" \
    "s_waitcnt lgkmcnt(0)\n\t" \
    "s_barrier\n\t" \
    "s_memrealtime s[16:17]\n\t" \
    "ds_read_b64_tr_b16 v[164:165], v49 offset:58368\n\t" \
    "ds_read_b64_tr_b16 v[166:167], v49 offset:62496\n\t" \
    "ds_read_b64_tr_b16 v[24:25], v49 offset:58624\n\t" \
    "ds_read_b64_tr_b16 v[26:27], v49 offset:62752\n\t" \
    "s_mov_b32 s47, 917504\n\t" \
    "buffer_load_dwordx4 v[4:7], v17, s[4:7], s47 offen offset:0 sc1\n\t" \
    "v_mfma_f32_16x16x32_bf16 v[64:67], v[100:103], v[152:155], v[64:67]\n\t" \
    "ds_read_b64_tr_b16 v[32:33], v49 offset:58384\n\t" \
    "ds_read_b64_tr_b16 v[34:35], v49 offset:62512\n\t" \
    "ds_read_b64_tr_b16 v[212:213], v49 offset:58640\n\t" \
    "ds_read_b64_tr_b16 v[214:215], v49 offset:62768\n\t" \
    "v_mfma_f32_16x16x32_bf16 v[180:183], v[100:103], v[160:163], v[180:183]\n\t" \
    "s_waitcnt vmcnt(10)\n\t" \
    "v_mfma_f32_16x16x32_bf16 v[68:71], v[12:15], v[144:147], v[68:71]\n\t" \
    "buffer_load_dwordx4 v[208:211], v17, s[4:7], s47 offen offset:1024 sc1\n\t" \
    "v_mfma_f32_16x16x32_bf16 v[148:151], v[12:15], v[172:175], v[148:151]\n\t" \
    "s_waitcnt lgkmcnt(6)\n\t" \
    "v_mfma_f32_16x16x32_bf16 v[72:75], v[164:167], v[204:207], 0\n\t" \
    "s_waitcnt lgkmcnt(4)\n\t" \
    "v_mfma_f32_16x16x32_bf16 v[88:91], v[24:27], v[204:207], 0\n\t" \
    "v_mfma_f32_16x16x32_bf16 v[136:139], v[12:15], v[160:163], v[136:139]\n\t" \
    "s_waitcnt lgkmcnt(2)\n\t" \
    "v_mfma_f32_16x16x32_bf16 v[56:59], v[32:35], v[204:207], 0\n\t" \
    "s_waitcnt lgkmcnt(0)\n\t" \
    "v_mfma_f32_16x16x32_bf16 v[216:219], v[212:215], v[204:207], 0\n\t" \
    "v_mfma_f32_16x16x32_bf16 v[180:183], v[12:15], v[152:155], v[180:183]\n\t" \
    "s_cmp_lg_u32 s24, 4\n\t" \
    "s_cbranch_scc1 .Lph_%=\n\t" \
    "s_waitcnt lgkmcnt(0)\n\t" \
    "v_mov_b32 v20, s16\n\t" \
    "v_mov_b32 v21, s16\n\t" \
    "v_mov_b32 v22, s16\n\t" \
    "v_mov_b32 v23, s16\n\t" \
    "buffer_store_dwordx4 v[20:23], v2, s[4:7], s32 offen sc1\n\t" \
    "s_nop 0\n\t" \
    ".Lph_%=:\n\t" \
    "buffer_load_dwordx4 v[76:79], v19, s[4:7], s45 offen offset:0 sc1\n\t" \
    "s_waitcnt vmcnt(11)\n\t" \
    "v_mfma_f32_16x16x32_bf16 v[140:143], v[192:195], v[220:223], v[140:143]\n\t" \
    "v_bfi_b32 v72, s41, v50, v72\n\t" \
    "v_bfi_b32 v88, s41, v50, v88\n\t" \
    "v_cvt_pk_bf16_f32 v45, v74, v75\n\t" \
    "v_mfma_f32_16x16x32_bf16 v[148:151], v[192:195], v[52:55], v[148:151]\n\t" \
    "v_cvt_pk_bf16_f32 v47, v90, v91\n\t" \
    "v_cvt_pk_bf16_f32 v44, v72, v73\n\t" \
    "v_cvt_pk_bf16_f32 v46, v88, v89\n\t" \
    "v_mfma_f32_16x16x32_bf16 v[64:67], v[192:195], v[80:83], v[64:67]\n\t" \
    "v_bfi_b32 v56, s41, v50, v56\n\t" \
    "v_bfi_b32 v216, s41, v50, v216\n\t" \
    "buffer_store_dwordx4 v[44:47], v42, s[4:7], s48 offen offset:0 sc1\n\t" \
    "v_mfma_f32_16x16x32_bf16 v[180:183], v[192:195], v[120:123], v[180:183]\n\t" \
    "v_cvt_pk_bf16_f32 v101, v58, v59\n\t" \
    "v_cvt_pk_bf16_f32 v103, v218, v219\n\t" \
    "v_cvt_pk_bf16_f32 v100, v56, v57\n\t" \
    "s_waitcnt vmcnt(11)\n\t" \
    "v_mfma_f32_16x16x32_bf16 v[68:71], v[196:199], v[52:55], v[68:71]\n\t" \
    "v_cvt_pk_bf16_f32 v102, v216, v217\n\t" \
    "buffer_store_dwordx4 v[100:103], v18, s[4:7], s48 offen offset:128 sc1\n\t" \
    "v_mfma_f32_16x16x32_bf16 v[148:151], v[196:199], v[220:223], v[148:151]\n\t" \
    "v_mfma_f32_16x16x32_bf16 v[136:139], v[196:199], v[120:123], v[136:139]\n\t" \
    "v_mfma_f32_16x16x32_bf16 v[180:183], v[196:199], v[80:83], v[180:183]\n\t" \
    "s_waitcnt vmcnt(11)\n\t" \
    "v_mfma_f32_16x16x32_bf16 v[164:167], v[200:203], v[96:99], 0\n\t" \
    "v_mfma_f32_16x16x32_bf16 v[24:27], v[200:203], v[124:127], 0\n\t" \
    "v_mfma_f32_16x16x32_bf16 v[32:35], v[200:203], v[116:119], 0\n\t" \
    "v_sub_f32 v140, v140, v68\n\t" \
    "v_sub_f32 v141, v141, v69\n\t" \
    "v_mfma_f32_16x16x32_bf16 v[212:215], v[200:203], v[128:131], 0\n\t" \
    "v_sub_f32 v142, v142, v70\n\t" \
    "v_sub_f32 v143, v143, v71\n\t" \
    "s_waitcnt vmcnt(10)\n\t" \
    "v_mfma_f32_16x16x32_bf16 v[164:167], v[104:107], v[176:179], v[164:167]\n\t" \
    "v_sub_f32 v64, v64, v136\n\t" \
    "v_sub_f32 v65, v65, v137\n\t" \
    "v_mfma_f32_16x16x32_bf16 v[24:27], v[104:107], v[184:187], v[24:27]\n\t" \
    "v_sub_f32 v66, v66, v138\n\t" \
    "v_sub_f32 v67, v67, v139\n\t" \
    "v_mfma_f32_16x16x32_bf16 v[32:35], v[104:107], v[188:191], v[32:35]\n\t" \
    "v_cvt_pk_bf16_f32 v14, v148, v149\n\t" \
    "v_cvt_pk_bf16_f32 v15, v150, v151\n\t" \
    "buffer_load_dwordx4 v[20:23], v19, s[4:7], s46 offen offset:0 sc1\n\t" \
    "v_mfma_f32_16x16x32_bf16 v[212:215], v[104:107], v[60:63], v[212:215]\n\t" \
    "v_cvt_pk_bf16_f32 v12, v140, v141\n\t" \
    "v_cvt_pk_bf16_f32 v13, v142, v143\n\t" \
    "s_waitcnt vmcnt(10)\n\t" \
    "v_mfma_f32_16x16x32_bf16 v[164:167], v[132:135], v[224:227], v[164:167]\n\t" \
    "v_cvt_pk_bf16_f32 v74, v180, v181\n\t" \
    "v_cvt_pk_bf16_f32 v75, v182, v183\n\t" \
    "v_mfma_f32_16x16x32_bf16 v[24:27], v[132:135], v[228:231], v[24:27]\n\t" \
    "ds_write_b128 v40, v[12:15] offset:0\n\t" \
    "v_cvt_pk_bf16_f32 v72, v64, v65\n\t" \
    "buffer_load_dwordx4 v[88:91], v19, s[4:7], s47 offen offset:0 sc1\n\t" \
    "v_mfma_f32_16x16x32_bf16 v[32:35], v[132:135], v[232:235], v[32:35]\n\t" \
    "v_cvt_pk_bf16_f32 v73, v66, v67\n\t" \
    "ds_write_b128 v40, v[72:75] offset:1024\n\t" \
    "v_mfma_f32_16x16x32_bf16 v[212:215], v[132:135], v[240:243], v[212:215]\n\t" \
    "s_waitcnt vmcnt(11)\n\t" \
    "s_cmp_ge_u32 s24, 4\n\t" \
    "s_cbranch_scc1 .Lk54_%=\n\t" \
    "v_mfma_f32_16x16x32_bf16 v[44:47], v[236:239], v[124:127], 0\n\t" \
    "v_mfma_f32_16x16x32_bf16 v[44:47], v[244:247], v[184:187], v[44:47]\n\t" \
    "v_mfma_f32_16x16x32_bf16 v[44:47], v[248:251], v[228:231], v[44:47]\n\t" \
    "v_mfma_f32_16x16x32_bf16 v[192:195], v[236:239], v[128:131], 0\n\t" \
    "v_mfma_f32_16x16x32_bf16 v[192:195], v[244:247], v[60:63], v[192:195]\n\t" \
    "v_mfma_f32_16x16x32_bf16 v[192:195], v[248:251], v[240:243], v[192:195]\n\t" \
    "v_mfma_f32_16x16x32_bf16 v[24:27], v[236:239], v[96:99], v[24:27]\n\t" \
    "v_mfma_f32_16x16x32_bf16 v[24:27], v[244:247], v[176:179], v[24:27]\n\t" \
    "v_mfma_f32_16x16x32_bf16 v[24:27], v[248:251], v[224:227], v[24:27]\n\t" \
    "v_mfma_f32_16x16x32_bf16 v[212:215], v[236:239], v[116:119], v[212:215]\n\t" \
    "s_nop 0\n\t" \
    "v_sub_f32 v164, v164, v44\n\t" \
    "v_sub_f32 v165, v165, v45\n\t" \
    "v_mfma_f32_16x16x32_bf16 v[212:215], v[244:247], v[188:191], v[212:215]\n\t" \
    "v_sub_f32 v166, v166, v46\n\t" \
    "v_sub_f32 v167, v167, v47\n\t" \
    "v_mfma_f32_16x16x32_bf16 v[212:215], v[248:251], v[232:235], v[212:215]\n\t" \
    "v_sub_f32 v32, v32, v192\n\t" \
    "v_sub_f32 v33, v33, v193\n\t" \
    "v_sub_f32 v34, v34, v194\n\t" \
    "v_sub_f32 v35, v35, v195\n\t" \
    ".Lk54_%=:\n\t" \
    "s_waitcnt vmcnt(10)\n\t" \
    "v_mfma_f32_16x16x32_bf16 v[56:59], v[252:255], v[92:95], 0\n\t" \
    "s_bitcmp1_b32 s24, 2\n\t" \
    "s_cbranch_scc1 .Ly35_%=\n\t" \
    "buffer_load_dwordx4 v[216:219], v19, s[4:7], s45 offen offset:1024 sc1\n\t" \
    ".Ly35_%=:\n\t" \
    "v_mfma_f32_16x16x32_bf16 v[100:103], v[252:255], v[108:111], 0\n\t" \
    "v_cvt_pk_bf16_f32 v198, v24, v25\n\t" \
    "v_cvt_pk_bf16_f32 v199, v26, v27\n\t" \
    "v_mfma_f32_16x16x32_bf16 v[200:203], v[252:255], v[112:115], 0\n\t" \
    "v_cvt_pk_bf16_f32 v196, v164, v165\n\t" \
    "v_cvt_pk_bf16_f32 v197, v166, v167\n\t" \
    "v_mfma_f32_16x16x32_bf16 v[68:71], v[252:255], v[84:87], 0\n\t" \
    "v_cvt_pk_bf16_f32 v138, v212, v213\n\t" \
    "v_cvt_pk_bf16_f32 v139, v214, v215\n\t" \
    "s_bitcmp1_b32 s24, 2\n\t" \
    "s_cbranch_scc1 .Ly36_%=\n\t" \
    "buffer_load_dwordx4 v[148:151], v19, s[4:7], s46 offen offset:1024 sc1\n\t" \
    ".Ly36_%=:\n\t" \
    "s_waitcnt vmcnt(9)\n\t" \
    "v_mfma_f32_16x16x32_bf16 v[104:107], v[156:159], v[108:111], 0\n\t" \
    "ds_write_b128 v40, v[196:199] offset:2064\n\t" \
    "v_cvt_pk_bf16_f32 v136, v32, v33\n\t" \
    "v_mfma_f32_16x16x32_bf16 v[100:103], v[156:159], v[92:95], v[100:103]\n\t" \
    "v_cvt_pk_bf16_f32 v137, v34, v35\n\t" \
    "v_mfma_f32_16x16x32_bf16 v[140:143], v[156:159], v[84:87], 0\n\t" \
    "ds_write_b128 v40, v[136:139] offset:3088\n\t" \
    "s_bitcmp1_b32 s24, 2\n\t" \
    "s_cbranch_scc1 .Ly37_%=\n\t" \
    "buffer_load_dwordx4 v[180:183], v19, s[4:7], s47 offen offset:1024 sc1\n\t" \
    ".Ly37_%=:\n\t" \
    "v_mfma_f32_16x16x32_bf16 v[68:71], v[156:159], v[112:115], v[68:71]\n\t" \
    "s_waitcnt vmcnt(8)\n\t" \
    "v_mfma_f32_16x16x32_bf16 v[56:59], v[28:31], v[172:175], v[56:59]\n\t" \
    "v_mfma_f32_16x16x32_bf16 v[100:103], v[28:31], v[144:147], v[100:103]\n\t" \
    "s_waitcnt lgkmcnt(0)\n\t" \
    "s_barrier\n\t" \
    "s_memrealtime s[18:19]\n\t" \
    "s_setprio 2\n\t" \
    "ds_read_b64_tr_b16 v[12:13], v49 offset:0\n\t" \
    "ds_read_b64_tr_b16 v[14:15], v49 offset:4128\n\t" \
    "ds_read_b64_tr_b16 v[64:65], v49 offset:256\n\t" \
    "ds_read_b64_tr_b16 v[66:67], v49 offset:4384\n\t" \
    "v_mfma_f32_16x16x32_bf16 v[200:203], v[28:31], v[152:155], v[200:203]\n\t" \
    "ds_read_b64_tr_b16 v[72:73], v49 offset:16\n\t" \
    "ds_read_b64_tr_b16 v[74:75], v49 offset:4144\n\t" \
    "ds_read_b64_tr_b16 v[132:133], v49 offset:272\n\t" \
    "ds_read_b64_tr_b16 v[134:135], v49 offset:4400\n\t" \
    "v_mfma_f32_16x16x32_bf16 v[68:71], v[28:31], v[160:163], v[68:71]\n\t" \
    "s_waitcnt vmcnt(7)\n\t" \
    "v_mfma_f32_16x16x32_bf16 v[104:107], v[168:171], v[144:147], v[104:107]\n\t" \
    "v_mfma_f32_16x16x32_bf16 v[100:103], v[168:171], v[172:175], v[100:103]\n\t" \
    "s_waitcnt lgkmcnt(6)\n\t" \
    "v_mfma_f32_16x16x32_bf16 v[236:239], v[12:15], v[204:207], 0\n\t" \
    "s_waitcnt lgkmcnt(4)\n\t" \
    "v_mfma_f32_16x16x32_bf16 v[244:247], v[64:67], v[204:207], 0\n\t" \
    "v_mfma_f32_16x16x32_bf16 v[140:143], v[168:171], v[160:163], v[140:143]\n\t" \
    "s_waitcnt lgkmcnt(2)\n\t" \
    "v_mfma_f32_16x16x32_bf16 v[44:47], v[72:75], v[204:207], 0\n\t" \
    "s_waitcnt lgkmcnt(0)\n\t" \
    "v_mfma_f32_16x16x32_bf16 v[248:251], v[132:135], v[204:207], 0\n\t" \
    "v_mfma_f32_16x16x32_bf16 v[68:71], v[168:171], v[152:155], v[68:71]\n\t" \
    "s_waitcnt vmcnt(6)\n\t" \
    "v_mfma_f32_16x16x32_bf16 v[56:59], v[4:7], v[220:223], v[56:59]\n\t" \
    "v_bfi_b32 v236, s41, v50, v236\n\t" \
    "v_bfi_b32 v244, s41, v50, v244\n\t" \
    "v_cvt_pk_bf16_f32 v193, v238, v239\n\t" \
    "v_mfma_f32_16x16x32_bf16 v[100:103], v[4:7], v[52:55], v[100:103]\n\t" \
    "v_cvt_pk_bf16_f32 v195, v246, v247\n\t" \
    "v_cvt_pk_bf16_f32 v192, v236, v237\n\t" \
    "v_cvt_pk_bf16_f32 v194, v244, v245\n\t" \
    "v_mfma_f32_16x16x32_bf16 v[200:203], v[4:7], v[80:83], v[200:203]\n\t" \
    "v_bfi_b32 v44, s41, v50, v44\n\t" \
    "v_bfi_b32 v248, s41, v50, v248\n\t" \
    "buffer_store_dwordx4 v[192:195], v42, s[4:7], s49 offen offset:0 sc1\n\t" \
    "s_add_u32 s37, s52, s33\n\t" \
    "s_buffer_load_dword s58, s[4:7], s37 offset:0x0\n\t" \
    "s_buffer_load_dword s59, s[4:7], s37 offset:0x400\n\t" \
    "s_buffer_load_dword s60, s[4:7], s37 offset:0x8000\n\t" \
    "s_buffer_load_dword s61, s[4:7], s37 offset:0x8400\n\t" \
    "v_mfma_f32_16x16x32_bf16 v[68:71], v[4:7], v[120:123], v[68:71]\n\t" \
    "v_cvt_pk_bf16_f32 v25, v46, v47\n\t" \
    "v_cvt_pk_bf16_f32 v27, v250, v251\n\t" \
    "v_cvt_pk_bf16_f32 v24, v44, v45\n\t" \
    "s_waitcnt vmcnt(6)\n\t" \
    "v_mfma_f32_16x16x32_bf16 v[104:107], v[208:211], v[52:55], v[104:107]\n\t" \
    "v_cvt_pk_bf16_f32 v26, v248, v249\n\t" \
    "buffer_store_dwordx4 v[24:27], v18, s[4:7], s49 offen offset:128 sc1\n\t" \
    "s_setprio 0\n\t" \
    "v_add_u32 v40, 91392, v40\n\t" \
    "ds_read_b128 v[164:167], v10 offset:3072\n\t" \
    "ds_read_b128 v[252:255], v10 offset:4096\n\t" \
    "v_mfma_f32_16x16x32_bf16 v[100:103], v[208:211], v[220:223], v[100:103]\n\t" \
    "s_cmp_lt_u32 s24, 4\n\t" \
    "s_cbranch_scc1 .Lp55_%=\n\t" \
    "s_setprio 1\n\t" \
    ".Lp55_%=:\n\t" \
    "v_mfma_f32_16x16x32_bf16 v[140:143], v[208:211], v[120:123], v[140:143]\n\t" \
    "v_mfma_f32_16x16x32_bf16 v[68:71], v[208:211], v[80:83], v[68:71]\n\t" \
    "s_waitcnt vmcnt(6)\n\t" \
    "v_mfma_f32_16x16x32_bf16 v[212:215], v[76:79], v[96:99], 0\n\t" \
    "v_mfma_f32_16x16x32_bf16 v[108:111], v[76:79], v[124:127], 0\n\t" \
    "v_mfma_f32_16x16x32_bf16 v[196:199], v[76:79], v[116:119], 0\n\t" \
    "v_sub_f32 v56, v56, v104\n\t" \
    ".Lz4_%=:\n\t" \
    "s_cbranch_execz .Lwk_%=\n\t" \
    "v_sub_f32 v57, v57, v105\n\t" \
    "v_mfma_f32_16x16x32_bf16 v[92:95], v[76:79], v[128:131], 0\n\t" \
    "v_sub_f32 v58, v58, v106\n\t" \
    "v_sub_f32 v59, v59, v107\n\t" \
    "s_waitcnt vmcnt(3)\n\t" \
    "v_mfma_f32_16x16x32_bf16 v[212:215], v[20:23], v[176:179], v[212:215]\n\t" \
    "v_sub_f32 v200, v200, v140\n\t" \
    "v_sub_f32 v201, v201, v141\n\t" \
    "v_mfma_f32_16x16x32_bf16 v[108:111], v[20:23], v[184:187], v[108:111]\n\t" \
    "v_sub_f32 v202, v202, v142\n\t" \
    "v_sub_f32 v203, v203, v143\n\t" \
    "v_mfma_f32_16x16x32_bf16 v[196:199], v[20:23], v[188:191], v[196:199]\n\t" \
    "v_cvt_pk_bf16_f32 v34, v100, v101\n\t" \
    "v_cvt_pk_bf16_f32 v35, v102, v103\n\t" \
    "v_mfma_f32_16x16x32_bf16 v[92:95], v[20:23], v[60:63], v[92:95]\n\t" \
    "v_cvt_pk_bf16_f32 v32, v56, v57\n\t" \
    "v_cvt_pk_bf16_f32 v33, v58, v59\n\t" \
    "s_waitcnt vmcnt(2)\n\t" \
    "v_mfma_f32_16x16x32_bf16 v[212:215], v[88:91], v[224:227], v[212:215]\n\t" \
    "v_cvt_pk_bf16_f32 v86, v68, v69\n\t" \
    "v_cvt_pk_bf16_f32 v87, v70, v71\n\t" \
    "v_mfma_f32_16x16x32_bf16 v[108:111], v[88:91], v[228:231], v[108:111]\n\t" \
    "ds_write_b128 v40, v[32:35] offset:0\n\t" \
    "v_cvt_pk_bf16_f32 v84, v200, v201\n\t" \
    "s_add_u32 s54, s53, 0\n\t" \
    "buffer_load_dwordx4 v[136:139], v0, s[4:7], s54 offen offset:0 sc1\n\t" \
    "s_add_u32 s54, s53, 4243456\n\t" \
    "buffer_load_dwordx4 v[112:115], v0, s[4:7], s54 offen offset:0 sc1\n\t" \
    "v_mfma_f32_16x16x32_bf16 v[196:199], v[88:91], v[232:235], v[196:199]\n\t" \
    "v_cvt_pk_bf16_f32 v85, v202, v203\n\t" \
    "ds_write_b128 v40, v[84:87] offset:1024\n\t" \
    "v_mfma_f32_16x16x32_bf16 v[92:95], v[88:91], v[240:243], v[92:95]\n\t" \
    "s_waitcnt vmcnt(4)\n\t" \
    "s_cmp_ge_u32 s24, 4\n\t" \
    "s_cbranch_scc1 .Lk56_%=\n\t" \
    "v_mfma_f32_16x16x32_bf16 v[156:159], v[216:219], v[124:127], 0\n\t" \
    "v_mfma_f32_16x16x32_bf16 v[156:159], v[148:151], v[184:187], v[156:159]\n\t" \
    "v_mfma_f32_16x16x32_bf16 v[156:159], v[180:183], v[228:231], v[156:159]\n\t" \
    "v_mfma_f32_16x16x32_bf16 v[28:31], v[216:219], v[128:131], 0\n\t" \
    "v_mfma_f32_16x16x32_bf16 v[28:31], v[148:151], v[60:63], v[28:31]\n\t" \
    "v_mfma_f32_16x16x32_bf16 v[28:31], v[180:183], v[240:243], v[28:31]\n\t" \
    "v_mfma_f32_16x16x32_bf16 v[108:111], v[216:219], v[96:99], v[108:111]\n\t" \
    "v_mfma_f32_16x16x32_bf16 v[108:111], v[148:151], v[176:179], v[108:111]\n\t" \
    "v_mfma_f32_16x16x32_bf16 v[108:111], v[180:183], v[224:227], v[108:111]\n\t" \
    "v_mfma_f32_16x16x32_bf16 v[92:95], v[216:219], v[116:119], v[92:95]\n\t" \
    "s_nop 0\n\t" \
    "v_sub_f32 v212, v212, v156\n\t" \
    "v_sub_f32 v213, v213, v157\n\t" \
    "v_mfma_f32_16x16x32_bf16 v[92:95], v[148:151], v[188:191], v[92:95]\n\t" \
    "v_sub_f32 v214, v214, v158\n\t" \
    "v_sub_f32 v215, v215, v159\n\t" \
    "v_mfma_f32_16x16x32_bf16 v[92:95], v[180:183], v[232:235], v[92:95]\n\t" \
    "v_sub_f32 v196, v196, v28\n\t" \
    "v_sub_f32 v197, v197, v29\n\t" \
    "v_sub_f32 v198, v198, v30\n\t" \
    "v_sub_f32 v199, v199, v31\n\t" \
    ".Lk56_%=:\n\t" \
    "v_cvt_pk_bf16_f32 v146, v108, v109\n\t" \
    "v_cvt_pk_bf16_f32 v147, v110, v111\n\t" \
    "v_cvt_pk_bf16_f32 v144, v212, v213\n\t" \
    "v_cvt_pk_bf16_f32 v145, v214, v215\n\t" \
    "s_nop 1\n\t" \
    "v_cvt_pk_bf16_f32 v174, v92, v93\n\t" \
    "v_cvt_pk_bf16_f32 v175, v94, v95\n\t" \
    "ds_write_b128 v40, v[144:147] offset:2064\n\t" \
    "v_cvt_pk_bf16_f32 v172, v196, v197\n\t" \
    "v_cvt_pk_bf16_f32 v173, v198, v199\n\t" \
    "v_add_u32 v49, 91392, v49\n\t" \
    "ds_write_b128 v40, v[172:175] offset:3088\n\t" \
    "s_waitcnt lgkmcnt(0)\n\t" \
    "s_barrier\n\t" \
    "ds_read_b64_tr_b16 v[12:13], v49 offset:0\n\t" \
    "ds_read_b64_tr_b16 v[14:15], v49 offset:4128\n\t" \
    "ds_read_b64_tr_b16 v[64:65], v49 offset:256\n\t" \
    "ds_read_b64_tr_b16 v[66:67], v49 offset:4384\n\t" \
    "ds_read_b64_tr_b16 v[160:161], v49 offset:16\n\t" \
    "ds_read_b64_tr_b16 v[162:163], v49 offset:4144\n\t" \
    "ds_read_b64_tr_b16 v[72:73], v49 offset:272\n\t" \
    "ds_read_b64_tr_b16 v[74:75], v49 offset:4400\n\t" \
    "s_memrealtime s[56:57]\n\t" \
    "s_waitcnt lgkmcnt(6)\n\t" \
    "v_mfma_f32_16x16x32_bf16 v[132:135], v[12:15], v[204:207], 0\n\t" \
    "s_waitcnt lgkmcnt(4)\n\t" \
    "v_mfma_f32_16x16x32_bf16 v[152:155], v[64:67], v[204:207], 0\n\t" \
    "s_waitcnt lgkmcnt(2)\n\t" \
    "v_mfma_f32_16x16x32_bf16 v[168:171], v[160:163], v[204:207], 0\n\t" \
    "s_waitcnt lgkmcnt(0)\n\t" \
    "v_mfma_f32_16x16x32_bf16 v[236:239], v[72:75], v[204:207], 0\n\t" \
    "s_nop 1\n\t" \
    "v_bfi_b32 v132, s41, v50, v132\n\t" \
    "s_nop 0\n\t" \
    "v_bfi_b32 v152, s41, v50, v152\n\t" \
    "v_cvt_pk_bf16_f32 v245, v134, v135\n\t" \
    "v_cvt_pk_bf16_f32 v247, v154, v155\n\t" \
    "v_cvt_pk_bf16_f32 v244, v132, v133\n\t" \
    "v_cvt_pk_bf16_f32 v246, v152, v153\n\t" \
    "v_bfi_b32 v168, s41, v50, v168\n\t" \
    "v_bfi_b32 v236, s41, v50, v236\n\t" \
    "buffer_store_dwordx4 v[244:247], v42, s[4:7], s50 offen offset:0 sc1\n\t" \
    "v_cvt_pk_bf16_f32 v193, v170, v171\n\t" \
    "v_cvt_pk_bf16_f32 v195, v238, v239\n\t" \
    "v_cvt_pk_bf16_f32 v192, v168, v169\n\t" \
    "v_cvt_pk_bf16_f32 v194, v236, v237\n\t" \
    "buffer_store_dwordx4 v[192:195], v18, s[4:7], s50 offen offset:128 sc1\n\t" \
    "s_cmp_ge_u32 s25, 126\n\t" \
    "s_cbranch_scc1 .Lend_%=\n\t" \
    "s_waitcnt lgkmcnt(0)\n\t" \
    "s_sub_u32 s37, s16, 300\n\t" \
    "s_mov_b32 s62, 300\n\t" \
    "s_sub_u32 s39, s58, s37\n\t" \
    "s_cmp_gt_u32 s39, 600\n\t" \
    "s_cselect_b32 s39, 400, s39\n\t" \
    "s_max_u32 s62, s62, s39\n\t" \
    "s_sub_u32 s39, s59, s37\n\t" \
    "s_cmp_gt_u32 s39, 600\n\t" \
    "s_cselect_b32 s39, 400, s39\n\t" \
    "s_max_u32 s62, s62, s39\n\t" \
    "s_sub_u32 s39, s60, s37\n\t" \
    "s_cmp_gt_u32 s39, 600\n\t" \
    "s_cselect_b32 s39, 400, s39\n\t" \
    "s_max_u32 s62, s62, s39\n\t" \
    "s_sub_u32 s39, s61, s37\n\t" \
    "s_cmp_gt_u32 s39, 600\n\t" \
    "s_cselect_b32 s39, 400, s39\n\t" \
    "s_max_u32 s62, s62, s39\n\t" \
    "s_sub_u32 s62, s62, 300\n\t" \
    "s_mul_i32 s62, s62, 5\n\t" \
    "s_lshr_b32 s62, s62, 2\n\t" \
    "s_waitcnt vmcnt(2)\n\t" \
    "v_and_b32 v41, v136, v138\n\t" \
    "v_bitop3_b32 v41, v41, v112, v114 bitop3:0x80\n\t" \
    "v_and_b32 v41, 1, v41\n\t" \
    "v_cmp_eq_u32 vcc, 0, v41\n\t" \
    "s_nop 1\n\t" \
    "s_cbranch_vccnz .Ls50_%=\n\t" \
    ".Lb50_%=:\n\t" \
    "ds_write_b128 v48, v[136:139] offset:33024\n\t" \
    "ds_write_b128 v48, v[112:115] offset:33152\n\t" \
    "s_add_u32 s37, s62, 125\n\t" \
    ".Lh57_%=:\n\t" \
    "s_memrealtime s[22:23]\n\t" \
    "s_waitcnt lgkmcnt(0)\n\t" \
    "s_sub_u32 s22, s22, s18\n\t" \
    "s_cmp_lt_u32 s22, s37\n\t" \
    "s_cbranch_scc1 .Lh57_%=\n\t" \
    "s_add_u32 s54, s53, 524288\n\t" \
    "buffer_load_dwordx4 v[4:7], v0, s[4:7], s54 offen offset:0 sc1\n\t" \
    "s_add_u32 s54, s53, 4767744\n\t" \
    "buffer_load_dwordx4 v[44:47], v0, s[4:7], s54 offen offset:0 sc1\n\t" \
    "s_waitcnt lgkmcnt(0)\n\t" \
    "s_barrier\n\t" \
    "ds_read_b64_tr_b16 v[52:53], v43 offset:33024\n\t" \
    "ds_read_b64_tr_b16 v[54:55], v43 offset:33280\n\t" \
    "ds_read_b64_tr_b16 v[248:249], v43 offset:33032\n\t" \
    "ds_read_b64_tr_b16 v[250:251], v43 offset:33288\n\t" \
    "s_waitcnt lgkmcnt(2)\n\t" \
    "v_mfma_f32_16x16x32_bf16 v[24:27], v[52:55], v[164:167], 0\n\t" \
    "s_waitcnt lgkmcnt(0)\n\t" \
    "v_mfma_f32_16x16x32_bf16 v[8:11], v[248:251], v[164:167], 0\n\t" \
    "v_mfma_f32_16x16x32_bf16 v[220:223], v[52:55], v[252:255], 0\n\t" \
    "v_mfma_f32_16x16x32_bf16 v[120:123], v[248:251], v[252:255], 0\n\t" \
    "s_nop 3\n\t" \
    "v_cvt_pk_bf16_f32 v80, v24, v25\n\t" \
    "v_cvt_pk_bf16_f32 v81, v26, v27\n\t" \
    "v_cvt_pk_bf16_f32 v82, v8, v9\n\t" \
    "v_cvt_pk_bf16_f32 v83, v10, v11\n\t" \
    "buffer_store_dwordx4 v[80:83], v38, s[8:11], 0 offen offset:0 sc1\n\t" \
    "v_cvt_pk_bf16_f32 v208, v220, v221\n\t" \
    "v_cvt_pk_bf16_f32 v209, v222, v223\n\t" \
    "v_cvt_pk_bf16_f32 v210, v120, v121\n\t" \
    "v_cvt_pk_bf16_f32 v211, v122, v123\n\t" \
    "buffer_store_dwordx4 v[208:211], v38, s[8:11], 0 offen offset:3072 sc1\n\t" \
    "s_waitcnt vmcnt(2)\n\t" \
    "v_and_b32 v39, v4, v6\n\t" \
    "v_bitop3_b32 v39, v39, v44, v46 bitop3:0x80\n\t" \
    "v_and_b32 v39, 1, v39\n\t" \
    "v_cmp_eq_u32 vcc, 0, v39\n\t" \
    "s_nop 1\n\t" \
    "s_cbranch_vccnz .Ls51_%=\n\t" \
    ".Lb51_%=:\n\t" \
    "ds_write_b128 v48, v[4:7] offset:0\n\t" \
    "ds_write_b128 v48, v[44:47] offset:128\n\t" \
    "s_add_u32 s37, s62, 100\n\t" \
    ".Lh58_%=:\n\t" \
    "s_memrealtime s[22:23]\n\t" \
    "s_waitcnt lgkmcnt(0)\n\t" \
    "s_sub_u32 s22, s22, s56\n\t" \
    "s_cmp_lt_u32 s22, s37\n\t" \
    "s_cbranch_scc1 .Lh58_%=\n\t" \
    "s_add_u32 s54, s53, 1048576\n\t" \
    "buffer_load_dwordx4 v[76:79], v0, s[4:7], s54 offen offset:0 sc1\n\t" \
    "s_add_u32 s54, s53, 5292032\n\t" \
    "buffer_load_dwordx4 v[104:107], v0, s[4:7], s54 offen offset:0 sc1\n\t" \
    "s_waitcnt lgkmcnt(0)\n\t" \
    "s_barrier\n\t" \
    "ds_read_b64_tr_b16 v[140:141], v43 offset:0\n\t" \
    "ds_read_b64_tr_b16 v[142:143], v43 offset:256\n\t" \
    "ds_read_b64_tr_b16 v[100:101], v43 offset:8\n\t" \
    "ds_read_b64_tr_b16 v[102:103], v43 offset:264\n\t" \
    "s_waitcnt lgkmcnt(2)\n\t" \
    "v_mfma_f32_16x16x32_bf16 v[20:23], v[140:143], v[164:167], 0\n\t" \
    "s_waitcnt lgkmcnt(0)\n\t" \
    "v_mfma_f32_16x16x32_bf16 v[56:59], v[100:103], v[164:167], 0\n\t" \
    "v_mfma_f32_16x16x32_bf16 v[68:71], v[140:143], v[252:255], 0\n\t" \
    "v_mfma_f32_16x16x32_bf16 v[32:35], v[100:103], v[252:255], 0\n\t" \
    "s_nop 3\n\t" \
    "v_cvt_pk_bf16_f32 v200, v20, v21\n\t" \
    "v_cvt_pk_bf16_f32 v201, v22, v23\n\t" \
    "v_cvt_pk_bf16_f32 v202, v56, v57\n\t" \
    "v_cvt_pk_bf16_f32 v203, v58, v59\n\t" \
    "buffer_store_dwordx4 v[200:203], v38, s[8:11], 0 offen offset:64 sc1\n\t" \
    "v_cvt_pk_bf16_f32 v84, v68, v69\n\t" \
    "v_cvt_pk_bf16_f32 v85, v70, v71\n\t" \
    "v_cvt_pk_bf16_f32 v86, v32, v33\n\t" \
    "v_cvt_pk_bf16_f32 v87, v34, v35\n\t" \
    "buffer_store_dwordx4 v[84:87], v38, s[8:11], 0 offen offset:3136 sc1\n\t" \
    "s_waitcnt vmcnt(2)\n\t" \
    "v_and_b32 v37, v76, v78\n\t" \
    "v_bitop3_b32 v37, v37, v104, v106 bitop3:0x80\n\t" \
    "v_and_b32 v37, 1, v37\n\t" \
    "v_cmp_eq_u32 vcc, 0, v37\n\t" \
    "s_nop 1\n\t" \
    "s_cbranch_vccnz .Ls52_%=\n\t" \
    ".Lb52_%=:\n\t" \
    "ds_write_b128 v48, v[76:79] offset:16512\n\t" \
    "ds_write_b128 v48, v[104:107] offset:16640\n\t" \
    "s_waitcnt lgkmcnt(0)\n\t" \
    "s_barrier\n\t" \
    "ds_read_b64_tr_b16 v[88:89], v43 offset:16512\n\t" \
    "ds_read_b64_tr_b16 v[90:91], v43 offset:16768\n\t" \
    "ds_read_b64_tr_b16 v[124:125], v43 offset:16520\n\t" \
    "ds_read_b64_tr_b16 v[126:127], v43 offset:16776\n\t" \
    "s_waitcnt lgkmcnt(2)\n\t" \
    "v_mfma_f32_16x16x32_bf16 v[184:187], v[88:91], v[164:167], 0\n\t" \
    "s_waitcnt lgkmcnt(0)\n\t" \
    "v_mfma_f32_16x16x32_bf16 v[228:231], v[124:127], v[164:167], 0\n\t" \
    "v_mfma_f32_16x16x32_bf16 v[128:131], v[88:91], v[252:255], 0\n\t" \
    "v_mfma_f32_16x16x32_bf16 v[60:63], v[124:127], v[252:255], 0\n\t" \
    "s_nop 3\n\t" \
    "v_cvt_pk_bf16_f32 v240, v184, v185\n\t" \
    "v_cvt_pk_bf16_f32 v241, v186, v187\n\t" \
    "v_cvt_pk_bf16_f32 v242, v228, v229\n\t" \
    "v_cvt_pk_bf16_f32 v243, v230, v231\n\t" \
    "buffer_store_dwordx4 v[240:243], v38, s[8:11], 0 offen offset:128 sc1\n\t" \
    "v_cvt_pk_bf16_f32 v96, v128, v129\n\t" \
    "v_cvt_pk_bf16_f32 v97, v130, v131\n\t" \
    "v_cvt_pk_bf16_f32 v98, v60, v61\n\t" \
    "v_cvt_pk_bf16_f32 v99, v62, v63\n\t" \
    "buffer_store_dwordx4 v[96:99], v38, s[8:11], 0 offen offset:3200 sc1\n\t" \
    "s_or_b32 s37, s2, s24\n\t" \
    "s_cbranch_scc1 .Lend_%=\n\t" \
    "s_mov_b32 s37, 34668544\n\t" \
    "v_mov_b32 v1, s34\n\t" \
    "s_mov_b64 exec, 1\n\t" \
    "buffer_store_dword v1, v0, s[4:7], s37 offen sc1\n\t" \
    "s_mov_b64 exec, -1\n\t" \
    ".Lend_%=:\n\t" \
    "s_endpgm\n\t" \
    ".Lq1028_%=:\n\t" \
    "s_mov_b32 s40, 0\n\t" \
    ".Lr1028_%=:\n\t" \
    "s_add_u32 s12, s55, 34603008\n\t" \
    "s_buffer_load_dword s13, s[4:7], s12 glc\n\t" \
    "s_buffer_load_dword s14, s[4:7], s12 offset:0x400 glc\n\t" \
    "s_waitcnt lgkmcnt(0)\n\t" \
    "s_xor_b32 s13, s13, s34\n\t" \
    "s_xor_b32 s14, s14, s34\n\t" \
    "s_or_b32 s13, s13, s14\n\t" \
    "s_cbranch_scc0 .Lb27_%=\n\t" \
    "s_add_u32 s40, s40, 1\n\t" \
    "s_cmp_lt_u32 s40, 1\n\t" \
    "s_cbranch_scc1 .Lr1028_%=\n\t" \
    "s_branch .Ls27_%=\n\t" \
    ".Lq1025_%=:\n\t" \
    "s_mov_b32 s40, 0\n\t" \
    ".Lr1025_%=:\n\t" \
    "s_nop 0\n\t" \
    "v_readlane_b32 s12, v11, 0\n\t" \
    "v_readlane_b32 s13, v11, 1\n\t" \
    "v_readlane_b32 s14, v11, 2\n\t" \
    "v_readlane_b32 s15, v11, 3\n\t" \
    "v_readlane_b32 s16, v11, 4\n\t" \
    "v_readlane_b32 s17, v11, 5\n\t" \
    "v_readlane_b32 s18, v11, 6\n\t" \
    "v_readlane_b32 s19, v11, 7\n\t" \
    "v_readlane_b32 s3, v11, 8\n\t" \
    "s_add_u32 s12, s12, s33\n\t" \
    "s_add_u32 s13, s13, s33\n\t" \
    "s_add_u32 s14, s14, s33\n\t" \
    "s_add_u32 s15, s15, s33\n\t" \
    "s_add_u32 s16, s16, s33\n\t" \
    "s_add_u32 s17, s17, s33\n\t" \
    "s_add_u32 s18, s18, s33\n\t" \
    "s_add_u32 s19, s19, s33\n\t" \
    "s_add_u32 s3, s3, s33\n\t" \
    "s_buffer_load_dword s12, s[4:7], s12 offset:0xc0000 glc\n\t" \
    "s_buffer_load_dword s13, s[4:7], s13 offset:0xc0000 glc\n\t" \
    "s_buffer_load_dword s14, s[4:7], s14 offset:0xc0000 glc\n\t" \
    "s_buffer_load_dword s15, s[4:7], s15 offset:0xc0000 glc\n\t" \
    "s_buffer_load_dword s16, s[4:7], s16 offset:0xc0000 glc\n\t" \
    "s_buffer_load_dword s17, s[4:7], s17 offset:0xc0000 glc\n\t" \
    "s_buffer_load_dword s18, s[4:7], s18 offset:0xc0000 glc\n\t" \
    "s_buffer_load_dword s19, s[4:7], s19 offset:0xc0000 glc\n\t" \
    "s_buffer_load_dword s3, s[4:7], s3 offset:0xc0000 glc\n\t" \
    "s_waitcnt lgkmcnt(0)\n\t" \
    "s_xor_b32 s12, s12, s34\n\t" \
    "s_xor_b32 s13, s13, s34\n\t" \
    "s_xor_b32 s14, s14, s34\n\t" \
    "s_xor_b32 s15, s15, s34\n\t" \
    "s_xor_b32 s16, s16, s34\n\t" \
    "s_xor_b32 s17, s17, s34\n\t" \
    "s_xor_b32 s18, s18, s34\n\t" \
    "s_xor_b32 s19, s19, s34\n\t" \
    "s_xor_b32 s3, s3, s34\n\t" \
    "s_or_b32 s12, s12, s13\n\t" \
    "s_or_b32 s12, s12, s14\n\t" \
    "s_or_b32 s12, s12, s15\n\t" \
    "s_or_b32 s12, s12, s16\n\t" \
    "s_or_b32 s12, s12, s17\n\t" \
    "s_or_b32 s12, s12, s18\n\t" \
    "s_or_b32 s12, s12, s19\n\t" \
    "s_or_b32 s12, s12, s3\n\t" \
    "s_cbranch_scc0 .Lb24_%=\n\t" \
    "s_add_u32 s40, s40, 1\n\t" \
    "s_cmp_lt_u32 s40, 1\n\t" \
    "s_cbranch_scc1 .Lr1025_%=\n\t" \
    "s_branch .Ls24_%=\n\t" \
    ".Lqa_%=:\n\t" \
    "s_sub_u32 s37, s24, 4\n\t" \
    "s_bfe_u32 s38, s2, 0x20002\n\t" \
    "s_lshl1_add_u32 s38, s38, s37\n\t" \
    "s_lshl_b32 s42, s38, 21\n\t" \
    "s_cmp_ge_u32 s38, 7\n\t" \
    "s_cbranch_scc1 .Lqd_%=\n\t" \
    "s_cmp_ge_u32 s37, 2\n\t" \
    "s_cbranch_scc1 .Lqd_%=\n\t" \
    "s_memtime s[62:63]\n\t" \
    "s_waitcnt lgkmcnt(0)\n\t" \
    "s_sleep 4\n\t" \
    "s_lshl_b32 s3, s38, 10\n\t" \
    "s_and_b32 s37, s8, 0x1fffff\n\t" \
    "s_add_u32 s38, s37, 16383\n\t" \
    "s_andn2_b32 s38, s38, 16383\n\t" \
    "s_add_u32 s39, s37, 9617408\n\t" \
    "s_andn2_b32 s39, s39, 16383\n\t" \
    "s_max_u32 s42, s42, s38\n\t" \
    "s_min_u32 s42, s42, s39\n\t" \
    "s_sub_u32 s42, s42, s37\n\t" \
    "s_cmp_eq_u32 s2, 255\n\t" \
    "s_cselect_b64 s[16:17], s[4:5], s[8:9]\n\t" \
    "s_cselect_b32 s42, 34684928, s42\n\t" \
    "s_add_u32 s16, s16, s42\n\t" \
    "s_addc_u32 s17, s17, 0\n\t" \
    "s_mov_b32 s18, 16384\n\t" \
    "s_mov_b32 s19, 0x20000\n\t" \
    "s_add_u32 s37, s3, 0x2000\n\t" \
    "buffer_store_dwordx4 v[20:23], v0, s[16:19], s3 offen sc1\n\t" \
    "buffer_store_dwordx4 v[20:23], v0, s[16:19], s37 offen sc1\n\t" \
    "s_nop 0\n\t" \
    "s_mov_b32 s39, 34668544\n\t" \
    "v_mov_b32 v20, 0\n\t" \
    "buffer_load_dword v21, v20, s[4:7], s39 offen sc1\n\t" \
    "s_mov_b32 s60, -1\n\t" \
    "s_mov_b32 s61, -1\n\t" \
    "s_mov_b32 s40, 0\n\t" \
    "s_waitcnt vmcnt(1) lgkmcnt(0)\n\t" \
    "s_memtime s[58:59]\n\t" \
    "s_waitcnt lgkmcnt(0)\n\t" \
    ".Lqc_%=:\n\t" \
    "s_buffer_load_dword s38, s[16:19], s3\n\t" \
    "s_mov_b32 s56, s58\n\t" \
    "s_xor_b32 s3, s3, 0x2000\n\t" \
    "s_waitcnt lgkmcnt(0)\n\t" \
    "s_memtime s[58:59]\n\t" \
    "s_add_u32 s40, s40, 1\n\t" \
    "s_waitcnt lgkmcnt(0)\n\t" \
    "s_sub_u32 s56, s58, s56\n\t" \
    "s_min_u32 s56, s56, s60\n\t" \
    "s_mov_b32 s60, s61\n\t" \
    "s_mov_b32 s61, s56\n\t" \
    "s_bitcmp1_b32 s40, 0\n\t" \
    "s_cbranch_scc1 .Lqc_%=\n\t" \
    "s_add_u32 s3, s3, 0x80\n\t" \
    "s_sub_u32 s37, s58, s62\n\t" \
    "s_cmp_gt_u32 s37, 5200\n\t" \
    "s_cselect_b32 s40, 98, s40\n\t" \
    "s_absdiff_i32 s37, s60, s61\n\t" \
    "s_min_u32 s38, s60, s61\n\t" \
    "s_cmp_gt_u32 s38, 500\n\t" \
    "s_cselect_b32 s37, 0, s37\n\t" \
    "s_cmp_lt_u32 s37, 80\n\t" \
    "s_cselect_b32 s37, 16, 4\n\t" \
    "s_cmp_lt_u32 s40, s37\n\t" \
    "s_cbranch_scc1 .Lqc_%=\n\t" \
    "s_waitcnt vmcnt(0)\n\t" \
    "v_readfirstlane_b32 s34, v21\n\t" \
    "s_cmp_gt_u32 s60, s61\n\t" \
    "s_cselect_b32 s0, 2, 0\n\t" \
    "s_mul_i32 s34, s34, 1664525\n\t" \
    "s_add_u32 s34, s34, 1013904223\n\t" \
    "s_or_b32 s34, s34, 1\n\t" \
    "s_cmp_eq_u32 s2, 255\n\t" \
    "s_cbranch_scc1 .Lqf_%=\n\t" \
    "s_xor_b32 s37, s0, s34\n\t" \
    "v_mov_b32 v21, s37\n\t" \
    "s_bfe_u32 s37, s2, 0x20002\n\t" \
    "s_lshl1_add_u32 s37, s37, s24\n\t" \
    "s_lshl_b32 s37, s37, 7\n\t" \
    "s_add_u32 s37, s37, 34670080\n\t" \
    "v_lshrrev_b32 v20, 2, v0\n\t" \
    "s_mov_b32 exec_hi, 0\n\t" \
    "buffer_store_dword v21, v20, s[4:7], s37 offen sc1\n\t" \
    "s_mov_b64 exec, -1\n\t" \
    ".Lqg_%=:\n\t" \
    "s_branch .Lqd_%=\n\t" \
    ".Lqf_%=:\n\t" \
    "s_getreg_b32 s37, hwreg(HW_REG_XCC_ID, 0, 2)\n\t" \
    "s_lshl1_add_u32 s37, s0, s37\n\t" \
    "s_add_u32 s37, s37, 1\n\t" \
    "s_and_b32 s37, s37, 7\n\t" \
    "s_lshl_b32 s37, s37, 1\n\t" \
    "s_xor_b32 s37, s37, s34\n\t" \
    "v_mov_b32 v20, s37\n\t" \
    "v_mov_b32 v21, s37\n\t" \
    "v_mov_b32 v22, s37\n\t" \
    "v_mov_b32 v23, s37\n\t" \
    "s_mov_b32 s39, 34669568\n\t" \
    "buffer_store_dwordx4 v[20:23], v0, s[4:7], s39 offen sc1\n\t" \
    "s_nop 0\n\t" \
    "s_branch .Lqg_%=\n\t" \
    ".Lz5_%=:\n\t" \
    "s_cbranch_execz .Lwk_%=\n\t" \
    ".Lr2_%=:\n\t" \
    "s_add_u32 s40, s40, 1\n\t" \
    "s_cmp_gt_u32 s40, 32\n\t" \
    "s_cbranch_scc1 .Lr3_%=\n\t" \
    "s_sleep 2\n\t" \
    "s_and_b32 s37, s2, 7\n\t" \
    "s_lshl_b32 s37, s37, 10\n\t" \
    "s_add_u32 s37, s37, 34676736\n\t" \
    "v_and_b32 v51, 0x70, v0\n\t" \
    "v_lshlrev_b32 v51, 3, v51\n\t" \
    "buffer_load_dword v50, v51, s[4:7], s37 offen sc1\n\t" \
    "s_waitcnt vmcnt(0)\n\t" \
    "v_readlane_b32 s56, v50, 0\n\t" \
    "v_readlane_b32 s57, v50, 1\n\t" \
    "v_readlane_b32 s58, v50, 2\n\t" \
    "v_readlane_b32 s59, v50, 3\n\t" \
    "v_readlane_b32 s60, v50, 4\n\t" \
    "v_readlane_b32 s61, v50, 5\n\t" \
    "v_readlane_b32 s62, v50, 6\n\t" \
    "v_readlane_b32 s63, v50, 7\n\t" \
    "s_branch .Lr1_%=\n\t" \
    ".Lq5_%=:\n\t" \
    "s_add_u32 s40, s40, 1\n\t" \
    "s_cmp_gt_u32 s40, 65536\n\t" \
    "s_cbranch_scc1 .Lq6_%=\n\t" \
    "s_sleep 4\n\t" \
    "v_and_b32 v16, 0x70, v0\n\t" \
    "v_lshlrev_b32 v16, 3, v16\n\t" \
    "s_mov_b32 s37, 34670592\n\t" \
    "buffer_load_dword v8, v16, s[4:7], s37 offen sc1\n\t" \
    "s_waitcnt vmcnt(0)\n\t" \
    "v_readlane_b32 s56, v8, 0\n\t" \
    "v_readlane_b32 s57, v8, 1\n\t" \
    "v_readlane_b32 s58, v8, 2\n\t" \
    "v_readlane_b32 s59, v8, 3\n\t" \
    "v_readlane_b32 s60, v8, 4\n\t" \
    "v_readlane_b32 s61, v8, 5\n\t" \
    "s_branch .Lq4_%=\n\t" \
    ".Lr5_%=:\n\t" \
    "s_add_u32 s40, s40, 1\n\t" \
    "s_cmp_gt_u32 s40, 65536\n\t" \
    "s_cbranch_scc1 .Lr6_%=\n\t" \
    "s_sleep 4\n\t" \
    "v_mov_b32 v16, 0\n\t" \
    "buffer_load_dword v8, v16, s[4:7], s52 offen sc1\n\t" \
    "s_waitcnt vmcnt(0)\n\t" \
    "v_readfirstlane_b32 s36, v8\n\t" \
    "s_branch .Lr4_%=\n\t" \
    ".Ls24_%=:\n\t" \
    "s_mov_b32 s40, 0\n\t" \
    "s_add_u32 s38, s33, 786432\n\t" \
    ".Ll24_%=:\n\t" \
    "s_sleep 2\n\t" \
    "buffer_load_dword v1, v11, s[4:7], s38 offen sc1\n\t" \
    "s_waitcnt vmcnt(0)\n\t" \
    "v_cmp_ne_u32 vcc, s34, v1\n\t" \
    "s_add_u32 s40, s40, 1\n\t" \
    "s_cmp_ge_u32 s40, 65536\n\t" \
    "s_cbranch_scc1 .Lb24_%=\n\t" \
    "s_cbranch_vccnz .Ll24_%=\n\t" \
    "s_branch .Lb24_%=\n\t" \
    ".Ls27_%=:\n\t" \
    "s_mov_b32 s40, 0\n\t" \
    "s_add_u32 s38, s55, 34603008\n\t" \
    ".Ll27_%=:\n\t" \
    "s_sleep 2\n\t" \
    "buffer_load_dword v36, v47, s[4:7], s38 offen sc1\n\t" \
    "s_waitcnt vmcnt(0)\n\t" \
    "v_cmp_ne_u32 vcc, s34, v36\n\t" \
    "s_add_u32 s40, s40, 1\n\t" \
    "s_cmp_ge_u32 s40, 65536\n\t" \
    "s_cbranch_scc1 .Lb27_%=\n\t" \
    "s_cbranch_vccnz .Ll27_%=\n\t" \
    "s_branch .Lb27_%=\n\t" \
    ".Ls50_%=:\n\t" \
    "s_mov_b32 s40, 0\n\t" \
    ".Ll50_%=:\n\t" \
    "s_add_u32 s54, s53, 0\n\t" \
    "buffer_load_dwordx4 v[136:139], v0, s[4:7], s54 offen offset:0 sc1\n\t" \
    "s_add_u32 s54, s53, 4243456\n\t" \
    "buffer_load_dwordx4 v[112:115], v0, s[4:7], s54 offen offset:0 sc1\n\t" \
    "s_waitcnt vmcnt(0)\n\t" \
    "v_and_b32 v41, v136, v138\n\t" \
    "v_bitop3_b32 v41, v41, v112, v114 bitop3:0x80\n\t" \
    "v_and_b32 v41, 1, v41\n\t" \
    "v_cmp_eq_u32 vcc, 0, v41\n\t" \
    "s_add_u32 s40, s40, 1\n\t" \
    "s_cmp_ge_u32 s40, 65536\n\t" \
    "s_cbranch_scc1 .Lb50_%=\n\t" \
    "s_cbranch_vccnz .Ll50_%=\n\t" \
    "s_branch .Lb50_%=\n\t" \
    ".Ls51_%=:\n\t" \
    "s_mov_b32 s40, 0\n\t" \
    ".Ll51_%=:\n\t" \
    "s_add_u32 s54, s53, 524288\n\t" \
    "buffer_load_dwordx4 v[4:7], v0, s[4:7], s54 offen offset:0 sc1\n\t" \
    "s_add_u32 s54, s53, 4767744\n\t" \
    "buffer_load_dwordx4 v[44:47], v0, s[4:7], s54 offen offset:0 sc1\n\t" \
    "s_waitcnt vmcnt(0)\n\t" \
    "v_and_b32 v39, v4, v6\n\t" \
    "v_bitop3_b32 v39, v39, v44, v46 bitop3:0x80\n\t" \
    "v_and_b32 v39, 1, v39\n\t" \
    "v_cmp_eq_u32 vcc, 0, v39\n\t" \
    "s_add_u32 s40, s40, 1\n\t" \
    "s_cmp_ge_u32 s40, 65536\n\t" \
    "s_cbranch_scc1 .Lb51_%=\n\t" \
    "s_cbranch_vccnz .Ll51_%=\n\t" \
    "s_branch .Lb51_%=\n\t" \
    ".Ls52_%=:\n\t" \
    "s_mov_b32 s40, 0\n\t" \
    ".Ll52_%=:\n\t" \
    "s_add_u32 s54, s53, 1048576\n\t" \
    "buffer_load_dwordx4 v[76:79], v0, s[4:7], s54 offen offset:0 sc1\n\t" \
    "s_add_u32 s54, s53, 5292032\n\t" \
    "buffer_load_dwordx4 v[104:107], v0, s[4:7], s54 offen offset:0 sc1\n\t" \
    "s_waitcnt vmcnt(0)\n\t" \
    "v_and_b32 v37, v76, v78\n\t" \
    "v_bitop3_b32 v37, v37, v104, v106 bitop3:0x80\n\t" \
    "v_and_b32 v37, 1, v37\n\t" \
    "v_cmp_eq_u32 vcc, 0, v37\n\t" \
    "s_add_u32 s40, s40, 1\n\t" \
    "s_cmp_ge_u32 s40, 65536\n\t" \
    "s_cbranch_scc1 .Lb52_%=\n\t" \
    "s_cbranch_vccnz .Ll52_%=\n\t" \
    "s_branch .Lb52_%=\n\t" \
    ".Lfin_%=:\n\t"
#define K_CLOBBERS "v1", "v2", "v3", "v4", "v5", "v6", "v7", "v8", "v9", "v10", "v11", "v12", "v13", "v14", "v15", "v16", "v17", "v18", "v19", "v20", "v21", "v22", "v23", "v24", "v25", "v26", "v27", "v28", "v29", "v30", "v31", "v32", "v33", "v34", "v35", "v36", "v37", "v38", "v39", "v40", "v41", "v42", "v43", "v44", "v45", "v46", "v47", "v48", "v49", "v50", "v51", "v52", "v53", "v54", "v55", "v56", "v57", "v58", "v59", "v60", "v61", "v62", "v63", "v64", "v65", "v66", "v67", "v68", "v69", "v70", "v71", "v72", "v73", "v74", "v75", "v76", "v77", "v78", "v79", "v80", "v81", "v82", "v83", "v84", "v85", "v86", "v87", "v88", "v89", "v90", "v91", "v92", "v93", "v94", "v95", "v96", "v97", "v98", "v99", "v100", "v101", "v102", "v103", "v104", "v105", "v106", "v107", "v108", "v109", "v110", "v111", "v112", "v113", "v114", "v115", "v116", "v117", "v118", "v119", "v120", "v121", "v122", "v123", "v124", "v125", "v126", "v127", "v128", "v129", "v130", "v131", "v132", "v133", "v134", "v135", "v136", "v137", "v138", "v139", "v140", "v141", "v142", "v143", "v144", "v145", "v146", "v147", "v148", "v149", "v150", "v151", "v152", "v153", "v154", "v155", "v156", "v157", "v158", "v159", "v160", "v161", "v162", "v163", "v164", "v165", "v166", "v167", "v168", "v169", "v170", "v171", "v172", "v173", "v174", "v175", "v176", "v177", "v178", "v179", "v180", "v181", "v182", "v183", "v184", "v185", "v186", "v187", "v188", "v189", "v190", "v191", "v192", "v193", "v194", "v195", "v196", "v197", "v198", "v199", "v200", "v201", "v202", "v203", "v204", "v205", "v206", "v207", "v208", "v209", "v210", "v211", "v212", "v213", "v214", "v215", "v216", "v217", "v218", "v219", "v220", "v221", "v222", "v223", "v224", "v225", "v226", "v227", "v228", "v229", "v230", "v231", "v232", "v233", "v234", "v235", "v236", "v237", "v238", "v239", "v240", "v241", "v242", "v243", "v244", "v245", "v246", "v247", "v248", "v249", "v250", "v251", "v252", "v253", "v254", "v255", "s3", "s4", "s5", "s6", "s7", "s8", "s9", "s10", "s11", "s12", "s13", "s14", "s15", "s16", "s17", "s18", "s19", "s20", "s21", "s22", "s23", "s24", "s25", "s26", "s27", "s28", "s29", "s30", "s31", "s32", "s33", "s34", "s35", "s36", "s37", "s38", "s39", "s40", "s41", "s42", "s43", "s44", "s45", "s46", "s47", "s48", "s49", "s50", "s51", "s52", "s53", "s54", "s55", "s56", "s57", "s58", "s59", "s60", "s61", "s62", "s63", "s64", "s65"

extern "C" __global__ __launch_bounds__(512, 2)
void k_conv(const char* __restrict__ x, const char* __restrict__ wgt, char* __restrict__ ws, char* __restrict__ out)
{
  __shared__ __attribute__((aligned(256))) char smem[157568];
  unsigned tid = threadIdx.x;
  auto kp = __builtin_amdgcn_kernarg_segment_ptr();
  asm volatile(K_BODY : "+{v0}"(tid), "+{s[0:1]}"(kp)
               : "{s2}"(blockIdx.x), "i"((__attribute__((address_space(3))) char*)smem)
               : "memory", "scc", "vcc", K_CLOBBERS);
}

extern "C" void kernel_launch(void* const* d_in, const int* in_sizes, int n_in,
                              void* d_out, int out_size, void* d_ws, size_t ws_size,
                              hipStream_t stream) {
  k_conv<<<dim3(256), dim3(512), 0, stream>>>((const char*)d_in[0], (const char*)d_in[1], (char*)d_ws, (char*)d_out);
}
```

```cpp
#include <hip/hip_runtime.h>
#include <hip/hip_bf16.h>

#define K_BODY \
    "s_load_dwordx2 s[16:17], s[0:1], 0x0\n\t" \
    "s_load_dwordx2 s[4:5], s[0:1], 0x10\n\t" \
    "s_load_dwordx2 s[12:13], s[0:1], 0x8\n\t" \
    "s_load_dwordx2 s[8:9], s[0:1], 0x18\n\t" \
    "s_getpc_b64 s[22:23]\n\t" \
    ".Lpc_%=:\n\t" \
    "s_cmp_ge_u32 s2, 16\n\t" \
    "s_cbranch_scc1 .Le1_%=\n\t" \
    "s_mov_b32 m0, 157312\n\t" \
    "v_lshlrev_b32 v8, 6, v0\n\t" \
    "v_min_u32 v8, .Lfin_%=-.Lpc_%=, v8\n\t" \
    "global_load_lds_dword v8, s[22:23]\n\t" \
    ".Le1_%=:\n\t" \
    "v_readfirstlane_b32 s24, v0\n\t" \
    "s_and_b32 s25, s2, 3\n\t" \
    "s_lshl_b32 s25, s25, 5\n\t" \
    "s_lshr_b32 s37, s2, 3\n\t" \
    "s_add_u32 s25, s25, s37\n\t" \
    "s_min_u32 s28, s25, 125\n\t" \
    "s_mul_i32 s26, s28, 57\n\t" \
    "s_lshr_b32 s26, s26, 9\n\t" \
    "s_mul_i32 s37, s26, 9\n\t" \
    "s_sub_u32 s27, s28, s37\n\t" \
    "s_bfe_u32 s37, s2, 0x10002\n\t" \
    "s_lshl1_add_u32 s26, s26, s37\n\t" \
    "v_and_b32 v0, 63, v0\n\t" \
    "v_lshlrev_b32 v0, 4, v0\n\t" \
    "s_lshr_b32 s24, s24, 6\n\t" \
    "s_sub_u32 s37, s24, 4\n\t" \
    "s_cbranch_scc1 .Le2_%=\n\t" \
    "s_cmp_ge_u32 s25, 126\n\t" \
    "s_cbranch_scc1 .Le2_%=\n\t" \
    "s_mul_i32 m0, s37, 12416\n\t" \
    "s_lshl2_add_u32 s37, s26, s37\n\t" \
    "s_mul_i32 s37, s37, 86016\n\t" \
    "s_mul_i32 s38, s27, 4992\n\t" \
    "s_add_u32 s37, s37, s38\n\t" \
    "s_addk_i32 s37, 896\n\t" \
    "v_add_u32 v9, s37, v0\n\t" \
    "s_cmp_eq_u32 s27, 0\n\t" \
    "s_cselect_b32 s38, 43008, 0\n\t" \
    "s_sub_u32 s39, s38, 2048\n\t" \
    "v_add_u32 v7, s39, v9\n\t" \
    "v_subrev_u32 v8, 0x400, v9\n\t" \
    "s_mov_b64 exec, 0xff\n\t" \
    "v_add_u32 v8, s38, v8\n\t" \
    "s_mov_b64 exec, -1\n\t" \
    "s_mov_b32 s18, 9633792\n\t" \
    "s_mov_b32 s19, 0x20000\n\t" \
    "s_mov_b32 s39, 43008\n\t" \
    "s_waitcnt lgkmcnt(0)\n\t" \
    "buffer_load_dwordx4 v7, s[16:19], 0 offen offset:0 sc1 nt lds\n\t" \
    "s_add_u32 m0, m0, 2080\n\t" \
    "s_nop 0\n\t" \
    "buffer_load_dwordx4 v9, s[16:19], 0 offen offset:3072 sc1 nt lds\n\t" \
    "buffer_load_dwordx4 v9, s[16:19], 0 offen offset:2048 sc1 nt lds\n\t" \
    "s_add_u32 m0, m0, 4128\n\t" \
    "s_nop 0\n\t" \
    "buffer_load_dwordx4 v7, s[16:19], s39 offen offset:0 sc1 nt lds\n\t" \
    "s_add_u32 m0, m0, 2080\n\t" \
    "s_nop 0\n\t" \
    "buffer_load_dwordx4 v9, s[16:19], s39 offen offset:3072 sc1 nt lds\n\t" \
    "buffer_load_dwordx4 v9, s[16:19], s39 offen offset:2048 sc1 nt lds\n\t" \
    "buffer_load_dwordx4 v9, s[16:19], s39 offen offset:1024 sc1 nt lds\n\t" \
    "s_add_u32 m0, m0, 4294966240\n\t" \
    "s_nop 0\n\t" \
    "buffer_load_dwordx4 v8, s[16:19], s39 offen offset:0 sc1 nt lds\n\t" \
    "s_add_u32 m0, m0, 1024\n\t" \
    "s_nop 0\n\t" \
    "buffer_load_dwordx4 v9, s[16:19], s39 offen offset:0 sc1 nt lds\n\t" \
    "s_add_u32 m0, m0, 4294960064\n\t" \
    "s_nop 0\n\t" \
    "buffer_load_dwordx4 v8, s[16:19], 0 offen offset:0 sc1 nt lds\n\t" \
    "s_add_u32 m0, m0, 1024\n\t" \
    "s_nop 0\n\t" \
    "buffer_load_dwordx4 v9, s[16:19], 0 offen offset:0 sc1 nt lds\n\t" \
    "s_add_u32 m0, m0, 32\n\t" \
    "s_nop 0\n\t" \
    "buffer_load_dwordx4 v9, s[16:19], 0 offen offset:1024 sc1 nt lds\n\t" \
    ".Le2_%=:\n\t" \
    "s_mov_b32 s6, 34701312\n\t" \
    "s_mov_b32 s7, 0x20000\n\t" \
    "v_lshrrev_b32 v8, 7, v0\n\t" \
    "v_and_b32 v9, 0x70, v0\n\t" \
    "v_lshrrev_b32 v2, 8, v0\n\t" \
    "v_lshl_or_b32 v2, v2, 16, v9\n\t" \
    "v_and_b32 v10, 0x80, v0\n\t" \
    "v_lshl_or_b32 v2, v10, 6, v2\n\t" \
    "s_lshl_b32 s32, s2, 7\n\t" \
    "s_andn2_b32 s37, s32, 0x1fff\n\t" \
    "s_add_u32 s32, s32, s37\n\t" \
    "s_add_u32 s32, s32, 33554432\n\t" \
    "s_cmp_ge_u32 s24, 4\n\t" \
    "s_cbranch_scc1 .Le3_%=\n\t" \
    "v_mov_b32 v12, 0\n\t" \
    "v_mov_b32 v13, 0\n\t" \
    "v_mov_b32 v14, 0\n\t" \
    "v_mov_b32 v15, 0\n\t" \
    "s_lshl_b32 s37, s24, 18\n\t" \
    "s_add_u32 s37, s37, s32\n\t" \
    "s_xor_b32 s38, s24, 2\n\t" \
    "s_lshr_b32 s39, s2, 3\n\t" \
    "s_or_b32 s38, s38, s39\n\t" \
    "s_memrealtime s[22:23]\n\t" \
    "s_waitcnt lgkmcnt(0)\n\t" \
    "s_cbranch_scc1 .Le4_%=\n\t" \
    "v_lshl_or_b32 v8, v8, 10, v9\n\t" \
    "v_mov_b32 v16, s22\n\t" \
    "v_mov_b32 v17, s22\n\t" \
    "v_mov_b32 v18, s22\n\t" \
    "v_mov_b32 v19, s22\n\t" \
    "s_add_u32 s38, s32, 1122304\n\t" \
    "buffer_store_dwordx4 v[16:19], v8, s[4:7], s38 offen sc1\n\t" \
    ".Le4_%=:\n\t" \
    ".Le3_%=:\n\t" \
    "s_bitcmp1_b32 s2, 2\n\t" \
    "s_cselect_b32 s38, 126, 0\n\t" \
    "s_add_u32 s28, s28, s38\n\t" \
    "s_mul_i32 s30, s28, 293\n\t" \
    "s_lshr_b32 s30, s30, 11\n\t" \
    "s_mul_i32 s37, s30, 7\n\t" \
    "s_sub_u32 s28, s28, s37\n\t" \
    "s_mul_i32 s29, s30, 43\n\t" \
    "s_lshr_b32 s29, s29, 7\n\t" \
    "s_mul_i32 s37, s29, 3\n\t" \
    "s_sub_u32 s30, s30, s37\n\t" \
    "s_and_b32 s37, s29, 12\n\t" \
    "s_add_u32 s37, s37, s29\n\t" \
    "s_mul_i32 s31, s37, 768\n\t" \
    "s_lshl_b32 s37, s30, 6\n\t" \
    "s_add_u32 s31, s31, s37\n\t" \
    "s_add_u32 s41, s29, s30\n\t" \
    "s_add_u32 s41, s41, s28\n\t" \
    "s_and_b32 s41, s41, 3\n\t" \
    "s_cmp_lt_u32 s25, 126\n\t" \
    "s_cselect_b32 s14, 903168, 0\n\t" \
    "s_mov_b32 s15, 0x20000\n\t" \
    "v_lshrrev_b32 v10, 6, v0\n\t" \
    "v_lshrrev_b32 v11, 9, v0\n\t" \
    "v_sub_u32 v8, v10, v11\n\t" \
    "v_mul_u32_u24 v8, 18432, v8\n\t" \
    "v_and_b32 v9, 0x30, v0\n\t" \
    "v_lshlrev_b32 v11, 5, v11\n\t" \
    "v_bitop3_b32 v1, v8, v9, v11 bitop3:0xf6\n\t" \
    "s_mov_b32 exec_lo, 0xf0000000\n\t" \
    "s_mov_b32 exec_hi, 0xf0000000\n\t" \
    "v_bfrev_b32 v1, 1\n\t" \
    "s_mov_b64 exec, -1\n\t" \
    "s_cmp_ge_u32 s24, 4\n\t" \
    "s_cbranch_scc1 .Le9_%=\n\t" \
    "v_bfe_u32 v20, v0, 4, 4\n\t" \
    "v_and_b32 v10, 12, v10\n\t" \
    "v_cvt_f32_ubyte0 v21, v10\n\t" \
    "v_and_b32 v22, 1, v20\n\t" \
    "v_cvt_f32_ubyte0 v22, v22\n\t" \
    "v_mul_f32 v22, 0x3e800000, v22\n\t" \
    "v_lshrrev_b32 v23, 1, v20\n\t" \
    "v_mov_b32 v32, 0\n\t" \
    "v_mov_b32 v33, -1\n\t" \
    "v_mov_b32 v31, 0x3e800000\n\t" \
    "s_mov_b32 s65, 0\n\t" \
    "s_mov_b32 s40, 0\n\t" \
    "s_cmp_eq_u32 s24, 1\n\t" \
    "s_cbranch_scc1 .Lm1_%=\n\t" \
    "s_cmp_eq_u32 s24, 2\n\t" \
    "s_cbranch_scc1 .Lm2_%=\n\t" \
    "s_cmp_eq_u32 s24, 3\n\t" \
    "s_cbranch_scc1 .Lm3_%=\n\t" \
    "s_add_u32 s37, s32, 1048576\n\t" \
    "s_mov_b64 exec, 0xff\n\t" \
    "s_mov_b64 exec, -1\n\t" \
    "v_or_b32 v24, 1, v20\n\t" \
    "v_cvt_f32_ubyte0 v24, v24\n\t" \
    "v_mul_f32 v24, 0x3c800000, v24\n\t" \
    "v_lshrrev_b32 v25, 4, v0\n\t" \
    "v_and_b32 v25, 16, v25\n\t" \
    "v_lshrrev_b32 v26, 2, v11\n\t" \
    "v_or_b32 v25, v25, v26\n\t" \
    "v_cvt_f32_ubyte0 v25, v25\n\t" \
    "v_add_f32 v26, 2.0, v25\n\t" \
    "v_add_f32 v27, 4.0, v25\n\t" \
    "v_add_f32 v28, 4.0, v26\n\t" \
    "v_mov_b32 v29, v22\n\t" \
    "v_mov_b32 v30, v22\n\t" \
    "v_mov_b32 v31, v24\n\t" \
    "s_mov_b32 s64, 0x3e800000\n\t" \
    "s_mov_b32 s65, 0x3e800000\n\t" \
    "v_add_u32 v34, 154880, v0\n\t" \
    "s_branch .Ltl_%=\n\t" \
    ".Lm1_%=:\n\t" \
    "v_lshrrev_b32 v24, 5, v0\n\t" \
    "v_and_b32 v24, 12, v24\n\t" \
    "v_and_or_b32 v24, v20, 3, v24\n\t" \
    "v_cvt_f32_ubyte0 v24, v24\n\t" \
    "v_mul_f32 v24, 0x3d924925, v24\n\t" \
    "v_mul_f32 v32, 0x41000000, v24\n\t" \
    "v_lshrrev_b32 v27, 3, v11\n\t" \
    "v_cvt_f32_ubyte0 v27, v27\n\t" \
    "v_add_f32 v25, -2.0, v27\n\t" \
    "v_add_f32 v26, -1.0, v27\n\t" \
    "v_add_f32 v28, 1.0, v27\n\t" \
    "v_and_b32 v29, 4, v20\n\t" \
    "v_cvt_f32_ubyte0 v29, v29\n\t" \
    "v_mul_f32 v29, 0x3d800000, v29\n\t" \
    "v_mov_b32 v30, v29\n\t" \
    "v_mov_b32 v31, 0xbe800000\n\t" \
    "s_mov_b32 s64, 0\n\t" \
    "s_mov_b32 exec_hi, 0\n\t" \
    "v_mov_b32 v33, 0\n\t" \
    "s_mov_b64 exec, -1\n\t" \
    "v_add_u32 v34, 149760, v0\n\t" \
    "s_branch .Ltl_%=\n\t" \
    ".Lm2_%=:\n\t" \
    "v_add_u32 v24, 6, v20\n\t" \
    "v_cvt_f32_ubyte0 v24, v24\n\t" \
    "v_mul_f32 v24, 0x3d000000, v24\n\t" \
    "v_add_f32 v25, 0.5, v21\n\t" \
    "v_add_f32 v26, 1.0, v25\n\t" \
    "v_add_f32 v27, 2.0, v25\n\t" \
    "v_add_f32 v28, 2.0, v26\n\t" \
    "v_mov_b32 v29, 0\n\t" \
    "v_mov_b32 v30, 0\n\t" \
    "s_mov_b32 s64, 0.5\n\t" \
    "v_add_u32 v34, 152832, v0\n\t" \
    "s_branch .Ltl_%=\n\t" \
    ".Lm3_%=:\n\t" \
    "s_mov_b32 s37, 34668544\n\t" \
    "buffer_load_dword v3, v12, s[4:7], s37 offen sc1\n\t" \
    "v_lshrrev_b32 v8, 2, v0\n\t" \
    "v_and_b32 v8, 28, v8\n\t" \
    "v_add_u32 v8, 156928, v8\n\t" \
    "ds_write_b32 v8, v12\n\t" \
    "v_add_u32 v24, 6, v23\n\t" \
    "v_cvt_f32_ubyte0 v24, v24\n\t" \
    "v_mul_f32 v24, 0x3d924925, v24\n\t" \
    "v_mov_b32 v25, v21\n\t" \
    "v_add_f32 v26, 1.0, v21\n\t" \
    "v_add_f32 v27, 2.0, v21\n\t" \
    "v_add_f32 v28, 2.0, v26\n\t" \
    "v_sub_f32 v29, 0, v22\n\t" \
    "v_sub_f32 v30, 0, v22\n\t" \
    "s_mov_b32 exec_lo, 0\n\t" \
    "v_fma_f32 v25, v21, 0.5, 4.0\n\t" \
    "v_add_f32 v27, 1.0, v25\n\t" \
    "v_fmaak_f32 v26, v21, v31, 0x41200000\n\t" \
    "v_mov_b32 v28, v26\n\t" \
    "v_add_f32 v30, v31, v30\n\t" \
    "s_mov_b64 exec, -1\n\t" \
    "s_mov_b32 s40, 0x11\n\t" \
    "v_add_u32 v34, 151808, v0\n\t" \
    ".Ltl_%=:\n\t" \
    "v_fma_f32 v36, v24, v25, v29\n\t" \
    "v_fma_f32 v38, v24, v26, v29\n\t" \
    "v_fma_f32 v40, v24, v27, v29\n\t" \
    "v_fma_f32 v42, v24, v28, v30\n\t" \
    "v_add_f32 v37, v36, v31\n\t" \
    "v_add_f32 v39, v38, v31\n\t" \
    "v_add_f32 v41, v40, v31\n\t" \
    "v_add_f32 v43, v42, v31\n\t" \
    "v_cos_f32 v36, v36\n\t" \
    "v_cos_f32 v37, v37\n\t" \
    "v_cos_f32 v38, v38\n\t" \
    "v_cos_f32 v39, v39\n\t" \
    "v_cos_f32 v40, v40\n\t" \
    "v_cos_f32 v41, v41\n\t" \
    "v_cos_f32 v42, v42\n\t" \
    "v_cos_f32 v43, v43\n\t" \
    "s_bitcmp0_b32 s40, 4\n\t" \
    "s_cbranch_scc1 .Lt2_%=\n\t" \
    "v_mul_f32 v36, 0x3b924925, v36\n\t" \
    "v_mul_f32 v37, 0x3b924925, v37\n\t" \
    "v_mul_f32 v38, 0x3b924925, v38\n\t" \
    "v_mul_f32 v39, 0x3b924925, v39\n\t" \
    "v_mul_f32 v40, 0x3b924925, v40\n\t" \
    "v_mul_f32 v41, 0x3b924925, v41\n\t" \
    "v_mul_f32 v42, 0x3b924925, v42\n\t" \
    "v_mul_f32 v43, 0x3b924925, v43\n\t" \
    ".Lt2_%=:\n\t" \
    "v_cvt_pk_bf16_f32 v44, v36, v37\n\t" \
    "v_cvt_pk_bf16_f32 v45, v38, v39\n\t" \
    "v_cvt_pk_bf16_f32 v46, v40, v41\n\t" \
    "v_cvt_pk_bf16_f32 v47, v42, v43\n\t" \
    "v_and_b32 v44, v33, v44\n\t" \
    "v_and_b32 v45, v33, v45\n\t" \
    "v_mov_b32 v33, -1\n\t" \
    "v_add_f32 v24, s64, v24\n\t" \
    "v_add_f32 v31, s65, v31\n\t" \
    "v_add_f32 v29, v32, v29\n\t" \
    "v_add_f32 v30, v32, v30\n\t" \
    "ds_write_b128 v34, v[44:47]\n\t" \
    "s_add_u32 s40, s40, 1\n\t" \
    "s_bitcmp1_b32 s40, 0\n\t" \
    "v_add_u32 v34, 0x400, v34\n\t" \
    "s_cbranch_scc1 .Ltl_%=\n\t" \
    "s_bitcmp1_b32 s40, 2\n\t" \
    "s_cbranch_scc1 .Lt4_%=\n\t" \
    "s_mov_b32 s40, 2\n\t" \
    "s_lshl2_add_u32 s37, s28, s24\n\t" \
    "s_lshl_b32 s37, s37, 2\n\t" \
    "v_lshrrev_b32 v25, 2, v20\n\t" \
    "v_add_u32 v25, s37, v25\n\t" \
    "v_mul_u32_u24 v26, 147, v25\n\t" \
    "v_lshrrev_b32 v26, 10, v26\n\t" \
    "v_lshl_or_b32 v24, v26, 1, 1\n\t" \
    "v_cvt_f32_ubyte0 v24, v24\n\t" \
    "v_mul_f32 v24, 0x3c800000, v24\n\t" \
    "v_mul_u32_u24 v26, 7, v26\n\t" \
    "v_sub_u32 v25, v25, v26\n\t" \
    "v_xor_b32 v22, s41, v20\n\t" \
    "v_bfe_u32 v32, v22, 1, 1\n\t" \
    "v_and_b32 v22, 1, v22\n\t" \
    "v_cvt_f32_ubyte0 v22, v22\n\t" \
    "v_mul_f32 v22, 0x3e800000, v22\n\t" \
    "v_lshl_or_b32 v32, v25, 1, v32\n\t" \
    "v_cvt_f32_ubyte0 v32, v32\n\t" \
    "v_mul_f32 v32, 0x3e924925, v32\n\t" \
    "v_mul_f32 v29, 0x3d800000, v21\n\t" \
    "v_fma_f32 v29, v29, v32, v22\n\t" \
    "v_mov_b32 v30, v29\n\t" \
    "v_mov_b32 v31, v24\n\t" \
    "v_mov_b32 v25, 0\n\t" \
    "v_mov_b32 v26, 2.0\n\t" \
    "v_mov_b32 v27, 4.0\n\t" \
    "v_mov_b32 v28, 0x40c00000\n\t" \
    "s_mov_b32 s64, 0\n\t" \
    "s_mov_b32 s65, 0\n\t" \
    "s_lshl_b32 s37, s24, 11\n\t" \
    "s_add_u32 s37, s37, 49664\n\t" \
    "v_add_u32 v34, s37, v0\n\t" \
    "s_branch .Ltl_%=\n\t" \
    ".Lt4_%=:\n\t" \
    ".Lt3_%=:\n\t" \
    "s_cmp_lg_u32 s24, 3\n\t" \
    "s_cbranch_scc1 .Lx2_%=\n\t" \
    "v_lshrrev_b32 v8, 7, v0\n\t" \
    "v_mul_u32_u24 v8, 96, v8\n\t" \
    "v_and_b32 v9, 16, v0\n\t" \
    "v_bfe_u32 v10, v0, 5, 2\n\t" \
    "v_add3_u32 v8, v8, v9, v10\n\t" \
    "v_mul_u32_u24 v8, 192, v8\n\t" \
    "s_mul_i32 s37, s28, 129024\n\t" \
    "s_add_u32 s37, s37, s31\n\t" \
    "s_mov_b32 m0, 157312\n\t" \
    "s_mov_b32 exec_hi, 0xffffff\n\t" \
    "buffer_load_dword v8, s[12:15], s37 offen lds\n\t" \
    "s_mov_b64 exec, -1\n\t" \
    ".Lx2_%=:\n\t" \
    ".Le9_%=:\n\t" \
    "s_lshr_b32 s37, s24, 2\n\t" \
    "s_and_b32 s38, s24, 2\n\t" \
    "s_mul_i32 s39, s37, 24832\n\t" \
    "s_lshl4_add_u32 s39, s38, s39\n\t" \
    "v_lshrrev_b32 v4, 6, v0\n\t" \
    "v_and_b32 v5, 12, v4\n\t" \
    "v_add_u32 v6, v4, v5\n\t" \
    "v_lshrrev_b32 v7, 1, v0\n\t" \
    "v_and_b32 v7, 24, v7\n\t" \
    "v_and_b32 v4, 11, v4\n\t" \
    "v_mul_u32_u24 v4, 192, v4\n\t" \
    "v_bfe_u32 v5, v0, 8, 1\n\t" \
    "v_mul_u32_u24 v5, 3104, v5\n\t" \
    "v_add3_u32 v4, v4, v5, v7\n\t" \
    "v_add_u32 v8, s39, v4\n\t" \
    "v_mov_b32 v9, 156928\n\t" \
    "v_add_u32 v10, 149760, v0\n\t" \
    "s_lshl2_add_u32 s37, s37, s38\n\t" \
    "s_lshl_b32 s37, s37, 7\n\t" \
    "s_mul_i32 s53, s27, 28\n\t" \
    "s_add_u32 s53, s53, s26\n\t" \
    "s_andn2_b32 s39, s53, 7\n\t" \
    "s_add_u32 s53, s53, s39\n\t" \
    "s_lshl_b32 s53, s53, 10\n\t" \
    "s_add_u32 s37, s37, s53\n\t" \
    "s_and_b32 s38, s24, 1\n\t" \
    "s_lshl_b32 s39, s38, 13\n\t" \
    "s_add_u32 s37, s37, s39\n\t" \
    "s_cmp_lt_u32 s25, 126\n\t" \
    "s_cselect_b32 s43, 0, 0x80000000\n\t" \
    "s_add_u32 s37, s37, s43\n\t" \
    "v_lshrrev_b32 v11, 8, v0\n\t" \
    "v_and_b32 v12, 0xf0, v0\n\t" \
    "v_mul_u32_u24 v4, 8486912, v11\n\t" \
    "v_add3_u32 v13, v4, v12, s37\n\t" \
    "s_lshl_b32 s39, s38, 10\n\t" \
    "v_add_u32 v14, s39, v10\n\t" \
    "s_lshl_b32 s39, s38, 5\n\t" \
    "s_add_u32 s39, s39, 116736\n\t" \
    "v_lshl_add_u32 v4, v6, 6, v7\n\t" \
    "v_lshrrev_b32 v5, 3, v0\n\t" \
    "v_and_b32 v5, 32, v5\n\t" \
    "v_xor_b32 v5, s39, v5\n\t" \
    "v_add_u32 v15, v4, v5\n\t" \
    "s_and_b32 s37, s24, 6\n\t" \
    "s_lshl_b32 s37, s37, 10\n\t" \
    "v_add_u32 v16, s37, v0\n\t" \
    "s_cmp_ge_u32 s25, 126\n\t" \
    "s_cbranch_scc1 .Lqa_%=\n\t" \
    ".Lqd_%=:\n\t" \
    "s_waitcnt lgkmcnt(0)\n\t" \
    "s_barrier\n\t" \
    "ds_read_b128 v[24:27], v14 offset:5120\n\t" \
    "ds_read_b128 v[28:31], v16 offset:49664\n\t" \
    "ds_read_b128 v[32:35], v16 offset:50688\n\t" \
    "s_and_b32 s33, s2, 7\n\t" \
    "s_lshl_b32 s33, s33, 15\n\t" \
    "s_add_u32 s33, s33, 33554432\n\t" \
    "s_lshr_b32 s37, s29, 2\n\t" \
    "s_mul_i32 s39, s30, 3\n\t" \
    "s_add_u32 s37, s37, s39\n\t" \
    "s_mul_i32 s37, s37, 114688\n\t" \
    "s_and_b32 s39, s29, 3\n\t" \
    "s_lshl_b32 s39, s39, 8\n\t" \
    "s_add_u32 s37, s37, s39\n\t" \
    "s_add_u32 s37, s37, s43\n\t" \
    "s_add_u32 s37, s37, 2097152\n\t" \
    "s_and_b32 s39, s24, 6\n\t" \
    "s_lshl3_add_u32 s39, s28, s39\n\t" \
    "s_lshl_b32 s39, s39, 1\n\t" \
    "v_add_u32 v14, s39, v11\n\t" \
    "v_mul_u32_u24 v17, 147, v14\n\t" \
    "v_lshrrev_b32 v17, 10, v17\n\t" \
    "s_and_b32 s39, s24, 1\n\t" \
    "v_subrev_u32 v18, s39, v17\n\t" \
    "v_mul_i32_i24 v18, 7, v18\n\t" \
    "v_sub_u32 v14, v14, v18\n\t" \
    "v_and_b32 v18, 14, v14\n\t" \
    "v_add_lshl_u32 v14, v14, v18, 12\n\t" \
    "v_and_b32 v18, 7, v17\n\t" \
    "v_mul_u32_u24 v18, 4243456, v18\n\t" \
    "v_add3_u32 v14, v14, v18, v12\n\t" \
    "v_and_b32 v17, 8, v17\n\t" \
    "v_xor_b32 v17, 8, v17\n\t" \
    "v_lshl_add_u32 v14, v17, 10, v14\n\t" \
    "v_add_u32 v19, s37, v14\n\t" \
    "s_lshl_b32 s43, s41, 10\n\t" \
    "s_and_b32 s37, s2, 0x70\n\t" \
    "s_lshl_b32 s37, s37, 1\n\t" \
    "s_lshl2_add_u32 s57, s24, s37\n\t" \
    "s_min_u32 s58, s57, 248\n\t" \
    "s_mul_i32 s59, s58, 147\n\t" \
    "s_lshr_b32 s59, s59, 12\n\t" \
    "s_mul_i32 s37, s59, 28\n\t" \
    "s_sub_u32 s60, s58, s37\n\t" \
    "s_andn2_b32 s39, s58, 7\n\t" \
    "s_add_u32 s51, s58, s39\n\t" \
    "s_lshl_b32 s51, s51, 10\n\t" \
    "s_sub_u32 s37, s60, 1\n\t" \
    "s_cselect_b32 s52, 27, -1\n\t" \
    "s_add_u32 s52, s52, s58\n\t" \
    "s_andn2_b32 s39, s52, 7\n\t" \
    "s_add_u32 s52, s52, s39\n\t" \
    "s_lshl_b32 s52, s52, 10\n\t" \
    "v_lshrrev_b32 v16, 9, v0\n\t" \
    "v_and_b32 v18, 0x2f0, v0\n\t" \
    "v_and_b32 v17, 1, v11\n\t" \
    "v_lshl_add_u32 v14, v17, 11, v18\n\t" \
    "v_add_u32 v36, s51, v14\n\t" \
    "v_add_u32 v37, s52, v0\n\t" \
    "s_mul_i32 s44, s24, 7296\n\t" \
    "s_mul_i32 s37, s60, 344064\n\t" \
    "s_mul_i32 s39, s59, 4992\n\t" \
    "s_add_u32 s37, s37, s39\n\t" \
    "s_cmp_eq_u32 s59, 8\n\t" \
    "s_movk_i32 s39, 2496\n\t" \
    "s_cselect_b32 s39, 1536, s39\n\t" \
    "s_add_u32 s37, s37, s39\n\t" \
    "s_and_b32 s39, s8, 0x1fffff\n\t" \
    "s_add_u32 s37, s37, s39\n\t" \
    "v_bfe_u32 v38, v0, 5, 2\n\t" \
    "v_lshl_add_u32 v38, v16, 4, v38\n\t" \
    "v_mul_u32_u24 v38, 43008, v38\n\t" \
    "v_add_u32 v39, s37, v38\n\t" \
    "s_lshr_b32 s37, s60, 1\n\t" \
    "s_mul_i32 s37, s37, 9\n\t" \
    "s_add_u32 s37, s37, s59\n\t" \
    "v_mad_u32_u24 v38, v16, 9, s37\n\t" \
    "v_lshrrev_b32 v18, 5, v38\n\t" \
    "s_cmp_lt_u32 s57, 252\n\t" \
    "s_cselect_b32 s51, s51, 0x80000000\n\t" \
    "s_lshl_b32 s37, s24, 2\n\t" \
    "s_lshl1_add_u32 s39, s24, 8\n\t" \
    "s_min_u32 s61, s37, s39\n\t" \
    "s_add_u32 s37, s37, 2\n\t" \
    "s_add_u32 s39, s24, 20\n\t" \
    "s_cmp_lt_u32 s24, 4\n\t" \
    "s_cselect_b32 s62, s37, s39\n\t" \
    "s_bfe_u32 s63, s2, 0x10003\n\t" \
    "s_mul_i32 s47, s63, 28\n\t" \
    "s_add_u32 s45, s47, s61\n\t" \
    "s_add_u32 s46, s47, s62\n\t" \
    "s_andn2_b32 s39, s45, 7\n\t" \
    "s_add_u32 s45, s45, s39\n\t" \
    "s_lshl_b32 s45, s45, 10\n\t" \
    "s_andn2_b32 s39, s46, 7\n\t" \
    "s_add_u32 s46, s46, s39\n\t" \
    "s_lshl_b32 s46, s46, 10\n\t" \
    "v_lshlrev_b32 v17, 4, v11\n\t" \
    "v_lshrrev_b32 v14, 6, v12\n\t" \
    "v_mul_u32_u24 v14, 7296, v14\n\t" \
    "v_and_b32 v38, 16, v0\n\t" \
    "v_mul_u32_u24 v38, 226, v38\n\t" \
    "v_and_b32 v40, 32, v0\n\t" \
    "v_lshl_add_u32 v38, v40, 1, v38\n\t" \
    "v_add3_u32 v14, v14, v38, v17\n\t" \
    "s_lshl_b32 s37, s61, 7\n\t" \
    "v_add_u32 v44, s37, v14\n\t" \
    "s_and_b32 s37, s62, 30\n\t" \
    "s_lshl_b32 s37, s37, 7\n\t" \
    "v_add_u32 v45, s37, v14\n\t" \
    "s_mov_b32 s1, 0\n\t" \
    "s_mov_b32 s41, 0x18000\n\t" \
    "s_lshl_b32 s37, s63, 9\n\t" \
    "s_add_u32 s51, s51, s37\n\t" \
    "s_and_b32 s37, s2, 0x87\n\t" \
    "s_lshl4_add_u32 s37, s24, s37\n\t" \
    "s_lshl_b32 s55, s37, 7\n\t" \
    "s_andn2_b32 s37, s55, 0x1fff\n\t" \
    "s_add_u32 s55, s55, s37\n\t" \
    "v_and_b32 v46, 16, v0\n\t" \
    "v_lshlrev_b32 v47, 6, v46\n\t" \
    "v_mad_u32_u24 v38, v12, 12, v17\n\t" \
    "v_and_b32 v14, 0x170, v0\n\t" \
    "v_and_b32 v41, 0x80, v0\n\t" \
    "v_lshl_add_u32 v14, v41, 3, v14\n\t" \
    "v_lshl_add_u32 v14, v16, 11, v14\n\t" \
    "v_add_u32 v42, s51, v14\n\t" \
    "s_mul_i32 s37, s24, 2064\n\t" \
    "v_and_b32 v14, 0x300, v0\n\t" \
    "v_add3_u32 v43, v14, v0, s37\n\t" \
    "s_lshl_b32 s37, s37, 1\n\t" \
    "v_lshlrev_b32 v14, 7, v16\n\t" \
    "v_xor_b32 v14, v14, v0\n\t" \
    "v_add_u32 v40, s37, v14\n\t" \
    "v_lshrrev_b32 v14, 5, v12\n\t" \
    "v_mul_u32_u24 v14, 2064, v14\n\t" \
    "v_lshl_add_u32 v14, v46, 2, v14\n\t" \
    "s_lshl_b32 s37, s24, 8\n\t" \
    "v_add3_u32 v48, v14, v17, s37\n\t" \
    "v_bfe_u32 v14, v0, 7, 1\n\t" \
    "v_lshl_add_u32 v14, v11, 2, v14\n\t" \
    "v_mul_u32_u24 v14, 2064, v14\n\t" \
    "v_lshrrev_b32 v41, 3, v0\n\t" \
    "v_and_or_b32 v14, v41, 8, v14\n\t" \
    "s_and_b32 s37, s24, 3\n\t" \
    "s_lshl_b32 s37, s37, 6\n\t" \
    "s_and_b32 s39, s24, 4\n\t" \
    "s_lshl_b32 s39, s39, 8\n\t" \
    "s_or_b32 s37, s37, s39\n\t" \
    "v_and_or_b32 v41, v0, 32, s37\n\t" \
    "v_mul_u32_u24 v46, 40, v46\n\t" \
    "v_xor_b32 v41, v41, v46\n\t" \
    "v_add_u32 v49, v14, v41\n\t" \
    "ds_read_b128 v[52:55], v10 offset:0\n\t" \
    "ds_read_b128 v[56:59], v10 offset:1024\n\t" \
    "s_mov_b64 exec, 0\n\t" \
    "s_cmp_eq_u32 s24, 0\n\t" \
    "s_cbranch_scc1 .Lz0_%=\n\t" \
    "s_cmp_eq_u32 s24, 1\n\t" \
    "s_cbranch_scc1 .Lz4_%=\n\t" \
    "s_cmp_eq_u32 s24, 2\n\t" \
    "s_cbranch_scc1 .Lz7_%=\n\t" \
    ".Lwk_%=:\n\t" \
    "s_mov_b64 exec, -1\n\t" \
    ".Lz0_%=:\n\t" \
    "s_cbranch_execz .Lz1_%=\n\t" \
    "s_and_b32 s37, s24, 1\n\t" \
    "s_lshl_b32 s37, s37, 4\n\t" \
    "s_lshr_b32 s39, s24, 1\n\t" \
    "s_add_u32 s37, s37, s39\n\t" \
    "s_mul_i32 s37, s37, 192\n\t" \
    "s_add_u32 s37, s37, s31\n\t" \
    "s_lshl_b32 s39, s24, 12\n\t" \
    "s_add_u32 m0, s39, 116736\n\t" \
    "s_add_u32 s38, s37, 0\n\t" \
    "buffer_load_dwordx4 v1, s[12:15], s38 offen  lds\n\t" \
    "s_add_u32 m0, s39, 117760\n\t" \
    "s_add_u32 s38, s37, 258048\n\t" \
    "buffer_load_dwordx4 v1, s[12:15], s38 offen  lds\n\t" \
    "s_add_u32 m0, s39, 118784\n\t" \
    "s_add_u32 s38, s37, 516096\n\t" \
    "buffer_load_dwordx4 v1, s[12:15], s38 offen  lds\n\t" \
    "s_add_u32 m0, s39, 119808\n\t" \
    "s_add_u32 s38, s37, 774144\n\t" \
    "buffer_load_dwordx4 v1, s[12:15], s38 offen  lds\n\t" \
    "s_cmp_lg_u32 s24, 3\n\t" \
    "s_cbranch_scc1 .Lr9_%=\n\t" \
    "s_and_b32 s37, s2, 7\n\t" \
    "s_lshl_b32 s37, s37, 10\n\t" \
    "s_add_u32 s37, s37, 34676736\n\t" \
    "s_buffer_load_dword s56, s[4:7], s37 offset:0x0\n\t" \
    "s_buffer_load_dword s57, s[4:7], s37 offset:0x80\n\t" \
    "s_buffer_load_dword s58, s[4:7], s37 offset:0x100\n\t" \
    "s_buffer_load_dword s59, s[4:7], s37 offset:0x180\n\t" \
    "s_buffer_load_dword s60, s[4:7], s37 offset:0x200\n\t" \
    "s_buffer_load_dword s61, s[4:7], s37 offset:0x280\n\t" \
    "s_buffer_load_dword s62, s[4:7], s37 offset:0x300\n\t" \
    "s_buffer_load_dword s63, s[4:7], s37 offset:0x380\n\t" \
    "s_mov_b32 s40, 0\n\t" \
    ".Lr1_%=:\n\t" \
    "s_waitcnt lgkmcnt(0)\n\t" \
    "s_memrealtime s[22:23]\n\t" \
    "s_waitcnt lgkmcnt(0)\n\t" \
    "s_mov_b32 s37, 1000\n\t" \
    "s_mov_b32 s38, 0\n\t" \
    "s_sub_u32 s39, s22, s56\n\t" \
    "s_min_u32 s37, s37, s39\n\t" \
    "s_max_u32 s38, s38, s39\n\t" \
    "s_sub_u32 s39, s22, s57\n\t" \
    "s_min_u32 s37, s37, s39\n\t" \
    "s_max_u32 s38, s38, s39\n\t" \
    "s_sub_u32 s39, s22, s58\n\t" \
    "s_min_u32 s37, s37, s39\n\t" \
    "s_max_u32 s38, s38, s39\n\t" \
    "s_sub_u32 s39, s22, s59\n\t" \
    "s_min_u32 s37, s37, s39\n\t" \
    "s_max_u32 s38, s38, s39\n\t" \
    "s_sub_u32 s39, s22, s60\n\t" \
    "s_min_u32 s37, s37, s39\n\t" \
    "s_max_u32 s38, s38, s39\n\t" \
    "s_sub_u32 s39, s22, s61\n\t" \
    "s_min_u32 s37, s37, s39\n\t" \
    "s_max_u32 s38, s38, s39\n\t" \
    "s_sub_u32 s39, s22, s62\n\t" \
    "s_min_u32 s37, s37, s39\n\t" \
    "s_max_u32 s38, s38, s39\n\t" \
    "s_sub_u32 s39, s22, s63\n\t" \
    "s_min_u32 s37, s37, s39\n\t" \
    "s_max_u32 s38, s38, s39\n\t" \
    "s_cmp_gt_u32 s38, 1000\n\t" \
    "s_cbranch_scc1 .Lr2_%=\n\t" \
    ".Lr3_%=:\n\t" \
    "s_sub_u32 s35, s22, s37\n\t" \
    "s_waitcnt vmcnt(5)\n\t" \
    "v_readfirstlane_b32 s34, v3\n\t" \
    "s_mul_i32 s34, s34, 1664525\n\t" \
    "s_add_u32 s34, s34, 1013904223\n\t" \
    "s_or_b32 s34, s34, 1\n\t" \
    "v_mov_b32 v60, s34\n\t" \
    "v_mov_b32 v61, s35\n\t" \
    "ds_write_b64 v9, v[60:61] offset:32\n\t" \
    ".Lr9_%=:\n\t" \
    "s_cmp_lt_u32 s24, 4\n\t" \
    "s_cbranch_scc1 .Lw_%=\n\t" \
    "s_waitcnt vmcnt(4)\n\t" \
    ".Lw_%=:\n\t" \
    "s_waitcnt lgkmcnt(0)\n\t" \
    "s_barrier\n\t" \
    "ds_read_b64_tr_b16 v[64:65], v8 offset:0\n\t" \
    "ds_read_b64_tr_b16 v[66:67], v8 offset:768\n\t" \
    "ds_read_b64_tr_b16 v[68:69], v8 offset:12416\n\t" \
    "ds_read_b64_tr_b16 v[70:71], v8 offset:13184\n\t" \
    "ds_read_b64_tr_b16 v[72:73], v8 offset:6208\n\t" \
    "ds_read_b64_tr_b16 v[74:75], v8 offset:6976\n\t" \
    "ds_read_b64_tr_b16 v[76:77], v8 offset:18624\n\t" \
    "ds_read_b64_tr_b16 v[78:79], v8 offset:19392\n\t" \
    "ds_read_b128 v[80:83], v9 offset:32\n\t" \
    "s_waitcnt lgkmcnt(7)\n\t" \
    "v_mfma_f32_16x16x32_bf16 v[84:87], v[24:27], v[64:67], 0\n\t" \
    "s_waitcnt lgkmcnt(5)\n\t" \
    "v_mfma_f32_16x16x32_bf16 v[88:91], v[24:27], v[68:71], 0\n\t" \
    "s_waitcnt lgkmcnt(3)\n\t" \
    "v_mfma_f32_16x16x32_bf16 v[92:95], v[24:27], v[72:75], 0\n\t" \
    "s_waitcnt lgkmcnt(1)\n\t" \
    "v_mfma_f32_16x16x32_bf16 v[96:99], v[24:27], v[76:79], 0\n\t" \
    "s_waitcnt lgkmcnt(0)\n\t" \
    "v_readfirstlane_b32 s34, v80\n\t" \
    "v_readfirstlane_b32 s35, v81\n\t" \
    "v_add_u32 v84, 0x8000, v84\n\t" \
    "v_add_u32 v86, 0x8000, v86\n\t" \
    "v_add_u32 v88, 0x8000, v88\n\t" \
    "v_add_u32 v90, 0x8000, v90\n\t" \
    "v_cvt_pk_bf16_f32 v108, v84, v85\n\t" \
    "v_cvt_pk_bf16_f32 v110, v88, v89\n\t" \
    "v_cvt_pk_bf16_f32 v109, v92, v93\n\t" \
    "v_cvt_pk_bf16_f32 v111, v96, v97\n\t" \
    "v_and_b32 v108, -2, v108\n\t" \
    "v_and_b32 v110, -2, v110\n\t" \
    "v_cvt_pk_bf16_f32 v112, v86, v87\n\t" \
    "v_cvt_pk_bf16_f32 v114, v90, v91\n\t" \
    "v_cvt_pk_bf16_f32 v113, v94, v95\n\t" \
    "v_cvt_pk_bf16_f32 v115, v98, v99\n\t" \
    "v_and_b32 v112, -2, v112\n\t" \
    "v_and_b32 v114, -2, v114\n\t" \
    ".Lh38_%=:\n\t" \
    "s_memrealtime s[22:23]\n\t" \
    "s_waitcnt lgkmcnt(0)\n\t" \
    "s_sub_u32 s22, s22, s35\n\t" \
    "s_cmp_lt_u32 s22, 205\n\t" \
    "s_cbranch_scc1 .Lh38_%=\n\t" \
    "buffer_store_dwordx4 v[108:111], v13, s[4:7], 0 offen offset:0 sc1\n\t" \
    "s_mov_b32 s42, 4243456\n\t" \
    "buffer_store_dwordx4 v[112:115], v13, s[4:7], s42 offen offset:0 sc1\n\t" \
    "ds_read_b64_tr_b16 v[116:117], v8 offset:64\n\t" \
    "ds_read_b64_tr_b16 v[118:119], v8 offset:832\n\t" \
    "ds_read_b64_tr_b16 v[120:121], v8 offset:12480\n\t" \
    "ds_read_b64_tr_b16 v[122:123], v8 offset:13248\n\t" \
    "ds_read_b64_tr_b16 v[124:125], v8 offset:6272\n\t" \
    "ds_read_b64_tr_b16 v[126:127], v8 offset:7040\n\t" \
    "ds_read_b64_tr_b16 v[128:129], v8 offset:18688\n\t" \
    "ds_read_b64_tr_b16 v[130:131], v8 offset:19456\n\t" \
    "ds_read_b64_tr_b16 v[132:133], v8 offset:128\n\t" \
    "ds_read_b64_tr_b16 v[134:135], v8 offset:896\n\t" \
    "ds_read_b64_tr_b16 v[136:137], v8 offset:12544\n\t" \
    "ds_read_b64_tr_b16 v[138:139], v8 offset:13312\n\t" \
    "ds_read_b64_tr_b16 v[140:141], v8 offset:6336\n\t" \
    "ds_read_b64_tr_b16 v[142:143], v8 offset:7104\n\t" \
    "ds_read_b64_tr_b16 v[144:145], v8 offset:18752\n\t" \
    "ds_read_b64_tr_b16 v[146:147], v8 offset:19520\n\t" \
    "v_mov_b32 v148, s34\n\t" \
    "v_mov_b32 v149, s34\n\t" \
    "v_mov_b32 v150, s34\n\t" \
    "v_mov_b32 v151, s34\n\t" \
    "s_and_b32 s37, s2, 243\n\t" \
    "s_cmp_eq_u32 s37, 243\n\t" \
    "s_cbranch_scc1 .Lskip_%=\n\t" \
    "s_waitcnt lgkmcnt(14)\n\t" \
    "v_mfma_f32_16x16x32_bf16 v[152:155], v[24:27], v[116:119], 0\n\t" \
    "s_waitcnt lgkmcnt(12)\n\t" \
    "v_mfma_f32_16x16x32_bf16 v[156:159], v[24:27], v[120:123], 0\n\t" \
    "s_waitcnt lgkmcnt(10)\n\t" \
    "v_mfma_f32_16x16x32_bf16 v[160:163], v[24:27], v[124:127], 0\n\t" \
    "s_waitcnt lgkmcnt(8)\n\t" \
    "v_mfma_f32_16x16x32_bf16 v[164:167], v[24:27], v[128:131], 0\n\t" \
    "s_waitcnt lgkmcnt(6)\n\t" \
    "v_mfma_f32_16x16x32_bf16 v[168:171], v[24:27], v[132:135], 0\n\t" \
    "s_waitcnt lgkmcnt(4)\n\t" \
    "v_mfma_f32_16x16x32_bf16 v[172:175], v[24:27], v[136:139], 0\n\t" \
    "s_waitcnt lgkmcnt(2)\n\t" \
    "v_mfma_f32_16x16x32_bf16 v[176:179], v[24:27], v[140:143], 0\n\t" \
    "s_waitcnt lgkmcnt(0)\n\t" \
    "v_mfma_f32_16x16x32_bf16 v[180:183], v[24:27], v[144:147], 0\n\t" \
    "v_add_u32 v152, 0x8000, v152\n\t" \
    "v_add_u32 v154, 0x8000, v154\n\t" \
    "v_add_u32 v156, 0x8000, v156\n\t" \
    "v_add_u32 v158, 0x8000, v158\n\t" \
    "v_cvt_pk_bf16_f32 v184, v152, v153\n\t" \
    "v_cvt_pk_bf16_f32 v186, v156, v157\n\t" \
    "v_cvt_pk_bf16_f32 v185, v160, v161\n\t" \
    "v_cvt_pk_bf16_f32 v187, v164, v165\n\t" \
    "v_and_b32 v184, -2, v184\n\t" \
    "v_and_b32 v186, -2, v186\n\t" \
    "v_cvt_pk_bf16_f32 v188, v154, v155\n\t" \
    "v_cvt_pk_bf16_f32 v190, v158, v159\n\t" \
    "v_cvt_pk_bf16_f32 v189, v162, v163\n\t" \
    "v_cvt_pk_bf16_f32 v191, v166, v167\n\t" \
    "v_and_b32 v188, -2, v188\n\t" \
    "v_and_b32 v190, -2, v190\n\t" \
    "v_add_u32 v168, 0x8000, v168\n\t" \
    "v_add_u32 v170, 0x8000, v170\n\t" \
    "v_add_u32 v172, 0x8000, v172\n\t" \
    "v_add_u32 v174, 0x8000, v174\n\t" \
    "v_cvt_pk_bf16_f32 v4, v168, v169\n\t" \
    "v_cvt_pk_bf16_f32 v6, v172, v173\n\t" \
    "v_cvt_pk_bf16_f32 v5, v176, v177\n\t" \
    "v_cvt_pk_bf16_f32 v7, v180, v181\n\t" \
    "v_and_b32 v4, -2, v4\n\t" \
    "v_and_b32 v6, -2, v6\n\t" \
    "v_cvt_pk_bf16_f32 v20, v170, v171\n\t" \
    "v_cvt_pk_bf16_f32 v22, v174, v175\n\t" \
    "v_cvt_pk_bf16_f32 v21, v178, v179\n\t" \
    "v_cvt_pk_bf16_f32 v23, v182, v183\n\t" \
    "v_and_b32 v20, -2, v20\n\t" \
    "v_and_b32 v22, -2, v22\n\t" \
    ".Lh39_%=:\n\t" \
    "s_memrealtime s[22:23]\n\t" \
    "s_waitcnt lgkmcnt(0)\n\t" \
    "s_sub_u32 s22, s22, s35\n\t" \
    "s_cmp_lt_u32 s22, 255\n\t" \
    "s_cbranch_scc1 .Lh39_%=\n\t" \
    "s_mov_b32 s42, 524288\n\t" \
    "buffer_store_dwordx4 v[184:187], v13, s[4:7], s42 offen offset:0 sc1\n\t" \
    "s_mov_b32 s42, 4767744\n\t" \
    "buffer_store_dwordx4 v[188:191], v13, s[4:7], s42 offen offset:0 sc1\n\t" \
    "s_waitcnt vmcnt(4)\n\t" \
    "s_waitcnt lgkmcnt(0)\n\t" \
    "s_barrier\n\t" \
    "s_lshl_b32 s37, s24, 18\n\t" \
    "s_add_u32 s37, s37, s32\n\t" \
    "s_bitcmp1_b32 13, s24\n\t" \
    "s_cselect_b32 s39, 0, 0x80000000\n\t" \
    "v_add_u32 v16, s39, v2\n\t" \
    "v_mov_b32 v60, 0\n\t" \
    "v_mov_b32 v61, 0\n\t" \
    "v_mov_b32 v62, 0\n\t" \
    "v_mov_b32 v63, 0\n\t" \
    "s_cmp_eq_u32 s24, 4\n\t" \
    "s_cselect_b64 exec, 0xff, -1\n\t" \
    "buffer_store_dwordx4 v[60:63], v16, s[4:7], s37 offen sc1\n\t" \
    "s_mov_b64 exec, -1\n\t" \
    ".Lh40_%=:\n\t" \
    "s_memrealtime s[22:23]\n\t" \
    "s_waitcnt lgkmcnt(0)\n\t" \
    "s_sub_u32 s22, s22, s35\n\t" \
    "s_cmp_lt_u32 s22, 305\n\t" \
    "s_cbranch_scc1 .Lh40_%=\n\t" \
    "s_mov_b32 s42, 1048576\n\t" \
    "buffer_store_dwordx4 v[4:7], v13, s[4:7], s42 offen offset:0 sc1\n\t" \
    "s_mov_b32 s42, 5292032\n\t" \
    "buffer_store_dwordx4 v[20:23], v13, s[4:7], s42 offen offset:0 sc1\n\t" \
    ".Lz1_%=:\n\t" \
    "s_cbranch_execz .Lz2_%=\n\t" \
    "ds_read_b64_tr_b16 v[64:65], v15 offset:0\n\t" \
    "ds_read_b64_tr_b16 v[66:67], v15 offset:256\n\t" \
    "ds_read_b64_tr_b16 v[68:69], v15 offset:2048\n\t" \
    "ds_read_b64_tr_b16 v[70:71], v15 offset:2304\n\t" \
    "ds_read_b64_tr_b16 v[72:73], v15 offset:4096\n\t" \
    "ds_read_b64_tr_b16 v[74:75], v15 offset:4352\n\t" \
    "ds_read_b64_tr_b16 v[76:77], v15 offset:6144\n\t" \
    "ds_read_b64_tr_b16 v[78:79], v15 offset:6400\n\t" \
    "ds_read_b64_tr_b16 v[80:81], v15 offset:8192\n\t" \
    "ds_read_b64_tr_b16 v[82:83], v15 offset:8448\n\t" \
    "ds_read_b64_tr_b16 v[100:101], v15 offset:10240\n\t" \
    "ds_read_b64_tr_b16 v[102:103], v15 offset:10496\n\t" \
    "ds_read_b64_tr_b16 v[104:105], v15 offset:12288\n\t" \
    "ds_read_b64_tr_b16 v[106:107], v15 offset:12544\n\t" \
    "s_waitcnt lgkmcnt(12)\n\t" \
    "v_mfma_f32_16x16x32_bf16 v[84:87], v[28:31], v[64:67], 0\n\t" \
    "ds_read_b64_tr_b16 v[88:89], v15 offset:14336\n\t" \
    ".Lz2_%=:\n\t" \
    "s_cbranch_execz .Lz3_%=\n\t" \
    "ds_read_b64_tr_b16 v[90:91], v15 offset:14592\n\t" \
    "s_waitcnt lgkmcnt(12)\n\t" \
    "v_mfma_f32_16x16x32_bf16 v[84:87], v[32:35], v[68:71], v[84:87]\n\t" \
    "ds_read_b64_tr_b16 v[92:93], v15 offset:16384\n\t" \
    "ds_read_b64_tr_b16 v[94:95], v15 offset:16640\n\t" \
    "s_waitcnt lgkmcnt(12)\n\t" \
    "v_mfma_f32_16x16x32_bf16 v[96:99], v[28:31], v[72:75], 0\n\t" \
    "ds_read_b64_tr_b16 v[108:109], v15 offset:18432\n\t" \
    "ds_read_b64_tr_b16 v[110:111], v15 offset:18688\n\t" \
    "s_waitcnt lgkmcnt(12)\n\t" \
    "v_mfma_f32_16x16x32_bf16 v[96:99], v[32:35], v[76:79], v[96:99]\n\t" \
    "ds_read_b64_tr_b16 v[112:113], v15 offset:20480\n\t" \
    "ds_read_b64_tr_b16 v[114:115], v15 offset:20736\n\t" \
    "s_waitcnt lgkmcnt(12)\n\t" \
    "v_mfma_f32_16x16x32_bf16 v[116:119], v[28:31], v[80:83], 0\n\t" \
    "ds_read_b64_tr_b16 v[120:121], v15 offset:22528\n\t" \
    "ds_read_b64_tr_b16 v[122:123], v15 offset:22784\n\t" \
    "s_waitcnt lgkmcnt(12)\n\t" \
    "v_mfma_f32_16x16x32_bf16 v[116:119], v[32:35], v[100:103], v[116:119]\n\t" \
    ".Lz3_%=:\n\t" \
    "s_cbranch_execz .Lwk_%=\n\t" \
    "ds_read_b64_tr_b16 v[124:125], v15 offset:24576\n\t" \
    "ds_read_b64_tr_b16 v[126:127], v15 offset:24832\n\t" \
    "s_waitcnt lgkmcnt(12)\n\t" \
    "v_mfma_f32_16x16x32_bf16 v[128:131], v[28:31], v[104:107], 0\n\t" \
    "ds_read_b64_tr_b16 v[132:133], v15 offset:26624\n\t" \
    "ds_read_b64_tr_b16 v[134:135], v15 offset:26880\n\t" \
    "s_waitcnt lgkmcnt(12)\n\t" \
    "v_mfma_f32_16x16x32_bf16 v[128:131], v[32:35], v[88:91], v[128:131]\n\t" \
    "ds_read_b64_tr_b16 v[136:137], v15 offset:28672\n\t" \
    "ds_read_b64_tr_b16 v[138:139], v15 offset:28928\n\t" \
    "s_waitcnt lgkmcnt(12)\n\t" \
    "v_mfma_f32_16x16x32_bf16 v[140:143], v[28:31], v[92:95], 0\n\t" \
    "ds_read_b64_tr_b16 v[24:25], v15 offset:30720\n\t" \
    "ds_read_b64_tr_b16 v[26:27], v15 offset:30976\n\t" \
    "s_waitcnt lgkmcnt(12)\n\t" \
    "v_mfma_f32_16x16x32_bf16 v[140:143], v[32:35], v[108:111], v[140:143]\n\t" \
    "v_cvt_pk_bf16_f32 v144, v84, v96\n\t" \
    "v_cvt_pk_bf16_f32 v152, v85, v97\n\t" \
    ".Lz4_%=:\n\t" \
    "s_cbranch_execz .Lz5_%=\n\t" \
    "s_waitcnt lgkmcnt(10)\n\t" \
    "v_mfma_f32_16x16x32_bf16 v[156:159], v[28:31], v[112:115], 0\n\t" \
    "v_cvt_pk_bf16_f32 v160, v86, v98\n\t" \
    "v_cvt_pk_bf16_f32 v164, v87, v99\n\t" \
    "s_waitcnt lgkmcnt(8)\n\t" \
    "v_mfma_f32_16x16x32_bf16 v[156:159], v[32:35], v[120:123], v[156:159]\n\t" \
    "v_cvt_pk_bf16_f32 v145, v116, v128\n\t" \
    "v_cvt_pk_bf16_f32 v153, v117, v129\n\t" \
    "s_waitcnt lgkmcnt(6)\n\t" \
    "v_mfma_f32_16x16x32_bf16 v[168:171], v[28:31], v[124:127], 0\n\t" \
    "v_cvt_pk_bf16_f32 v161, v118, v130\n\t" \
    "v_cvt_pk_bf16_f32 v165, v119, v131\n\t" \
    "s_waitcnt lgkmcnt(4)\n\t" \
    "v_mfma_f32_16x16x32_bf16 v[168:171], v[32:35], v[132:135], v[168:171]\n\t" \
    "s_waitcnt lgkmcnt(2)\n\t" \
    "v_mfma_f32_16x16x32_bf16 v[172:175], v[28:31], v[136:139], 0\n\t" \
    "s_waitcnt lgkmcnt(0)\n\t" \
    "v_mfma_f32_16x16x32_bf16 v[172:175], v[32:35], v[24:27], v[172:175]\n\t" \
    "v_cvt_pk_bf16_f32 v146, v140, v156\n\t" \
    ".Lz5_%=:\n\t" \
    "s_cbranch_execz .Lz6_%=\n\t" \
    "v_cvt_pk_bf16_f32 v154, v141, v157\n\t" \
    "v_cvt_pk_bf16_f32 v162, v142, v158\n\t" \
    "v_cvt_pk_bf16_f32 v166, v143, v159\n\t" \
    "s_nop 3\n\t" \
    "v_cvt_pk_bf16_f32 v147, v168, v172\n\t" \
    "v_cvt_pk_bf16_f32 v155, v169, v173\n\t" \
    "v_cvt_pk_bf16_f32 v163, v170, v174\n\t" \
    "v_cvt_pk_bf16_f32 v167, v171, v175\n\t" \
    ".Lh41_%=:\n\t" \
    "s_memrealtime s[22:23]\n\t" \
    "s_waitcnt lgkmcnt(0)\n\t" \
    "s_sub_u32 s22, s22, s35\n\t" \
    "s_cmp_lt_u32 s22, 365\n\t" \
    "s_cbranch_scc1 .Lh41_%=\n\t" \
    "s_cmp_eq_u32 s24, 0\n\t" \
    "s_cbranch_scc1 .Lj43_%=\n\t" \
    ".Lh42_%=:\n\t" \
    "s_memrealtime s[22:23]\n\t" \
    "s_waitcnt lgkmcnt(0)\n\t" \
    "s_sub_u32 s22, s22, s35\n\t" \
    "s_cmp_lt_u32 s22, 380\n\t" \
    "s_cbranch_scc1 .Lh42_%=\n\t" \
    ".Lj43_%=:\n\t" \
    "s_waitcnt lgkmcnt(0)\n\t" \
    "s_xor_b32 s42, s43, 0\n\t" \
    "buffer_store_dwordx4 v[144:147], v19, s[4:7], s42 offen offset:0 sc1\n\t" \
    "s_waitcnt vmcnt(1)\n\t" \
    ".Lz6_%=:\n\t" \
    "s_cbranch_execz .Lwk_%=\n\t" \
    "s_mov_b64 exec, 1\n\t" \
    "ds_add_rtn_u32 v17, v9, v9 offset:8\n\t" \
    "s_add_u32 s37, s32, 524288\n\t" \
    "s_waitcnt lgkmcnt(0)\n\t" \
    "v_cmp_eq_u32 vcc, 1098496, v17\n\t" \
    "s_mov_b64 exec, -1\n\t" \
    "s_nop 0\n\t" \
    "s_cbranch_vccz .La13_%=\n\t" \
    "buffer_store_dwordx4 v[148:151], v2, s[4:7], s37 offen sc1\n\t" \
    ".La13_%=:\n\t" \
    "s_mov_b64 exec, -1\n\t" \
    "s_xor_b32 s42, s43, 1024\n\t" \
    "buffer_store_dwordx4 v[152:155], v19, s[4:7], s42 offen offset:0 sc1\n\t" \
    "s_cmp_eq_u32 s24, 0\n\t" \
    "s_cbranch_scc1 .Lj45_%=\n\t" \
    ".Lh44_%=:\n\t" \
    "s_memrealtime s[22:23]\n\t" \
    "s_waitcnt lgkmcnt(0)\n\t" \
    "s_sub_u32 s22, s22, s35\n\t" \
    "s_cmp_lt_u32 s22, 427\n\t" \
    "s_cbranch_scc1 .Lh44_%=\n\t" \
    ".Lj45_%=:\n\t" \
    "s_waitcnt lgkmcnt(0)\n\t" \
    "s_xor_b32 s42, s43, 2048\n\t" \
    "buffer_store_dwordx4 v[160:163], v19, s[4:7], s42 offen offset:0 sc1\n\t" \
    "s_xor_b32 s42, s43, 3072\n\t" \
    "buffer_store_dwordx4 v[164:167], v19, s[4:7], s42 offen offset:0 sc1\n\t" \
    ".Lskip_%=:\n\t" \
    "s_waitcnt lgkmcnt(0)\n\t" \
    "s_nop 7\n\t" \
    ".Lh46_%=:\n\t" \
    "s_memrealtime s[22:23]\n\t" \
    "s_waitcnt lgkmcnt(0)\n\t" \
    "s_sub_u32 s22, s22, s35\n\t" \
    "s_cmp_lt_u32 s22, 405\n\t" \
    "s_cbranch_scc1 .Lh46_%=\n\t" \
    "s_and_b32 s52, s2, 7\n\t" \
    "s_lshl_b32 s52, s52, 7\n\t" \
    "s_add_u32 s52, s52, 34669568\n\t" \
    "s_buffer_load_dword s36, s[4:7], s52\n\t" \
    "s_and_b32 s37, s24, 3\n\t" \
    "s_mul_i32 s37, s37, 8486912\n\t" \
    "s_add_u32 s53, s53, s37\n\t" \
    "s_and_b32 s37, s24, 4\n\t" \
    "s_lshl_b32 s39, s37, 11\n\t" \
    "s_add_u32 s53, s53, s39\n\t" \
    "s_add_u32 s37, s37, s24\n\t" \
    "s_andn2_b32 s39, s26, 1\n\t" \
    "s_lshl3_add_u32 s37, s39, s37\n\t" \
    "s_mul_i32 s37, s37, 43008\n\t" \
    "s_mul_i32 s39, s27, 4992\n\t" \
    "s_add_u32 s51, s37, s39\n\t" \
    "s_cmp_eq_u32 s27, 8\n\t" \
    "s_movk_i32 s10, 4992\n\t" \
    "s_cselect_b32 s10, 3072, s10\n\t" \
    "s_mov_b32 s11, 0x20000\n\t" \
    "s_and_b32 s37, s2, 0x70\n\t" \
    "s_lshl_b32 s37, s37, 1\n\t" \
    "v_lshrrev_b32 v11, 7, v0\n\t" \
    "v_lshl_add_u32 v11, v11, 2, s37\n\t" \
    "v_min_u32 v11, 248, v11\n\t" \
    "v_mul_u32_u24 v46, 147, v11\n\t" \
    "v_lshrrev_b32 v46, 12, v46\n\t" \
    "v_mul_u32_u24 v41, 28, v46\n\t" \
    "v_sub_u32 v11, v11, v41\n\t" \
    "v_bfe_u32 v41, v0, 4, 3\n\t" \
    "v_min_u32 v41, 4, v41\n\t" \
    "v_add3_u32 v11, v11, v41, -1\n\t" \
    "v_add_u32 v41, 28, v11\n\t" \
    "v_min_u32 v11, v11, v41\n\t" \
    "v_and_b32 v1, 1, v11\n\t" \
    "v_lshrrev_b32 v11, 1, v11\n\t" \
    "v_mad_u32_u24 v11, v11, 9, v46\n\t" \
    "v_lshl_add_u32 v11, v1, 7, v11\n\t" \
    "v_and_b32 v3, 31, v11\n\t" \
    "v_and_b32 v50, 24, v3\n\t" \
    "v_add_u32 v3, v3, v50\n\t" \
    "v_lshrrev_b32 v50, 5, v11\n\t" \
    "v_lshlrev_b32 v50, 7, v50\n\t" \
    "v_lshl_or_b32 v51, v3, 10, v50\n\t" \
    "s_mov_b32 s40, 0\n\t" \
    "s_waitcnt lgkmcnt(0)\n\t" \
    ".Lr4_%=:\n\t" \
    "s_xor_b32 s36, s36, s34\n\t" \
    "s_andn2_b32 s37, s36, 14\n\t" \
    "s_cbranch_scc1 .Lr5_%=\n\t" \
    ".Lr6_%=:\n\t" \
    "s_bfe_u32 s36, s36, 0x30001\n\t" \
    "s_add_u32 s56, s2, s36\n\t" \
    "s_and_b32 s56, s56, 7\n\t" \
    "s_lshr_b32 s37, s2, 7\n\t" \
    "s_lshl1_add_u32 s56, s56, s37\n\t" \
    "s_and_b32 s37, s56, 7\n\t" \
    "s_mul_i32 s48, s37, 4243456\n\t" \
    "s_and_b32 s37, s56, 8\n\t" \
    "s_lshl_b32 s37, s37, 10\n\t" \
    "s_add_u32 s48, s48, s37\n\t" \
    "s_add_u32 s49, s48, 524288\n\t" \
    "s_add_u32 s50, s48, 1048576\n\t" \
    "s_xor_b32 s37, s48, 0x2000\n\t" \
    "s_add_u32 s37, s37, 2097152\n\t" \
    "s_add_u32 s45, s45, s37\n\t" \
    "s_add_u32 s46, s46, s37\n\t" \
    "s_cmp_lt_u32 s24, 4\n\t" \
    "s_cselect_b32 s47, s46, 0x80000000\n\t" \
    "v_add_u32 v17, s45, v0\n\t" \
    "v_add_u32 v19, s46, v0\n\t" \
    "s_lshr_b32 s37, s24, 2\n\t" \
    "s_mul_i32 s37, s37, 6\n\t" \
    "s_mul_i32 s39, s56, 7\n\t" \
    "s_add_u32 s37, s37, s39\n\t" \
    "s_lshr_b32 s37, s37, 4\n\t" \
    "s_and_b32 s39, s24, 3\n\t" \
    "s_mul_i32 s39, s39, 63\n\t" \
    "s_add_u32 s37, s37, s39\n\t" \
    "v_lshrrev_b32 v41, 4, v0\n\t" \
    "v_min_u32 v41, 8, v41\n\t" \
    "v_mad_u32_u24 v41, v41, 7, s37\n\t" \
    "v_add_u32 v46, 2, v41\n\t" \
    "v_lshrrev_b32 v46, 7, v46\n\t" \
    "v_lshl_add_u32 v41, v46, 1, v41\n\t" \
    "v_and_b32 v46, 31, v41\n\t" \
    "v_and_b32 v1, 24, v46\n\t" \
    "v_add_u32 v46, v46, v1\n\t" \
    "v_lshrrev_b32 v1, 5, v41\n\t" \
    "v_lshlrev_b32 v1, 7, v1\n\t" \
    "v_lshl_or_b32 v11, v46, 10, v1\n\t" \
    "v_add_u32 v18, s36, v18\n\t" \
    "v_lshlrev_b32 v18, 8, v18\n\t" \
    "v_and_b32 v18, 0x400, v18\n\t" \
    "v_xor_b32 v42, v42, v18\n\t" \
    "s_sub_u32 s37, s24, s36\n\t" \
    "s_and_b32 s37, s37, 7\n\t" \
    "s_mul_i32 s39, s27, 28\n\t" \
    "s_add_u32 s39, s39, s26\n\t" \
    "s_lshr_b32 s39, s39, 5\n\t" \
    "s_lshl4_add_u32 s37, s39, s37\n\t" \
    "s_lshl_b32 s52, s37, 7\n\t" \
    "s_andn2_b32 s37, s52, 0x1fff\n\t" \
    "s_add_u32 s52, s52, s37\n\t" \
    "s_and_b32 s37, s56, 6\n\t" \
    "s_lshl_b32 s37, s37, 15\n\t" \
    "s_and_b32 s39, s48, 0x2000\n\t" \
    "s_add_u32 s37, s37, s39\n\t" \
    "s_add_u32 s33, s37, 33554432\n\t" \
    "s_add_u32 s55, s55, s39\n\t" \
    "s_nop 0\n\t" \
    "v_readlane_b32 s12, v11, 0\n\t" \
    "v_readlane_b32 s13, v11, 1\n\t" \
    "v_readlane_b32 s14, v11, 2\n\t" \
    "v_readlane_b32 s15, v11, 3\n\t" \
    "v_readlane_b32 s16, v11, 4\n\t" \
    "v_readlane_b32 s17, v11, 5\n\t" \
    "v_readlane_b32 s18, v11, 6\n\t" \
    "v_readlane_b32 s19, v11, 7\n\t" \
    "v_readlane_b32 s3, v11, 8\n\t" \
    "s_add_u32 s12, s12, s33\n\t" \
    "s_add_u32 s13, s13, s33\n\t" \
    "s_add_u32 s14, s14, s33\n\t" \
    "s_add_u32 s15, s15, s33\n\t" \
    "s_add_u32 s16, s16, s33\n\t" \
    "s_add_u32 s17, s17, s33\n\t" \
    "s_add_u32 s18, s18, s33\n\t" \
    "s_add_u32 s19, s19, s33\n\t" \
    "s_add_u32 s3, s3, s33\n\t" \
    "s_waitcnt vmcnt(0)\n\t" \
    "s_mov_b64 exec, 1\n\t" \
    "ds_add_rtn_u32 v3, v9, v9 offset:12\n\t" \
    "s_add_u32 s37, s32, 786432\n\t" \
    "s_waitcnt lgkmcnt(0)\n\t" \
    "v_cmp_eq_u32 vcc, 1098496, v3\n\t" \
    "s_mov_b64 exec, -1\n\t" \
    "s_nop 0\n\t" \
    "s_cbranch_vccz .La22_%=\n\t" \
    "buffer_store_dwordx4 v[148:151], v2, s[4:7], s37 offen sc1\n\t" \
    ".La22_%=:\n\t" \
    "s_mov_b64 exec, -1\n\t" \
    ".Lh47_%=:\n\t" \
    "s_memrealtime s[22:23]\n\t" \
    "s_waitcnt lgkmcnt(0)\n\t" \
    "s_sub_u32 s22, s22, s35\n\t" \
    "s_cmp_lt_u32 s22, 472\n\t" \
    "s_cbranch_scc1 .Lh47_%=\n\t" \
    "s_mov_b32 s37, 34670592\n\t" \
    "s_buffer_load_dword s56, s[4:7], s37 offset:0x0\n\t" \
    "s_buffer_load_dword s57, s[4:7], s37 offset:0x80\n\t" \
    "s_buffer_load_dword s58, s[4:7], s37 offset:0x100\n\t" \
    "s_buffer_load_dword s59, s[4:7], s37 offset:0x180\n\t" \
    "s_buffer_load_dword s60, s[4:7], s37 offset:0x200\n\t" \
    "s_buffer_load_dword s61, s[4:7], s37 offset:0x280\n\t" \
    "s_cmp_lg_u32 s24, 0\n\t" \
    "s_cbranch_scc1 .Lb15_%=\n\t" \
    "s_mov_b32 s40, 0\n\t" \
    "s_add_u32 s38, s33, 524288\n\t" \
    ".Ll15_%=:\n\t" \
    "buffer_load_dword v50, v51, s[4:7], s38 offen sc1\n\t" \
    "s_waitcnt vmcnt(0)\n\t" \
    "v_cmp_ne_u32 vcc, s34, v50\n\t" \
    "s_add_u32 s40, s40, 1\n\t" \
    "s_cmp_ge_u32 s40, 65536\n\t" \
    "s_cbranch_scc1 .Lb15_%=\n\t" \
    "s_cbranch_vccz .Lb15_%=\n\t" \
    "s_sleep 2\n\t" \
    "s_branch .Ll15_%=\n\t" \
    ".Lb15_%=:\n\t" \
    "s_waitcnt lgkmcnt(0)\n\t" \
    "s_barrier\n\t" \
    "buffer_load_dwordx4 v[176:179], v36, s[4:7], s48 offen offset:0 sc1\n\t" \
    "buffer_load_dwordx4 v[180:183], v36, s[4:7], s48 offen offset:1024 sc1\n\t" \
    "buffer_load_dwordx4 v[184:187], v36, s[4:7], s48 offen offset:256 sc1\n\t" \
    "buffer_load_dwordx4 v[188:191], v36, s[4:7], s48 offen offset:1280 sc1\n\t" \
    "buffer_load_dwordx4 v[60:63], v37, s[4:7], s48 offen offset:0 sc1\n\t" \
    "s_cmp_lt_u32 s24, 4\n\t" \
    "s_cbranch_scc1 .Lp48_%=\n\t" \
    "s_setprio 1\n\t" \
    ".Lp48_%=:\n\t" \
    "s_mov_b32 s40, 0\n\t" \
    "s_waitcnt lgkmcnt(0)\n\t" \
    ".Lq4_%=:\n\t" \
    "s_mov_b32 s37, 0\n\t" \
    "s_mov_b32 s0, 0\n\t" \
    "s_xor_b32 s61, s61, s34\n\t" \
    "s_or_b32 s37, s37, s61\n\t" \
    "s_lshl1_add_u32 s0, s0, s61\n\t" \
    "s_xor_b32 s60, s60, s34\n\t" \
    "s_or_b32 s37, s37, s60\n\t" \
    "s_lshl1_add_u32 s0, s0, s60\n\t" \
    "s_xor_b32 s59, s59, s34\n\t" \
    "s_or_b32 s37, s37, s59\n\t" \
    "s_lshl1_add_u32 s0, s0, s59\n\t" \
    "s_xor_b32 s58, s58, s34\n\t" \
    "s_or_b32 s37, s37, s58\n\t" \
    "s_lshl1_add_u32 s0, s0, s58\n\t" \
    "s_xor_b32 s57, s57, s34\n\t" \
    "s_or_b32 s37, s37, s57\n\t" \
    "s_lshl1_add_u32 s0, s0, s57\n\t" \
    "s_xor_b32 s56, s56, s34\n\t" \
    "s_or_b32 s37, s37, s56\n\t" \
    "s_lshl1_add_u32 s0, s0, s56\n\t" \
    "s_andn2_b32 s37, s37, 2\n\t" \
    "s_cbranch_scc1 .Lq5_%=\n\t" \
    ".Lq6_%=:\n\t" \
    "s_lshr_b32 s0, s0, 1\n\t" \
    "s_add_u32 s37, s36, 3\n\t" \
    "s_bitcmp1_b32 s37, 2\n\t" \
    "s_cselect_b32 s37, 51, 12\n\t" \
    "s_xor_b32 s0, s0, s37\n\t" \
    "v_add_u32 v41, 344064, v39\n\t" \
    "v_lshrrev_b32 v46, 21, v41\n\t" \
    "v_bfe_u32 v46, s0, v46, 1\n\t" \
    "v_lshrrev_b32 v1, 3, v41\n\t" \
    "v_lshlrev_b32 v46, 10, v46\n\t" \
    "v_and_b32 v1, 0x400, v1\n\t" \
    "v_xor_b32 v46, v46, v1\n\t" \
    "v_xor_b32 v18, v42, v46\n\t" \
    "v_lshrrev_b32 v46, 21, v39\n\t" \
    "v_bfe_u32 v46, s0, v46, 1\n\t" \
    "v_lshrrev_b32 v1, 3, v39\n\t" \
    "v_lshlrev_b32 v46, 10, v46\n\t" \
    "v_and_b32 v1, 0x400, v1\n\t" \
    "v_xor_b32 v46, v46, v1\n\t" \
    "v_xor_b32 v42, v42, v46\n\t" \
    "s_lshr_b32 s37, s10, 1\n\t" \
    "s_add_u32 s37, s37, s51\n\t" \
    "s_and_b32 s39, s8, 0x1fffff\n\t" \
    "s_add_u32 s37, s37, s39\n\t" \
    "s_lshr_b32 s39, s37, 21\n\t" \
    "s_lshr_b32 s39, s0, s39\n\t" \
    "s_xor_b32 s37, s37, s48\n\t" \
    "s_lshr_b32 s37, s37, 13\n\t" \
    "s_xor_b32 s37, s37, s39\n\t" \
    "s_and_b32 s37, s37, 1\n\t" \
    "s_mul_i32 s37, s37, 172032\n\t" \
    "s_add_u32 s37, s37, s51\n\t" \
    "s_add_u32 s8, s8, s37\n\t" \
    "s_addc_u32 s9, s9, 0\n\t" \
    "s_waitcnt vmcnt(4)\n\t" \
    "v_mfma_f32_32x32x16_bf16 v[192:207], v[52:55], v[176:179], 0\n\t" \
    "buffer_load_dwordx4 v[4:7], v36, s[4:7], s49 offen offset:0 sc1\n\t" \
    "s_waitcnt vmcnt(4)\n\t" \
    "v_mfma_f32_32x32x16_bf16 v[192:207], v[56:59], v[180:183], v[192:207]\n\t" \
    "buffer_load_dwordx4 v[20:23], v36, s[4:7], s49 offen offset:1024 sc1\n\t" \
    "s_waitcnt vmcnt(4)\n\t" \
    "v_mfma_f32_32x32x16_bf16 v[208:223], v[52:55], v[184:187], 0\n\t" \
    "buffer_load_dwordx4 v[64:67], v36, s[4:7], s49 offen offset:256 sc1\n\t" \
    "s_waitcnt vmcnt(4)\n\t" \
    "v_mfma_f32_32x32x16_bf16 v[208:223], v[56:59], v[188:191], v[208:223]\n\t" \
    "buffer_load_dwordx4 v[68:71], v36, s[4:7], s49 offen offset:1280 sc1\n\t" \
    "v_mfma_f32_32x32x16_bf16 v[224:239], v[56:59], v[176:179], 0\n\t" \
    "s_add_u32 m0, s44, 0\n\t" \
    "s_nop 8\n\t" \
    "v_cvt_pk_bf16_f32 v9, v192, v208\n\t" \
    "v_cvt_pk_bf16_f32 v3, v193, v209\n\t" \
    "v_cvt_pk_bf16_f32 v51, v194, v210\n\t" \
    "v_cvt_pk_bf16_f32 v50, v195, v211\n\t" \
    "v_cvt_pk_bf16_f32 v8, v196, v212\n\t" \
    "s_waitcnt vmcnt(4)\n\t" \
    "v_permlane16_swap_b32 v60, v180\n\t" \
    "v_permlane16_swap_b32 v61, v181\n\t" \
    "ds_write_addtid_b32 v9 offset:3616\n\t" \
    "ds_write_addtid_b32 v3 offset:3872\n\t" \
    "ds_write_addtid_b32 v51 offset:4128\n\t" \
    "ds_write_addtid_b32 v50 offset:4384\n\t" \
    "ds_write_addtid_b32 v8 offset:4640\n\t" \
    "v_permlane16_swap_b32 v62, v182\n\t" \
    "v_permlane16_swap_b32 v63, v183\n\t" \
    "v_cvt_pk_bf16_f32 v16, v197, v213\n\t" \
    "v_cvt_pk_bf16_f32 v41, v198, v214\n\t" \
    "v_cvt_pk_bf16_f32 v39, v199, v215\n\t" \
    "v_cvt_pk_bf16_f32 v1, v200, v216\n\t" \
    "v_cvt_pk_bf16_f32 v46, v201, v217\n\t" \
    "v_mfma_f32_32x32x16_bf16 v[240:255], v[56:59], v[184:187], 0\n\t" \
    "v_permlane16_swap_b32 v180, v188\n\t" \
    "buffer_load_dwordx4 v[72:75], v37, s[4:7], s49 offen offset:0 sc1\n\t" \
    "ds_write_addtid_b32 v16 offset:4896\n\t" \
    "ds_write_addtid_b32 v41 offset:5152\n\t" \
    "ds_write_addtid_b32 v39 offset:5408\n\t" \
    "ds_write_addtid_b32 v1 offset:5664\n\t" \
    "ds_write_addtid_b32 v46 offset:5920\n\t" \
    "v_permlane16_swap_b32 v181, v189\n\t" \
    "v_permlane16_swap_b32 v182, v190\n\t" \
    "v_cvt_pk_bf16_f32 v9, v202, v218\n\t" \
    "v_cvt_pk_bf16_f32 v3, v203, v219\n\t" \
    "v_cvt_pk_bf16_f32 v51, v204, v220\n\t" \
    "v_cvt_pk_bf16_f32 v50, v205, v221\n\t" \
    "v_permlane16_swap_b32 v183, v191\n\t" \
    "v_mfma_f32_32x32x16_bf16 v[224:239], v[52:55], v[60:63], v[224:239]\n\t" \
    "ds_write_addtid_b32 v9 offset:6176\n\t" \
    "ds_write_addtid_b32 v3 offset:6432\n\t" \
    "ds_write_addtid_b32 v51 offset:6688\n\t" \
    "ds_write_addtid_b32 v50 offset:6944\n\t" \
    "v_mfma_f32_32x32x16_bf16 v[240:255], v[52:55], v[180:183], v[240:255]\n\t" \
    ".Lh49_%=:\n\t" \
    "s_memrealtime s[22:23]\n\t" \
    "s_waitcnt lgkmcnt(0)\n\t" \
    "s_sub_u32 s22, s22, s35\n\t" \
    "s_cmp_lt_u32 s22, 585\n\t" \
    "s_cbranch_scc1 .Lh49_%=\n\t" \
    "s_buffer_load_dword s12, s[4:7], s12 offset:0xc0000\n\t" \
    "s_buffer_load_dword s13, s[4:7], s13 offset:0xc0000\n\t" \
    "s_buffer_load_dword s14, s[4:7], s14 offset:0xc0000\n\t" \
    "s_buffer_load_dword s15, s[4:7], s15 offset:0xc0000\n\t" \
    "s_buffer_load_dword s16, s[4:7], s16 offset:0xc0000\n\t" \
    "s_buffer_load_dword s17, s[4:7], s17 offset:0xc0000\n\t" \
    "s_buffer_load_dword s18, s[4:7], s18 offset:0xc0000\n\t" \
    "s_buffer_load_dword s19, s[4:7], s19 offset:0xc0000\n\t" \
    "s_buffer_load_dword s3, s[4:7], s3 offset:0xc0000\n\t" \
    "s_waitcnt vmcnt(4)\n\t" \
    "v_mfma_f32_32x32x16_bf16 v[192:207], v[52:55], v[4:7], 0\n\t" \
    "v_cvt_pk_bf16_f32 v8, v224, v240\n\t" \
    "v_cvt_pk_bf16_f32 v16, v225, v241\n\t" \
    "v_cvt_pk_bf16_f32 v41, v226, v242\n\t" \
    "v_cvt_pk_bf16_f32 v39, v227, v243\n\t" \
    "v_cvt_pk_bf16_f32 v1, v228, v244\n\t" \
    "buffer_load_dwordx4 v[76:79], v36, s[4:7], s50 offen offset:0 sc1\n\t" \
    "ds_write_addtid_b32 v8 offset:0\n\t" \
    "ds_write_addtid_b32 v16 offset:256\n\t" \
    "ds_write_addtid_b32 v41 offset:512\n\t" \
    "ds_write_addtid_b32 v39 offset:768\n\t" \
    "ds_write_addtid_b32 v1 offset:1024\n\t" \
    "s_waitcnt vmcnt(4)\n\t" \
    "v_mfma_f32_32x32x16_bf16 v[192:207], v[56:59], v[20:23], v[192:207]\n\t" \
    "buffer_load_dwordx4 v[80:83], v36, s[4:7], s50 offen offset:1024 sc1\n\t" \
    "v_cvt_pk_bf16_f32 v46, v229, v245\n\t" \
    "v_cvt_pk_bf16_f32 v9, v230, v246\n\t" \
    "v_cvt_pk_bf16_f32 v3, v231, v247\n\t" \
    "v_cvt_pk_bf16_f32 v51, v232, v248\n\t" \
    "v_cvt_pk_bf16_f32 v50, v233, v249\n\t" \
    "s_waitcnt vmcnt(4)\n\t" \
    "v_mfma_f32_32x32x16_bf16 v[208:223], v[52:55], v[64:67], 0\n\t" \
    "buffer_load_dwordx4 v[100:103], v36, s[4:7], s50 offen offset:256 sc1\n\t" \
    "ds_write_addtid_b32 v46 offset:1280\n\t" \
    "ds_write_addtid_b32 v9 offset:1536\n\t" \
    "ds_write_addtid_b32 v3 offset:1792\n\t" \
    "ds_write_addtid_b32 v51 offset:2048\n\t" \
    "ds_write_addtid_b32 v50 offset:2304\n\t" \
    "v_cvt_pk_bf16_f32 v8, v234, v250\n\t" \
    "v_cvt_pk_bf16_f32 v16, v235, v251\n\t" \
    "v_cvt_pk_bf16_f32 v41, v236, v252\n\t" \
    "v_cvt_pk_bf16_f32 v39, v237, v253\n\t" \
    "buffer_load_dwordx4 v[104:107], v36, s[4:7], s50 offen offset:1280 sc1\n\t" \
    "s_waitcnt vmcnt(5)\n\t" \
    "v_mfma_f32_32x32x16_bf16 v[208:223], v[56:59], v[68:71], v[208:223]\n\t" \
    "ds_write_addtid_b32 v8 offset:2560\n\t" \
    "ds_write_addtid_b32 v16 offset:2816\n\t" \
    "ds_write_addtid_b32 v41 offset:3072\n\t" \
    "ds_write_addtid_b32 v39 offset:3328\n\t" \
    "buffer_load_dwordx4 v[88:91], v37, s[4:7], s50 offen offset:0 sc1\n\t" \
    "s_waitcnt lgkmcnt(0)\n\t" \
    "s_xor_b32 s12, s12, s34\n\t" \
    "s_xor_b32 s13, s13, s34\n\t" \
    "s_xor_b32 s14, s14, s34\n\t" \
    "s_xor_b32 s15, s15, s34\n\t" \
    "s_xor_b32 s16, s16, s34\n\t" \
    "s_xor_b32 s17, s17, s34\n\t" \
    "s_xor_b32 s18, s18, s34\n\t" \
    "s_xor_b32 s19, s19, s34\n\t" \
    "s_xor_b32 s3, s3, s34\n\t" \
    "s_or_b32 s12, s12, s13\n\t" \
    "s_or_b32 s12, s12, s14\n\t" \
    "s_or_b32 s12, s12, s15\n\t" \
    "s_or_b32 s12, s12, s16\n\t" \
    "s_or_b32 s12, s12, s17\n\t" \
    "s_or_b32 s12, s12, s18\n\t" \
    "s_or_b32 s12, s12, s19\n\t" \
    "s_or_b32 s12, s12, s3\n\t" \
    "s_cbranch_scc1 .Lq1025_%=\n\t" \
    ".Lb24_%=:\n\t" \
    "s_waitcnt lgkmcnt(0)\n\t" \
    "s_barrier\n\t" \
    "ds_read_b128 v[92:95], v44 offset:0\n\t" \
    "s_mov_b32 s45, 0\n\t" \
    "buffer_load_dwordx4 v[12:15], v17, s[4:7], s45 offen offset:0 sc1\n\t" \
    "ds_read_b128 v[108:111], v44 offset:128\n\t" \
    "ds_read_b128 v[112:115], v44 offset:29184\n\t" \
    "ds_read_b128 v[84:87], v44 offset:29312\n\t" \
    "ds_read_b128 v[96:99], v45 offset:0\n\t" \
    "buffer_load_dwordx4 v[120:123], v17, s[4:7], s45 offen offset:1024 sc1\n\t" \
    "ds_read_b128 v[124:127], v45 offset:128\n\t" \
    "ds_read_b128 v[116:119], v45 offset:29184\n\t" \
    "ds_read_b128 v[128:131], v45 offset:29312\n\t" \
    "v_mfma_f32_32x32x16_bf16 v[224:239], v[56:59], v[4:7], 0\n\t" \
    "v_cvt_pk_bf16_f32 v46, v192, v208\n\t" \
    "v_cvt_pk_bf16_f32 v9, v193, v209\n\t" \
    "v_cvt_pk_bf16_f32 v3, v194, v210\n\t" \
    "v_cvt_pk_bf16_f32 v51, v195, v211\n\t" \
    "v_cvt_pk_bf16_f32 v50, v196, v212\n\t" \
    "s_waitcnt vmcnt(7)\n\t" \
    "v_permlane16_swap_b32 v72, v20\n\t" \
    "v_permlane16_swap_b32 v73, v21\n\t" \
    "s_mov_b32 s46, 114688\n\t" \
    "buffer_load_dwordx4 v[132:135], v17, s[4:7], s46 offen offset:0 sc1\n\t" \
    "ds_write_addtid_b32 v46 offset:61984\n\t" \
    "ds_write_addtid_b32 v9 offset:62240\n\t" \
    "ds_write_addtid_b32 v3 offset:62496\n\t" \
    "ds_write_addtid_b32 v51 offset:62752\n\t" \
    "ds_write_addtid_b32 v50 offset:63008\n\t" \
    "v_permlane16_swap_b32 v74, v22\n\t" \
    "v_permlane16_swap_b32 v75, v23\n\t" \
    "v_cvt_pk_bf16_f32 v36, v197, v213\n\t" \
    "v_cvt_pk_bf16_f32 v8, v198, v214\n\t" \
    "v_cvt_pk_bf16_f32 v16, v199, v215\n\t" \
    "v_cvt_pk_bf16_f32 v41, v200, v216\n\t" \
    "v_cvt_pk_bf16_f32 v39, v201, v217\n\t" \
    "v_mfma_f32_32x32x16_bf16 v[240:255], v[56:59], v[64:67], 0\n\t" \
    "v_permlane16_swap_b32 v20, v68\n\t" \
    "buffer_load_dwordx4 v[28:31], v17, s[4:7], s46 offen offset:1024 sc1\n\t" \
    "ds_write_addtid_b32 v36 offset:63264\n\t" \
    "ds_write_addtid_b32 v8 offset:63520\n\t" \
    "ds_write_addtid_b32 v16 offset:63776\n\t" \
    "ds_write_addtid_b32 v41 offset:64032\n\t" \
    "ds_write_addtid_b32 v39 offset:64288\n\t" \
    "v_permlane16_swap_b32 v21, v69\n\t" \
    "v_permlane16_swap_b32 v22, v70\n\t" \
    "v_cvt_pk_bf16_f32 v37, v202, v218\n\t" \
    "v_cvt_pk_bf16_f32 v11, v203, v219\n\t" \
    "v_cvt_pk_bf16_f32 v1, v204, v220\n\t" \
    "v_cvt_pk_bf16_f32 v46, v205, v221\n\t" \
    "v_permlane16_swap_b32 v23, v71\n\t" \
    "v_mfma_f32_32x32x16_bf16 v[224:239], v[52:55], v[72:75], v[224:239]\n\t" \
    "buffer_load_dwordx4 v[136:139], v19, s[4:7], s45 offen offset:0 sc1\n\t" \
    "ds_write_addtid_b32 v37 offset:64544\n\t" \
    "ds_write_addtid_b32 v11 offset:64800\n\t" \
    "ds_write_addtid_b32 v1 offset:65056\n\t" \
    "ds_write_addtid_b32 v46 offset:65312\n\t" \
    "v_mfma_f32_32x32x16_bf16 v[240:255], v[52:55], v[20:23], v[240:255]\n\t" \
    "s_waitcnt vmcnt(9)\n\t" \
    "v_mfma_f32_32x32x16_bf16 v[192:207], v[52:55], v[76:79], 0\n\t" \
    "s_nop 9\n\t" \
    "v_cvt_pk_bf16_f32 v9, v224, v240\n\t" \
    "v_cvt_pk_bf16_f32 v3, v225, v241\n\t" \
    "v_cvt_pk_bf16_f32 v51, v226, v242\n\t" \
    "v_cvt_pk_bf16_f32 v50, v227, v243\n\t" \
    "v_cvt_pk_bf16_f32 v36, v228, v244\n\t" \
    "buffer_load_dwordx4 v[24:27], v19, s[4:7], s46 offen offset:0 sc1\n\t" \
    "ds_write_addtid_b32 v9 offset:58368\n\t" \
    "ds_write_addtid_b32 v3 offset:58624\n\t" \
    "ds_write_addtid_b32 v51 offset:58880\n\t" \
    "ds_write_addtid_b32 v50 offset:59136\n\t" \
    "ds_write_addtid_b32 v36 offset:59392\n\t" \
    "s_waitcnt vmcnt(9)\n\t" \
    "v_mfma_f32_32x32x16_bf16 v[192:207], v[56:59], v[80:83], v[192:207]\n\t" \
    "v_cvt_pk_bf16_f32 v8, v229, v245\n\t" \
    "v_cvt_pk_bf16_f32 v16, v230, v246\n\t" \
    "v_cvt_pk_bf16_f32 v41, v231, v247\n\t" \
    "v_cvt_pk_bf16_f32 v39, v232, v248\n\t" \
    "v_cvt_pk_bf16_f32 v37, v233, v249\n\t" \
    "s_mov_b32 s47, 229376\n\t" \
    "buffer_load_dwordx4 v[32:35], v17, s[4:7], s47 offen offset:0 sc1\n\t" \
    "s_waitcnt vmcnt(9)\n\t" \
    "v_mfma_f32_32x32x16_bf16 v[208:223], v[52:55], v[100:103], 0\n\t" \
    "ds_write_addtid_b32 v8 offset:59648\n\t" \
    "ds_write_addtid_b32 v16 offset:59904\n\t" \
    "ds_write_addtid_b32 v41 offset:60160\n\t" \
    "ds_write_addtid_b32 v39 offset:60416\n\t" \
    "ds_write_addtid_b32 v37 offset:60672\n\t" \
    "buffer_load_dwordx4 v[140:143], v17, s[4:7], s47 offen offset:1024 sc1\n\t" \
    "v_cvt_pk_bf16_f32 v11, v234, v250\n\t" \
    "v_cvt_pk_bf16_f32 v1, v235, v251\n\t" \
    "v_cvt_pk_bf16_f32 v46, v236, v252\n\t" \
    "v_cvt_pk_bf16_f32 v9, v237, v253\n\t" \
    "s_waitcnt vmcnt(9)\n\t" \
    "v_mfma_f32_32x32x16_bf16 v[208:223], v[56:59], v[104:107], v[208:223]\n\t" \
    "ds_write_addtid_b32 v11 offset:60928\n\t" \
    "ds_write_addtid_b32 v1 offset:61184\n\t" \
    "ds_write_addtid_b32 v46 offset:61440\n\t" \
    "ds_write_addtid_b32 v9 offset:61696\n\t" \
    "buffer_load_dwordx4 v[156:159], v19, s[4:7], s47 offen offset:0 sc1\n\t" \
    "s_waitcnt vmcnt(9)\n\t" \
    "s_waitcnt lgkmcnt(0)\n\t" \
    "s_barrier\n\t" \
    "s_memrealtime s[64:65]\n\t" \
    "s_cmp_lg_u32 s24, 7\n\t" \
    "s_cbranch_scc1 .Lar_%=\n\t" \
    "s_waitcnt lgkmcnt(0)\n\t" \
    "v_mov_b32 v149, s64\n\t" \
    "s_and_b32 s37, s48, 8192\n\t" \
    "s_add_u32 s37, s37, s32\n\t" \
    "s_add_u32 s37, s37, 1048576\n\t" \
    "s_mov_b64 exec, 0xff\n\t" \
    "buffer_store_dwordx4 v[148:151], v0, s[4:7], s37 offen sc1\n\t" \
    "s_mov_b64 exec, -1\n\t" \
    ".Lar_%=:\n\t" \
    "v_add_u32 v44, 58368, v44\n\t" \
    "s_bitcmp1_b32 s24, 2\n\t" \
    "s_cbranch_scc1 .Ly29_%=\n\t" \
    "buffer_load_dwordx4 v[168:171], v19, s[4:7], s45 offen offset:1024 sc1\n\t" \
    ".Ly29_%=:\n\t" \
    ".Lz7_%=:\n\t" \
    "s_cbranch_execz .Lz8_%=\n\t" \
    "v_add_u32 v45, 58368, v45\n\t" \
    "ds_read_b128 v[172:175], v44 offset:0\n\t" \
    "ds_read_b128 v[144:147], v44 offset:128\n\t" \
    "ds_read_b128 v[152:155], v44 offset:29184\n\t" \
    "ds_read_b128 v[160:163], v44 offset:29312\n\t" \
    "s_bitcmp1_b32 s24, 2\n\t" \
    "s_cbranch_scc1 .Ly30_%=\n\t" \
    "buffer_load_dwordx4 v[164:167], v19, s[4:7], s46 offen offset:1024 sc1\n\t" \
    ".Ly30_%=:\n\t" \
    "ds_read_b128 v[176:179], v45 offset:0\n\t" \
    "ds_read_b128 v[184:187], v45 offset:128\n\t" \
    "ds_read_b128 v[188:191], v45 offset:29184\n\t" \
    "ds_read_b128 v[60:63], v45 offset:29312\n\t" \
    "v_mfma_f32_32x32x16_bf16 v[224:239], v[56:59], v[76:79], 0\n\t" \
    "v_cvt_pk_bf16_f32 v3, v192, v208\n\t" \
    "v_cvt_pk_bf16_f32 v51, v193, v209\n\t" \
    "v_cvt_pk_bf16_f32 v50, v194, v210\n\t" \
    "v_cvt_pk_bf16_f32 v36, v195, v211\n\t" \
    "v_cvt_pk_bf16_f32 v8, v196, v212\n\t" \
    "s_waitcnt vmcnt(8)\n\t" \
    "v_mfma_f32_16x16x32_bf16 v[180:183], v[12:15], v[92:95], 0\n\t" \
    "v_permlane16_swap_b32 v88, v80\n\t" \
    "s_bitcmp1_b32 s24, 2\n\t" \
    "s_cbranch_scc1 .Ly31_%=\n\t" \
    "buffer_load_dwordx4 v[4:7], v19, s[4:7], s47 offen offset:1024 sc1\n\t" \
    ".Ly31_%=:\n\t" \
    "v_permlane16_swap_b32 v89, v81\n\t" \
    "ds_write_addtid_b32 v3 offset:3616\n\t" \
    "ds_write_addtid_b32 v51 offset:3872\n\t" \
    "ds_write_addtid_b32 v50 offset:4128\n\t" \
    "ds_write_addtid_b32 v36 offset:4384\n\t" \
    "ds_write_addtid_b32 v8 offset:4640\n\t" \
    "v_mfma_f32_16x16x32_bf16 v[64:67], v[12:15], v[108:111], 0\n\t" \
    "v_permlane16_swap_b32 v90, v82\n\t" \
    "v_mfma_f32_16x16x32_bf16 v[68:71], v[12:15], v[112:115], 0\n\t" \
    "v_permlane16_swap_b32 v91, v83\n\t" \
    "v_cvt_pk_bf16_f32 v16, v197, v213\n\t" \
    "v_cvt_pk_bf16_f32 v41, v198, v214\n\t" \
    "v_cvt_pk_bf16_f32 v39, v199, v215\n\t" \
    "v_cvt_pk_bf16_f32 v37, v200, v216\n\t" \
    "v_cvt_pk_bf16_f32 v11, v201, v217\n\t" \
    "s_mov_b32 s45, 344064\n\t" \
    "buffer_load_dwordx4 v[72:75], v17, s[4:7], s45 offen offset:0 sc1\n\t" \
    "v_mfma_f32_32x32x16_bf16 v[240:255], v[56:59], v[100:103], 0\n\t" \
    "v_mfma_f32_16x16x32_bf16 v[20:23], v[12:15], v[84:87], 0\n\t" \
    "v_permlane16_swap_b32 v80, v104\n\t" \
    "ds_write_addtid_b32 v16 offset:4896\n\t" \
    "ds_write_addtid_b32 v41 offset:5152\n\t" \
    "ds_write_addtid_b32 v39 offset:5408\n\t" \
    "ds_write_addtid_b32 v37 offset:5664\n\t" \
    "ds_write_addtid_b32 v11 offset:5920\n\t" \
    "s_waitcnt vmcnt(8)\n\t" \
    "v_mfma_f32_16x16x32_bf16 v[148:151], v[120:123], v[108:111], 0\n\t" \
    "v_permlane16_swap_b32 v81, v105\n\t" \
    "buffer_load_dwordx4 v[76:79], v17, s[4:7], s45 offen offset:1024 sc1\n\t" \
    "v_permlane16_swap_b32 v82, v106\n\t" \
    "v_cvt_pk_bf16_f32 v1, v202, v218\n\t" \
    "v_cvt_pk_bf16_f32 v46, v203, v219\n\t" \
    "v_cvt_pk_bf16_f32 v9, v204, v220\n\t" \
    "v_cvt_pk_bf16_f32 v3, v205, v221\n\t" \
    "v_mfma_f32_16x16x32_bf16 v[64:67], v[120:123], v[92:95], v[64:67]\n\t" \
    "v_permlane16_swap_b32 v83, v107\n\t" \
    "v_mfma_f32_16x16x32_bf16 v[56:59], v[120:123], v[84:87], 0\n\t" \
    "v_mfma_f32_32x32x16_bf16 v[224:239], v[52:55], v[88:91], v[224:239]\n\t" \
    "ds_write_addtid_b32 v1 offset:6176\n\t" \
    "ds_write_addtid_b32 v46 offset:6432\n\t" \
    "ds_write_addtid_b32 v9 offset:6688\n\t" \
    "ds_write_addtid_b32 v3 offset:6944\n\t" \
    "s_mov_b32 s46, 458752\n\t" \
    "buffer_load_dwordx4 v[100:103], v17, s[4:7], s46 offen offset:0 sc1\n\t" \
    "v_mfma_f32_32x32x16_bf16 v[240:255], v[52:55], v[80:83], v[240:255]\n\t" \
    "v_mfma_f32_16x16x32_bf16 v[20:23], v[120:123], v[112:115], v[20:23]\n\t" \
    "s_nop 10\n\t" \
    "v_cvt_pk_bf16_f32 v51, v224, v240\n\t" \
    "v_cvt_pk_bf16_f32 v50, v225, v241\n\t" \
    "v_cvt_pk_bf16_f32 v36, v226, v242\n\t" \
    "v_cvt_pk_bf16_f32 v8, v227, v243\n\t" \
    "v_cvt_pk_bf16_f32 v16, v228, v244\n\t" \
    "s_waitcnt vmcnt(9) lgkmcnt(15)\n\t" \
    "v_mfma_f32_16x16x32_bf16 v[180:183], v[132:135], v[172:175], v[180:183]\n\t" \
    "buffer_load_dwordx4 v[12:15], v17, s[4:7], s46 offen offset:1024 sc1\n\t" \
    "v_mfma_f32_16x16x32_bf16 v[64:67], v[132:135], v[144:147], v[64:67]\n\t" \
    "ds_write_addtid_b32 v51 offset:0\n\t" \
    "ds_write_addtid_b32 v50 offset:256\n\t" \
    "ds_write_addtid_b32 v36 offset:512\n\t" \
    "ds_write_addtid_b32 v8 offset:768\n\t" \
    "ds_write_addtid_b32 v16 offset:1024\n\t" \
    "v_mfma_f32_16x16x32_bf16 v[68:71], v[132:135], v[152:155], v[68:71]\n\t" \
    "v_cvt_pk_bf16_f32 v41, v229, v245\n\t" \
    "v_cvt_pk_bf16_f32 v39, v230, v246\n\t" \
    "v_cvt_pk_bf16_f32 v37, v231, v247\n\t" \
    "v_cvt_pk_bf16_f32 v11, v232, v248\n\t" \
    "v_cvt_pk_bf16_f32 v1, v233, v249\n\t" \
    "s_mov_b32 s47, 573440\n\t" \
    "buffer_load_dwordx4 v[192:195], v17, s[4:7], s47 offen offset:0 sc1\n\t" \
    "v_mfma_f32_16x16x32_bf16 v[20:23], v[132:135], v[160:163], v[20:23]\n\t" \
    "ds_write_addtid_b32 v41 offset:1280\n\t" \
    "ds_write_addtid_b32 v39 offset:1536\n\t" \
    "ds_write_addtid_b32 v37 offset:1792\n\t" \
    "ds_write_addtid_b32 v11 offset:2048\n\t" \
    "ds_write_addtid_b32 v1 offset:2304\n\t" \
    "s_waitcnt vmcnt(10)\n\t" \
    "v_mfma_f32_16x16x32_bf16 v[148:151], v[28:31], v[144:147], v[148:151]\n\t" \
    "buffer_load_dwordx4 v[196:199], v17, s[4:7], s47 offen offset:1024 sc1\n\t" \
    "v_mfma_f32_16x16x32_bf16 v[64:67], v[28:31], v[172:175], v[64:67]\n\t" \
    "v_cvt_pk_bf16_f32 v46, v234, v250\n\t" \
    "v_cvt_pk_bf16_f32 v9, v235, v251\n\t" \
    "v_cvt_pk_bf16_f32 v3, v236, v252\n\t" \
    "v_cvt_pk_bf16_f32 v51, v237, v253\n\t" \
    "v_mfma_f32_16x16x32_bf16 v[56:59], v[28:31], v[160:163], v[56:59]\n\t" \
    "ds_write_addtid_b32 v46 offset:2560\n\t" \
    "ds_write_addtid_b32 v9 offset:2816\n\t" \
    "ds_write_addtid_b32 v3 offset:3072\n\t" \
    "ds_write_addtid_b32 v51 offset:3328\n\t" \
    "buffer_load_dwordx4 v[200:203], v19, s[4:7], s45 offen offset:0 sc1\n\t" \
    "v_mfma_f32_16x16x32_bf16 v[20:23], v[28:31], v[152:155], v[20:23]\n\t" \
    "s_waitcnt lgkmcnt(0)\n\t" \
    "s_barrier\n\t" \
    "ds_read_b128 v[204:207], v10 offset:2048\n\t" \
    "v_mov_b32 v50, 0x10000\n\t" \
    "s_waitcnt vmcnt(11)\n\t" \
    "v_mfma_f32_16x16x32_bf16 v[208:211], v[136:139], v[96:99], 0\n\t" \
    "v_subrev_u32 v44, 58368, v44\n\t" \
    "v_mfma_f32_16x16x32_bf16 v[212:215], v[136:139], v[124:127], 0\n\t" \
    "v_subrev_u32 v45, 58368, v45\n\t" \
    "v_mfma_f32_16x16x32_bf16 v[216:219], v[136:139], v[116:119], 0\n\t" \
    "ds_read_b128 v[220:223], v44 offset:0\n\t" \
    "buffer_load_dwordx4 v[104:107], v19, s[4:7], s46 offen offset:0 sc1\n\t" \
    "v_mfma_f32_16x16x32_bf16 v[88:91], v[136:139], v[128:131], 0\n\t" \
    "ds_read_b128 v[52:55], v44 offset:128\n\t" \
    "s_waitcnt vmcnt(11)\n\t" \
    "v_mfma_f32_16x16x32_bf16 v[208:211], v[24:27], v[176:179], v[208:211]\n\t" \
    "ds_read_b128 v[80:83], v44 offset:29184\n\t" \
    "v_mfma_f32_16x16x32_bf16 v[212:215], v[24:27], v[184:187], v[212:215]\n\t" \
    "ds_read_b128 v[120:123], v44 offset:29312\n\t" \
    "buffer_load_dwordx4 v[132:135], v19, s[4:7], s47 offen offset:0 sc1\n\t" \
    "v_mfma_f32_16x16x32_bf16 v[216:219], v[24:27], v[188:191], v[216:219]\n\t" \
    "ds_read_b128 v[224:227], v45 offset:0\n\t" \
    "v_mfma_f32_16x16x32_bf16 v[88:91], v[24:27], v[60:63], v[88:91]\n\t" \
    "ds_read_b128 v[228:231], v45 offset:128\n\t" \
    "s_waitcnt vmcnt(11) lgkmcnt(5)\n\t" \
    "v_mfma_f32_16x16x32_bf16 v[180:183], v[32:35], v[220:223], v[180:183]\n\t" \
    "ds_read_b128 v[232:235], v45 offset:29184\n\t" \
    "s_bitcmp1_b32 s24, 2\n\t" \
    "s_cbranch_scc1 .Ly32_%=\n\t" \
    "buffer_load_dwordx4 v[236:239], v19, s[4:7], s45 offen offset:1024 sc1\n\t" \
    ".Ly32_%=:\n\t" \
    "s_waitcnt lgkmcnt(5)\n\t" \
    "v_mfma_f32_16x16x32_bf16 v[64:67], v[32:35], v[52:55], v[64:67]\n\t" \
    "ds_read_b128 v[240:243], v45 offset:29312\n\t" \
    "s_waitcnt lgkmcnt(5)\n\t" \
    "v_mfma_f32_16x16x32_bf16 v[68:71], v[32:35], v[80:83], v[68:71]\n\t" \
    "s_waitcnt lgkmcnt(4)\n\t" \
    "v_mfma_f32_16x16x32_bf16 v[20:23], v[32:35], v[120:123], v[20:23]\n\t" \
    "s_bitcmp1_b32 s24, 2\n\t" \
    "s_cbranch_scc1 .Ly33_%=\n\t" \
    "buffer_load_dwordx4 v[244:247], v19, s[4:7], s46 offen offset:1024 sc1\n\t" \
    ".Ly33_%=:\n\t" \
    "s_waitcnt vmcnt(10)\n\t" \
    "v_mfma_f32_16x16x32_bf16 v[148:151], v[140:143], v[52:55], v[148:151]\n\t" \
    "s_waitcnt lgkmcnt(0)\n\t" \
    "s_add_u32 s12, s55, 34603008\n\t" \
    "s_buffer_load_dword s13, s[4:7], s12\n\t" \
    "s_buffer_load_dword s14, s[4:7], s12 offset:0x400\n\t" \
    "v_mfma_f32_16x16x32_bf16 v[64:67], v[140:143], v[220:223], v[64:67]\n\t" \
    "v_mfma_f32_16x16x32_bf16 v[56:59], v[140:143], v[120:123], v[56:59]\n\t" \
    "s_bitcmp1_b32 s24, 2\n\t" \
    "s_cbranch_scc1 .Ly34_%=\n\t" \
    "buffer_load_dwordx4 v[248:251], v19, s[4:7], s47 offen offset:1024 sc1\n\t" \
    ".Ly34_%=:\n\t" \
    "v_mfma_f32_16x16x32_bf16 v[20:23], v[140:143], v[80:83], v[20:23]\n\t" \
    "s_waitcnt vmcnt(9)\n\t" \
    "v_mfma_f32_16x16x32_bf16 v[208:211], v[156:159], v[224:227], v[208:211]\n\t" \
    "v_mfma_f32_16x16x32_bf16 v[212:215], v[156:159], v[228:231], v[212:215]\n\t" \
    "s_mov_b32 s45, 688128\n\t" \
    "buffer_load_dwordx4 v[252:255], v17, s[4:7], s45 offen offset:0 sc1\n\t" \
    "v_mfma_f32_16x16x32_bf16 v[216:219], v[156:159], v[232:235], v[216:219]\n\t" \
    "v_sub_f32 v180, v180, v148\n\t" \
    "v_sub_f32 v181, v181, v149\n\t" \
    "v_sub_f32 v182, v182, v150\n\t" \
    "v_mfma_f32_16x16x32_bf16 v[88:91], v[156:159], v[240:243], v[88:91]\n\t" \
    "v_sub_f32 v183, v183, v151\n\t" \
    "v_sub_f32 v68, v68, v56\n\t" \
    "v_sub_f32 v69, v69, v57\n\t" \
    "v_sub_f32 v70, v70, v58\n\t" \
    "v_sub_f32 v71, v71, v59\n\t" \
    "v_cvt_pk_bf16_f32 v30, v64, v65\n\t" \
    "v_cvt_pk_bf16_f32 v31, v66, v67\n\t" \
    "v_cvt_pk_bf16_f32 v28, v180, v181\n\t" \
    "v_cvt_pk_bf16_f32 v29, v182, v183\n\t" \
    "v_cvt_pk_bf16_f32 v138, v20, v21\n\t" \
    "v_cvt_pk_bf16_f32 v139, v22, v23\n\t" \
    "ds_write_b128 v40, v[28:31] offset:58368\n\t" \
    "v_cvt_pk_bf16_f32 v136, v68, v69\n\t" \
    "v_cvt_pk_bf16_f32 v137, v70, v71\n\t" \
    "ds_write_b128 v40, v[136:139] offset:59392\n\t" \
    "s_waitcnt vmcnt(10)\n\t" \
    "s_cmp_ge_u32 s24, 4\n\t" \
    "s_cbranch_scc1 .Lk53_%=\n\t" \
    "v_mfma_f32_16x16x32_bf16 v[24:27], v[168:171], v[124:127], 0\n\t" \
    "v_mfma_f32_16x16x32_bf16 v[24:27], v[164:167], v[184:187], v[24:27]\n\t" \
    "v_mfma_f32_16x16x32_bf16 v[24:27], v[4:7], v[228:231], v[24:27]\n\t" \
    "v_mfma_f32_16x16x32_bf16 v[32:35], v[168:171], v[128:131], 0\n\t" \
    "v_mfma_f32_16x16x32_bf16 v[32:35], v[164:167], v[60:63], v[32:35]\n\t" \
    "v_mfma_f32_16x16x32_bf16 v[32:35], v[4:7], v[240:243], v[32:35]\n\t" \
    "v_mfma_f32_16x16x32_bf16 v[212:215], v[168:171], v[96:99], v[212:215]\n\t" \
    "v_mfma_f32_16x16x32_bf16 v[212:215], v[164:167], v[176:179], v[212:215]\n\t" \
    "v_mfma_f32_16x16x32_bf16 v[212:215], v[4:7], v[224:227], v[212:215]\n\t" \
    "v_mfma_f32_16x16x32_bf16 v[88:91], v[168:171], v[116:119], v[88:91]\n\t" \
    "s_nop 0\n\t" \
    "v_sub_f32 v208, v208, v24\n\t" \
    "v_sub_f32 v209, v209, v25\n\t" \
    "v_mfma_f32_16x16x32_bf16 v[88:91], v[164:167], v[188:191], v[88:91]\n\t" \
    "v_sub_f32 v210, v210, v26\n\t" \
    "v_sub_f32 v211, v211, v27\n\t" \
    "v_mfma_f32_16x16x32_bf16 v[88:91], v[4:7], v[232:235], v[88:91]\n\t" \
    "v_sub_f32 v216, v216, v32\n\t" \
    "v_sub_f32 v217, v217, v33\n\t" \
    "v_sub_f32 v218, v218, v34\n\t" \
    "v_sub_f32 v219, v219, v35\n\t" \
    ".Lk53_%=:\n\t" \
    "s_waitcnt vmcnt(9)\n\t" \
    "v_mfma_f32_16x16x32_bf16 v[140:143], v[72:75], v[92:95], 0\n\t" \
    "buffer_load_dwordx4 v[156:159], v17, s[4:7], s45 offen offset:1024 sc1\n\t" \
    "v_mfma_f32_16x16x32_bf16 v[148:151], v[72:75], v[108:111], 0\n\t" \
    "v_cvt_pk_bf16_f32 v58, v212, v213\n\t" \
    "v_cvt_pk_bf16_f32 v59, v214, v215\n\t" \
    "v_mfma_f32_16x16x32_bf16 v[64:67], v[72:75], v[112:115], 0\n\t" \
    "v_cvt_pk_bf16_f32 v56, v208, v209\n\t" \
    "v_cvt_pk_bf16_f32 v57, v210, v211\n\t" \
    "v_mfma_f32_16x16x32_bf16 v[180:183], v[72:75], v[84:87], 0\n\t" \
    "v_cvt_pk_bf16_f32 v22, v88, v89\n\t" \
    "v_cvt_pk_bf16_f32 v23, v90, v91\n\t" \
    "s_mov_b32 s46, 802816\n\t" \
    "buffer_load_dwordx4 v[28:31], v17, s[4:7], s46 offen offset:0 sc1\n\t" \
    "s_waitcnt vmcnt(10)\n\t" \
    "v_mfma_f32_16x16x32_bf16 v[68:71], v[76:79], v[108:111], 0\n\t" \
    "ds_write_b128 v40, v[56:59] offset:60432\n\t" \
    "v_cvt_pk_bf16_f32 v20, v216, v217\n\t" \
    "v_mfma_f32_16x16x32_bf16 v[148:151], v[76:79], v[92:95], v[148:151]\n\t" \
    "v_cvt_pk_bf16_f32 v21, v218, v219\n\t" \
    "v_mfma_f32_16x16x32_bf16 v[136:139], v[76:79], v[84:87], 0\n\t" \
    "ds_write_b128 v40, v[20:23] offset:61456\n\t" \
    "buffer_load_dwordx4 v[168:171], v17, s[4:7], s46 offen offset:1024 sc1\n\t" \
    "v_mfma_f32_16x16x32_bf16 v[180:183], v[76:79], v[112:115], v[180:183]\n\t" \
    "s_waitcnt vmcnt(10)\n\t" \
    "v_mfma_f32_16x16x32_bf16 v[140:143], v[100:103], v[172:175], v[140:143]\n\t" \
    "v_mfma_f32_16x16x32_bf16 v[148:151], v[100:103], v[144:147], v[148:151]\n\t" \
    "s_waitcnt lgkmcnt(0)\n\t" \
    "s_xor_b32 s13, s13, s34\n\t" \
    "s_xor_b32 s14, s14, s34\n\t" \
    "s_or_b32 s13, s13, s14\n\t" \
    "s_cbranch_scc1 .Lq1028_%=\n\t" \
    ".Lb27_%=:\n\t" \
    "s_waitcnt lgkmcnt(0)\n\t" \
    "s_barrier\n\t" \
    "s_memrealtime s[16:17]\n\t" \
    "ds_read_b64_tr_b16 v[164:165], v49 offset:58368\n\t" \
    "ds_read_b64_tr_b16 v[166:167], v49 offset:62496\n\t" \
    "ds_read_b64_tr_b16 v[24:25], v49 offset:58624\n\t" \
    "ds_read_b64_tr_b16 v[26:27], v49 offset:62752\n\t" \
    "s_mov_b32 s47, 917504\n\t" \
    "buffer_load_dwordx4 v[4:7], v17, s[4:7], s47 offen offset:0 sc1\n\t" \
    "v_mfma_f32_16x16x32_bf16 v[64:67], v[100:103], v[152:155], v[64:67]\n\t" \
    "ds_read_b64_tr_b16 v[32:33], v49 offset:58384\n\t" \
    "ds_read_b64_tr_b16 v[34:35], v49 offset:62512\n\t" \
    "ds_read_b64_tr_b16 v[212:213], v49 offset:58640\n\t" \
    "ds_read_b64_tr_b16 v[214:215], v49 offset:62768\n\t" \
    "v_mfma_f32_16x16x32_bf16 v[180:183], v[100:103], v[160:163], v[180:183]\n\t" \
    "s_waitcnt vmcnt(10)\n\t" \
    "v_mfma_f32_16x16x32_bf16 v[68:71], v[12:15], v[144:147], v[68:71]\n\t" \
    "buffer_load_dwordx4 v[208:211], v17, s[4:7], s47 offen offset:1024 sc1\n\t" \
    "v_mfma_f32_16x16x32_bf16 v[148:151], v[12:15], v[172:175], v[148:151]\n\t" \
    "s_waitcnt lgkmcnt(6)\n\t" \
    "v_mfma_f32_16x16x32_bf16 v[72:75], v[164:167], v[204:207], 0\n\t" \
    "s_waitcnt lgkmcnt(4)\n\t" \
    "v_mfma_f32_16x16x32_bf16 v[88:91], v[24:27], v[204:207], 0\n\t" \
    "v_mfma_f32_16x16x32_bf16 v[136:139], v[12:15], v[160:163], v[136:139]\n\t" \
    "s_waitcnt lgkmcnt(2)\n\t" \
    "v_mfma_f32_16x16x32_bf16 v[56:59], v[32:35], v[204:207], 0\n\t" \
    "s_waitcnt lgkmcnt(0)\n\t" \
    "v_mfma_f32_16x16x32_bf16 v[216:219], v[212:215], v[204:207], 0\n\t" \
    "v_mfma_f32_16x16x32_bf16 v[180:183], v[12:15], v[152:155], v[180:183]\n\t" \
    "s_cmp_lg_u32 s24, 4\n\t" \
    "s_cbranch_scc1 .Lph_%=\n\t" \
    "s_waitcnt lgkmcnt(0)\n\t" \
    "v_mov_b32 v20, s16\n\t" \
    "v_mov_b32 v21, s16\n\t" \
    "v_mov_b32 v22, s16\n\t" \
    "v_mov_b32 v23, s16\n\t" \
    "buffer_store_dwordx4 v[20:23], v2, s[4:7], s32 offen sc1\n\t" \
    "s_nop 0\n\t" \
    ".Lph_%=:\n\t" \
    "buffer_load_dwordx4 v[76:79], v19, s[4:7], s45 offen offset:0 sc1\n\t" \
    "s_waitcnt vmcnt(11)\n\t" \
    "v_mfma_f32_16x16x32_bf16 v[140:143], v[192:195], v[220:223], v[140:143]\n\t" \
    "v_bfi_b32 v72, s41, v50, v72\n\t" \
    "v_bfi_b32 v88, s41, v50, v88\n\t" \
    "v_cvt_pk_bf16_f32 v45, v74, v75\n\t" \
    "v_mfma_f32_16x16x32_bf16 v[148:151], v[192:195], v[52:55], v[148:151]\n\t" \
    "v_cvt_pk_bf16_f32 v47, v90, v91\n\t" \
    "v_cvt_pk_bf16_f32 v44, v72, v73\n\t" \
    "v_cvt_pk_bf16_f32 v46, v88, v89\n\t" \
    "v_mfma_f32_16x16x32_bf16 v[64:67], v[192:195], v[80:83], v[64:67]\n\t" \
    "v_bfi_b32 v56, s41, v50, v56\n\t" \
    "v_bfi_b32 v216, s41, v50, v216\n\t" \
    "buffer_store_dwordx4 v[44:47], v42, s[4:7], s48 offen offset:0 sc1\n\t" \
    "v_mfma_f32_16x16x32_bf16 v[180:183], v[192:195], v[120:123], v[180:183]\n\t" \
    "v_cvt_pk_bf16_f32 v101, v58, v59\n\t" \
    "v_cvt_pk_bf16_f32 v103, v218, v219\n\t" \
    "v_cvt_pk_bf16_f32 v100, v56, v57\n\t" \
    "s_waitcnt vmcnt(11)\n\t" \
    "v_mfma_f32_16x16x32_bf16 v[68:71], v[196:199], v[52:55], v[68:71]\n\t" \
    "v_cvt_pk_bf16_f32 v102, v216, v217\n\t" \
    "buffer_store_dwordx4 v[100:103], v18, s[4:7], s48 offen offset:128 sc1\n\t" \
    "v_mfma_f32_16x16x32_bf16 v[148:151], v[196:199], v[220:223], v[148:151]\n\t" \
    "v_mfma_f32_16x16x32_bf16 v[136:139], v[196:199], v[120:123], v[136:139]\n\t" \
    "v_mfma_f32_16x16x32_bf16 v[180:183], v[196:199], v[80:83], v[180:183]\n\t" \
    "s_waitcnt vmcnt(11)\n\t" \
    "v_mfma_f32_16x16x32_bf16 v[164:167], v[200:203], v[96:99], 0\n\t" \
    "v_mfma_f32_16x16x32_bf16 v[24:27], v[200:203], v[124:127], 0\n\t" \
    "v_mfma_f32_16x16x32_bf16 v[32:35], v[200:203], v[116:119], 0\n\t" \
    "v_sub_f32 v140, v140, v68\n\t" \
    "v_sub_f32 v141, v141, v69\n\t" \
    "v_mfma_f32_16x16x32_bf16 v[212:215], v[200:203], v[128:131], 0\n\t" \
    "v_sub_f32 v142, v142, v70\n\t" \
    "v_sub_f32 v143, v143, v71\n\t" \
    "s_waitcnt vmcnt(10)\n\t" \
    "v_mfma_f32_16x16x32_bf16 v[164:167], v[104:107], v[176:179], v[164:167]\n\t" \
    "v_sub_f32 v64, v64, v136\n\t" \
    "v_sub_f32 v65, v65, v137\n\t" \
    "v_mfma_f32_16x16x32_bf16 v[24:27], v[104:107], v[184:187], v[24:27]\n\t" \
    "v_sub_f32 v66, v66, v138\n\t" \
    "v_sub_f32 v67, v67, v139\n\t" \
    "v_mfma_f32_16x16x32_bf16 v[32:35], v[104:107], v[188:191], v[32:35]\n\t" \
    "v_cvt_pk_bf16_f32 v14, v148, v149\n\t" \
    "v_cvt_pk_bf16_f32 v15, v150, v151\n\t" \
    "buffer_load_dwordx4 v[20:23], v19, s[4:7], s46 offen offset:0 sc1\n\t" \
    "v_mfma_f32_16x16x32_bf16 v[212:215], v[104:107], v[60:63], v[212:215]\n\t" \
    "v_cvt_pk_bf16_f32 v12, v140, v141\n\t" \
    "v_cvt_pk_bf16_f32 v13, v142, v143\n\t" \
    "s_waitcnt vmcnt(10)\n\t" \
    "v_mfma_f32_16x16x32_bf16 v[164:167], v[132:135], v[224:227], v[164:167]\n\t" \
    "v_cvt_pk_bf16_f32 v74, v180, v181\n\t" \
    "v_cvt_pk_bf16_f32 v75, v182, v183\n\t" \
    "v_mfma_f32_16x16x32_bf16 v[24:27], v[132:135], v[228:231], v[24:27]\n\t" \
    "ds_write_b128 v40, v[12:15] offset:0\n\t" \
    "v_cvt_pk_bf16_f32 v72, v64, v65\n\t" \
    "buffer_load_dwordx4 v[88:91], v19, s[4:7], s47 offen offset:0 sc1\n\t" \
    "v_mfma_f32_16x16x32_bf16 v[32:35], v[132:135], v[232:235], v[32:35]\n\t" \
    "v_cvt_pk_bf16_f32 v73, v66, v67\n\t" \
    "ds_write_b128 v40, v[72:75] offset:1024\n\t" \
    "v_mfma_f32_16x16x32_bf16 v[212:215], v[132:135], v[240:243], v[212:215]\n\t" \
    "s_waitcnt vmcnt(11)\n\t" \
    "s_cmp_ge_u32 s24, 4\n\t" \
    "s_cbranch_scc1 .Lk54_%=\n\t" \
    "v_mfma_f32_16x16x32_bf16 v[44:47], v[236:239], v[124:127], 0\n\t" \
    "v_mfma_f32_16x16x32_bf16 v[44:47], v[244:247], v[184:187], v[44:47]\n\t" \
    "v_mfma_f32_16x16x32_bf16 v[44:47], v[248:251], v[228:231], v[44:47]\n\t" \
    "v_mfma_f32_16x16x32_bf16 v[192:195], v[236:239], v[128:131], 0\n\t" \
    "v_mfma_f32_16x16x32_bf16 v[192:195], v[244:247], v[60:63], v[192:195]\n\t" \
    "v_mfma_f32_16x16x32_bf16 v[192:195], v[248:251], v[240:243], v[192:195]\n\t" \
    "v_mfma_f32_16x16x32_bf16 v[24:27], v[236:239], v[96:99], v[24:27]\n\t" \
    "v_mfma_f32_16x16x32_bf16 v[24:27], v[244:247], v[176:179], v[24:27]\n\t" \
    "v_mfma_f32_16x16x32_bf16 v[24:27], v[248:251], v[224:227], v[24:27]\n\t" \
    "v_mfma_f32_16x16x32_bf16 v[212:215], v[236:239], v[116:119], v[212:215]\n\t" \
    "s_nop 0\n\t" \
    "v_sub_f32 v164, v164, v44\n\t" \
    "v_sub_f32 v165, v165, v45\n\t" \
    "v_mfma_f32_16x16x32_bf16 v[212:215], v[244:247], v[188:191], v[212:215]\n\t" \
    "v_sub_f32 v166, v166, v46\n\t" \
    "v_sub_f32 v167, v167, v47\n\t" \
    "v_mfma_f32_16x16x32_bf16 v[212:215], v[248:251], v[232:235], v[212:215]\n\t" \
    "v_sub_f32 v32, v32, v192\n\t" \
    "v_sub_f32 v33, v33, v193\n\t" \
    "v_sub_f32 v34, v34, v194\n\t" \
    "v_sub_f32 v35, v35, v195\n\t" \
    ".Lk54_%=:\n\t" \
    "s_waitcnt vmcnt(10)\n\t" \
    "v_mfma_f32_16x16x32_bf16 v[56:59], v[252:255], v[92:95], 0\n\t" \
    "s_bitcmp1_b32 s24, 2\n\t" \
    "s_cbranch_scc1 .Ly35_%=\n\t" \
    "buffer_load_dwordx4 v[216:219], v19, s[4:7], s45 offen offset:1024 sc1\n\t" \
    ".Ly35_%=:\n\t" \
    "v_mfma_f32_16x16x32_bf16 v[100:103], v[252:255], v[108:111], 0\n\t" \
    "v_cvt_pk_bf16_f32 v198, v24, v25\n\t" \
    "v_cvt_pk_bf16_f32 v199, v26, v27\n\t" \
    "v_mfma_f32_16x16x32_bf16 v[200:203], v[252:255], v[112:115], 0\n\t" \
    "v_cvt_pk_bf16_f32 v196, v164, v165\n\t" \
    "v_cvt_pk_bf16_f32 v197, v166, v167\n\t" \
    "v_mfma_f32_16x16x32_bf16 v[68:71], v[252:255], v[84:87], 0\n\t" \
    "v_cvt_pk_bf16_f32 v138, v212, v213\n\t" \
    "v_cvt_pk_bf16_f32 v139, v214, v215\n\t" \
    "s_bitcmp1_b32 s24, 2\n\t" \
    "s_cbranch_scc1 .Ly36_%=\n\t" \
    "buffer_load_dwordx4 v[148:151], v19, s[4:7], s46 offen offset:1024 sc1\n\t" \
    ".Ly36_%=:\n\t" \
    "s_waitcnt vmcnt(9)\n\t" \
    "v_mfma_f32_16x16x32_bf16 v[104:107], v[156:159], v[108:111], 0\n\t" \
    "ds_write_b128 v40, v[196:199] offset:2064\n\t" \
    "v_cvt_pk_bf16_f32 v136, v32, v33\n\t" \
    "v_mfma_f32_16x16x32_bf16 v[100:103], v[156:159], v[92:95], v[100:103]\n\t" \
    "v_cvt_pk_bf16_f32 v137, v34, v35\n\t" \
    "v_mfma_f32_16x16x32_bf16 v[140:143], v[156:159], v[84:87], 0\n\t" \
    "ds_write_b128 v40, v[136:139] offset:3088\n\t" \
    "s_bitcmp1_b32 s24, 2\n\t" \
    "s_cbranch_scc1 .Ly37_%=\n\t" \
    "buffer_load_dwordx4 v[180:183], v19, s[4:7], s47 offen offset:1024 sc1\n\t" \
    ".Ly37_%=:\n\t" \
    "v_mfma_f32_16x16x32_bf16 v[68:71], v[156:159], v[112:115], v[68:71]\n\t" \
    "s_waitcnt vmcnt(8)\n\t" \
    "v_mfma_f32_16x16x32_bf16 v[56:59], v[28:31], v[172:175], v[56:59]\n\t" \
    "v_mfma_f32_16x16x32_bf16 v[100:103], v[28:31], v[144:147], v[100:103]\n\t" \
    "s_waitcnt lgkmcnt(0)\n\t" \
    "s_barrier\n\t" \
    "s_memrealtime s[18:19]\n\t" \
    "s_setprio 2\n\t" \
    "ds_read_b64_tr_b16 v[12:13], v49 offset:0\n\t" \
    "ds_read_b64_tr_b16 v[14:15], v49 offset:4128\n\t" \
    "ds_read_b64_tr_b16 v[64:65], v49 offset:256\n\t" \
    "ds_read_b64_tr_b16 v[66:67], v49 offset:4384\n\t" \
    "v_mfma_f32_16x16x32_bf16 v[200:203], v[28:31], v[152:155], v[200:203]\n\t" \
    "ds_read_b64_tr_b16 v[72:73], v49 offset:16\n\t" \
    "ds_read_b64_tr_b16 v[74:75], v49 offset:4144\n\t" \
    "ds_read_b64_tr_b16 v[132:133], v49 offset:272\n\t" \
    "ds_read_b64_tr_b16 v[134:135], v49 offset:4400\n\t" \
    "v_mfma_f32_16x16x32_bf16 v[68:71], v[28:31], v[160:163], v[68:71]\n\t" \
    "s_waitcnt vmcnt(7)\n\t" \
    "v_mfma_f32_16x16x32_bf16 v[104:107], v[168:171], v[144:147], v[104:107]\n\t" \
    "v_mfma_f32_16x16x32_bf16 v[100:103], v[168:171], v[172:175], v[100:103]\n\t" \
    "s_waitcnt lgkmcnt(6)\n\t" \
    "v_mfma_f32_16x16x32_bf16 v[236:239], v[12:15], v[204:207], 0\n\t" \
    "s_waitcnt lgkmcnt(4)\n\t" \
    "v_mfma_f32_16x16x32_bf16 v[244:247], v[64:67], v[204:207], 0\n\t" \
    "v_mfma_f32_16x16x32_bf16 v[140:143], v[168:171], v[160:163], v[140:143]\n\t" \
    "s_waitcnt lgkmcnt(2)\n\t" \
    "v_mfma_f32_16x16x32_bf16 v[44:47], v[72:75], v[204:207], 0\n\t" \
    "s_waitcnt lgkmcnt(0)\n\t" \
    "v_mfma_f32_16x16x32_bf16 v[248:251], v[132:135], v[204:207], 0\n\t" \
    "v_mfma_f32_16x16x32_bf16 v[68:71], v[168:171], v[152:155], v[68:71]\n\t" \
    "s_waitcnt vmcnt(6)\n\t" \
    "v_mfma_f32_16x16x32_bf16 v[56:59], v[4:7], v[220:223], v[56:59]\n\t" \
    "v_bfi_b32 v236, s41, v50, v236\n\t" \
    "v_bfi_b32 v244, s41, v50, v244\n\t" \
    "v_cvt_pk_bf16_f32 v193, v238, v239\n\t" \
    "v_mfma_f32_16x16x32_bf16 v[100:103], v[4:7], v[52:55], v[100:103]\n\t" \
    "v_cvt_pk_bf16_f32 v195, v246, v247\n\t" \
    "v_cvt_pk_bf16_f32 v192, v236, v237\n\t" \
    "v_cvt_pk_bf16_f32 v194, v244, v245\n\t" \
    "v_mfma_f32_16x16x32_bf16 v[200:203], v[4:7], v[80:83], v[200:203]\n\t" \
    "v_bfi_b32 v44, s41, v50, v44\n\t" \
    ".Lz8_%=:\n\t" \
    "s_cbranch_execz .Lz9_%=\n\t" \
    "v_bfi_b32 v248, s41, v50, v248\n\t" \
    "buffer_store_dwordx4 v[192:195], v42, s[4:7], s49 offen offset:0 sc1\n\t" \
    "s_add_u32 s37, s52, s33\n\t" \
    "s_buffer_load_dword s58, s[4:7], s37 offset:0x0\n\t" \
    "s_buffer_load_dword s59, s[4:7], s37 offset:0x400\n\t" \
    "s_buffer_load_dword s60, s[4:7], s37 offset:0x8000\n\t" \
    "s_buffer_load_dword s61, s[4:7], s37 offset:0x8400\n\t" \
    "v_mfma_f32_16x16x32_bf16 v[68:71], v[4:7], v[120:123], v[68:71]\n\t" \
    "v_cvt_pk_bf16_f32 v25, v46, v47\n\t" \
    "v_cvt_pk_bf16_f32 v27, v250, v251\n\t" \
    "v_cvt_pk_bf16_f32 v24, v44, v45\n\t" \
    "s_waitcnt vmcnt(6)\n\t" \
    "v_mfma_f32_16x16x32_bf16 v[104:107], v[208:211], v[52:55], v[104:107]\n\t" \
    "v_cvt_pk_bf16_f32 v26, v248, v249\n\t" \
    "buffer_store_dwordx4 v[24:27], v18, s[4:7], s49 offen offset:128 sc1\n\t" \
    "s_setprio 0\n\t" \
    "v_add_u32 v40, 91392, v40\n\t" \
    "ds_read_b128 v[164:167], v10 offset:3072\n\t" \
    "ds_read_b128 v[252:255], v10 offset:4096\n\t" \
    "v_mfma_f32_16x16x32_bf16 v[100:103], v[208:211], v[220:223], v[100:103]\n\t" \
    "s_cmp_lt_u32 s24, 4\n\t" \
    "s_cbranch_scc1 .Lp55_%=\n\t" \
    "s_setprio 1\n\t" \
    ".Lp55_%=:\n\t" \
    "v_mfma_f32_16x16x32_bf16 v[140:143], v[208:211], v[120:123], v[140:143]\n\t" \
    "v_mfma_f32_16x16x32_bf16 v[68:71], v[208:211], v[80:83], v[68:71]\n\t" \
    "s_waitcnt vmcnt(6)\n\t" \
    "v_mfma_f32_16x16x32_bf16 v[212:215], v[76:79], v[96:99], 0\n\t" \
    "v_mfma_f32_16x16x32_bf16 v[108:111], v[76:79], v[124:127], 0\n\t" \
    "v_mfma_f32_16x16x32_bf16 v[196:199], v[76:79], v[116:119], 0\n\t" \
    "v_sub_f32 v56, v56, v104\n\t" \
    "v_sub_f32 v57, v57, v105\n\t" \
    "v_mfma_f32_16x16x32_bf16 v[92:95], v[76:79], v[128:131], 0\n\t" \
    "v_sub_f32 v58, v58, v106\n\t" \
    "v_sub_f32 v59, v59, v107\n\t" \
    "s_waitcnt vmcnt(3)\n\t" \
    "v_mfma_f32_16x16x32_bf16 v[212:215], v[20:23], v[176:179], v[212:215]\n\t" \
    "v_sub_f32 v200, v200, v140\n\t" \
    "v_sub_f32 v201, v201, v141\n\t" \
    "v_mfma_f32_16x16x32_bf16 v[108:111], v[20:23], v[184:187], v[108:111]\n\t" \
    "v_sub_f32 v202, v202, v142\n\t" \
    "v_sub_f32 v203, v203, v143\n\t" \
    "v_mfma_f32_16x16x32_bf16 v[196:199], v[20:23], v[188:191], v[196:199]\n\t" \
    "v_cvt_pk_bf16_f32 v34, v100, v101\n\t" \
    "v_cvt_pk_bf16_f32 v35, v102, v103\n\t" \
    "v_mfma_f32_16x16x32_bf16 v[92:95], v[20:23], v[60:63], v[92:95]\n\t" \
    "v_cvt_pk_bf16_f32 v32, v56, v57\n\t" \
    "v_cvt_pk_bf16_f32 v33, v58, v59\n\t" \
    "s_waitcnt vmcnt(2)\n\t" \
    "v_mfma_f32_16x16x32_bf16 v[212:215], v[88:91], v[224:227], v[212:215]\n\t" \
    "v_cvt_pk_bf16_f32 v86, v68, v69\n\t" \
    "v_cvt_pk_bf16_f32 v87, v70, v71\n\t" \
    "v_mfma_f32_16x16x32_bf16 v[108:111], v[88:91], v[228:231], v[108:111]\n\t" \
    "ds_write_b128 v40, v[32:35] offset:0\n\t" \
    "v_cvt_pk_bf16_f32 v84, v200, v201\n\t" \
    "s_add_u32 s54, s53, 0\n\t" \
    "buffer_load_dwordx4 v[136:139], v0, s[4:7], s54 offen offset:0 sc1\n\t" \
    "s_add_u32 s54, s53, 4243456\n\t" \
    "buffer_load_dwordx4 v[112:115], v0, s[4:7], s54 offen offset:0 sc1\n\t" \
    "v_mfma_f32_16x16x32_bf16 v[196:199], v[88:91], v[232:235], v[196:199]\n\t" \
    "v_cvt_pk_bf16_f32 v85, v202, v203\n\t" \
    "ds_write_b128 v40, v[84:87] offset:1024\n\t" \
    "v_mfma_f32_16x16x32_bf16 v[92:95], v[88:91], v[240:243], v[92:95]\n\t" \
    "s_waitcnt vmcnt(4)\n\t" \
    "s_cmp_ge_u32 s24, 4\n\t" \
    "s_cbranch_scc1 .Lk56_%=\n\t" \
    "v_mfma_f32_16x16x32_bf16 v[156:159], v[216:219], v[124:127], 0\n\t" \
    "v_mfma_f32_16x16x32_bf16 v[156:159], v[148:151], v[184:187], v[156:159]\n\t" \
    "v_mfma_f32_16x16x32_bf16 v[156:159], v[180:183], v[228:231], v[156:159]\n\t" \
    "v_mfma_f32_16x16x32_bf16 v[28:31], v[216:219], v[128:131], 0\n\t" \
    "v_mfma_f32_16x16x32_bf16 v[28:31], v[148:151], v[60:63], v[28:31]\n\t" \
    "v_mfma_f32_16x16x32_bf16 v[28:31], v[180:183], v[240:243], v[28:31]\n\t" \
    "v_mfma_f32_16x16x32_bf16 v[108:111], v[216:219], v[96:99], v[108:111]\n\t" \
    "v_mfma_f32_16x16x32_bf16 v[108:111], v[148:151], v[176:179], v[108:111]\n\t" \
    "v_mfma_f32_16x16x32_bf16 v[108:111], v[180:183], v[224:227], v[108:111]\n\t" \
    "v_mfma_f32_16x16x32_bf16 v[92:95], v[216:219], v[116:119], v[92:95]\n\t" \
    "s_nop 0\n\t" \
    "v_sub_f32 v212, v212, v156\n\t" \
    "v_sub_f32 v213, v213, v157\n\t" \
    "v_mfma_f32_16x16x32_bf16 v[92:95], v[148:151], v[188:191], v[92:95]\n\t" \
    "v_sub_f32 v214, v214, v158\n\t" \
    "v_sub_f32 v215, v215, v159\n\t" \
    "v_mfma_f32_16x16x32_bf16 v[92:95], v[180:183], v[232:235], v[92:95]\n\t" \
    "v_sub_f32 v196, v196, v28\n\t" \
    "v_sub_f32 v197, v197, v29\n\t" \
    "v_sub_f32 v198, v198, v30\n\t" \
    "v_sub_f32 v199, v199, v31\n\t" \
    ".Lk56_%=:\n\t" \
    "v_cvt_pk_bf16_f32 v146, v108, v109\n\t" \
    "v_cvt_pk_bf16_f32 v147, v110, v111\n\t" \
    "v_cvt_pk_bf16_f32 v144, v212, v213\n\t" \
    "v_cvt_pk_bf16_f32 v145, v214, v215\n\t" \
    "s_nop 1\n\t" \
    "v_cvt_pk_bf16_f32 v174, v92, v93\n\t" \
    "v_cvt_pk_bf16_f32 v175, v94, v95\n\t" \
    "ds_write_b128 v40, v[144:147] offset:2064\n\t" \
    "v_cvt_pk_bf16_f32 v172, v196, v197\n\t" \
    "v_cvt_pk_bf16_f32 v173, v198, v199\n\t" \
    "v_add_u32 v49, 91392, v49\n\t" \
    "ds_write_b128 v40, v[172:175] offset:3088\n\t" \
    "s_waitcnt lgkmcnt(0)\n\t" \
    "s_barrier\n\t" \
    "ds_read_b64_tr_b16 v[12:13], v49 offset:0\n\t" \
    "ds_read_b64_tr_b16 v[14:15], v49 offset:4128\n\t" \
    "ds_read_b64_tr_b16 v[64:65], v49 offset:256\n\t" \
    "ds_read_b64_tr_b16 v[66:67], v49 offset:4384\n\t" \
    "ds_read_b64_tr_b16 v[160:161], v49 offset:16\n\t" \
    "ds_read_b64_tr_b16 v[162:163], v49 offset:4144\n\t" \
    "ds_read_b64_tr_b16 v[72:73], v49 offset:272\n\t" \
    "ds_read_b64_tr_b16 v[74:75], v49 offset:4400\n\t" \
    "s_memrealtime s[56:57]\n\t" \
    "s_waitcnt lgkmcnt(6)\n\t" \
    "v_mfma_f32_16x16x32_bf16 v[132:135], v[12:15], v[204:207], 0\n\t" \
    "s_waitcnt lgkmcnt(4)\n\t" \
    "v_mfma_f32_16x16x32_bf16 v[152:155], v[64:67], v[204:207], 0\n\t" \
    "s_waitcnt lgkmcnt(2)\n\t" \
    "v_mfma_f32_16x16x32_bf16 v[168:171], v[160:163], v[204:207], 0\n\t" \
    "s_waitcnt lgkmcnt(0)\n\t" \
    "v_mfma_f32_16x16x32_bf16 v[236:239], v[72:75], v[204:207], 0\n\t" \
    "s_nop 1\n\t" \
    "v_bfi_b32 v132, s41, v50, v132\n\t" \
    "s_nop 0\n\t" \
    "v_bfi_b32 v152, s41, v50, v152\n\t" \
    "v_cvt_pk_bf16_f32 v245, v134, v135\n\t" \
    "v_cvt_pk_bf16_f32 v247, v154, v155\n\t" \
    "v_cvt_pk_bf16_f32 v244, v132, v133\n\t" \
    "v_cvt_pk_bf16_f32 v246, v152, v153\n\t" \
    "v_bfi_b32 v168, s41, v50, v168\n\t" \
    "v_bfi_b32 v236, s41, v50, v236\n\t" \
    "buffer_store_dwordx4 v[244:247], v42, s[4:7], s50 offen offset:0 sc1\n\t" \
    "v_cvt_pk_bf16_f32 v193, v170, v171\n\t" \
    "v_cvt_pk_bf16_f32 v195, v238, v239\n\t" \
    "v_cvt_pk_bf16_f32 v192, v168, v169\n\t" \
    "v_cvt_pk_bf16_f32 v194, v236, v237\n\t" \
    "buffer_store_dwordx4 v[192:195], v18, s[4:7], s50 offen offset:128 sc1\n\t" \
    "s_cmp_ge_u32 s25, 126\n\t" \
    "s_cbranch_scc1 .Lend_%=\n\t" \
    "s_waitcnt lgkmcnt(0)\n\t" \
    "s_sub_u32 s37, s16, 300\n\t" \
    "s_mov_b32 s62, 300\n\t" \
    "s_sub_u32 s39, s58, s37\n\t" \
    "s_cmp_gt_u32 s39, 600\n\t" \
    "s_cselect_b32 s39, 400, s39\n\t" \
    "s_max_u32 s62, s62, s39\n\t" \
    "s_sub_u32 s39, s59, s37\n\t" \
    "s_cmp_gt_u32 s39, 600\n\t" \
    "s_cselect_b32 s39, 400, s39\n\t" \
    "s_max_u32 s62, s62, s39\n\t" \
    "s_sub_u32 s39, s60, s37\n\t" \
    "s_cmp_gt_u32 s39, 600\n\t" \
    "s_cselect_b32 s39, 400, s39\n\t" \
    "s_max_u32 s62, s62, s39\n\t" \
    "s_sub_u32 s39, s61, s37\n\t" \
    "s_cmp_gt_u32 s39, 600\n\t" \
    "s_cselect_b32 s39, 400, s39\n\t" \
    "s_max_u32 s62, s62, s39\n\t" \
    "s_sub_u32 s62, s62, 300\n\t" \
    "s_mul_i32 s62, s62, 5\n\t" \
    "s_lshr_b32 s62, s62, 2\n\t" \
    "s_waitcnt vmcnt(2)\n\t" \
    "v_and_b32 v41, v136, v138\n\t" \
    "v_bitop3_b32 v41, v41, v112, v114 bitop3:0x80\n\t" \
    "v_and_b32 v41, 1, v41\n\t" \
    "v_cmp_eq_u32 vcc, 0, v41\n\t" \
    "s_nop 1\n\t" \
    "s_cbranch_vccnz .Ls50_%=\n\t" \
    ".Lb50_%=:\n\t" \
    "ds_write_b128 v48, v[136:139] offset:33024\n\t" \
    "ds_write_b128 v48, v[112:115] offset:33152\n\t" \
    "s_add_u32 s37, s62, 125\n\t" \
    ".Lh57_%=:\n\t" \
    "s_memrealtime s[22:23]\n\t" \
    "s_waitcnt lgkmcnt(0)\n\t" \
    "s_sub_u32 s22, s22, s18\n\t" \
    "s_cmp_lt_u32 s22, s37\n\t" \
    "s_cbranch_scc1 .Lh57_%=\n\t" \
    "s_add_u32 s54, s53, 524288\n\t" \
    "buffer_load_dwordx4 v[4:7], v0, s[4:7], s54 offen offset:0 sc1\n\t" \
    "s_add_u32 s54, s53, 4767744\n\t" \
    "buffer_load_dwordx4 v[44:47], v0, s[4:7], s54 offen offset:0 sc1\n\t" \
    "s_waitcnt lgkmcnt(0)\n\t" \
    "s_barrier\n\t" \
    "ds_read_b64_tr_b16 v[52:53], v43 offset:33024\n\t" \
    "ds_read_b64_tr_b16 v[54:55], v43 offset:33280\n\t" \
    "ds_read_b64_tr_b16 v[248:249], v43 offset:33032\n\t" \
    "ds_read_b64_tr_b16 v[250:251], v43 offset:33288\n\t" \
    "s_waitcnt lgkmcnt(2)\n\t" \
    "v_mfma_f32_16x16x32_bf16 v[24:27], v[52:55], v[164:167], 0\n\t" \
    "s_waitcnt lgkmcnt(0)\n\t" \
    "v_mfma_f32_16x16x32_bf16 v[8:11], v[248:251], v[164:167], 0\n\t" \
    "v_mfma_f32_16x16x32_bf16 v[220:223], v[52:55], v[252:255], 0\n\t" \
    "v_mfma_f32_16x16x32_bf16 v[120:123], v[248:251], v[252:255], 0\n\t" \
    "s_nop 3\n\t" \
    "v_cvt_pk_bf16_f32 v80, v24, v25\n\t" \
    "v_cvt_pk_bf16_f32 v81, v26, v27\n\t" \
    "v_cvt_pk_bf16_f32 v82, v8, v9\n\t" \
    "v_cvt_pk_bf16_f32 v83, v10, v11\n\t" \
    "buffer_store_dwordx4 v[80:83], v38, s[8:11], 0 offen offset:0 sc1\n\t" \
    "v_cvt_pk_bf16_f32 v208, v220, v221\n\t" \
    "v_cvt_pk_bf16_f32 v209, v222, v223\n\t" \
    "v_cvt_pk_bf16_f32 v210, v120, v121\n\t" \
    "v_cvt_pk_bf16_f32 v211, v122, v123\n\t" \
    "buffer_store_dwordx4 v[208:211], v38, s[8:11], 0 offen offset:3072 sc1\n\t" \
    "s_waitcnt vmcnt(2)\n\t" \
    "v_and_b32 v39, v4, v6\n\t" \
    "v_bitop3_b32 v39, v39, v44, v46 bitop3:0x80\n\t" \
    "v_and_b32 v39, 1, v39\n\t" \
    "v_cmp_eq_u32 vcc, 0, v39\n\t" \
    "s_nop 1\n\t" \
    "s_cbranch_vccnz .Ls51_%=\n\t" \
    ".Lb51_%=:\n\t" \
    "ds_write_b128 v48, v[4:7] offset:0\n\t" \
    "ds_write_b128 v48, v[44:47] offset:128\n\t" \
    "s_add_u32 s37, s62, 100\n\t" \
    ".Lh58_%=:\n\t" \
    "s_memrealtime s[22:23]\n\t" \
    "s_waitcnt lgkmcnt(0)\n\t" \
    "s_sub_u32 s22, s22, s56\n\t" \
    "s_cmp_lt_u32 s22, s37\n\t" \
    "s_cbranch_scc1 .Lh58_%=\n\t" \
    "s_add_u32 s54, s53, 1048576\n\t" \
    "buffer_load_dwordx4 v[76:79], v0, s[4:7], s54 offen offset:0 sc1\n\t" \
    "s_add_u32 s54, s53, 5292032\n\t" \
    "buffer_load_dwordx4 v[104:107], v0, s[4:7], s54 offen offset:0 sc1\n\t" \
    "s_waitcnt lgkmcnt(0)\n\t" \
    "s_barrier\n\t" \
    "ds_read_b64_tr_b16 v[140:141], v43 offset:0\n\t" \
    "ds_read_b64_tr_b16 v[142:143], v43 offset:256\n\t" \
    "ds_read_b64_tr_b16 v[100:101], v43 offset:8\n\t" \
    "ds_read_b64_tr_b16 v[102:103], v43 offset:264\n\t" \
    "s_waitcnt lgkmcnt(2)\n\t" \
    "v_mfma_f32_16x16x32_bf16 v[20:23], v[140:143], v[164:167], 0\n\t" \
    "s_waitcnt lgkmcnt(0)\n\t" \
    "v_mfma_f32_16x16x32_bf16 v[56:59], v[100:103], v[164:167], 0\n\t" \
    "v_mfma_f32_16x16x32_bf16 v[68:71], v[140:143], v[252:255], 0\n\t" \
    "v_mfma_f32_16x16x32_bf16 v[32:35], v[100:103], v[252:255], 0\n\t" \
    "s_nop 3\n\t" \
    "v_cvt_pk_bf16_f32 v200, v20, v21\n\t" \
    "v_cvt_pk_bf16_f32 v201, v22, v23\n\t" \
    "v_cvt_pk_bf16_f32 v202, v56, v57\n\t" \
    "v_cvt_pk_bf16_f32 v203, v58, v59\n\t" \
    "buffer_store_dwordx4 v[200:203], v38, s[8:11], 0 offen offset:64 sc1\n\t" \
    "v_cvt_pk_bf16_f32 v84, v68, v69\n\t" \
    "v_cvt_pk_bf16_f32 v85, v70, v71\n\t" \
    "v_cvt_pk_bf16_f32 v86, v32, v33\n\t" \
    "v_cvt_pk_bf16_f32 v87, v34, v35\n\t" \
    "buffer_store_dwordx4 v[84:87], v38, s[8:11], 0 offen offset:3136 sc1\n\t" \
    "s_waitcnt vmcnt(2)\n\t" \
    "v_and_b32 v37, v76, v78\n\t" \
    "v_bitop3_b32 v37, v37, v104, v106 bitop3:0x80\n\t" \
    "v_and_b32 v37, 1, v37\n\t" \
    "v_cmp_eq_u32 vcc, 0, v37\n\t" \
    "s_nop 1\n\t" \
    "s_cbranch_vccnz .Ls52_%=\n\t" \
    ".Lb52_%=:\n\t" \
    "ds_write_b128 v48, v[76:79] offset:16512\n\t" \
    "ds_write_b128 v48, v[104:107] offset:16640\n\t" \
    "s_waitcnt lgkmcnt(0)\n\t" \
    "s_barrier\n\t" \
    "ds_read_b64_tr_b16 v[88:89], v43 offset:16512\n\t" \
    "ds_read_b64_tr_b16 v[90:91], v43 offset:16768\n\t" \
    "ds_read_b64_tr_b16 v[124:125], v43 offset:16520\n\t" \
    "ds_read_b64_tr_b16 v[126:127], v43 offset:16776\n\t" \
    "s_waitcnt lgkmcnt(2)\n\t" \
    "v_mfma_f32_16x16x32_bf16 v[184:187], v[88:91], v[164:167], 0\n\t" \
    "s_waitcnt lgkmcnt(0)\n\t" \
    "v_mfma_f32_16x16x32_bf16 v[228:231], v[124:127], v[164:167], 0\n\t" \
    "v_mfma_f32_16x16x32_bf16 v[128:131], v[88:91], v[252:255], 0\n\t" \
    "v_mfma_f32_16x16x32_bf16 v[60:63], v[124:127], v[252:255], 0\n\t" \
    "s_nop 3\n\t" \
    "v_cvt_pk_bf16_f32 v240, v184, v185\n\t" \
    "v_cvt_pk_bf16_f32 v241, v186, v187\n\t" \
    "v_cvt_pk_bf16_f32 v242, v228, v229\n\t" \
    "v_cvt_pk_bf16_f32 v243, v230, v231\n\t" \
    "buffer_store_dwordx4 v[240:243], v38, s[8:11], 0 offen offset:128 sc1\n\t" \
    "v_cvt_pk_bf16_f32 v96, v128, v129\n\t" \
    "v_cvt_pk_bf16_f32 v97, v130, v131\n\t" \
    "v_cvt_pk_bf16_f32 v98, v60, v61\n\t" \
    "v_cvt_pk_bf16_f32 v99, v62, v63\n\t" \
    "buffer_store_dwordx4 v[96:99], v38, s[8:11], 0 offen offset:3200 sc1\n\t" \
    "s_or_b32 s37, s2, s24\n\t" \
    "s_cbranch_scc1 .Lend_%=\n\t" \
    "s_mov_b32 s37, 34668544\n\t" \
    "v_mov_b32 v1, s34\n\t" \
    "s_mov_b64 exec, 1\n\t" \
    "buffer_store_dword v1, v0, s[4:7], s37 offen sc1\n\t" \
    "s_mov_b64 exec, -1\n\t" \
    ".Lend_%=:\n\t" \
    "s_endpgm\n\t" \
    ".Lq1028_%=:\n\t" \
    "s_mov_b32 s40, 0\n\t" \
    ".Lr1028_%=:\n\t" \
    "s_add_u32 s12, s55, 34603008\n\t" \
    "s_buffer_load_dword s13, s[4:7], s12 glc\n\t" \
    "s_buffer_load_dword s14, s[4:7], s12 offset:0x400 glc\n\t" \
    "s_waitcnt lgkmcnt(0)\n\t" \
    "s_xor_b32 s13, s13, s34\n\t" \
    "s_xor_b32 s14, s14, s34\n\t" \
    "s_or_b32 s13, s13, s14\n\t" \
    "s_cbranch_scc0 .Lb27_%=\n\t" \
    "s_add_u32 s40, s40, 1\n\t" \
    "s_cmp_lt_u32 s40, 1\n\t" \
    "s_cbranch_scc1 .Lr1028_%=\n\t" \
    "s_branch .Ls27_%=\n\t" \
    ".Lq1025_%=:\n\t" \
    "s_mov_b32 s40, 0\n\t" \
    ".Lr1025_%=:\n\t" \
    "s_nop 0\n\t" \
    "v_readlane_b32 s12, v11, 0\n\t" \
    "v_readlane_b32 s13, v11, 1\n\t" \
    "v_readlane_b32 s14, v11, 2\n\t" \
    "v_readlane_b32 s15, v11, 3\n\t" \
    "v_readlane_b32 s16, v11, 4\n\t" \
    "v_readlane_b32 s17, v11, 5\n\t" \
    "v_readlane_b32 s18, v11, 6\n\t" \
    "v_readlane_b32 s19, v11, 7\n\t" \
    "v_readlane_b32 s3, v11, 8\n\t" \
    "s_add_u32 s12, s12, s33\n\t" \
    "s_add_u32 s13, s13, s33\n\t" \
    "s_add_u32 s14, s14, s33\n\t" \
    "s_add_u32 s15, s15, s33\n\t" \
    "s_add_u32 s16, s16, s33\n\t" \
    "s_add_u32 s17, s17, s33\n\t" \
    "s_add_u32 s18, s18, s33\n\t" \
    "s_add_u32 s19, s19, s33\n\t" \
    "s_add_u32 s3, s3, s33\n\t" \
    "s_buffer_load_dword s12, s[4:7], s12 offset:0xc0000 glc\n\t" \
    "s_buffer_load_dword s13, s[4:7], s13 offset:0xc0000 glc\n\t" \
    "s_buffer_load_dword s14, s[4:7], s14 offset:0xc0000 glc\n\t" \
    "s_buffer_load_dword s15, s[4:7], s15 offset:0xc0000 glc\n\t" \
    "s_buffer_load_dword s16, s[4:7], s16 offset:0xc0000 glc\n\t" \
    "s_buffer_load_dword s17, s[4:7], s17 offset:0xc0000 glc\n\t" \
    "s_buffer_load_dword s18, s[4:7], s18 offset:0xc0000 glc\n\t" \
    "s_buffer_load_dword s19, s[4:7], s19 offset:0xc0000 glc\n\t" \
    "s_buffer_load_dword s3, s[4:7], s3 offset:0xc0000 glc\n\t" \
    "s_waitcnt lgkmcnt(0)\n\t" \
    "s_xor_b32 s12, s12, s34\n\t" \
    "s_xor_b32 s13, s13, s34\n\t" \
    "s_xor_b32 s14, s14, s34\n\t" \
    "s_xor_b32 s15, s15, s34\n\t" \
    "s_xor_b32 s16, s16, s34\n\t" \
    "s_xor_b32 s17, s17, s34\n\t" \
    "s_xor_b32 s18, s18, s34\n\t" \
    "s_xor_b32 s19, s19, s34\n\t" \
    "s_xor_b32 s3, s3, s34\n\t" \
    "s_or_b32 s12, s12, s13\n\t" \
    "s_or_b32 s12, s12, s14\n\t" \
    "s_or_b32 s12, s12, s15\n\t" \
    "s_or_b32 s12, s12, s16\n\t" \
    "s_or_b32 s12, s12, s17\n\t" \
    "s_or_b32 s12, s12, s18\n\t" \
    "s_or_b32 s12, s12, s19\n\t" \
    "s_or_b32 s12, s12, s3\n\t" \
    "s_cbranch_scc0 .Lb24_%=\n\t" \
    "s_add_u32 s40, s40, 1\n\t" \
    "s_cmp_lt_u32 s40, 1\n\t" \
    "s_cbranch_scc1 .Lr1025_%=\n\t" \
    "s_branch .Ls24_%=\n\t" \
    ".Lqa_%=:\n\t" \
    "s_sub_u32 s37, s24, 4\n\t" \
    "s_bfe_u32 s38, s2, 0x20002\n\t" \
    "s_lshl1_add_u32 s38, s38, s37\n\t" \
    "s_lshl_b32 s42, s38, 21\n\t" \
    "s_cmp_ge_u32 s38, 7\n\t" \
    "s_cbranch_scc1 .Lqd_%=\n\t" \
    "s_cmp_ge_u32 s37, 2\n\t" \
    "s_cbranch_scc1 .Lqd_%=\n\t" \
    "s_memtime s[62:63]\n\t" \
    "s_waitcnt lgkmcnt(0)\n\t" \
    "s_sleep 4\n\t" \
    "s_lshl_b32 s3, s38, 10\n\t" \
    "s_and_b32 s37, s8, 0x1fffff\n\t" \
    "s_add_u32 s38, s37, 16383\n\t" \
    "s_andn2_b32 s38, s38, 16383\n\t" \
    "s_add_u32 s39, s37, 9617408\n\t" \
    "s_andn2_b32 s39, s39, 16383\n\t" \
    "s_max_u32 s42, s42, s38\n\t" \
    "s_min_u32 s42, s42, s39\n\t" \
    "s_sub_u32 s42, s42, s37\n\t" \
    "s_cmp_eq_u32 s2, 255\n\t" \
    "s_cselect_b64 s[16:17], s[4:5], s[8:9]\n\t" \
    "s_cselect_b32 s42, 34684928, s42\n\t" \
    "s_add_u32 s16, s16, s42\n\t" \
    "s_addc_u32 s17, s17, 0\n\t" \
    "s_mov_b32 s18, 16384\n\t" \
    "s_mov_b32 s19, 0x20000\n\t" \
    "s_add_u32 s37, s3, 0x2000\n\t" \
    "buffer_store_dwordx4 v[20:23], v0, s[16:19], s3 offen sc1\n\t" \
    "buffer_store_dwordx4 v[20:23], v0, s[16:19], s37 offen sc1\n\t" \
    "s_nop 0\n\t" \
    "s_mov_b32 s39, 34668544\n\t" \
    "v_mov_b32 v20, 0\n\t" \
    "buffer_load_dword v21, v20, s[4:7], s39 offen sc1\n\t" \
    "s_mov_b32 s60, -1\n\t" \
    "s_mov_b32 s61, -1\n\t" \
    "s_mov_b32 s40, 0\n\t" \
    "s_waitcnt vmcnt(1) lgkmcnt(0)\n\t" \
    "s_memtime s[58:59]\n\t" \
    "s_waitcnt lgkmcnt(0)\n\t" \
    ".Lqc_%=:\n\t" \
    "s_buffer_load_dword s38, s[16:19], s3\n\t" \
    "s_mov_b32 s56, s58\n\t" \
    "s_xor_b32 s3, s3, 0x2000\n\t" \
    "s_waitcnt lgkmcnt(0)\n\t" \
    "s_memtime s[58:59]\n\t" \
    "s_add_u32 s40, s40, 1\n\t" \
    "s_waitcnt lgkmcnt(0)\n\t" \
    "s_sub_u32 s56, s58, s56\n\t" \
    "s_min_u32 s56, s56, s60\n\t" \
    "s_mov_b32 s60, s61\n\t" \
    "s_mov_b32 s61, s56\n\t" \
    "s_bitcmp1_b32 s40, 0\n\t" \
    "s_cbranch_scc1 .Lqc_%=\n\t" \
    "s_add_u32 s3, s3, 0x80\n\t" \
    "s_sub_u32 s37, s58, s62\n\t" \
    "s_cmp_gt_u32 s37, 5200\n\t" \
    "s_cselect_b32 s40, 98, s40\n\t" \
    "s_absdiff_i32 s37, s60, s61\n\t" \
    "s_min_u32 s38, s60, s61\n\t" \
    "s_cmp_gt_u32 s38, 500\n\t" \
    "s_cselect_b32 s37, 0, s37\n\t" \
    "s_cmp_lt_u32 s37, 80\n\t" \
    "s_cselect_b32 s37, 16, 4\n\t" \
    "s_cmp_lt_u32 s40, s37\n\t" \
    "s_cbranch_scc1 .Lqc_%=\n\t" \
    "s_waitcnt vmcnt(0)\n\t" \
    "v_readfirstlane_b32 s34, v21\n\t" \
    "s_cmp_gt_u32 s60, s61\n\t" \
    "s_cselect_b32 s0, 2, 0\n\t" \
    "s_mul_i32 s34, s34, 1664525\n\t" \
    "s_add_u32 s34, s34, 1013904223\n\t" \
    "s_or_b32 s34, s34, 1\n\t" \
    "s_cmp_eq_u32 s2, 255\n\t" \
    "s_cbranch_scc1 .Lqf_%=\n\t" \
    "s_xor_b32 s37, s0, s34\n\t" \
    "v_mov_b32 v21, s37\n\t" \
    "s_bfe_u32 s37, s2, 0x20002\n\t" \
    "s_lshl1_add_u32 s37, s37, s24\n\t" \
    "s_lshl_b32 s37, s37, 7\n\t" \
    "s_add_u32 s37, s37, 34670080\n\t" \
    "v_lshrrev_b32 v20, 2, v0\n\t" \
    "s_mov_b32 exec_hi, 0\n\t" \
    "buffer_store_dword v21, v20, s[4:7], s37 offen sc1\n\t" \
    "s_mov_b64 exec, -1\n\t" \
    ".Lqg_%=:\n\t" \
    "s_branch .Lqd_%=\n\t" \
    ".Lqf_%=:\n\t" \
    "s_getreg_b32 s37, hwreg(HW_REG_XCC_ID, 0, 2)\n\t" \
    "s_lshl1_add_u32 s37, s0, s37\n\t" \
    "s_add_u32 s37, s37, 1\n\t" \
    "s_and_b32 s37, s37, 7\n\t" \
    "s_lshl_b32 s37, s37, 1\n\t" \
    "s_xor_b32 s37, s37, s34\n\t" \
    "v_mov_b32 v20, s37\n\t" \
    "v_mov_b32 v21, s37\n\t" \
    "v_mov_b32 v22, s37\n\t" \
    "v_mov_b32 v23, s37\n\t" \
    "s_mov_b32 s39, 34669568\n\t" \
    "buffer_store_dwordx4 v[20:23], v0, s[4:7], s39 offen sc1\n\t" \
    "s_nop 0\n\t" \
    "s_branch .Lqg_%=\n\t" \
    ".Lr2_%=:\n\t" \
    "s_add_u32 s40, s40, 1\n\t" \
    "s_cmp_gt_u32 s40, 32\n\t" \
    "s_cbranch_scc1 .Lr3_%=\n\t" \
    "s_sleep 2\n\t" \
    "s_and_b32 s37, s2, 7\n\t" \
    "s_lshl_b32 s37, s37, 10\n\t" \
    "s_add_u32 s37, s37, 34676736\n\t" \
    "v_and_b32 v51, 0x70, v0\n\t" \
    "v_lshlrev_b32 v51, 3, v51\n\t" \
    "buffer_load_dword v50, v51, s[4:7], s37 offen sc1\n\t" \
    "s_waitcnt vmcnt(0)\n\t" \
    "v_readlane_b32 s56, v50, 0\n\t" \
    "v_readlane_b32 s57, v50, 1\n\t" \
    "v_readlane_b32 s58, v50, 2\n\t" \
    "v_readlane_b32 s59, v50, 3\n\t" \
    "v_readlane_b32 s60, v50, 4\n\t" \
    "v_readlane_b32 s61, v50, 5\n\t" \
    "v_readlane_b32 s62, v50, 6\n\t" \
    "v_readlane_b32 s63, v50, 7\n\t" \
    "s_branch .Lr1_%=\n\t" \
    ".Lq5_%=:\n\t" \
    "s_add_u32 s40, s40, 1\n\t" \
    "s_cmp_gt_u32 s40, 65536\n\t" \
    "s_cbranch_scc1 .Lq6_%=\n\t" \
    "s_sleep 4\n\t" \
    "v_and_b32 v16, 0x70, v0\n\t" \
    "v_lshlrev_b32 v16, 3, v16\n\t" \
    "s_mov_b32 s37, 34670592\n\t" \
    "buffer_load_dword v8, v16, s[4:7], s37 offen sc1\n\t" \
    "s_waitcnt vmcnt(0)\n\t" \
    "v_readlane_b32 s56, v8, 0\n\t" \
    "v_readlane_b32 s57, v8, 1\n\t" \
    "v_readlane_b32 s58, v8, 2\n\t" \
    "v_readlane_b32 s59, v8, 3\n\t" \
    "v_readlane_b32 s60, v8, 4\n\t" \
    "v_readlane_b32 s61, v8, 5\n\t" \
    "s_branch .Lq4_%=\n\t" \
    ".Lr5_%=:\n\t" \
    "s_add_u32 s40, s40, 1\n\t" \
    "s_cmp_gt_u32 s40, 65536\n\t" \
    "s_cbranch_scc1 .Lr6_%=\n\t" \
    "s_sleep 4\n\t" \
    "v_mov_b32 v16, 0\n\t" \
    "buffer_load_dword v8, v16, s[4:7], s52 offen sc1\n\t" \
    "s_waitcnt vmcnt(0)\n\t" \
    "v_readfirstlane_b32 s36, v8\n\t" \
    "s_branch .Lr4_%=\n\t" \
    ".Ls24_%=:\n\t" \
    "s_mov_b32 s40, 0\n\t" \
    "s_add_u32 s38, s33, 786432\n\t" \
    ".Ll24_%=:\n\t" \
    "s_sleep 2\n\t" \
    "buffer_load_dword v1, v11, s[4:7], s38 offen sc1\n\t" \
    "s_waitcnt vmcnt(0)\n\t" \
    "v_cmp_ne_u32 vcc, s34, v1\n\t" \
    "s_add_u32 s40, s40, 1\n\t" \
    "s_cmp_ge_u32 s40, 65536\n\t" \
    "s_cbranch_scc1 .Lb24_%=\n\t" \
    "s_cbranch_vccnz .Ll24_%=\n\t" \
    "s_branch .Lb24_%=\n\t" \
    ".Ls27_%=:\n\t" \
    "s_mov_b32 s40, 0\n\t" \
    "s_add_u32 s38, s55, 34603008\n\t" \
    ".Ll27_%=:\n\t" \
    "s_sleep 2\n\t" \
    "buffer_load_dword v36, v47, s[4:7], s38 offen sc1\n\t" \
    "s_waitcnt vmcnt(0)\n\t" \
    "v_cmp_ne_u32 vcc, s34, v36\n\t" \
    "s_add_u32 s40, s40, 1\n\t" \
    "s_cmp_ge_u32 s40, 65536\n\t" \
    "s_cbranch_scc1 .Lb27_%=\n\t" \
    "s_cbranch_vccnz .Ll27_%=\n\t" \
    "s_branch .Lb27_%=\n\t" \
    ".Ls50_%=:\n\t" \
    "s_mov_b32 s40, 0\n\t" \
    ".Ll50_%=:\n\t" \
    "s_add_u32 s54, s53, 0\n\t" \
    "buffer_load_dwordx4 v[136:139], v0, s[4:7], s54 offen offset:0 sc1\n\t" \
    "s_add_u32 s54, s53, 4243456\n\t" \
    "buffer_load_dwordx4 v[112:115], v0, s[4:7], s54 offen offset:0 sc1\n\t" \
    "s_waitcnt vmcnt(0)\n\t" \
    "v_and_b32 v41, v136, v138\n\t" \
    "v_bitop3_b32 v41, v41, v112, v114 bitop3:0x80\n\t" \
    "v_and_b32 v41, 1, v41\n\t" \
    "v_cmp_eq_u32 vcc, 0, v41\n\t" \
    "s_add_u32 s40, s40, 1\n\t" \
    "s_cmp_ge_u32 s40, 65536\n\t" \
    "s_cbranch_scc1 .Lb50_%=\n\t" \
    "s_cbranch_vccnz .Ll50_%=\n\t" \
    "s_branch .Lb50_%=\n\t" \
    ".Ls51_%=:\n\t" \
    "s_mov_b32 s40, 0\n\t" \
    ".Ll51_%=:\n\t" \
    "s_add_u32 s54, s53, 524288\n\t" \
    "buffer_load_dwordx4 v[4:7], v0, s[4:7], s54 offen offset:0 sc1\n\t" \
    "s_add_u32 s54, s53, 4767744\n\t" \
    "buffer_load_dwordx4 v[44:47], v0, s[4:7], s54 offen offset:0 sc1\n\t" \
    "s_waitcnt vmcnt(0)\n\t" \
    "v_and_b32 v39, v4, v6\n\t" \
    "v_bitop3_b32 v39, v39, v44, v46 bitop3:0x80\n\t" \
    "v_and_b32 v39, 1, v39\n\t" \
    "v_cmp_eq_u32 vcc, 0, v39\n\t" \
    "s_add_u32 s40, s40, 1\n\t" \
    "s_cmp_ge_u32 s40, 65536\n\t" \
    "s_cbranch_scc1 .Lb51_%=\n\t" \
    "s_cbranch_vccnz .Ll51_%=\n\t" \
    "s_branch .Lb51_%=\n\t" \
    ".Lz9_%=:\n\t" \
    "s_cbranch_execz .Lwk_%=\n\t" \
    ".Ls52_%=:\n\t" \
    "s_mov_b32 s40, 0\n\t" \
    ".Ll52_%=:\n\t" \
    "s_add_u32 s54, s53, 1048576\n\t" \
    "buffer_load_dwordx4 v[76:79], v0, s[4:7], s54 offen offset:0 sc1\n\t" \
    "s_add_u32 s54, s53, 5292032\n\t" \
    "buffer_load_dwordx4 v[104:107], v0, s[4:7], s54 offen offset:0 sc1\n\t" \
    "s_waitcnt vmcnt(0)\n\t" \
    "v_and_b32 v37, v76, v78\n\t" \
    "v_bitop3_b32 v37, v37, v104, v106 bitop3:0x80\n\t" \
    "v_and_b32 v37, 1, v37\n\t" \
    "v_cmp_eq_u32 vcc, 0, v37\n\t" \
    "s_add_u32 s40, s40, 1\n\t" \
    "s_cmp_ge_u32 s40, 65536\n\t" \
    "s_cbranch_scc1 .Lb52_%=\n\t" \
    "s_cbranch_vccnz .Ll52_%=\n\t" \
    "s_branch .Lb52_%=\n\t" \
    ".Lfin_%=:\n\t"
#define K_CLOBBERS "v1", "v2", "v3", "v4", "v5", "v6", "v7", "v8", "v9", "v10", "v11", "v12", "v13", "v14", "v15", "v16", "v17", "v18", "v19", "v20", "v21", "v22", "v23", "v24", "v25", "v26", "v27", "v28", "v29", "v30", "v31", "v32", "v33", "v34", "v35", "v36", "v37", "v38", "v39", "v40", "v41", "v42", "v43", "v44", "v45", "v46", "v47", "v48", "v49", "v50", "v51", "v52", "v53", "v54", "v55", "v56", "v57", "v58", "v59", "v60", "v61", "v62", "v63", "v64", "v65", "v66", "v67", "v68", "v69", "v70", "v71", "v72", "v73", "v74", "v75", "v76", "v77", "v78", "v79", "v80", "v81", "v82", "v83", "v84", "v85", "v86", "v87", "v88", "v89", "v90", "v91", "v92", "v93", "v94", "v95", "v96", "v97", "v98", "v99", "v100", "v101", "v102", "v103", "v104", "v105", "v106", "v107", "v108", "v109", "v110", "v111", "v112", "v113", "v114", "v115", "v116", "v117", "v118", "v119", "v120", "v121", "v122", "v123", "v124", "v125", "v126", "v127", "v128", "v129", "v130", "v131", "v132", "v133", "v134", "v135", "v136", "v137", "v138", "v139", "v140", "v141", "v142", "v143", "v144", "v145", "v146", "v147", "v148", "v149", "v150", "v151", "v152", "v153", "v154", "v155", "v156", "v157", "v158", "v159", "v160", "v161", "v162", "v163", "v164", "v165", "v166", "v167", "v168", "v169", "v170", "v171", "v172", "v173", "v174", "v175", "v176", "v177", "v178", "v179", "v180", "v181", "v182", "v183", "v184", "v185", "v186", "v187", "v188", "v189", "v190", "v191", "v192", "v193", "v194", "v195", "v196", "v197", "v198", "v199", "v200", "v201", "v202", "v203", "v204", "v205", "v206", "v207", "v208", "v209", "v210", "v211", "v212", "v213", "v214", "v215", "v216", "v217", "v218", "v219", "v220", "v221", "v222", "v223", "v224", "v225", "v226", "v227", "v228", "v229", "v230", "v231", "v232", "v233", "v234", "v235", "v236", "v237", "v238", "v239", "v240", "v241", "v242", "v243", "v244", "v245", "v246", "v247", "v248", "v249", "v250", "v251", "v252", "v253", "v254", "v255", "s3", "s4", "s5", "s6", "s7", "s8", "s9", "s10", "s11", "s12", "s13", "s14", "s15", "s16", "s17", "s18", "s19", "s20", "s21", "s22", "s23", "s24", "s25", "s26", "s27", "s28", "s29", "s30", "s31", "s32", "s33", "s34", "s35", "s36", "s37", "s38", "s39", "s40", "s41", "s42", "s43", "s44", "s45", "s46", "s47", "s48", "s49", "s50", "s51", "s52", "s53", "s54", "s55", "s56", "s57", "s58", "s59", "s60", "s61", "s62", "s63", "s64", "s65"

extern "C" __global__ __launch_bounds__(512, 2)
void k_conv(const char* __restrict__ x, const char* __restrict__ wgt, char* __restrict__ ws, char* __restrict__ out)
{
  __shared__ __attribute__((aligned(256))) char smem[157568];
  unsigned tid = threadIdx.x;
  auto kp = __builtin_amdgcn_kernarg_segment_ptr();
  asm volatile(K_BODY : "+{v0}"(tid), "+{s[0:1]}"(kp)
               : "{s2}"(blockIdx.x), "i"((__attribute__((address_space(3))) char*)smem)
               : "memory", "scc", "vcc", K_CLOBBERS);
}

extern "C" void kernel_launch(void* const* d_in, const int* in_sizes, int n_in,
                              void* d_out, int out_size, void* d_ws, size_t ws_size,
                              hipStream_t stream) {
  k_conv<<<dim3(256), dim3(512), 0, stream>>>((const char*)d_in[0], (const char*)d_in[1], (char*)d_ws, (char*)d_out);
}
```
